```python
import torch

def _softmax(x, axis=-1):
    return torch.softmax(x, dim=axis)
def _gelu(x):
    return torch.nn.functional.gelu(x, approximate="tanh")
def _silu(x):
    return torch.nn.functional.silu(x)
def _sigmoid(x):
    return torch.sigmoid(x)
def _layernorm(x, axis=-1, eps=1e-5):
    m = x.mean(dim=axis, keepdim=True)
    v = x.var(dim=axis, keepdim=True, unbiased=False)
    return (x - m) / torch.sqrt(v + eps)
def _scatter_add(tgt, idx, src, axis=0):
    return tgt.clone().index_add_(axis, idx, src)
def _segment_sum(src, seg, n):
    out = torch.zeros((n,) + src.shape[1:], dtype=src.dtype)
    return out.index_add_(0, seg, src)
def _pad(x, pw):
    flat = tuple(v for pair in reversed(pw) for v in pair)
    return torch.nn.functional.pad(x, flat)

kernel_name = 'sequential_preLN__softmax_full_mha__swiglu_ffn__prompt'

def setup_inputs():

    _w = (lambda a: (lambda t: t.to(torch.bfloat16) if t.is_floating_point() else t)(torch.as_tensor(a)))
    def _A_setup():
        B, S, D = 1, 2048, 576
        return {
            "x": _w((lambda *s: torch.randn(*s, dtype=torch.bfloat16) * 0.05)(B, S, D)),
            "wqkv": _w((lambda *s: torch.randn(*s, dtype=torch.bfloat16) * 0.05)(D, 3 * D)),
            "wo": _w((lambda *s: torch.randn(*s, dtype=torch.bfloat16) * 0.05)(D, D)),
        }
    _A_in = _A_setup()

    def _B_setup():
        B, S, D, F = 1, 2048, 576, 4608
        return {
            "x": _w((lambda *s: torch.randn(*s, dtype=torch.bfloat16) * 0.05)(B, S, D)),
            "w_gate": _w((lambda *s: torch.randn(*s, dtype=torch.bfloat16) * 0.05)(D, F)),
            "w_up": _w((lambda *s: torch.randn(*s, dtype=torch.bfloat16) * 0.05)(D, F)),
            "w_down": _w((lambda *s: torch.randn(*s, dtype=torch.bfloat16) * 0.05)(F, D)),
        }
    _B_in = _B_setup()

    return {
        "A_x": _A_in["x"],
        "A_wqkv": _A_in["wqkv"],
        "A_wo": _A_in["wo"],
        "B_x": _B_in["x"],
        "B_w_gate": _B_in["w_gate"],
        "B_w_up": _B_in["w_up"],
        "B_w_down": _B_in["w_down"],
    }

def reference(A_x, A_wqkv, A_wo, B_x, B_w_gate, B_w_up, B_w_down):
    def _A_ref(x, wqkv, wo):
        B, S, D = x.shape
        H = 32; Dh = D // H
        qkv = (lambda x, *d: x.permute(*d))((x @ wqkv).reshape(B, S, 3, H, Dh), 2, 0, 3, 1, 4)
        q, k, v = qkv[0], qkv[1], qkv[2]
        scale = Dh ** -0.5
        scores = torch.einsum("bhqd,bhkd->bhqk", q, k) * scale
        p = _softmax(scores, axis=-1)
        o = torch.einsum("bhqk,bhkd->bhqd", p, v)
        return (lambda x, *d: x.permute(*d))(o, 0, 2, 1, 3).reshape(B, S, D) @ wo

    def _B_ref(x, w_gate, w_up, w_down):
        g = _silu(x @ w_gate)
        u = x @ w_up
        return (g * u) @ w_down

    x = A_x
    h = x + _A_ref(_layernorm(x, axis=-1), A_wqkv, A_wo)
    h2 = h + _B_ref(_layernorm(h, axis=-1), B_w_gate, B_w_up, B_w_down)
    return h2
```

```cpp
#include <hip/hip_runtime.h>
#include <hip/hip_bf16.h>
#include <stdint.h>

typedef __bf16 bf16x8 __attribute__((ext_vector_type(8)));
typedef __bf16 bf16x2 __attribute__((ext_vector_type(2)));
typedef short s16x4 __attribute__((ext_vector_type(4)));
typedef float f32x16 __attribute__((ext_vector_type(16)));
typedef float f32x4 __attribute__((ext_vector_type(4)));
typedef float f32x2 __attribute__((ext_vector_type(2)));
typedef unsigned u32x4 __attribute__((ext_vector_type(4)));
typedef unsigned u32x2 __attribute__((ext_vector_type(2)));
typedef unsigned short u16;
#define LDSP __attribute__((address_space(3)))
#define DI __device__ __forceinline__
#define SBAR() __builtin_amdgcn_sched_barrier(0)

constexpr int SEQ = 2048, DM = 576, NH = 32, DH = 18, FF = 4608;
constexpr float LOG2E = 1.4426950408889634f;
constexpr float QSCALE = 0.23570226039551584f * LOG2E;
constexpr float LN_EPS = 1e-5f;

constexpr size_t CHUNK_B = 37888;
constexpr size_t KV_HEAD_B = 4 * CHUNK_B;
constexpr size_t WS_QM = 0;
constexpr size_t WS_QT = WS_QM + (size_t)NH * 65536;
constexpr size_t WS_KV = WS_QT + (size_t)NH * 8192;
constexpr size_t WS_KMAX = WS_KV + (size_t)NH * KV_HEAD_B;
constexpr size_t WS_XMEAN = WS_KMAX + 4096;
constexpr size_t WS_OFR = WS_XMEAN + 8192;
constexpr size_t WS_WO = WS_OFR + (size_t)128 * 18 * 1024;
constexpr size_t WS_WBAR = WS_WO + (size_t)12 * 54 * 1024;
constexpr size_t WS_HC = WS_WBAR + 3072;
constexpr size_t WS_SSQ = WS_HC + (size_t)64 * 36 * 1024;
constexpr size_t WS_W1 = WS_SSQ + (size_t)12 * 8192;
constexpr size_t WS_WD = WS_W1 + (size_t)288 * 36 * 1024;
constexpr size_t WS_ACT = WS_WD + (size_t)4 * 144 * 9 * 1024;
constexpr size_t WS_PART = WS_ACT + (size_t)128 * 144 * 1024;
constexpr size_t WS_END = WS_PART + (size_t)4 * 128 * 4 * 4608;

DI unsigned cvtpk(float lo, float hi) {
  f32x2 v = {lo, hi};
  bf16x2 b = __builtin_convertvector(v, bf16x2);
  return __builtin_bit_cast(unsigned, b);
}
DI float bflo(unsigned u) { return __uint_as_float(u << 16); }
DI float bfhi(unsigned u) { return __uint_as_float(u & 0xffff0000u); }
DI float dot2(unsigned a, unsigned b, float c) {
  return __builtin_amdgcn_fdot2_f32_bf16(__builtin_bit_cast(bf16x2, a), __builtin_bit_cast(bf16x2, b), c, false);
}
DI f32x16 mfma32(u32x4 a, u32x4 b, f32x16 c) {
  return __builtin_amdgcn_mfma_f32_32x32x16_bf16(__builtin_bit_cast(bf16x8, a), __builtin_bit_cast(bf16x8, b), c, 0, 0, 0);
}
DI f32x4 mfma16(u32x4 a, u32x4 b, f32x4 c) {
  return __builtin_amdgcn_mfma_f32_16x16x32_bf16(__builtin_bit_cast(bf16x8, a), __builtin_bit_cast(bf16x8, b), c, 0, 0, 0);
}
DI unsigned rfl(unsigned x) { return __builtin_amdgcn_readfirstlane(x); }
DI const char* uniptr(const char* p) {
  uint64_t v = (uint64_t)p;
  unsigned lo = rfl((unsigned)v), hi = rfl((unsigned)(v >> 32));
  return (const char*)(((uint64_t)hi << 32) | lo);
}
DI void dma16(const char* sbase, unsigned voff, unsigned lds) {
  unsigned sv;
  asm volatile("s_mov_b32 %0, m0\n\ts_mov_b32 m0, %3\n\ts_nop 0\n\tglobal_load_lds_dwordx4 %1, %2\n\ts_mov_b32 m0, %0"
               : "=&s"(sv) : "v"(voff), "s"(sbase), "s"(lds));
}
DI u32x4 gld16(const char* sbase, unsigned voff) {
  u32x4 r;
  asm volatile("global_load_dwordx4 %0, %1, %2" : "=v"(r) : "v"(voff), "s"(sbase));
  return r;
}
DI unsigned gld4(const char* sbase, unsigned voff) {
  unsigned r;
  asm volatile("global_load_dword %0, %1, %2" : "=v"(r) : "v"(voff), "s"(sbase));
  return r;
}
#define WAITV(n) asm volatile("s_waitcnt vmcnt(" #n ")" ::: "memory")
#define BARRIER() asm volatile("s_barrier" ::: "memory")
#define PIN(x) asm volatile("" : "+v"(x))
DI u32x4 ldsr16(LDSP const char* p) { return *(LDSP const u32x4*)p; }
DI float xor32_sum(float v) {
  auto r = __builtin_amdgcn_permlane32_swap(__float_as_uint(v), __float_as_uint(v), false, false);
  return __uint_as_float(r[0]) + __uint_as_float(r[1]);
}
DI float xor32_max(float v) {
  auto r = __builtin_amdgcn_permlane32_swap(__float_as_uint(v), __float_as_uint(v), false, false);
  return fmaxf(__uint_as_float(r[0]), __uint_as_float(r[1]));
}
DI float xor16_sum(float v) {
  auto r = __builtin_amdgcn_permlane16_swap(__float_as_uint(v), __float_as_uint(v), false, false);
  return __uint_as_float(r[0]) + __uint_as_float(r[1]);
}

DI int wo_row(int step, int g, int j) {
  if (step < 16) return 18 * (2 * step + (g >> 1)) + 4 * (g & 1) + (j & 3) + 8 * (j >> 2);
  return 18 * (4 * (4 * (step - 16) + g) + (j >> 1)) + 16 + (j & 1);
}

constexpr int NFR_W1 = 288 * 36, NFR_WD = 4 * 144 * 9, NFR_WO = 12 * 54;
__global__ __launch_bounds__(256) void k_prep(const u16* __restrict__ wo, const u16* __restrict__ wg, const u16* __restrict__ wu,
                                              const u16* __restrict__ wd, char* __restrict__ ws) {
  const int lane = threadIdx.x & 63;
  const int job = blockIdx.x * 4 + (threadIdx.x >> 6);
  u16 e[8];
  char* dst;
  if (job < NFR_W1) {
    const int T = job / 36, s = job % 36, r = lane & 31, h = lane >> 5;
    const u16* src = (r < 16 ? wg : wu) + (size_t)(16 * s + 8 * h) * FF + 16 * T + (r & 15);
#pragma unroll
    for (int j = 0; j < 8; ++j) e[j] = src[(size_t)j * FF];
    dst = ws + WS_W1 + (size_t)job * 1024;
  } else if (job < NFR_W1 + NFR_WD) {
    const int q = job - NFR_W1, nb = q / 1296, c = (q % 1296) / 9, t = q % 9, g = lane >> 4;
    const u16* src = wd + (size_t)(32 * c + 16 * (g >> 1) + 4 * (g & 1)) * DM + 144 * nb + 16 * t + (lane & 15);
#pragma unroll
    for (int j = 0; j < 8; ++j) e[j] = src[(size_t)((j & 3) + 8 * (j >> 2)) * DM];
    dst = ws + WS_WD + (size_t)q * 1024;
  } else if (job < NFR_W1 + NFR_WD + NFR_WO) {
    const int q = job - NFR_W1 - NFR_WD, nb = q / 54, step = (q % 54) / 3, t = q % 3, g = lane >> 4;
#pragma unroll
    for (int j = 0; j < 8; ++j) e[j] = wo[(size_t)wo_row(step, g, j) * DM + 48 * nb + 16 * t + (lane & 15)];
    dst = ws + WS_WO + (size_t)q * 1024;
  } else {
    const int q = job - NFR_W1 - NFR_WD - NFR_WO;
    if (q >= 72) return;
    const int step = q >> 2, g = q & 3;
    u16* wb = (u16*)(ws + WS_WBAR);
    for (int j = 0; j < 8; ++j) {
      const u16* src = wo + (size_t)wo_row(step, g, j) * DM;
      float a = 0.f;
      for (int i = lane; i < DM; i += 64) a += __uint_as_float((unsigned)src[i] << 16);
      for (int o = 32; o > 0; o >>= 1) a += __shfl_xor(a, o);
      a *= (1.0f / DM);
      const unsigned hi = cvtpk(a, 0.f) & 0xffffu;
      const unsigned lo = cvtpk(a - bflo(hi), 0.f) & 0xffffu;
      if (lane == 0) { wb[q * 8 + j] = (u16)hi; wb[576 + q * 8 + j] = (u16)lo; }
    }
    return;
  }
  u32x4 v;
#pragma unroll
  for (int j = 0; j < 4; ++j) v[j] = (unsigned)e[2 * j] | ((unsigned)e[2 * j + 1] << 16);
  *(u32x4*)(dst + lane * 16) = v;
}

constexpr int K1_PITCH = 224, K1_WB = 576 * K1_PITCH  , K1_CS = K1_WB + 2048  , K1_LDS = K1_CS + 512;
constexpr int K1_SP = 232;
__global__ __launch_bounds__(512, 2) void k_qkv(const u16* __restrict__ x, const u16* __restrict__ wqkv, char* __restrict__ ws) {
  extern __shared__ __attribute__((aligned(16))) char smem[];
  LDSP char* lds = (LDSP char*)smem;
  const unsigned lds0 = (unsigned)(uintptr_t)lds;
  const int tid = threadIdx.x, lane = tid & 63, w = rfl(tid >> 6);
  const int xcd = blockIdx.x & 7, bi = blockIdx.x >> 3;
  const int rb = 4 * (xcd >> 1) + (bi & 3), cb = 8 * (xcd & 1) + (bi >> 2);
  const int c0 = 108 * cb - 4 * (cb & 1);
  const int m = lane & 15, g = lane >> 4;
  const int row = 128 * rb + 16 * w + m;
  if (tid < 128) *(LDSP float*)(lds + K1_CS + tid * 4) = 0.f;

  u32x4 X[18];
  {
    const char* xb = (const char*)x;
    const char* wb = (const char*)wqkv + c0 * 2;
    const unsigned xo = row * 1152 + g * 16;
#pragma unroll
    for (int i = 0; i < 18; ++i) {
      X[i] = gld16(xb, xo + i * 64);
      if (i < 16) {
        int p = 8 * i + w; if (p > 125) p = 125;
        const unsigned u = 64 * p + lane, rho = u / 14, cu = u - rho * 14;
        const unsigned k = (rho & ~12u) | ((rho & 4u) << 1) | ((rho & 8u) >> 1);
        dma16(wb, k * 3456 + cu * 16, lds0 + p * 1024);
      }
    }
  }
  f32x4 acc[7], cs[7];
#pragma unroll
  for (int t = 0; t < 7; ++t) { acc[t] = f32x4{0, 0, 0, 0}; cs[t] = f32x4{0, 0, 0, 0}; }
  float sum = 0.f, sq = 0.f;
  const u32x4 ones = {0x3f803f80u, 0x3f803f80u, 0x3f803f80u, 0x3f803f80u};
  LDSP const char* wl = lds + (16 * (g >> 1) + 4 * (g & 1) + (m >> 2)) * K1_PITCH + (m & 3) * 8;
#define K1_STAGE(z, cnt)                                                                                   \
  {                                                                                                        \
    asm volatile("s_waitcnt vmcnt(" #cnt ")\n\ts_barrier" : "+v"(X[3 * z]), "+v"(X[3 * z + 1]), "+v"(X[3 * z + 2])::"memory"); \
    _Pragma("unroll") for (int s = 3 * z; s < 3 * z + 3; ++s) {                                            \
      const bool docs = ((s & 7) == w);                                                                    \
      _Pragma("unroll") for (int t = 0; t < 7; ++t) {                                                      \
        s16x4 lo = __builtin_amdgcn_ds_read_tr16_b64_v4i16((LDSP s16x4*)(wl + s * 7168 + t * 32));         \
        s16x4 hi = __builtin_amdgcn_ds_read_tr16_b64_v4i16((LDSP s16x4*)(wl + s * 7168 + 1792 + t * 32));  \
        u32x4 a = __builtin_bit_cast(u32x4, __builtin_shufflevector(lo, hi, 0, 1, 2, 3, 4, 5, 6, 7));      \
        acc[t] = mfma16(a, X[s], acc[t]);                                                                  \
        if (docs) cs[t] = mfma16(a, ones, cs[t]);                                                          \
      }                                                                                                    \
      _Pragma("unroll") for (int j = 0; j < 4; ++j) { sum = dot2(X[s][j], 0x3f803f80u, sum); sq = dot2(X[s][j], X[s][j], sq); } \
    }                                                                                                      \
  }
  K1_STAGE(0, 28) K1_STAGE(1, 22) K1_STAGE(2, 17) K1_STAGE(3, 11) K1_STAGE(4, 5) K1_STAGE(5, 0)
#undef K1_STAGE
  if (m == 0) {
#pragma unroll
    for (int t = 0; t < 7; ++t)
#pragma unroll
      for (int i = 0; i < 4; ++i)
        __hip_atomic_fetch_add((LDSP float*)(lds + K1_CS + (16 * t + 4 * g + i) * 4), cs[t][i], __ATOMIC_RELAXED, __HIP_MEMORY_SCOPE_WORKGROUP);
  }
  sum = xor32_sum(xor16_sum(sum));
  sq = xor32_sum(xor16_sum(sq));
  const float mean = sum * (1.0f / DM);
  const float var = fmaxf(sq * (1.0f / DM) - mean * mean, 0.f);
  const float rstd = __builtin_amdgcn_rsqf(var + LN_EPS);
  if (cb == 0 && g == 0) ((float*)(ws + WS_XMEAN))[row] = mean;
  __syncthreads();
#pragma unroll
  for (int t = 0; t < 7; ++t) {
    const f32x4 c = *(LDSP const f32x4*)(lds + K1_CS + (16 * t + 4 * g) * 4);
    const float sc = (c0 + 16 * t + 4 * g < DM) ? rstd * QSCALE : rstd;
    u32x2 o;
    o[0] = cvtpk((acc[t][0] - mean * c[0]) * sc, (acc[t][1] - mean * c[1]) * sc);
    o[1] = cvtpk((acc[t][2] - mean * c[2]) * sc, (acc[t][3] - mean * c[3]) * sc);
    *(LDSP u32x2*)(lds + (16 * w + m) * K1_SP + (16 * t + 4 * g) * 2) = o;
  }
  __syncthreads();
  const int off = 4 * (cb & 1);
#pragma unroll 1
  for (int hd = 0; hd < 6; ++hd) {
    const int G = 6 * cb + hd, type = G >> 5, head = G & 31;
    if (type < 2) {
      if ((tid >> 7) == (hd & 3)) {
        const int tok = tid & 127, T = 128 * rb + tok;
        LDSP const unsigned* sp = (LDSP const unsigned*)(lds + tok * K1_SP + (off + 18 * hd) * 2);
        unsigned d[9];
#pragma unroll
        for (int i = 0; i < 9; ++i) d[i] = sp[i];
        const u32x4 u0 = {d[0], d[1], d[2], d[3]}, u1 = {d[4], d[5], d[6], d[7]};
        if (type == 0) {
          char* qm = ws + WS_QM + (size_t)head * 65536 + (T >> 5) * 1024 + (T & 31) * 16;
          *(u32x4*)qm = u0; *(u32x4*)(qm + 512) = u1;
          ((unsigned*)(ws + WS_QT + (size_t)head * 8192))[T] = d[8];
        } else {
          char* ck = ws + WS_KV + (size_t)head * KV_HEAD_B + (T >> 9) * CHUNK_B;
          char* km = ck + ((T >> 5) & 15) * 1024 + (T & 31) * 16;
          *(u32x4*)km = u0; *(u32x4*)(km + 512) = u1;
          *(unsigned*)(ck + 16384 + ((T >> 7) & 3) * 512 + (T & 31) * 16 + ((T >> 5) & 3) * 4) = d[8];
          float n2 = 0.f;
#pragma unroll
          for (int i = 0; i < 9; ++i) n2 = dot2(d[i], d[i], n2);
#pragma unroll
          for (int o = 32; o > 0; o >>= 1) n2 = fmaxf(n2, __shfl_xor(n2, o));
          if (lane == 0) ((float*)(ws + WS_KMAX))[(2 * rb + (tok >> 6)) * 32 + head] = n2;
        }
      }
    } else if (tid < 304) {
      const int s = tid / 38, rem = tid - s * 38, h = rem / 19, d = rem - h * 19;
      u32x4 v = ones;
      if (d < 18) {
        LDSP const u16* sp = (LDSP const u16*)(lds + (16 * s + 4 * h) * K1_SP + (off + 18 * hd + d) * 2);
#pragma unroll
        for (int j = 0; j < 4; ++j) {
          const int t0 = ((2 * j) & 3) + 8 * (j >> 1);
          v[j] = (unsigned)sp[t0 * (K1_SP / 2)] | ((unsigned)sp[(t0 + 1) * (K1_SP / 2)] << 16);
        }
      }
      const int T0 = 128 * rb;
      char* vb = ws + WS_KV + (size_t)head * KV_HEAD_B + (T0 >> 9) * CHUNK_B + 18432 + ((T0 >> 4) & 31) * 608;
      *(u32x4*)(vb + tid * 16) = v;
    }
  }
}

constexpr int K2_LDS = (int)KV_HEAD_B;
struct AttnState { f32x16 o; };
DI void attn_group(LDSP const char* km, LDSP const char* kt, LDSP const char* vp, const u32x4& qm, const u32x4 (&qt)[4], const f32x16& negm, f32x16& o) {
  const u32x4 ktl = ldsr16(kt);
#pragma unroll
  for (int t = 0; t < 4; ++t) {
    const u32x4 kf = ldsr16(km + t * 1024);
    const u32x4 v0 = ldsr16(vp + t * 1216), v1 = ldsr16(vp + t * 1216 + 608);
    f32x16 s = mfma32(ktl, qt[t], negm);
    s = mfma32(kf, qm, s);
    u32x4 p0, p1;
#pragma unroll
    for (int i = 0; i < 4; ++i) {
      p0[i] = cvtpk(__builtin_amdgcn_exp2f(s[2 * i]), __builtin_amdgcn_exp2f(s[2 * i + 1]));
      p1[i] = cvtpk(__builtin_amdgcn_exp2f(s[8 + 2 * i]), __builtin_amdgcn_exp2f(s[8 + 2 * i + 1]));
    }
    o = mfma32(v0, p0, o);
    o = mfma32(v1, p1, o);
  }
}
__global__ __launch_bounds__(512, 2) void k_attn(char* __restrict__ ws) {
  extern __shared__ __attribute__((aligned(16))) char smem[];
  LDSP char* lds = (LDSP char*)smem;
  const unsigned lds0 = (unsigned)(uintptr_t)lds;
  const int tid = threadIdx.x, lane = tid & 63, w = rfl(tid >> 6);
  const int xcd = blockIdx.x & 7, bi = blockIdx.x >> 3;
  const int head = 4 * xcd + (bi & 3), qb = bi >> 2;
  const int r = lane & 31, h = lane >> 5;
  const int qt32 = 8 * qb + w;

  u32x4 qm = gld16(ws + WS_QM, head * 65536 + qt32 * 1024 + lane * 16);
  unsigned qtl = gld4(ws + WS_QT, head * 8192 + (32 * qt32 + r) * 4);
  unsigned kmx = gld4(ws + WS_KMAX, (r * 32 + head) * 4);
  {
    const char* kv = ws + WS_KV + (size_t)head * KV_HEAD_B;
#pragma unroll
    for (int c = 0; c < 4; ++c)
#pragma unroll
      for (int i = 0; i < 5; ++i) {
        int p = 8 * i + w; if (p > 36) p = 36;
        const unsigned o = c * (unsigned)CHUNK_B + p * 1024;
        dma16(kv, o + lane * 16, lds0 + o);
      }
  }
  asm volatile("s_waitcnt vmcnt(20)" : "+v"(qm), "+v"(qtl), "+v"(kmx)::"memory");
  float k2 = __uint_as_float(kmx);
#pragma unroll
  for (int o = 16; o > 0; o >>= 1) k2 = fmaxf(k2, __shfl_xor(k2, o));
  if (h) qtl = 0;
  float q2 = dot2(qtl, qtl, 0.f);
#pragma unroll
  for (int i = 0; i < 4; ++i) q2 = dot2(qm[i], qm[i], q2);
  q2 = xor32_sum(q2);
  float mshift = __builtin_amdgcn_sqrtf(q2 * k2) * 1.001f;
  u32x4 qt[4];
#pragma unroll
  for (int t = 0; t < 4; ++t) { qt[t] = u32x4{0, 0, 0, 0}; qt[t][t] = qtl; }
  f32x16 negm, o;
#pragma unroll
  for (int i = 0; i < 16; ++i) { negm[i] = -mshift; o[i] = 0.f; }
  const int vrow = r < 19 ? r : ((r >= 20 && r < 28) ? 0 : 18);
  LDSP const char* kmb = lds + lane * 16;
  LDSP const char* ktb = lds + 16384 + r * 16;
  LDSP const char* vb = lds + 18432 + h * 304 + vrow * 16;
#define K2_CHUNK(c, cnt)                                                            \
  {                                                                                 \
    asm volatile("s_waitcnt vmcnt(" #cnt ")\n\ts_barrier" ::: "memory");            \
    _Pragma("unroll 1") for (int gq = 0; gq < 4; ++gq)                              \
      attn_group(kmb + c * CHUNK_B + gq * 4096, ktb + c * CHUNK_B + gq * 512, vb + c * CHUNK_B + gq * 4864, qm, qt, negm, o); \
  }
  K2_CHUNK(0, 15) K2_CHUNK(1, 10) K2_CHUNK(2, 5) K2_CHUNK(3, 0)
#undef K2_CHUNK
  float l;
  { auto rr = __builtin_amdgcn_permlane32_swap(__float_as_uint(o[10]), __float_as_uint(o[10]), false, false); l = __uint_as_float(rr[0]); }
  if (__builtin_expect(__any(!(l >= 9.094947e-13f)), 0)) {
    float mx = -INFINITY;
    f32x16 z;
#pragma unroll
    for (int i = 0; i < 16; ++i) z[i] = 0.f;
#pragma unroll 1
    for (int tt = 0; tt < 64; ++tt) {
      const int c = tt >> 4, tl = tt & 15;
      const u32x4 ktl = ldsr16(ktb + c * CHUNK_B + (tl >> 2) * 512);
      const u32x4 kf = ldsr16(kmb + c * CHUNK_B + tl * 1024);
      u32x4 qq = {0, 0, 0, 0};
#pragma unroll
      for (int i = 0; i < 4; ++i) qq[i] = ((tl & 3) == i) ? qtl : 0u;
      f32x16 s = mfma32(ktl, qq, z);
      s = mfma32(kf, qm, s);
#pragma unroll
      for (int i = 0; i < 16; ++i) mx = fmaxf(mx, s[i]);
    }
    mx = xor32_max(mx);
#pragma unroll
    for (int i = 0; i < 16; ++i) { negm[i] = -mx; o[i] = 0.f; }
#pragma unroll 1
    for (int gq = 0; gq < 16; ++gq) {
      const int c = gq >> 2, gg = gq & 3;
      attn_group(kmb + c * CHUNK_B + gg * 4096, ktb + c * CHUNK_B + gg * 512, vb + c * CHUNK_B + gg * 4864, qm, qt, negm, o);
    }
    auto rr = __builtin_amdgcn_permlane32_swap(__float_as_uint(o[10]), __float_as_uint(o[10]), false, false);
    l = __uint_as_float(rr[0]);
  }
  const float rl = 1.0f / l;
  u32x4 ou;
#pragma unroll
  for (int i = 0; i < 4; ++i) ou[i] = cvtpk(o[2 * i] * rl, o[2 * i + 1] * rl);
  const int q = 32 * qt32 + r;
  char* ob = ws + WS_OFR + (size_t)(q >> 4) * 18432 + (q & 15) * 16;
  *(u32x4*)(ob + (head >> 1) * 1024 + (2 * (head & 1) + h) * 256) = ou;
  if (h == 0) *(unsigned*)(ob + (16 + (head >> 4)) * 1024 + ((head >> 2) & 3) * 256 + (head & 3) * 4) = cvtpk(o[8] * rl, o[9] * rl);
}

constexpr int K3_WBAR = 54 * 1024, K3_LDS = K3_WBAR + 3072;
__global__ __launch_bounds__(512, 2) void k_oproj(const u16* __restrict__ x, u16* __restrict__ hout, char* __restrict__ ws) {
  extern __shared__ __attribute__((aligned(16))) char smem[];
  LDSP char* lds = (LDSP char*)smem;
  const unsigned lds0 = (unsigned)(uintptr_t)lds;
  const int tid = threadIdx.x, lane = tid & 63, w = rfl(tid >> 6);
  const int xcd = blockIdx.x & 7, bi = blockIdx.x >> 3;
  const int rb = 2 * xcd + (bi & 1), nb = bi >> 1;
  const int m = lane & 15, g = lane >> 4;
  const int mt = 8 * rb + w, row = 16 * mt + m;

  u32x4 O[18];
  u32x2 xr[3];
  unsigned xm;
  {
    const char* wb = ws + WS_WO + (size_t)nb * 54 * 1024;
    const char* ob = ws + WS_OFR + (size_t)mt * 18432;
    if (w < 3) dma16(ws + WS_WBAR, w * 1024 + lane * 16, lds0 + K3_WBAR + w * 1024);
#pragma unroll
    for (int i = 0; i < 18; ++i) {
      O[i] = gld16(ob, i * 1024 + lane * 16);
      if (i < 7) { int p = 8 * i + w; if (p > 53) p = 53; dma16(wb, p * 1024 + lane * 16, lds0 + p * 1024); }
    }
#pragma unroll
    for (int t = 0; t < 3; ++t)
      asm volatile("global_load_dwordx2 %0, %1, %2" : "=v"(xr[t]) : "v"(row * 1152 + (48 * nb + 16 * t + 4 * g) * 2), "s"((const char*)x));
    xm = gld4(ws + WS_XMEAN, row * 4);
  }
  f32x4 acc[4];
#pragma unroll
  for (int t = 0; t < 4; ++t) acc[t] = f32x4{0, 0, 0, 0};
  LDSP const char* wl = lds + lane * 16;
  LDSP const char* wbl = lds + K3_WBAR + (m & 1) * 1152 + g * 16;
  asm volatile("s_waitcnt vmcnt(15)\n\ts_barrier" : "+v"(O[0]), "+v"(O[1]), "+v"(O[2]), "+v"(O[3]), "+v"(O[4]), "+v"(O[5]), "+v"(O[6])::"memory");
#pragma unroll
  for (int s = 0; s < 7; ++s) {
#pragma unroll
    for (int t = 0; t < 3; ++t) acc[t] = mfma16(ldsr16(wl + (3 * s + t) * 1024), O[s], acc[t]);
    acc[3] = mfma16(ldsr16(wbl + s * 64), O[s], acc[3]);
  }
  asm volatile("s_waitcnt vmcnt(4)" : "+v"(O[7]), "+v"(O[8]), "+v"(O[9]), "+v"(O[10]), "+v"(O[11]), "+v"(O[12]), "+v"(O[13]), "+v"(O[14]),
               "+v"(O[15]), "+v"(O[16]), "+v"(O[17])::"memory");
#pragma unroll
  for (int s = 7; s < 18; ++s) {
#pragma unroll
    for (int t = 0; t < 3; ++t) acc[t] = mfma16(ldsr16(wl + (3 * s + t) * 1024), O[s], acc[t]);
    acc[3] = mfma16(ldsr16(wbl + s * 64), O[s], acc[3]);
  }
  asm volatile("s_waitcnt vmcnt(0)" : "+v"(xr[0]), "+v"(xr[1]), "+v"(xr[2]), "+v"(xm)::"memory");
  const float mean = __uint_as_float(xm) + acc[3][0] + acc[3][1];
  float ssq = 0.f;
#pragma unroll
  for (int t = 0; t < 3; ++t) {
    u32x2 hb, hc;
    hb[0] = cvtpk(bflo(xr[t][0]) + acc[t][0], bfhi(xr[t][0]) + acc[t][1]);
    hb[1] = cvtpk(bflo(xr[t][1]) + acc[t][2], bfhi(xr[t][1]) + acc[t][3]);
    hc[0] = cvtpk(bflo(hb[0]) - mean, bfhi(hb[0]) - mean);
    hc[1] = cvtpk(bflo(hb[1]) - mean, bfhi(hb[1]) - mean);
    ssq = dot2(hc[0], hc[0], ssq); ssq = dot2(hc[1], hc[1], ssq);
    *(u32x2*)(hout + (size_t)row * DM + 48 * nb + 16 * t + 4 * g) = hb;
    *(u32x2*)(ws + WS_HC + (size_t)(row >> 5) * 36864 + (3 * nb + t) * 1024 + (g >> 1) * 512 + (row & 31) * 16 + (g & 1) * 8) = hc;
  }
  ssq = xor32_sum(xor16_sum(ssq));
  if (g == 0) ((float*)(ws + WS_SSQ))[nb * 2048 + row] = ssq;
}

constexpr int K4_SLOT = 36864, K4_LDS = 4 * K4_SLOT;
DI void k4_dma_tile(const char* wb, int c, int w, unsigned lane16, unsigned lds0) {
  const char* src = wb + (size_t)c * K4_SLOT;
  const unsigned dst = lds0 + (c & 3) * K4_SLOT;
#pragma unroll
  for (int i = 0; i < 4; ++i) dma16(src, (8 * i + w) * 1024 + lane16, dst + (8 * i + w) * 1024);
  if ((w >> 2) == (c & 1)) dma16(src, (32 + (w & 3)) * 1024 + lane16, dst + (32 + (w & 3)) * 1024);
}
DI unsigned k4_epi_pair(float g0, float u0, float g1, float u1, float c1, float c2) {
  const float a0 = g0 * c1, a1 = g1 * c1;
  const float e0 = __builtin_amdgcn_exp2f(a0), e1 = __builtin_amdgcn_exp2f(a1);
  const float r0 = __builtin_amdgcn_rcpf(__builtin_fmaf(e0, c2, c2)), r1 = __builtin_amdgcn_rcpf(__builtin_fmaf(e1, c2, c2));
  return cvtpk(a0 * u0 * r0, a1 * u1 * r1);
}
template <bool EPI>
DI void k4_tile(LDSP const char* cur, LDSP const char* nxt, const u32x4 (&X)[36], u32x4 (&wf)[4], f32x16& acc, const f32x16& prev,
                float c1, float c2, char* dst) {
  u32x4 ou;
#pragma unroll
  for (int s = 0; s < 36; ++s) {
    const u32x4 a = wf[s & 3];
    if (s == 0) { f32x16 z; _Pragma("unroll") for (int i = 0; i < 16; ++i) z[i] = 0.f; acc = mfma32(a, X[s], z); }
    else acc = mfma32(a, X[s], acc);
    wf[s & 3] = (s + 4 < 36) ? ldsr16(cur + (s + 4) * 1024) : ldsr16(nxt + (s + 4 - 36) * 1024);
    if (EPI) {
      if (s >= 4 && s < 36 && (s & 7) == 4) { const int i = (s - 4) >> 3; ou[i] = k4_epi_pair(prev[2 * i], prev[8 + 2 * i], prev[2 * i + 1], prev[9 + 2 * i], c1, c2); PIN(ou[i]); }
      if (s == 33) *(u32x4*)dst = ou;
    }
    SBAR();
  }
}
__global__ __launch_bounds__(512, 2) void k_gateup(char* __restrict__ ws) {
  extern __shared__ __attribute__((aligned(16))) char smem[];
  LDSP char* lds = (LDSP char*)smem;
  const unsigned lds0 = (unsigned)(uintptr_t)lds;
  const int tid = threadIdx.x, lane = tid & 63, w = rfl(tid >> 6);
  const int xcd = blockIdx.x & 7, bi = blockIdx.x >> 3;
  const int rb = 4 * (xcd & 1) + (bi & 3), fs = 8 * (xcd >> 1) + (bi >> 2);
  const int r = lane & 31, h = lane >> 5;
  const int mt32 = 8 * rb + w, row = 32 * mt32 + r;
  const unsigned lane16 = lane * 16;
  const char* wb = ws + WS_W1 + (size_t)fs * 9 * K4_SLOT;

  u32x4 X[36];
  unsigned sq[6];
  {
#pragma unroll
    for (int i = 0; i < 6; ++i) sq[i] = gld4(ws + WS_SSQ, (6 * h + i) * 8192 + row * 4);
    k4_dma_tile(wb, 0, w, lane16, lds0);
    const char* xb = ws + WS_HC + (size_t)mt32 * 36864;
#pragma unroll
    for (int i = 0; i < 36; ++i) X[i] = gld16(xb, i * 1024 + lane16);
    k4_dma_tile(wb, 1, w, lane16, lds0);
    k4_dma_tile(wb, 2, w, lane16, lds0);
  }
  asm volatile("s_waitcnt vmcnt(4)" : "+v"(sq[0]), "+v"(sq[1]), "+v"(sq[2]), "+v"(sq[3]), "+v"(sq[4]), "+v"(sq[5]),
               "+v"(X[0]), "+v"(X[1]), "+v"(X[2]), "+v"(X[3]), "+v"(X[4]), "+v"(X[5]), "+v"(X[6]), "+v"(X[7]), "+v"(X[8]),
               "+v"(X[9]), "+v"(X[10]), "+v"(X[11]), "+v"(X[12]), "+v"(X[13]), "+v"(X[14]), "+v"(X[15]), "+v"(X[16]), "+v"(X[17])::"memory");
  asm volatile("s_barrier" : "+v"(X[18]), "+v"(X[19]), "+v"(X[20]), "+v"(X[21]), "+v"(X[22]), "+v"(X[23]), "+v"(X[24]), "+v"(X[25]), "+v"(X[26]),
               "+v"(X[27]), "+v"(X[28]), "+v"(X[29]), "+v"(X[30]), "+v"(X[31]), "+v"(X[32]), "+v"(X[33]), "+v"(X[34]), "+v"(X[35])::"memory");
  float ss = (__uint_as_float(sq[0]) + __uint_as_float(sq[1])) + (__uint_as_float(sq[2]) + __uint_as_float(sq[3])) + (__uint_as_float(sq[4]) + __uint_as_float(sq[5]));
  ss = xor32_sum(ss);
  const float sd = __builtin_amdgcn_sqrtf(ss * (1.0f / DM) + LN_EPS);
  const float c1 = -LOG2E / sd, c2 = -LOG2E * sd;
  LDSP const char* wl = lds + lane16;
  u32x4 wf[4];
#pragma unroll
  for (int i = 0; i < 4; ++i) wf[i] = ldsr16(wl + i * 1024);
  char* ab = ws + WS_ACT + (size_t)(row >> 4) * (144 * 1024) + (row & 15) * 16 + h * 256;
  const int T0 = 9 * fs;
#define K4_DST(T) (ab + ((T) >> 1) * 1024 + ((T) & 1) * 512)
  f32x16 accA, accB;
  k4_dma_tile(wb, 3, w, lane16, lds0);
  k4_tile<false>(wl, wl + K4_SLOT, X, wf, accA, accA, c1, c2, nullptr);
#pragma unroll 1
  for (int c = 1; c < 9; c += 2) {
    if (c < 7) asm volatile("s_waitcnt vmcnt(4)\n\ts_barrier" ::: "memory");
    else asm volatile("s_waitcnt vmcnt(0)\n\ts_barrier" ::: "memory");
    if (c + 3 < 9) k4_dma_tile(wb, c + 3, w, lane16, lds0);
    k4_tile<true>(wl + (c & 3) * K4_SLOT, wl + ((c + 1) & 3) * K4_SLOT, X, wf, accB, accA, c1, c2, K4_DST(T0 + c - 1));
    if (c < 7) asm volatile("s_waitcnt vmcnt(4)\n\ts_barrier" ::: "memory");
    if (c + 4 < 9) k4_dma_tile(wb, c + 4, w, lane16, lds0);
    k4_tile<true>(wl + ((c + 1) & 3) * K4_SLOT, wl + ((c + 2) & 3) * K4_SLOT, X, wf, accA, accB, c1, c2, K4_DST(T0 + c));
  }
  u32x4 ou;
#pragma unroll
  for (int i = 0; i < 4; ++i) ou[i] = k4_epi_pair(accA[2 * i], accA[8 + 2 * i], accA[2 * i + 1], accA[9 + 2 * i], c1, c2);
  *(u32x4*)K4_DST(T0 + 8) = ou;
#undef K4_DST
}

constexpr int K5_SLOT = 36864, K5_LDS = 3 * K5_SLOT;
DI void k5_issue(const char* wb, const char* ab, const char* ab2, int z, int slot, int w, unsigned lane16, unsigned lds0, u32x4 (&A)[4]) {
  const unsigned dst = lds0 + slot * K5_SLOT;
#pragma unroll
  for (int i = 0; i < 4; ++i) {
    const int p = 8 * i + w;
    const int kh = p >= 18, q = p - 18 * kh;
    dma16(wb, (kh * 162 + z * 18 + q) * 1024 + lane16, dst + p * 1024);
    A[i] = gld16((i >> 1) ? ab2 : ab, (2 * z + (i & 1)) * 1024 + lane16);
  }
  if ((w >> 2) == (z & 1)) { const int p = 32 + (w & 3); dma16(wb, (162 + z * 18 + p - 18) * 1024 + lane16, dst + p * 1024); }
}
template <int Z>
DI void k5_stage(const char* wb, const char* ab, const char* ab2, int w, unsigned lane16, unsigned lds0, LDSP const char* wl, u32x4 (&A)[3][4], u32x4 (&wf)[4],
                 f32x4 (&acc)[2][9]) {
  LDSP const char* cur = wl + (Z % 3) * K5_SLOT;
  LDSP const char* nxt = wl + ((Z + 1) % 3) * K5_SLOT;
#pragma unroll
  for (int f = 0; f < 18; ++f) {
    const u32x4 a = wf[(18 * Z + f) & 3];
    acc[0][f % 9] = mfma16(a, A[Z % 3][f / 9], acc[0][f % 9]);
    acc[1][f % 9] = mfma16(a, A[Z % 3][2 + f / 9], acc[1][f % 9]);
    if (f == 14 && Z < 8) {
      u32x4(&N)[4] = A[(Z + 1) % 3];
      if (Z < 7) asm volatile("s_waitcnt vmcnt(8) lgkmcnt(0)\n\ts_barrier" : "+v"(N[0]), "+v"(N[1]), "+v"(N[2]), "+v"(N[3])::"memory");
      else asm volatile("s_waitcnt vmcnt(0) lgkmcnt(0)\n\ts_barrier" : "+v"(N[0]), "+v"(N[1]), "+v"(N[2]), "+v"(N[3])::"memory");
    }
    if (f + 4 < 18) wf[(18 * Z + f) & 3] = ldsr16(cur + (f + 4) * 1024);
    else if (Z < 8) wf[(18 * Z + f) & 3] = ldsr16(nxt + (f + 4 - 18) * 1024);
    SBAR();
  }
  if (Z + 3 < 9) k5_issue(wb, ab, ab2, Z + 3, Z % 3, w, lane16, lds0, A[Z % 3]);
  SBAR();
}
__global__ __launch_bounds__(512, 2) void k_down(char* __restrict__ ws) {
  extern __shared__ __attribute__((aligned(16))) char smem[];
  LDSP char* lds = (LDSP char*)smem;
  const unsigned lds0 = (unsigned)(uintptr_t)lds;
  const int tid = threadIdx.x, lane = tid & 63, w = rfl(tid >> 6);
  const int xcd = blockIdx.x & 7, bi = blockIdx.x >> 3;
  const int kq = xcd >> 1, rb = 8 * (xcd & 1) + (bi & 7), nb = bi >> 3;
  const int mg = w & 3, kh = w >> 2;
  const unsigned lane16 = lane * 16;
  const char* wb = ws + WS_WD + ((size_t)nb * 144 + 36 * kq) * 9 * 1024;
  const char* ab = ws + WS_ACT + ((size_t)(8 * rb + 2 * mg + kh) * 144 + 36 * kq + 18 * kh) * 1024;
  const char* ab2 = ws + WS_ACT + ((size_t)(8 * rb + 2 * mg + (kh ^ 1)) * 144 + 36 * kq + 18 * kh) * 1024;
  u32x4 A[3][4], wf[4];
  f32x4 acc[2][9];
#pragma unroll
  for (int t = 0; t < 9; ++t) { acc[0][t] = f32x4{0, 0, 0, 0}; acc[1][t] = f32x4{0, 0, 0, 0}; }
  k5_issue(wb, ab, ab2, 0, 0, w, lane16, lds0, A[0]);
  k5_issue(wb, ab, ab2, 1, 1, w, lane16, lds0, A[1]);
  k5_issue(wb, ab, ab2, 2, 2, w, lane16, lds0, A[2]);
  LDSP const char* wl = lds + kh * 18432 + lane16;
  asm volatile("s_waitcnt vmcnt(17)\n\ts_barrier" : "+v"(A[0][0]), "+v"(A[0][1]), "+v"(A[0][2]), "+v"(A[0][3])::"memory");
#pragma unroll
  for (int i = 0; i < 4; ++i) wf[i] = ldsr16(wl + i * 1024);
  k5_stage<0>(wb, ab, ab2, w, lane16, lds0, wl, A, wf, acc);
  k5_stage<1>(wb, ab, ab2, w, lane16, lds0, wl, A, wf, acc);
  k5_stage<2>(wb, ab, ab2, w, lane16, lds0, wl, A, wf, acc);
  k5_stage<3>(wb, ab, ab2, w, lane16, lds0, wl, A, wf, acc);
  k5_stage<4>(wb, ab, ab2, w, lane16, lds0, wl, A, wf, acc);
  k5_stage<5>(wb, ab, ab2, w, lane16, lds0, wl, A, wf, acc);
  k5_stage<6>(wb, ab, ab2, w, lane16, lds0, wl, A, wf, acc);
  k5_stage<7>(wb, ab, ab2, w, lane16, lds0, wl, A, wf, acc);
  k5_stage<8>(wb, ab, ab2, w, lane16, lds0, wl, A, wf, acc);
  __syncthreads();
  {
    LDSP char* xw = lds + w * 9216 + lane16;
#pragma unroll
    for (int t = 0; t < 9; ++t) *(LDSP f32x4*)(xw + t * 1024) = acc[1][t];
  }
  __syncthreads();
  LDSP const char* xr = lds + (w ^ 4) * 9216 + lane16;
  unsigned o[18];
#pragma unroll
  for (int t = 0; t < 9; ++t) {
    const f32x4 p = *(LDSP const f32x4*)(xr + t * 1024);
    const f32x4 a = acc[0][t];
    o[2 * t] = cvtpk(a[0] + p[0], a[1] + p[1]);
    o[2 * t + 1] = cvtpk(a[2] + p[2], a[3] + p[3]);
  }
  char* pb = ws + WS_PART + (((size_t)kq * 128 + 8 * rb + 2 * mg + kh) * 4 + nb) * 4608;
#pragma unroll
  for (int j = 0; j < 4; ++j) *(u32x4*)(pb + j * 1024 + lane16) = u32x4{o[4 * j], o[4 * j + 1], o[4 * j + 2], o[4 * j + 3]};
  *(u32x2*)(pb + 4096 + lane * 8) = u32x2{o[16], o[17]};
}

__global__ __launch_bounds__(320) void k_combine(u16* __restrict__ out, const char* __restrict__ ws) {
  const int lane = threadIdx.x & 63, j = threadIdx.x >> 6;
  const int mt = blockIdx.x >> 2, nb = blockIdx.x & 3;
  const int m = lane & 15, g = lane >> 4;
  const char* pb = ws + WS_PART + ((size_t)mt * 4 + nb) * 4608;
  constexpr size_t KQ = (size_t)128 * 4 * 4608;
  u16* ob = out + (size_t)(16 * mt + m) * DM + 144 * nb + 32 * j + 4 * g;
  if (j < 4) {
    u32x4 p[4];
#pragma unroll
    for (int k = 0; k < 4; ++k) p[k] = *(const u32x4*)(pb + k * KQ + j * 1024 + lane * 16);
    const u32x2 h0 = *(const u32x2*)ob, h1 = *(const u32x2*)(ob + 16);
    const unsigned hh[4] = {h0[0], h0[1], h1[0], h1[1]};
    unsigned o[4];
#pragma unroll
    for (int i = 0; i < 4; ++i)
      o[i] = cvtpk(bflo(hh[i]) + ((bflo(p[0][i]) + bflo(p[1][i])) + (bflo(p[2][i]) + bflo(p[3][i]))),
                   bfhi(hh[i]) + ((bfhi(p[0][i]) + bfhi(p[1][i])) + (bfhi(p[2][i]) + bfhi(p[3][i]))));
    *(u32x2*)ob = u32x2{o[0], o[1]};
    *(u32x2*)(ob + 16) = u32x2{o[2], o[3]};
  } else {
    u32x2 p[4];
#pragma unroll
    for (int k = 0; k < 4; ++k) p[k] = *(const u32x2*)(pb + k * KQ + 4096 + lane * 8);
    const u32x2 h0 = *(const u32x2*)ob;
    unsigned o[2];
#pragma unroll
    for (int i = 0; i < 2; ++i)
      o[i] = cvtpk(bflo(h0[i]) + ((bflo(p[0][i]) + bflo(p[1][i])) + (bflo(p[2][i]) + bflo(p[3][i]))),
                   bfhi(h0[i]) + ((bfhi(p[0][i]) + bfhi(p[1][i])) + (bfhi(p[2][i]) + bfhi(p[3][i]))));
    *(u32x2*)ob = u32x2{o[0], o[1]};
  }
}

extern "C" void kernel_launch(void* const* d_in, const int* in_sizes, int n_in, void* d_out, int out_size, void* d_ws, size_t ws_size,
                              hipStream_t stream) {
  const u16* x = (const u16*)d_in[0];
  const u16* wqkv = (const u16*)d_in[1];
  const u16* wo = (const u16*)d_in[2];
  const u16* wg = (const u16*)d_in[4];
  const u16* wu = (const u16*)d_in[5];
  const u16* wd = (const u16*)d_in[6];
  char* ws = (char*)d_ws;
  (void)hipFuncSetAttribute((const void*)k_qkv, hipFuncAttributeMaxDynamicSharedMemorySize, K1_LDS);
  (void)hipFuncSetAttribute((const void*)k_attn, hipFuncAttributeMaxDynamicSharedMemorySize, K2_LDS);
  (void)hipFuncSetAttribute((const void*)k_oproj, hipFuncAttributeMaxDynamicSharedMemorySize, K3_LDS);
  (void)hipFuncSetAttribute((const void*)k_gateup, hipFuncAttributeMaxDynamicSharedMemorySize, K4_LDS);
  (void)hipFuncSetAttribute((const void*)k_down, hipFuncAttributeMaxDynamicSharedMemorySize, K5_LDS);
  constexpr int NJOB = NFR_W1 + NFR_WD + NFR_WO + 72;
  k_prep<<<(NJOB + 3) / 4, 256, 0, stream>>>(wo, wg, wu, wd, ws);
  k_qkv<<<256, 512, K1_LDS, stream>>>(x, wqkv, ws);
  k_attn<<<256, 512, K2_LDS, stream>>>(ws);
  k_oproj<<<192, 512, K3_LDS, stream>>>(x, (u16*)d_out, ws);
  k_gateup<<<256, 512, K4_LDS, stream>>>(ws);
  k_down<<<256, 512, K5_LDS, stream>>>(ws);
  k_combine<<<512, 320, 0, stream>>>((u16*)d_out, ws);
}
```

```cpp
#include <hip/hip_runtime.h>
#include <hip/hip_bf16.h>
#include <stdint.h>
#include <utility>
#include <type_traits>

typedef __bf16 bf16x8 __attribute__((ext_vector_type(8)));
typedef __bf16 bf16x2 __attribute__((ext_vector_type(2)));
typedef short s16x4 __attribute__((ext_vector_type(4)));
typedef float f32x16 __attribute__((ext_vector_type(16)));
typedef float f32x4 __attribute__((ext_vector_type(4)));
typedef float f32x2 __attribute__((ext_vector_type(2)));
typedef unsigned u32x4 __attribute__((ext_vector_type(4)));
typedef unsigned u32x2 __attribute__((ext_vector_type(2)));
typedef unsigned short u16;
#define LDSP __attribute__((address_space(3)))
#define DI __device__ __forceinline__
#define SBAR() __builtin_amdgcn_sched_barrier(0)

constexpr int SEQ = 2048, DM = 576, NH = 32, DH = 18, FF = 4608;
constexpr float LOG2E = 1.4426950408889634f;
constexpr float QSCALE = 0.23570226039551584f * LOG2E;
constexpr float LN_EPS = 1e-5f;

constexpr size_t CHUNK_B = 37888;
constexpr size_t KV_HEAD_B = 4 * CHUNK_B;
constexpr size_t WS_QM = 0;
constexpr size_t WS_QT = WS_QM + (size_t)NH * 65536;
constexpr size_t WS_KV = WS_QT + (size_t)NH * 8192;
constexpr size_t WS_KMAX = WS_KV + (size_t)NH * KV_HEAD_B;
constexpr size_t WS_XMEAN = WS_KMAX + 65536;
constexpr size_t WS_OFR = WS_XMEAN + 8192;
constexpr size_t WS_WO = WS_OFR + (size_t)128 * 18 * 1024;
constexpr size_t WS_WBAR = WS_WO + (size_t)12 * 54 * 1024;
constexpr size_t WS_HC = WS_WBAR + 3072;
constexpr size_t WS_SSQ = WS_HC + (size_t)64 * 36 * 1024;
constexpr size_t WS_W1 = WS_SSQ + (size_t)12 * 8192;
constexpr size_t WS_WD = WS_W1 + (size_t)288 * 36 * 1024;
constexpr size_t WS_ACT = WS_WD + (size_t)4 * 144 * 9 * 1024;
constexpr size_t WS_PART = WS_ACT + (size_t)128 * 144 * 1024;
constexpr size_t WS_END = WS_PART + (size_t)4 * 128 * 4 * 4608;

DI unsigned cvtpk(float lo, float hi) {
  f32x2 v = {lo, hi};
  bf16x2 b = __builtin_convertvector(v, bf16x2);
  return __builtin_bit_cast(unsigned, b);
}
DI float bflo(unsigned u) { return __uint_as_float(u << 16); }
DI float bfhi(unsigned u) { return __uint_as_float(u & 0xffff0000u); }
DI float dot2(unsigned a, unsigned b, float c) {
  return __builtin_amdgcn_fdot2_f32_bf16(__builtin_bit_cast(bf16x2, a), __builtin_bit_cast(bf16x2, b), c, false);
}
DI f32x16 mfma32(u32x4 a, u32x4 b, f32x16 c) {
  return __builtin_amdgcn_mfma_f32_32x32x16_bf16(__builtin_bit_cast(bf16x8, a), __builtin_bit_cast(bf16x8, b), c, 0, 0, 0);
}
DI f32x4 mfma16(u32x4 a, u32x4 b, f32x4 c) {
  return __builtin_amdgcn_mfma_f32_16x16x32_bf16(__builtin_bit_cast(bf16x8, a), __builtin_bit_cast(bf16x8, b), c, 0, 0, 0);
}
DI unsigned rfl(unsigned x) { return __builtin_amdgcn_readfirstlane(x); }
DI const char* uniptr(const char* p) {
  uint64_t v = (uint64_t)p;
  unsigned lo = rfl((unsigned)v), hi = rfl((unsigned)(v >> 32));
  return (const char*)(((uint64_t)hi << 32) | lo);
}
DI void dma16(const char* sbase, unsigned voff, unsigned lds) {
  unsigned sv;
  asm volatile("s_mov_b32 %0, m0\n\ts_mov_b32 m0, %3\n\ts_nop 0\n\tglobal_load_lds_dwordx4 %1, %2\n\ts_mov_b32 m0, %0"
               : "=&s"(sv) : "v"(voff), "s"(sbase), "s"(lds));
}
DI u32x4 gld16(const char* sbase, unsigned voff) {
  u32x4 r;
  asm volatile("global_load_dwordx4 %0, %1, %2" : "=v"(r) : "v"(voff), "s"(sbase));
  return r;
}
DI unsigned gld4(const char* sbase, unsigned voff) {
  unsigned r;
  asm volatile("global_load_dword %0, %1, %2" : "=v"(r) : "v"(voff), "s"(sbase));
  return r;
}
#define WAITV(n) asm volatile("s_waitcnt vmcnt(" #n ")" ::: "memory")
#define BARRIER() asm volatile("s_barrier" ::: "memory")
#define PIN(x) asm volatile("" : "+v"(x))
DI u32x4 ldsr16(LDSP const char* p) { return *(LDSP const u32x4*)p; }
DI float xor32_sum(float v) {
  auto r = __builtin_amdgcn_permlane32_swap(__float_as_uint(v), __float_as_uint(v), false, false);
  return __uint_as_float(r[0]) + __uint_as_float(r[1]);
}
DI float xor32_max(float v) {
  auto r = __builtin_amdgcn_permlane32_swap(__float_as_uint(v), __float_as_uint(v), false, false);
  return fmaxf(__uint_as_float(r[0]), __uint_as_float(r[1]));
}
DI float xor16_sum(float v) {
  auto r = __builtin_amdgcn_permlane16_swap(__float_as_uint(v), __float_as_uint(v), false, false);
  return __uint_as_float(r[0]) + __uint_as_float(r[1]);
}

#define STAMP_DECL
#define STAMP(i)
#define STAMP_OUT(kid, n)

DI int wo_row(int step, int g, int j) {
  if (step < 16) return 18 * (2 * step + (g >> 1)) + 4 * (g & 1) + (j & 3) + 8 * (j >> 2);
  return 18 * (4 * (4 * (step - 16) + g) + (j >> 1)) + 16 + (j & 1);
}

#define K4_ROT 0
#define K5_ROT 0
#define K4_PFWD 1
#define PART_AUX 16
#define ACT_AUX 16
#define PREP_AUX 16
#define CODE_PF(dst, first, last, extra, blk)                                                                              \
  do {                                                                                                                     \
    uint64_t pc_; int b_, e_;                                                                                              \
    asm volatile("s_getpc_b64 %0\n.Lpf%=:\n\ts_mov_b32 %1, " #first "-.Lpf%=\n\ts_mov_b32 %2, " #last "-.Lpf%=" : "=s"(pc_), "=s"(b_), "=s"(e_)); \
    const unsigned o_ = (blk) * 8192u + (threadIdx.x & 63) * 128u, n_ = (unsigned)(e_ - b_) + (extra) - 4u;                \
    dst = gld4((const char*)(pc_ + b_), o_ < n_ ? o_ : n_);                                                                \
  } while (0)
#define G_MFMA(d, a, b) asm volatile("v_mfma_f32_16x16x32_bf16 %0, %1, %2, %0" : "+v"(d) : "v"(a), "v"(b))
#define G_MFMA0(d, a, b) asm volatile("v_mfma_f32_16x16x32_bf16 %0, %1, %2, 0" : "=&v"(d) : "v"(a), "v"(b))
template <int AUX> DI void bst16(u32x4 v, __amdgpu_buffer_rsrc_t rs, unsigned voff, unsigned soff) {
  __builtin_amdgcn_raw_buffer_store_b128(v, rs, voff, soff, AUX);
  asm volatile("s_nop 1" ::"v"(v) : "memory");
}
DI __amdgpu_buffer_rsrc_t mkrsrc(const void* p, unsigned bytes) {
  return __builtin_amdgcn_make_buffer_rsrc((void*)p, 0, bytes, 0x00020000);
}

#define K1_DEPTH 2
constexpr int K1_PITCH = 224, K1_SLOT = 16384 + 64 * K1_PITCH  , K1_NS = K1_DEPTH + 1;
constexpr int K1_SP = 232;
constexpr int K1_STG = K1_NS * K1_SLOT, K1_CS = K1_STG + 128 * K1_SP + 64, K1_ST = K1_CS + 512, K1_LDS = K1_ST + 2048;
struct K1C {
  const char* xb; const char* wb; unsigned xo0, xo1, wo0, wo1, xd, wd0, wd1;
  LDSP const char *xa[2], *wl, *wc0, *wc1;
  u32x4 X[2][2], wf[4], ones;
  f32x4 acc[4][2], sm[2], sq[2];
};
template <int Z, int I> DI void k1_issue(const K1C& c) {
  constexpr unsigned so = (Z % K1_NS) * K1_SLOT;
  if (I == 0) dma16(c.xb + Z * 128, c.xo0, c.xd + so);
  if (I == 1) dma16(c.wb + Z * (64 * 3456), c.wo0, c.wd0 + so);
  if (I == 2) dma16(c.xb + Z * 128, c.xo1, c.xd + so + 1024);
  if (I == 3) dma16(c.wb + Z * (64 * 3456), c.wo1, c.wd1 + so);
}
DI u32x4 tr_frag(LDSP const char* p, int d) {
  s16x4 lo = __builtin_amdgcn_ds_read_tr16_b64_v4i16((LDSP s16x4*)p);
  s16x4 hi = __builtin_amdgcn_ds_read_tr16_b64_v4i16((LDSP s16x4*)(p + d));
  return __builtin_bit_cast(u32x4, __builtin_shufflevector(lo, hi, 0, 1, 2, 3, 4, 5, 6, 7));
}
template <int NHG, int F> DI void k1_read(K1C& c) {
  constexpr int NF = 4 + NHG, S = F / NF, j = F % NF, Z = S >> 1, so = (Z % K1_NS) * K1_SLOT;
  if constexpr (j == 0) {
    c.X[S & 1][0] = ldsr16(c.xa[S & 1] + so);
    c.X[S & 1][1] = ldsr16(c.xa[S & 1] + so + 2048);
  }
  constexpr int wo = so + (S & 1) * 7168;
  if constexpr (NHG == 0 || j < 3) c.wf[F & 3] = tr_frag(c.wl + wo + j * 32, 1792);
  else c.wf[F & 3] = tr_frag((j == 3 ? c.wc0 : c.wc1) + wo, 1792);
}
template <int NHG, int F> DI void k1_frag(K1C& c) {
  constexpr int NF = 4 + NHG, S = F / NF, j = F % NF, Z = S >> 1, N = F + 3, fz = F % (2 * NF);
  if constexpr (S == 0) {
    if constexpr (NHG == 0 || j < 3) { G_MFMA0(c.acc[j][0], c.wf[F & 3], c.X[0][0]); G_MFMA0(c.acc[j][1], c.wf[F & 3], c.X[0][1]); }
    else G_MFMA0(c.acc[3][j - 3], c.wf[F & 3], c.ones);
  } else {
    if constexpr (NHG == 0 || j < 3) { G_MFMA(c.acc[j][0], c.wf[F & 3], c.X[S & 1][0]); G_MFMA(c.acc[j][1], c.wf[F & 3], c.X[S & 1][1]); }
    else G_MFMA(c.acc[3][j - 3], c.wf[F & 3], c.ones);
  }
  if constexpr ((S & 1) == NHG && j < 2) {
    if constexpr (S < 2) { G_MFMA0(c.sm[j], c.ones, c.X[S & 1][j]); G_MFMA0(c.sq[j], c.X[S & 1][j], c.X[S & 1][j]); }
    else { G_MFMA(c.sm[j], c.ones, c.X[S & 1][j]); G_MFMA(c.sq[j], c.X[S & 1][j], c.X[S & 1][j]); }
  }
  if constexpr (N < 18 * NF) {
    if constexpr (N % (2 * NF) == 0) {
      constexpr int ZN = N / (2 * NF), rem = (8 - ZN < K1_DEPTH - 1 ? 8 - ZN : K1_DEPTH - 1) * 4;
      asm volatile("s_waitcnt vmcnt(%0) lgkmcnt(0)\n\ts_barrier" ::"i"(rem) : "memory");
    }
    k1_read<NHG, N>(c);
  }
  if constexpr (Z + K1_DEPTH < 9 && fz < 4) k1_issue<Z + K1_DEPTH, fz>(c);
  SBAR();
}
template <int NHG, int... Fs> DI void k1_run(K1C& c, std::integer_sequence<int, Fs...>) {
  k1_read<NHG, 0>(c); k1_read<NHG, 1>(c); k1_read<NHG, 2>(c);
  SBAR();
  (k1_frag<NHG, Fs>(c), ...);
  asm volatile("s_nop 7" : "+v"(c.acc[0][0]), "+v"(c.acc[0][1]), "+v"(c.acc[1][0]), "+v"(c.acc[1][1]), "+v"(c.acc[2][0]), "+v"(c.acc[2][1]),
               "+v"(c.acc[3][0]), "+v"(c.acc[3][1]), "+v"(c.sm[0]), "+v"(c.sm[1]), "+v"(c.sq[0]), "+v"(c.sq[1]));
  SBAR();
}
template <int... Is> DI void k1_prologue(const K1C& c, std::integer_sequence<int, Is...>) { (k1_issue<Is / 4, Is % 4>(c), ...); }
extern "C" __global__ __launch_bounds__(512, 2) void k_qkv(const u16* __restrict__ x, const u16* __restrict__ wqkv, char* __restrict__ ws) {
  extern __shared__ __attribute__((aligned(16))) char smem[];
  LDSP char* lds = (LDSP char*)smem;
  const unsigned lds0 = (unsigned)(uintptr_t)lds;
  const int tid = threadIdx.x, lane = tid & 63, w = rfl(tid >> 6);
  const int xcd = blockIdx.x & 7, bi = blockIdx.x >> 3;
  const int rb = 4 * (xcd >> 1) + (bi & 3), cb = 8 * (xcd & 1) + (bi >> 2);
  const int c0 = 108 * cb - 4 * (cb & 1);
  const int m = lane & 15, g = lane >> 4, mg = w & 3, nh = w >> 2;
  STAMP_DECL; STAMP(0);
  unsigned pf;
  CODE_PF(pf, k_qkv, k_combine, 1024, w);
  K1C c;
  c.xb = (const char*)x + (128 * rb + 16 * w) * 1152;
  c.wb = (const char*)wqkv + c0 * 2;
  c.xo0 = (lane >> 3) * 1152 + (((lane & 7) ^ g) * 16);
  c.xo1 = (8 + (lane >> 3)) * 1152 + (((lane & 7) ^ (4 + g)) * 16);
  c.xd = lds0 + w * 2048;
  {
    const int p1 = w < 6 ? w + 8 : 13;
    const unsigned u0 = 64 * w + lane, r0 = u0 / 14, u1 = 64 * p1 + lane, r1 = u1 / 14;
    c.wo0 = ((r0 & ~12u) | ((r0 & 4u) << 1) | ((r0 & 8u) >> 1)) * 3456 + (u0 - r0 * 14) * 16;
    c.wo1 = ((r1 & ~12u) | ((r1 & 4u) << 1) | ((r1 & 8u) >> 1)) * 3456 + (u1 - r1 * 14) * 16;
    c.wd0 = lds0 + 16384 + w * 1024;
    c.wd1 = lds0 + 16384 + p1 * 1024;
  }
  k1_prologue(c, std::make_integer_sequence<int, 4 * K1_DEPTH>{});
  c.ones = u32x4{0x3f803f80u, 0x3f803f80u, 0x3f803f80u, 0x3f803f80u};
  PIN(c.ones);
  c.xa[0] = lds + (32 * mg + m) * 128 + ((g ^ (m >> 1)) * 16);
  c.xa[1] = lds + (32 * mg + m) * 128 + (((4 + g) ^ (m >> 1)) * 16);
  LDSP const char* wbase = lds + 16384 + (16 * (g >> 1) + 4 * (g & 1) + (m >> 2)) * K1_PITCH + (m & 3) * 8;
  c.wl = wbase + nh * 128;
  c.wc0 = wbase + mg * 32;
  c.wc1 = wbase + (mg < 3 ? mg + 4 : 6) * 32;
  STAMP(1);
  asm volatile("s_waitcnt vmcnt(%1)\n\ts_barrier" : "+v"(pf) : "i"(4 * (K1_DEPTH - 1)) : "memory");
  STAMP(2);
  if (nh) k1_run<1>(c, std::make_integer_sequence<int, 90>{});
  else k1_run<0>(c, std::make_integer_sequence<int, 72>{});
  STAMP(3);
  f32x2 st[2];
#pragma unroll
  for (int t = 0; t < 2; ++t) {
    const float d = (m & 2) ? ((m & 1) ? c.sq[t][3] : c.sq[t][2]) : ((m & 1) ? c.sq[t][1] : c.sq[t][0]);
    st[t] = f32x2{c.sm[t][0], d};
    if (g == (m >> 2)) *(LDSP f32x2*)(lds + K1_ST + nh * 1024 + (32 * mg + 16 * t + m) * 8) = st[t];
  }
  if (nh && m == 0) {
    *(LDSP f32x4*)(lds + K1_CS + (16 * mg + 4 * g) * 4) = c.acc[3][0];
    if (mg < 3) *(LDSP f32x4*)(lds + K1_CS + (16 * (mg + 4) + 4 * g) * 4) = c.acc[3][1];
  }
  asm volatile("s_waitcnt lgkmcnt(0)\n\ts_barrier" ::: "memory");
  LDSP char* stg = lds + K1_STG;
#pragma unroll
  for (int t = 0; t < 2; ++t) {
    const f32x2 p0 = *(LDSP const f32x2*)(lds + K1_ST + (32 * mg + 16 * t + m) * 8);
    const f32x2 p1 = *(LDSP const f32x2*)(lds + K1_ST + 1024 + (32 * mg + 16 * t + m) * 8);
    const float mean = (p0[0] + p1[0]) * (1.0f / DM);
    const float var = fmaxf((p0[1] + p1[1]) * (1.0f / DM) - mean * mean, 0.f);
    const float rstd = __builtin_amdgcn_rsqf(var + LN_EPS);
    if (cb == 0 && nh == 0 && g == 0) ((float*)(ws + WS_XMEAN))[128 * rb + 32 * mg + 16 * t + m] = mean;
#pragma unroll
    for (int j = 0; j < 4; ++j) {
      if (j < 3 || nh == 0) {
        const int col = 64 * nh + 16 * j + 4 * g;
        const f32x4 cc = *(LDSP const f32x4*)(lds + K1_CS + col * 4);
        const float sc = (c0 + col < DM) ? rstd * QSCALE : rstd;
        const f32x4 a = c.acc[j][t];
        u32x2 o;
        o[0] = cvtpk((a[0] - mean * cc[0]) * sc, (a[1] - mean * cc[1]) * sc);
        o[1] = cvtpk((a[2] - mean * cc[2]) * sc, (a[3] - mean * cc[3]) * sc);
        *(LDSP u32x2*)(stg + (32 * mg + 16 * t + m) * K1_SP + col * 2) = o;
      }
    }
  }
  const int off = 4 * (cb & 1), T0 = 128 * rb;
  char* kvb = ws + WS_KV + (size_t)(T0 >> 9) * CHUNK_B;
  if (cb >= 10) {
    asm volatile("s_waitcnt lgkmcnt(0)" ::: "memory");
    char* vb = kvb + 18432 + ((T0 >> 4) & 31) * 608;
#pragma unroll
    for (int tt = 0; tt < 2; ++tt) {
      const int tile = 4 * nh + 2 * tt + (g >> 1), cc = 16 * tile + m - off;
      const int hd = (cc + 18) / 18 - 1, d = cc - 18 * hd, G = 6 * cb + hd;
      const bool ok = cc >= 0 && cc < 108 && G >= 64;
#pragma unroll
      for (int t = 0; t < 2; ++t) {
        const u32x4 v = tr_frag(stg + (32 * mg + 16 * t + 4 * (g & 1) + (m >> 2)) * K1_SP + (16 * tile + 4 * (m & 3)) * 2, 8 * K1_SP);
        if (ok) *(u32x4*)(vb + (size_t)(G - 64) * KV_HEAD_B + (2 * mg + t) * 608 + (g & 1) * 304 + d * 16) = v;
      }
    }
    if (tid < 96 && 6 * cb + (tid >> 4) >= 64)
      *(u32x4*)(vb + (size_t)(6 * cb + (tid >> 4) - 64) * KV_HEAD_B + (tid & 15) * 304 + 288) = c.ones;
  }
  STAMP(4);
  if (cb <= 10) {
    asm volatile("s_waitcnt lgkmcnt(0)\n\ts_barrier" ::: "memory");
    const int tok = tid & 127, T = T0 + tok;
    unsigned d[2][9];
#pragma unroll
    for (int it = 0; it < 2; ++it) {
      const int hd = (w >> 1) + 4 * it;
      LDSP const unsigned* sp = (LDSP const unsigned*)(stg + tok * K1_SP + (off + 18 * (hd < 6 ? hd : 0)) * 2);
#pragma unroll
      for (int i = 0; i < 9; ++i) d[it][i] = sp[i];
    }
#pragma unroll
    for (int it = 0; it < 2; ++it) {
      const int hd = (w >> 1) + 4 * it, G = 6 * cb + hd, head = G & 31;
      if (hd < 6 && G < 64) {
        const u32x4 u0 = {d[it][0], d[it][1], d[it][2], d[it][3]}, u1 = {d[it][4], d[it][5], d[it][6], d[it][7]};
        if (G < 32) {
          char* qm = ws + WS_QM + (size_t)head * 65536 + (T >> 5) * 1024 + (T & 31) * 16;
          *(u32x4*)qm = u0; *(u32x4*)(qm + 512) = u1;
          ((unsigned*)(ws + WS_QT + (size_t)head * 8192))[T] = d[it][8];
        } else {
          char* ck = kvb + (size_t)head * KV_HEAD_B;
          char* km = ck + ((T >> 5) & 15) * 1024 + (T & 31) * 16;
          *(u32x4*)km = u0; *(u32x4*)(km + 512) = u1;
          *(unsigned*)(ck + 16384 + ((T >> 7) & 3) * 512 + (T & 31) * 16 + ((T >> 5) & 3) * 4) = d[it][8];
          float n2 = 0.f;
#pragma unroll
          for (int i = 0; i < 9; ++i) n2 = dot2(d[it][i], d[it][i], n2);
          n2 = xor32_max(n2);
          { auto r = __builtin_amdgcn_permlane16_swap(__float_as_uint(n2), __float_as_uint(n2), false, false); n2 = fmaxf(__uint_as_float(r[0]), __uint_as_float(r[1])); }
          if (lane < 16) ((float*)(ws + WS_KMAX))[((2 * rb + (w & 1)) * 32 + head) * 16 + lane] = n2;
        }
      }
    }
  }
  STAMP(5); STAMP_OUT(1, 6);
}

constexpr int K2_LDS = (int)KV_HEAD_B;
constexpr int NJ_W1 = 72 * 36, NJ_WD = 9 * 144, NJ_WO = 9 * 18, NJ_ALL = NJ_W1 + NJ_WD + NJ_WO;
struct PrepJob { const char* src; unsigned rs, c1, c2, voff, dst[4]; };
DI void prep_setup(PrepJob& j, int J, int lane, const u16* wo, const u16* wg, const u16* wu, const u16* wd) {
  if (J >= NJ_ALL) J = NJ_ALL - 1;
  const int n = lane & 15, g = lane >> 4;
  unsigned lrow = 8 * g;
  j.c1 = 0; j.c2 = 0;
  if (J < NJ_W1) {
    const int tg = J / 36, sm = J - tg * 36;
    j.src = (const char*)((sm & 1) ? wu : wg) + (size_t)(sm >> 1) * (32 * FF * 2) + tg * 128;
    j.rs = FF * 2;
#pragma unroll
    for (int t = 0; t < 4; ++t) j.dst[t] = (unsigned)WS_W1 + (4 * tg + t) * 36864 + sm * 1024;
  } else if (J < NJ_W1 + NJ_WD) {
    const int q = J - NJ_W1, cc = q / 9, tg = q - cc * 9;
    j.src = (const char*)wd + (size_t)cc * (32 * DM * 2) + tg * 128;
    j.rs = DM * 2;
#pragma unroll
    for (int t = 0; t < 4; ++t) { const int nt = 4 * tg + t, nb = nt / 9; j.dst[t] = (unsigned)WS_WD + ((nb * 144 + cc) * 9 + (nt - nb * 9)) * 1024; }
  } else {
    const int q = J - NJ_W1 - NJ_WD, step = q / 9, tg = q - step * 9;
    j.rs = DM * 2;
    if (step < 16) { j.src = (const char*)wo + (size_t)step * (36 * DM * 2) + tg * 128; lrow = 18 * (g >> 1) + 4 * (g & 1); j.c2 = 4; }
    else { j.src = (const char*)wo + (size_t)(288 * (step - 16) + 16) * (DM * 2) + tg * 128; lrow = 72 * g; j.c1 = 16; }
#pragma unroll
    for (int t = 0; t < 4; ++t) { const int nt = 4 * tg + t, nb = nt / 3; j.dst[t] = (unsigned)WS_WO + ((nb * 18 + step) * 3 + (nt - nb * 3)) * 1024; }
  }
#pragma unroll
  for (int t = 0; t < 4; ++t) j.dst[t] = rfl(j.dst[t]);
  j.voff = lrow * j.rs + n * 2;
}
template <int L>
DI void prep_load(const PrepJob& j, u32x4 (&r)[8]) {
  constexpr int jj = L >> 2, t = L & 3;
  const char* b = j.src + (jj + j.c1 * (jj >> 1) + j.c2 * (jj >> 2)) * j.rs;
  unsigned v;
  asm volatile("global_load_ushort %0, %1, %2 offset:%3" : "=v"(v) : "v"(j.voff), "s"(b), "i"(t * 32));
  r[jj][t] = v;
}
DI void prep_store(const PrepJob& j, const u32x4 (&r)[8], __amdgpu_buffer_rsrc_t rs, unsigned lane16) {
#pragma unroll
  for (int t = 0; t < 4; ++t) {
    const u32x4 v = {r[0][t] | (r[1][t] << 16), r[2][t] | (r[3][t] << 16), r[4][t] | (r[5][t] << 16), r[6][t] | (r[7][t] << 16)};
    bst16<PREP_AUX>(v, rs, lane16, j.dst[t]);
  }
}
#define PIN8(r) "+v"(r[0]), "+v"(r[1]), "+v"(r[2]), "+v"(r[3]), "+v"(r[4]), "+v"(r[5]), "+v"(r[6]), "+v"(r[7])

#define A_EXP(d, s) asm volatile("v_exp_f32 %0, %1" : "=v"(d) : "v"(s))
#define A_CVT(d, a, b) asm volatile("v_cvt_pk_bf16_f32 %0, %1, %2" : "=v"(d) : "v"(a), "v"(b))
#define A_MFMA_NEW(d, a, b, c) asm volatile("v_mfma_f32_32x32x16_bf16 %0, %1, %2, %3" : "=&v"(d) : "v"(a), "v"(b), "v"(c))
#define A_MFMA_ACC(d, a, b) asm volatile("v_mfma_f32_32x32x16_bf16 %0, %1, %2, %0" : "+v"(d) : "v"(a), "v"(b))
struct AttnC {
  LDSP const char *kmb, *ktb, *vb;
  u32x4 qm, qt[4], kf, ktl, v0, v1, p1;
  f32x16 negm, o;
};
constexpr int k_off(int T) { return (T >> 4) * (int)CHUNK_B + (T & 15) * 1024; }
constexpr int kt_off(int T) { return (T >> 4) * (int)CHUNK_B + ((T >> 2) & 3) * 512; }
constexpr int v_off(int T) { return (T >> 4) * (int)CHUNK_B + (T & 15) * 1216; }
template <int T, class F0, class F1>
DI void attn_tile(AttnC& c, const f32x16& S, f32x16& Sn, F0 hook0, F1 hook1) {
  float e0, e1, e2, e3, e4, e5, e6, e7;
  unsigned a0, a1, a2, a3;
  if (T < 63) A_MFMA_NEW(Sn, c.ktl, c.qt[(T + 1) & 3], c.negm);
  A_EXP(e0, S[0]); A_EXP(e1, S[1]);
  if (T < 62 && ((T + 2) & 3) == 0) c.ktl = ldsr16(c.ktb + kt_off(T + 2));
  A_EXP(e2, S[2]); A_EXP(e3, S[3]);
  if (T > 0) { A_MFMA_ACC(c.o, c.v1, c.p1); }
  c.v1 = ldsr16(c.vb + v_off(T) + 608);
  A_CVT(a0, e0, e1); A_EXP(e4, S[4]); A_CVT(a1, e2, e3); A_EXP(e5, S[5]);
  hook0();
  A_EXP(e6, S[6]); A_EXP(e7, S[7]);
  if (T < 63) A_MFMA_ACC(Sn, c.kf, c.qm);
  if (T < 62) c.kf = ldsr16(c.kmb + k_off(T + 2));
  A_CVT(a2, e4, e5); A_EXP(e0, S[8]); A_CVT(a3, e6, e7); A_EXP(e1, S[9]);
  A_EXP(e2, S[10]); A_EXP(e3, S[11]);
  { const u32x4 p0 = {a0, a1, a2, a3}; A_MFMA_ACC(c.o, c.v0, p0); }
  if (T < 63) c.v0 = ldsr16(c.vb + v_off(T + 1));
  A_CVT(a0, e0, e1); A_EXP(e4, S[12]); A_CVT(a1, e2, e3); A_EXP(e5, S[13]);
  hook1();
  A_EXP(e6, S[14]); A_EXP(e7, S[15]);
  A_CVT(a2, e4, e5);
  A_CVT(a3, e6, e7);
  c.p1 = u32x4{a0, a1, a2, a3};
}
DI void attn_group_slow(LDSP const char* km, LDSP const char* kt, LDSP const char* vp, const u32x4& qm, const u32x4 (&qt)[4], const f32x16& negm, f32x16& o) {
  const u32x4 ktl = ldsr16(kt);
#pragma unroll
  for (int t = 0; t < 4; ++t) {
    const u32x4 kf = ldsr16(km + t * 1024);
    const u32x4 v0 = ldsr16(vp + t * 1216), v1 = ldsr16(vp + t * 1216 + 608);
    f32x16 s = mfma32(ktl, qt[t], negm);
    s = mfma32(kf, qm, s);
    u32x4 p0, p1;
#pragma unroll
    for (int i = 0; i < 4; ++i) {
      p0[i] = cvtpk(__builtin_amdgcn_exp2f(s[2 * i]), __builtin_amdgcn_exp2f(s[2 * i + 1]));
      p1[i] = cvtpk(__builtin_amdgcn_exp2f(s[8 + 2 * i]), __builtin_amdgcn_exp2f(s[8 + 2 * i + 1]));
    }
    o = mfma32(v0, p0, o);
    o = mfma32(v1, p1, o);
  }
}
struct AttnSide { const char* kv; unsigned lane16, lds0; int w; PrepJob ja, jb; u32x4 ra[8], rb[8]; };
DI void attn_dma(const AttnSide& s, int chunk, int i) {
  int p = 8 * i + s.w; if (p > 36) p = 36;
  const unsigned o = chunk * (unsigned)CHUNK_B + p * 1024;
  dma16(s.kv, o + s.lane16, s.lds0 + o);
}
template <int G>
DI void attn_group(AttnC& c, f32x16& SA, f32x16& SB, AttnSide& s) {
  auto h = [&](auto I) {
    constexpr int i = decltype(I)::value;
    if constexpr (G < 3) { if constexpr (i < 5) attn_dma(s, G + 1, i); }
    else if constexpr (G >= 5 && G < 9) prep_load<8 * (G - 5) + i>(s.ja, s.ra);
    else if constexpr (G >= 9 && G < 13) prep_load<8 * (G - 9) + i>(s.jb, s.rb);
  };
#define IC(n) std::integral_constant<int, n>{}
  attn_tile<4 * G + 0>(c, SA, SB, [&] { h(IC(0)); }, [&] { h(IC(1)); });
  attn_tile<4 * G + 1>(c, SB, SA, [&] { h(IC(2)); }, [&] { h(IC(3)); });
  attn_tile<4 * G + 2>(c, SA, SB, [&] { h(IC(4)); }, [&] { h(IC(5)); });
  attn_tile<4 * G + 3>(c, SB, SA, [&] { h(IC(6)); }, [&] { h(IC(7)); });
#undef IC
}
extern "C" __global__ __launch_bounds__(512, 2) void k_attn(const u16* __restrict__ wo, const u16* __restrict__ wg, const u16* __restrict__ wu,
                                                 const u16* __restrict__ wd, char* __restrict__ ws) {
  extern __shared__ __attribute__((aligned(16))) char smem[];
  LDSP char* lds = (LDSP char*)smem;
  const int tid = threadIdx.x, lane = tid & 63, w = rfl(tid >> 6);
  const int xcd = blockIdx.x & 7, bi = blockIdx.x >> 3;
  const int head = 4 * xcd + (bi & 3), qb = bi >> 2;
  const int r = lane & 31, h = lane >> 5;
  const int qt32 = 8 * qb + w;
  STAMP_DECL; STAMP(0);
  AttnSide s;
  s.kv = ws + WS_KV + (size_t)head * KV_HEAD_B; s.lane16 = lane * 16; s.lds0 = (unsigned)(uintptr_t)lds; s.w = w;
  AttnC c;
  c.qm = gld16(ws + WS_QM, head * 65536 + qt32 * 1024 + lane * 16);
  unsigned qtl = gld4(ws + WS_QT, head * 8192 + (32 * qt32 + r) * 4);
  u32x4 km0 = gld16(ws + WS_KMAX, ((lane >> 1) * 32 + head) * 64 + (lane & 1) * 32);
  u32x4 km1 = gld16(ws + WS_KMAX, ((lane >> 1) * 32 + head) * 64 + (lane & 1) * 32 + 16);
#pragma unroll
  for (int i = 0; i < 5; ++i) attn_dma(s, 0, i);
  const int gw = blockIdx.x * 8 + w;
  prep_setup(s.ja, 2 * gw, lane, wo, wg, wu, wd);
  prep_setup(s.jb, 2 * gw + 1, lane, wo, wg, wu, wd);
  asm volatile("s_waitcnt vmcnt(5)" : "+v"(c.qm), "+v"(qtl), "+v"(km0), "+v"(km1)::"memory");
  float k2 = 0.f;
#pragma unroll
  for (int i = 0; i < 4; ++i) k2 = fmaxf(k2, fmaxf(__uint_as_float(km0[i]), __uint_as_float(km1[i])));
  k2 = xor32_max(k2);
  { auto rr = __builtin_amdgcn_permlane16_swap(__float_as_uint(k2), __float_as_uint(k2), false, false); k2 = fmaxf(__uint_as_float(rr[0]), __uint_as_float(rr[1])); }
  k2 = fmaxf(k2, __uint_as_float(__builtin_amdgcn_update_dpp(0u, __float_as_uint(k2), 0x128, 0xf, 0xf, false)));
  k2 = fmaxf(k2, __uint_as_float(__builtin_amdgcn_update_dpp(0u, __float_as_uint(k2), 0x124, 0xf, 0xf, false)));
  k2 = fmaxf(k2, __uint_as_float(__builtin_amdgcn_update_dpp(0u, __float_as_uint(k2), 0x122, 0xf, 0xf, false)));
  k2 = fmaxf(k2, __uint_as_float(__builtin_amdgcn_update_dpp(0u, __float_as_uint(k2), 0x121, 0xf, 0xf, false)));
  if (h) qtl = 0;
  float q2 = dot2(qtl, qtl, 0.f);
#pragma unroll
  for (int i = 0; i < 4; ++i) q2 = dot2(c.qm[i], c.qm[i], q2);
  q2 = xor32_sum(q2);
  const float mshift = __builtin_amdgcn_sqrtf(q2 * k2) * 1.001f;
#pragma unroll
  for (int t = 0; t < 4; ++t) { c.qt[t] = u32x4{0, 0, 0, 0}; c.qt[t][t] = qtl; PIN(c.qt[t]); }
#pragma unroll
  for (int i = 0; i < 16; ++i) { c.negm[i] = -mshift; c.o[i] = 0.f; }
  PIN(c.negm); PIN(c.o);
  const int vrow = r < 19 ? r : ((r >= 20 && r < 28) ? 0 : 18);
  c.kmb = lds + lane * 16;
  c.ktb = lds + 16384 + r * 16;
  c.vb = lds + 18432 + h * 304 + vrow * 16;
  STAMP(1);
  asm volatile("s_waitcnt vmcnt(0)\n\ts_barrier" ::: "memory");
  STAMP(2);
  f32x16 SA, SB;
  {
    c.ktl = ldsr16(c.ktb);
    const u32x4 k0 = ldsr16(c.kmb);
    c.kf = ldsr16(c.kmb + 1024);
    c.v0 = ldsr16(c.vb);
    A_MFMA_NEW(SA, c.ktl, c.qt[0], c.negm);
    A_MFMA_ACC(SA, k0, c.qm);
    asm volatile("s_nop 15");
  }
  attn_group<0>(c, SA, SB, s); attn_group<1>(c, SA, SB, s); attn_group<2>(c, SA, SB, s);
  asm volatile("s_waitcnt vmcnt(10)\n\ts_barrier" ::: "memory");
  attn_group<3>(c, SA, SB, s); attn_group<4>(c, SA, SB, s);
  asm volatile("s_waitcnt vmcnt(0)\n\ts_barrier" ::: "memory");
  STAMP(3);
  unsigned wr[5];
  const bool dobar = gw < DM;
  if (dobar) {
    const char* b = (const char*)wo + (size_t)wo_row(gw >> 5, (gw >> 3) & 3, gw & 7) * (DM * 2);
#pragma unroll
    for (int i = 0; i < 5; ++i) wr[i] = gld4(b, (i < 4 ? lane + 64 * i : 256 + r) * 4);
  }
  attn_group<5>(c, SA, SB, s); attn_group<6>(c, SA, SB, s); attn_group<7>(c, SA, SB, s); attn_group<8>(c, SA, SB, s);
  attn_group<9>(c, SA, SB, s); attn_group<10>(c, SA, SB, s);
  const __amdgpu_buffer_rsrc_t wrs = mkrsrc(ws, (unsigned)WS_END);
  asm volatile("s_waitcnt vmcnt(16)" : PIN8(s.ra), "+v"(wr[0]), "+v"(wr[1]), "+v"(wr[2]), "+v"(wr[3]), "+v"(wr[4])::"memory");
  prep_store(s.ja, s.ra, wrs, s.lane16);
  if (dobar) {
    float a = 0.f;
#pragma unroll
    for (int i = 0; i < 4; ++i) a = dot2(wr[i], 0x3f803f80u, a);
    a = dot2(h ? 0u : wr[4], 0x3f803f80u, a);
    a = xor16_sum(xor32_sum(a));
    a += __uint_as_float(__builtin_amdgcn_update_dpp(0u, __float_as_uint(a), 0x128, 0xf, 0xf, false));
    a += __uint_as_float(__builtin_amdgcn_update_dpp(0u, __float_as_uint(a), 0x124, 0xf, 0xf, false));
    a += __uint_as_float(__builtin_amdgcn_update_dpp(0u, __float_as_uint(a), 0x122, 0xf, 0xf, false));
    a += __uint_as_float(__builtin_amdgcn_update_dpp(0u, __float_as_uint(a), 0x121, 0xf, 0xf, false));
    a *= (1.0f / DM);
    const unsigned hi = cvtpk(a, 0.f) & 0xffffu;
    const unsigned lo = cvtpk(a - bflo(hi), 0.f) & 0xffffu;
    if (lane < 2) ((u16*)(ws + WS_WBAR))[lane * DM + gw] = (u16)(lane ? lo : hi);
  }
  attn_group<11>(c, SA, SB, s); attn_group<12>(c, SA, SB, s); attn_group<13>(c, SA, SB, s); attn_group<14>(c, SA, SB, s);
  asm volatile("s_waitcnt vmcnt(0)" : PIN8(s.rb)::"memory");
  prep_store(s.jb, s.rb, wrs, s.lane16);
  attn_group<15>(c, SA, SB, s);
  asm volatile("s_nop 1");
  A_MFMA_ACC(c.o, c.v1, c.p1);
  asm volatile("s_nop 15" : "+v"(c.o) : "v"(c.negm));
  STAMP(4);
  f32x16 o = c.o;
  float l;
  { auto rr = __builtin_amdgcn_permlane32_swap(__float_as_uint(o[10]), __float_as_uint(o[10]), false, false); l = __uint_as_float(rr[0]); }
  if (__builtin_expect(__any(!(l >= 9.094947e-13f)), 0)) {
    float mx = -INFINITY;
    f32x16 z, negm;
#pragma unroll
    for (int i = 0; i < 16; ++i) z[i] = 0.f;
#pragma unroll 1
    for (int tt = 0; tt < 64; ++tt) {
      const int ck = tt >> 4, tl = tt & 15;
      const u32x4 ktl = ldsr16(c.ktb + ck * CHUNK_B + (tl >> 2) * 512);
      const u32x4 kf = ldsr16(c.kmb + ck * CHUNK_B + tl * 1024);
      u32x4 qq = {0, 0, 0, 0};
#pragma unroll
      for (int i = 0; i < 4; ++i) qq[i] = ((tl & 3) == i) ? qtl : 0u;
      f32x16 sc = mfma32(ktl, qq, z);
      sc = mfma32(kf, c.qm, sc);
#pragma unroll
      for (int i = 0; i < 16; ++i) mx = fmaxf(mx, sc[i]);
    }
    mx = xor32_max(mx);
#pragma unroll
    for (int i = 0; i < 16; ++i) { negm[i] = -mx; o[i] = 0.f; }
#pragma unroll 1
    for (int gq = 0; gq < 16; ++gq) {
      const int ck = gq >> 2, gg = gq & 3;
      attn_group_slow(c.kmb + ck * CHUNK_B + gg * 4096, c.ktb + ck * CHUNK_B + gg * 512, c.vb + ck * CHUNK_B + gg * 4864, c.qm, c.qt, negm, o);
    }
    auto rr = __builtin_amdgcn_permlane32_swap(__float_as_uint(o[10]), __float_as_uint(o[10]), false, false);
    l = __uint_as_float(rr[0]);
  }
  const float rl = 1.0f / l;
  u32x4 ou;
#pragma unroll
  for (int i = 0; i < 4; ++i) ou[i] = cvtpk(o[2 * i] * rl, o[2 * i + 1] * rl);
  const int q = 32 * qt32 + r;
  char* ob = ws + WS_OFR + (size_t)(q >> 4) * 18432 + (q & 15) * 16;
  *(u32x4*)(ob + (head >> 1) * 1024 + (2 * (head & 1) + h) * 256) = ou;
  if (h == 0) *(unsigned*)(ob + (16 + (head >> 4)) * 1024 + ((head >> 2) & 3) * 256 + (head & 3) * 4) = cvtpk(o[8] * rl, o[9] * rl);
  STAMP(5); STAMP_OUT(2, 6);
}

constexpr int K3_WBAR = 54 * 1024, K3_LDS = K3_WBAR + 3072;
extern "C" __global__ __launch_bounds__(512, 2) void k_oproj(const u16* __restrict__ x, u16* __restrict__ hout, char* __restrict__ ws) {
  extern __shared__ __attribute__((aligned(16))) char smem[];
  LDSP char* lds = (LDSP char*)smem;
  const unsigned lds0 = (unsigned)(uintptr_t)lds;
  const int tid = threadIdx.x, lane = tid & 63, w = rfl(tid >> 6);
  const int xcd = blockIdx.x & 7, bi = blockIdx.x >> 3;
  const int rb = 2 * xcd + (bi & 1), nb = bi >> 1;
  const int m = lane & 15, g = lane >> 4;
  const int mt = 8 * rb + w, row = 16 * mt + m;
  STAMP_DECL; STAMP(0);
  u32x4 O[18];
  u32x2 xr[3];
  unsigned xm;
  {
    const char* wb = ws + WS_WO + (size_t)nb * 54 * 1024;
    const char* ob = ws + WS_OFR + (size_t)mt * 18432;
    if (w < 3) dma16(ws + WS_WBAR, w * 1024 + lane * 16, lds0 + K3_WBAR + w * 1024);
#pragma unroll
    for (int i = 0; i < 18; ++i) {
      if (i < 7) { int p = 8 * i + w; if (p > 53) p = 53; dma16(wb, p * 1024 + lane * 16, lds0 + p * 1024); }
      O[i] = gld16(ob, i * 1024 + lane * 16);
      if (i == 8) {
#pragma unroll
        for (int t = 0; t < 3; ++t)
          asm volatile("global_load_dwordx2 %0, %1, %2" : "=v"(xr[t]) : "v"(row * 1152 + (48 * nb + 16 * t + 4 * g) * 2), "s"((const char*)x));
        xm = gld4(ws + WS_XMEAN, row * 4);
      }
    }
  }
  f32x4 acc[4];
  LDSP const char* wl = lds + lane * 16;
  LDSP const char* wbl = lds + K3_WBAR + (m & 1) * 1152 + g * 16;
  STAMP(1);
  asm volatile("s_waitcnt vmcnt(15)\n\ts_barrier" : "+v"(O[0]), "+v"(O[1]), "+v"(O[2]), "+v"(O[3]), "+v"(O[4]), "+v"(O[5]), "+v"(O[6])::"memory");
  u32x4 wf[6];
#define K3_RD(f) (((f) & 3) == 3 ? ldsr16(wbl + ((f) >> 2) * 64) : ldsr16(wl + (3 * ((f) >> 2) + ((f) & 3)) * 1024))
#pragma unroll
  for (int f = 0; f < 5; ++f) wf[f] = K3_RD(f);
#pragma unroll
  for (int f = 0; f < 72; ++f) {
    if (f == 28) asm volatile("s_waitcnt vmcnt(5)" : "+v"(O[7]), "+v"(O[8]), "+v"(xr[0]), "+v"(xr[1]), "+v"(xr[2]), "+v"(xm), "+v"(O[9]), "+v"(O[10]), "+v"(O[11]), "+v"(O[12]));
    if (f == 52) asm volatile("s_waitcnt vmcnt(0)" : "+v"(O[13]), "+v"(O[14]), "+v"(O[15]), "+v"(O[16]), "+v"(O[17]));
    if (f + 5 < 72) wf[(f + 5) % 6] = K3_RD(f + 5);
    if (f < 4) G_MFMA0(acc[f & 3], wf[f % 6], O[f >> 2]);
    else G_MFMA(acc[f & 3], wf[f % 6], O[f >> 2]);
    SBAR();
  }
  asm volatile("s_nop 7" : "+v"(acc[0]), "+v"(acc[1]), "+v"(acc[2]), "+v"(acc[3]));
#undef K3_RD
  STAMP(2);
  const float mean = __uint_as_float(xm) + acc[3][0] + acc[3][1];
  float ssq = 0.f;
#pragma unroll
  for (int t = 0; t < 3; ++t) {
    u32x2 hb, hc;
    hb[0] = cvtpk(bflo(xr[t][0]) + acc[t][0], bfhi(xr[t][0]) + acc[t][1]);
    hb[1] = cvtpk(bflo(xr[t][1]) + acc[t][2], bfhi(xr[t][1]) + acc[t][3]);
    hc[0] = cvtpk(bflo(hb[0]) - mean, bfhi(hb[0]) - mean);
    hc[1] = cvtpk(bflo(hb[1]) - mean, bfhi(hb[1]) - mean);
    ssq = dot2(hc[0], hc[0], ssq); ssq = dot2(hc[1], hc[1], ssq);
    *(u32x2*)(hout + (size_t)row * DM + 48 * nb + 16 * t + 4 * g) = hb;
    *(u32x2*)(ws + WS_HC + (size_t)mt * 18432 + (6 * nb + 2 * t + (g >> 1)) * 256 + m * 16 + (g & 1) * 8) = hc;
  }
  ssq = xor32_sum(xor16_sum(ssq));
  if (g == 0) ((float*)(ws + WS_SSQ))[nb * 2048 + row] = ssq;
  STAMP(3); STAMP_OUT(3, 4);
}

constexpr int K4_SLOT = 36864, K4_LDS = 4 * K4_SLOT;
constexpr int K4_PF = 4;
struct K4C {
  const char *wb, *xb; unsigned lane16, lds0; int w, rot;
  const char* pfb; unsigned pfo, pf0, pf1;
  LDSP const char* wl[4];
  u32x4 X[2][18], wf[K4_PF + 1];
  f32x4 acc[4][4];
  float c1[2], c2[2];
  __amdgpu_buffer_rsrc_t ars; unsigned avo; int T0;
  float ea[2][2], et[2][2]; unsigned pk[2][4];
};
DI void k4_piece(const K4C& c, int tile, int p) {
  int q = p + 4 * c.rot; if (q >= 36) q -= 36;
  dma16(c.wb + (tile * K4_SLOT + q * 1024), c.lane16, c.lds0 + (tile & 3) * K4_SLOT + p * 1024);
}
template <int Z, int I> DI void k4_issue1(K4C& c) {
  if (I == 0) k4_piece(c, c.w >> 2, 4 * Z + (c.w & 3));
  else if (I < 5) {
    int z = Z + c.rot; if (z >= 9) z -= 9;
    c.X[(I - 1) >> 1][2 * Z + ((I - 1) & 1)] = gld16(c.xb + (((I - 1) >> 1) * 18432 + (2 * z + ((I - 1) & 1)) * 1024), c.lane16);
  }
  else if ((c.w >> 2) == (Z & 1)) k4_piece(c, 2, 4 * Z + (c.w & 3));
}
template <int TL, int I> DI void k4_issue2(K4C& c) {
  if (I < 4) k4_piece(c, TL, 8 * I + c.w);
  else if ((c.w >> 2) == (TL & 1)) k4_piece(c, TL, 32 + (c.w & 3));
}
template <int TL, int E, int U> DI void k4_epi(K4C& c) {
  constexpr int e = U >> 2, u = U & 3, mt = e >> 2, i = e & 3, q = e & 1;
  if constexpr (U < 32) {
    if (u == 0) { const float a = c.acc[TL & 3][mt][i] * c.c1[mt]; c.ea[E][q] = a; c.et[E][q] = a * c.acc[TL & 3][2 + mt][i]; PIN(c.ea[E][q]); PIN(c.et[E][q]); }
    if (u == 1) { c.ea[E][q] = __builtin_amdgcn_exp2f(c.ea[E][q]); PIN(c.ea[E][q]); }
    if (u == 2) { c.ea[E][q] = __builtin_amdgcn_rcpf(__builtin_fmaf(c.ea[E][q], c.c2[mt], c.c2[mt])); PIN(c.ea[E][q]); }
    if (u == 3) { c.et[E][q] *= c.ea[E][q]; PIN(c.et[E][q]); if (q) { c.pk[E][e >> 1] = cvtpk(c.et[E][0], c.et[E][1]); PIN(c.pk[E][e >> 1]); } }
  } else if constexpr (U == 32) {
    auto r0 = __builtin_amdgcn_permlane16_swap(c.pk[E][0], c.pk[E][2], false, false);
    auto r1 = __builtin_amdgcn_permlane16_swap(c.pk[E][1], c.pk[E][3], false, false);
    c.pk[E][0] = r0[0]; c.pk[E][1] = r1[0]; c.pk[E][2] = r0[1]; c.pk[E][3] = r1[1];
  } else if constexpr (U == 33) {
    const int T = c.T0 + TL;
    bst16<ACT_AUX>(u32x4{c.pk[E][0], c.pk[E][1], c.pk[E][2], c.pk[E][3]}, c.ars, c.avo, (T >> 1) * 1024 + (T & 1) * 512);
  }
}
constexpr int k4_tile(int F) { return F < 108 ? (F % 6) >> 1 : 3 + (F - 108) / 36; }
constexpr int k4_step(int F) { return F < 108 ? F / 6 : ((F - 108) % 36) >> 1; }
constexpr int k4_addr(int F) { return k4_step(F) * 2048 + (F & 1) * 1024; }
template <int F> DI void k4_frag(K4C& c) {
  constexpr int TL = k4_tile(F), S = k4_step(F), M = F & 1, N = F + K4_PF;
  const u32x4 a = c.wf[F % (K4_PF + 1)];
  if (S == 0) {
    G_MFMA0(c.acc[TL & 3][2 * M], a, c.X[0][S]);
    G_MFMA0(c.acc[TL & 3][2 * M + 1], a, c.X[1][S]);
  } else {
    G_MFMA(c.acc[TL & 3][2 * M], a, c.X[0][S]);
    G_MFMA(c.acc[TL & 3][2 * M + 1], a, c.X[1][S]);
  }
  if constexpr (N < 108 && N % 12 == 0) {
    constexpr int Z = N / 12;
    if (Z < 8) asm volatile("s_waitcnt vmcnt(5) lgkmcnt(0)\n\ts_barrier" : "+v"(c.X[0][2 * Z]), "+v"(c.X[0][2 * Z + 1]), "+v"(c.X[1][2 * Z]), "+v"(c.X[1][2 * Z + 1])::"memory");
    else asm volatile("s_waitcnt vmcnt(4) lgkmcnt(0)\n\ts_barrier" : "+v"(c.X[0][2 * Z]), "+v"(c.X[0][2 * Z + 1]), "+v"(c.X[1][2 * Z]), "+v"(c.X[1][2 * Z + 1])::"memory");
  } else if constexpr (N >= 108 && N < 324 && (N - 108) % 36 == 0) {
    constexpr int NT = k4_tile(N);
    if (NT == 3) asm volatile("s_waitcnt vmcnt(0) lgkmcnt(0)\n\ts_barrier" ::: "memory");
    else if (NT == 8) asm volatile("s_waitcnt vmcnt(%0) lgkmcnt(0)\n\ts_barrier" ::"i"(2 + K4_PFWD) : "memory");
    else asm volatile("s_waitcnt vmcnt(4) lgkmcnt(0)\n\ts_barrier" ::: "memory");
  }
  if constexpr (N < 324) c.wf[N % (K4_PF + 1)] = ldsr16(c.wl[k4_tile(N) & 3] + k4_addr(N));
  if constexpr (F < 108) {
    constexpr int Z = F / 12, I = F % 12;
    if constexpr (Z < 7 && I < 6) k4_issue1<Z + 2, I>(c);
    if constexpr (Z == 7 && I < 5) k4_issue2<3, I>(c);
  } else {
    constexpr int P = (F - 108) % 36;
    if constexpr (TL == 3) { if constexpr (P % 3 == 0 && P < 15) k4_issue2<4, P / 3>(c); if constexpr (P % 3 == 0 && P >= 15 && P < 30) k4_issue2<5, (P - 15) / 3>(c); }
    else if constexpr (TL + 2 < 9 && P % 4 == 0 && P < 20) k4_issue2<TL + 2, P / 4>(c);
    if constexpr (TL == 7 && P == 0) {
      if (K4_PFWD) c.pf0 = gld4(c.pfb, c.pfo);
      CODE_PF(c.pf1, k_down, k_combine, 1024, c.w & 1);
    }
    if constexpr (P >= 2) {
      if constexpr (TL == 3) { k4_epi<0, 0, P - 2>(c); k4_epi<1, 1, P - 2>(c); }
      else if constexpr (TL == 4) { k4_epi<2, 0, P - 2>(c); k4_epi<3, 1, P - 2>(c); }
      else k4_epi<TL - 1, 0, P - 2>(c);
    }
  }
  SBAR();
}
template <int B, int... Fs> DI void k4_run(K4C& c, std::integer_sequence<int, Fs...>) { (k4_frag<B + Fs>(c), ...); }
template <int... Us> DI void k4_last(K4C& c, std::integer_sequence<int, Us...>) { (k4_epi<8, 0, Us>(c), ...); }
extern "C" __global__ __launch_bounds__(512, 2) void k_gateup(char* __restrict__ ws) {
  extern __shared__ __attribute__((aligned(16))) char smem[];
  LDSP char* lds = (LDSP char*)smem;
  const int tid = threadIdx.x, lane = tid & 63, w = rfl(tid >> 6);
  const int xcd = blockIdx.x & 7, bi = blockIdx.x >> 3;
  const int rb = 4 * (xcd & 1) + (bi & 3), fs = 8 * (xcd >> 1) + (bi >> 2);
  const int m = lane & 15, g = lane >> 4;
  const int mt = 16 * rb + 2 * w;
  STAMP_DECL; STAMP(0);
  K4C c;
  c.lds0 = (unsigned)(uintptr_t)lds; c.lane16 = lane * 16; c.w = w;
  c.wb = ws + WS_W1 + (size_t)fs * 9 * K4_SLOT;
  c.xb = ws + WS_HC + (size_t)mt * 18432;
  c.rot = K4_ROT ? ((bi >> 2) + 2 * (bi & 3)) % 9 : 0;
  k4_issue1<0, 0>(c); k4_issue1<0, 1>(c); k4_issue1<0, 2>(c); k4_issue1<0, 3>(c); k4_issue1<0, 4>(c); k4_issue1<0, 5>(c);
  unsigned sq[6];
#pragma unroll
  for (int i = 0; i < 6; ++i) sq[i] = gld4(ws + WS_SSQ, (3 * g + (i >> 1)) * 8192 + (16 * mt + 16 * (i & 1) + m) * 4);
  k4_issue1<1, 0>(c); k4_issue1<1, 1>(c); k4_issue1<1, 2>(c); k4_issue1<1, 3>(c); k4_issue1<1, 4>(c); k4_issue1<1, 5>(c);
#pragma unroll
  for (int i = 0; i < 4; ++i) { c.wl[i] = lds + c.lane16 + i * K4_SLOT; PIN(c.wl[i]); }
  c.ars = mkrsrc(ws + WS_ACT, 128u * 144 * 1024);
  c.avo = (mt + (g & 1)) * (144 * 1024) + (g >> 1) * 256 + m * 16;
  c.T0 = 9 * fs;
  {
    const unsigned ln = (8 * bi + w) * 41 + (lane < 41 ? lane : 40), lc = ln < 10368 ? ln : 10367, nbq = lc / 2592;
    c.pfb = ws + WS_WD + (size_t)(xcd >> 1) * 331776;
    c.pfo = nbq * 1327104 + (lc - nbq * 2592) * 128;
    PIN(c.pfo);
  }
  STAMP(1);
  asm volatile("s_waitcnt vmcnt(5)\n\ts_barrier" : "+v"(sq[0]), "+v"(sq[1]), "+v"(sq[2]), "+v"(sq[3]), "+v"(sq[4]), "+v"(sq[5]),
               "+v"(c.X[0][0]), "+v"(c.X[0][1]), "+v"(c.X[1][0]), "+v"(c.X[1][1])::"memory");
  STAMP(2);
#pragma unroll
  for (int i = 0; i < K4_PF; ++i) c.wf[i] = ldsr16(c.wl[k4_tile(i)] + k4_addr(i));
#pragma unroll
  for (int t = 0; t < 2; ++t) {
    float ss = __uint_as_float(sq[t]) + __uint_as_float(sq[2 + t]) + __uint_as_float(sq[4 + t]);
    ss = xor32_sum(xor16_sum(ss));
    const float v = ss * (1.0f / DM) + LN_EPS, rs = __builtin_amdgcn_rsqf(v);
    c.c1[t] = -LOG2E * rs; c.c2[t] = -LOG2E * (v * rs);
    PIN(c.c1[t]); PIN(c.c2[t]);
  }
  SBAR();
  k4_run<0>(c, std::make_integer_sequence<int, 108>{});
  STAMP(3);
  k4_run<108>(c, std::make_integer_sequence<int, 72>{});
  STAMP(4);
  k4_run<180>(c, std::make_integer_sequence<int, 72>{});
  STAMP(5);
  k4_run<252>(c, std::make_integer_sequence<int, 72>{});
  asm volatile("s_nop 7" : "+v"(c.acc[0][0]), "+v"(c.acc[0][1]), "+v"(c.acc[0][2]), "+v"(c.acc[0][3]));
  STAMP(6);
  k4_last(c, std::make_integer_sequence<int, 34>{});
  asm volatile("s_waitcnt vmcnt(2)" : "+v"(c.pf0), "+v"(c.pf1));
  STAMP(7); STAMP_OUT(4, 8);
}

constexpr int K5_SLOT = 36864, K5_LDS = 4 * K5_SLOT;
struct K5C {
  const char *wb, *ab, *ab2; unsigned lane16, lds0; int w, rot;
  LDSP const char* wl[4];
  LDSP char* xw;
  u32x4 A[3][4], wf[5];
  f32x4 acc[2][9];
};
template <int Z, int I> DI void k5_issue(K5C& c) {
  int z = Z + c.rot; if (z >= 9) z -= 9;
  if (I < 8) {
    if (I & 1) c.A[Z % 3][I >> 1] = gld16(((I >> 2) ? c.ab2 : c.ab) + (2 * z + ((I >> 1) & 1)) * 1024, c.lane16);
    else { const int p = 4 * I + c.w, q = p >= 18 ? p + 144 : p; dma16(c.wb + (z * 18432 + q * 1024), c.lane16, c.lds0 + (Z & 3) * K5_SLOT + p * 1024); }
  } else if ((c.w >> 2) == (Z & 1)) {
    const int p = 32 + (c.w & 3); dma16(c.wb + (z * 18432 + (p + 144) * 1024), c.lane16, c.lds0 + (Z & 3) * K5_SLOT + p * 1024);
  }
}
template <int F> DI void k5_frag(K5C& c) {
  constexpr int Z = F / 18, f = F % 18, N = F + 4;
  const u32x4 a = c.wf[F % 5];
  if (F < 9) {
    G_MFMA0(c.acc[0][f % 9], a, c.A[Z % 3][f / 9]);
    G_MFMA0(c.acc[1][f % 9], a, c.A[Z % 3][2 + f / 9]);
  } else {
    G_MFMA(c.acc[1][f % 9], a, c.A[Z % 3][2 + f / 9]);
    G_MFMA(c.acc[0][f % 9], a, c.A[Z % 3][f / 9]);
  }
  if constexpr (N < 162 && N % 18 == 0) {
    u32x4(&A)[4] = c.A[(Z + 1) % 3];
    if (Z < 7) asm volatile("s_waitcnt vmcnt(8) lgkmcnt(0)\n\ts_barrier" : "+v"(A[0]), "+v"(A[1]), "+v"(A[2]), "+v"(A[3])::"memory");
    else asm volatile("s_waitcnt vmcnt(0) lgkmcnt(0)\n\ts_barrier" : "+v"(A[0]), "+v"(A[1]), "+v"(A[2]), "+v"(A[3])::"memory");
  }
  if constexpr (N < 162) c.wf[N % 5] = ldsr16(c.wl[(N / 18) & 3] + (N % 18) * 1024);
  if constexpr (Z + 2 < 9 && f < 14 && (f % 3) != 2) { constexpr int I = f - f / 3; if constexpr (I < 9) k5_issue<Z + 2, I>(c); }
  if constexpr (F >= 157) { PIN(c.acc[1][F - 157]); *(LDSP f32x4*)(c.xw + (F - 157) * 1024) = c.acc[1][F - 157]; }
  SBAR();
}
template <int... Fs> DI void k5_run(K5C& c, std::integer_sequence<int, Fs...>) { (k5_frag<Fs>(c), ...); }
template <int Z, int... Is> DI void k5_issue_all(K5C& c, std::integer_sequence<int, Is...>) { (k5_issue<Z, Is>(c), ...); }
extern "C" __global__ __launch_bounds__(512, 2) void k_down(char* __restrict__ ws) {
  extern __shared__ __attribute__((aligned(16))) char smem[];
  LDSP char* lds = (LDSP char*)smem;
  const int tid = threadIdx.x, lane = tid & 63, w = rfl(tid >> 6);
  const int xcd = blockIdx.x & 7, bi = blockIdx.x >> 3;
  const int kq = xcd >> 1, rb = 8 * (xcd & 1) + (bi & 7), nb = bi >> 3;
  const int mg = w & 3, kh = w >> 2;
  STAMP_DECL; STAMP(0);
  K5C c;
  c.lds0 = (unsigned)(uintptr_t)lds; c.lane16 = lane * 16; c.w = w;
  c.rot = K5_ROT ? ((bi & 7) + 2 * (bi >> 3)) % 9 : 0;
  c.wb = ws + WS_WD + ((size_t)nb * 144 + 36 * kq) * 9 * 1024;
  c.ab = ws + WS_ACT + ((size_t)(8 * rb + 2 * mg + kh) * 144 + 36 * kq + 18 * kh) * 1024;
  c.ab2 = ws + WS_ACT + ((size_t)(8 * rb + 2 * mg + (kh ^ 1)) * 144 + 36 * kq + 18 * kh) * 1024;
  k5_issue_all<0>(c, std::make_integer_sequence<int, 9>{});
  k5_issue_all<1>(c, std::make_integer_sequence<int, 9>{});
#pragma unroll
  for (int i = 0; i < 4; ++i) { c.wl[i] = lds + kh * 18432 + c.lane16 + i * K5_SLOT; PIN(c.wl[i]); }
  c.xw = lds + K5_SLOT + w * 9216 + c.lane16; PIN(c.xw);
  LDSP const char* xr = lds + K5_SLOT + (w ^ 4) * 9216 + c.lane16; PIN(xr);
  const __amdgpu_buffer_rsrc_t prs = mkrsrc(ws + WS_PART, 4u * 128 * 4 * 4608);
  const unsigned po = ((kq * 128 + 8 * rb + 2 * mg + kh) * 4 + nb) * 4608;
  STAMP(1);
  asm volatile("s_waitcnt vmcnt(8)\n\ts_barrier" : "+v"(c.A[0][0]), "+v"(c.A[0][1]), "+v"(c.A[0][2]), "+v"(c.A[0][3])::"memory");
  STAMP(2);
#pragma unroll
  for (int i = 0; i < 4; ++i) c.wf[i] = ldsr16(c.wl[0] + i * 1024);
  SBAR();
  k5_run(c, std::make_integer_sequence<int, 162>{});
  asm volatile("s_nop 7" : "+v"(c.acc[0][8]), "+v"(c.acc[1][8]), "+v"(c.acc[0][7]), "+v"(c.acc[1][7]), "+v"(c.acc[1][5]), "+v"(c.acc[1][6]));
  STAMP(4);
#pragma unroll
  for (int t = 5; t < 9; ++t) *(LDSP f32x4*)(c.xw + t * 1024) = c.acc[1][t];
  asm volatile("s_waitcnt lgkmcnt(0)\n\ts_barrier" ::: "memory");
  f32x4 p[9];
#pragma unroll
  for (int t = 0; t < 9; ++t) p[t] = *(LDSP const f32x4*)(xr + t * 1024);
  SBAR();
  unsigned o[18];
#pragma unroll
  for (int t = 0; t < 9; ++t) {
    const f32x4 a = c.acc[0][t] + p[t];
    o[2 * t] = cvtpk(a[0], a[1]);
    o[2 * t + 1] = cvtpk(a[2], a[3]);
    if (t & 1) bst16<PART_AUX>(u32x4{o[2 * t - 2], o[2 * t - 1], o[2 * t], o[2 * t + 1]}, prs, c.lane16, po + (t >> 1) * 1024);
  }
  __builtin_amdgcn_raw_buffer_store_b64(u32x2{o[16], o[17]}, prs, lane * 8, po + 4096, PART_AUX);
  STAMP(5); STAMP_OUT(5, 6);
}

extern "C" __global__ __launch_bounds__(320) void k_combine(u16* __restrict__ out, const char* __restrict__ ws) {
  const int lane = threadIdx.x & 63, j = threadIdx.x >> 6;
  const int mt = blockIdx.x >> 2, nb = blockIdx.x & 3;
  const int m = lane & 15, g = lane >> 4;
  const char* pb = ws + WS_PART + ((size_t)mt * 4 + nb) * 4608;
  constexpr size_t KQ = (size_t)128 * 4 * 4608;
  u16* ob = out + (size_t)(16 * mt + m) * DM + 144 * nb + 32 * j + 4 * g;
  if (j < 4) {
    u32x4 p[4];
#pragma unroll
    for (int k = 0; k < 4; ++k) p[k] = *(const u32x4*)(pb + k * KQ + j * 1024 + lane * 16);
    const u32x2 h0 = *(const u32x2*)ob, h1 = *(const u32x2*)(ob + 16);
    const unsigned hh[4] = {h0[0], h0[1], h1[0], h1[1]};
    unsigned o[4];
#pragma unroll
    for (int i = 0; i < 4; ++i)
      o[i] = cvtpk(bflo(hh[i]) + ((bflo(p[0][i]) + bflo(p[1][i])) + (bflo(p[2][i]) + bflo(p[3][i]))),
                   bfhi(hh[i]) + ((bfhi(p[0][i]) + bfhi(p[1][i])) + (bfhi(p[2][i]) + bfhi(p[3][i]))));
    *(u32x2*)ob = u32x2{o[0], o[1]};
    *(u32x2*)(ob + 16) = u32x2{o[2], o[3]};
  } else {
    u32x2 p[4];
#pragma unroll
    for (int k = 0; k < 4; ++k) p[k] = *(const u32x2*)(pb + k * KQ + 4096 + lane * 8);
    const u32x2 h0 = *(const u32x2*)ob;
    unsigned o[2];
#pragma unroll
    for (int i = 0; i < 2; ++i)
      o[i] = cvtpk(bflo(h0[i]) + ((bflo(p[0][i]) + bflo(p[1][i])) + (bflo(p[2][i]) + bflo(p[3][i]))),
                   bfhi(h0[i]) + ((bfhi(p[0][i]) + bfhi(p[1][i])) + (bfhi(p[2][i]) + bfhi(p[3][i]))));
    *(u32x2*)ob = u32x2{o[0], o[1]};
  }
}

extern "C" void kernel_launch(void* const* d_in, const int* in_sizes, int n_in, void* d_out, int out_size, void* d_ws, size_t ws_size,
                              hipStream_t stream) {
  const u16* x = (const u16*)d_in[0];
  const u16* wqkv = (const u16*)d_in[1];
  const u16* wo = (const u16*)d_in[2];
  const u16* wg = (const u16*)d_in[4];
  const u16* wu = (const u16*)d_in[5];
  const u16* wd = (const u16*)d_in[6];
  char* ws = (char*)d_ws;
  (void)hipFuncSetAttribute((const void*)k_qkv, hipFuncAttributeMaxDynamicSharedMemorySize, K1_LDS);
  (void)hipFuncSetAttribute((const void*)k_attn, hipFuncAttributeMaxDynamicSharedMemorySize, K2_LDS);
  (void)hipFuncSetAttribute((const void*)k_oproj, hipFuncAttributeMaxDynamicSharedMemorySize, K3_LDS);
  (void)hipFuncSetAttribute((const void*)k_gateup, hipFuncAttributeMaxDynamicSharedMemorySize, K4_LDS);
  (void)hipFuncSetAttribute((const void*)k_down, hipFuncAttributeMaxDynamicSharedMemorySize, K5_LDS);
  k_qkv<<<256, 512, K1_LDS, stream>>>(x, wqkv, ws);
  k_attn<<<256, 512, K2_LDS, stream>>>(wo, wg, wu, wd, ws);
  k_oproj<<<192, 512, K3_LDS, stream>>>(x, (u16*)d_out, ws);
  k_gateup<<<256, 512, K4_LDS, stream>>>(ws);
  k_down<<<256, 512, K5_LDS, stream>>>(ws);
  k_combine<<<512, 320, 0, stream>>>((u16*)d_out, ws);
}
```

```cpp
#include <hip/hip_runtime.h>
#include <hip/hip_bf16.h>
#include <stdint.h>
#include <utility>
#include <type_traits>

typedef __bf16 bf16x8 __attribute__((ext_vector_type(8)));
typedef __bf16 bf16x2 __attribute__((ext_vector_type(2)));
typedef short s16x4 __attribute__((ext_vector_type(4)));
typedef float f32x16 __attribute__((ext_vector_type(16)));
typedef float f32x4 __attribute__((ext_vector_type(4)));
typedef float f32x2 __attribute__((ext_vector_type(2)));
typedef unsigned u32x4 __attribute__((ext_vector_type(4)));
typedef unsigned u32x2 __attribute__((ext_vector_type(2)));
typedef unsigned short u16;
#define LDSP __attribute__((address_space(3)))
#define DI __device__ __forceinline__
#define SBAR() __builtin_amdgcn_sched_barrier(0)

constexpr int SEQ = 2048, DM = 576, NH = 32, DH = 18, FF = 4608;
constexpr float LOG2E = 1.4426950408889634f;
constexpr float QSCALE = 0.23570226039551584f * LOG2E;
constexpr float LN_EPS = 1e-5f;

constexpr size_t CHUNK_B = 37888;
constexpr size_t KV_HEAD_B = 4 * CHUNK_B;
constexpr size_t WS_QM = 0;
constexpr size_t WS_QT = WS_QM + (size_t)NH * 65536;
constexpr size_t WS_KV = WS_QT + (size_t)NH * 8192;
constexpr size_t WS_KMAX = WS_KV + (size_t)NH * KV_HEAD_B;
constexpr size_t WS_XMEAN = WS_KMAX + 65536;
constexpr size_t WS_OFR = WS_XMEAN + 8192;
constexpr size_t WS_WO = WS_OFR + (size_t)128 * 18 * 1024;
constexpr size_t WS_WBAR = WS_WO + (size_t)12 * 54 * 1024;
constexpr size_t WS_HC = WS_WBAR + 3072;
constexpr size_t WS_SSQ = WS_HC + (size_t)64 * 36 * 1024;
constexpr size_t WS_W1 = WS_SSQ + (size_t)12 * 8192;
constexpr size_t WS_WD = WS_W1 + (size_t)288 * 36 * 1024;
constexpr size_t WS_ACT = WS_WD + (size_t)4 * 144 * 9 * 1024;
constexpr size_t UNIT_B = (size_t)128 * 18432;
constexpr size_t WS_PART = WS_ACT + (size_t)128 * 144 * 1024;
constexpr size_t WS_PART8 = WS_PART + (size_t)4 * 64 * 8 * 3 * 1024;
constexpr size_t WS_CNT = WS_PART8 + (size_t)64 * 8 * 4 * 512;
constexpr size_t WS_XU = WS_CNT + 32768;
constexpr size_t WS_HU = WS_XU + UNIT_B;
constexpr size_t WS_END = WS_HU + UNIT_B;

DI unsigned cvtpk(float lo, float hi) {
  f32x2 v = {lo, hi};
  bf16x2 b = __builtin_convertvector(v, bf16x2);
  return __builtin_bit_cast(unsigned, b);
}
DI float bflo(unsigned u) { return __uint_as_float(u << 16); }
DI float bfhi(unsigned u) { return __uint_as_float(u & 0xffff0000u); }
DI float dot2(unsigned a, unsigned b, float c) {
  return __builtin_amdgcn_fdot2_f32_bf16(__builtin_bit_cast(bf16x2, a), __builtin_bit_cast(bf16x2, b), c, false);
}
DI f32x16 mfma32(u32x4 a, u32x4 b, f32x16 c) {
  return __builtin_amdgcn_mfma_f32_32x32x16_bf16(__builtin_bit_cast(bf16x8, a), __builtin_bit_cast(bf16x8, b), c, 0, 0, 0);
}
DI f32x4 mfma16(u32x4 a, u32x4 b, f32x4 c) {
  return __builtin_amdgcn_mfma_f32_16x16x32_bf16(__builtin_bit_cast(bf16x8, a), __builtin_bit_cast(bf16x8, b), c, 0, 0, 0);
}
DI unsigned rfl(unsigned x) { return __builtin_amdgcn_readfirstlane(x); }
DI const char* uniptr(const char* p) {
  uint64_t v = (uint64_t)p;
  unsigned lo = rfl((unsigned)v), hi = rfl((unsigned)(v >> 32));
  return (const char*)(((uint64_t)hi << 32) | lo);
}
DI void dma16(const char* sbase, unsigned voff, unsigned lds) {
  asm volatile("s_mov_b32 m0, %2\n\ts_nop 0\n\tglobal_load_lds_dwordx4 %0, %1" ::"v"(voff), "s"(sbase), "s"(lds));
}
DI u32x4 gld16(const char* sbase, unsigned voff) {
  u32x4 r;
  asm volatile("global_load_dwordx4 %0, %1, %2" : "=v"(r) : "v"(voff), "s"(sbase));
  return r;
}
DI unsigned gld4(const char* sbase, unsigned voff) {
  unsigned r;
  asm volatile("global_load_dword %0, %1, %2" : "=v"(r) : "v"(voff), "s"(sbase));
  return r;
}
#define WAITV(n) asm volatile("s_waitcnt vmcnt(" #n ")" ::: "memory")
#define BARRIER() asm volatile("s_barrier" ::: "memory")
#define PIN(x) asm volatile("" : "+v"(x))
DI u32x4 ldsr16(LDSP const char* p) { return *(LDSP const u32x4*)p; }
DI float xor32_sum(float v) {
  auto r = __builtin_amdgcn_permlane32_swap(__float_as_uint(v), __float_as_uint(v), false, false);
  return __uint_as_float(r[0]) + __uint_as_float(r[1]);
}
DI float xor32_max(float v) {
  auto r = __builtin_amdgcn_permlane32_swap(__float_as_uint(v), __float_as_uint(v), false, false);
  return fmaxf(__uint_as_float(r[0]), __uint_as_float(r[1]));
}
DI float xor16_sum(float v) {
  auto r = __builtin_amdgcn_permlane16_swap(__float_as_uint(v), __float_as_uint(v), false, false);
  return __uint_as_float(r[0]) + __uint_as_float(r[1]);
}

#define STAMP_DECL
#define STAMP(i)
#define STAMP_OUT(kid, n)

DI int wo_row(int step, int g, int j) {
  if (step < 16) return 18 * (2 * step + (g >> 1)) + 4 * (g & 1) + (j & 3) + 8 * (j >> 2);
  return 18 * (4 * (4 * (step - 16) + g) + (j >> 1)) + 16 + (j & 1);
}

#define K4_SPREAD 1
#define K4_PM 1
#define K4_WD 1
#define K3_PF 0
#define K3_SLEEP 32
#define K1_ROT 0
#define K4_ROT 0
#define K5_FLAG 0
#define K5_SPREAD 1
#define K5_PM 0
#define OUT_AUX 16
#define K4_PFWD 1
#define QKV_AUX 16
#define QKT_AUX 16
#define V_AUX 0
#define O_AUX 16
#define H_AUX 16
#define PART_AUX 16
#define ACT_AUX 16
#define PREP_AUX 16
#define CODE_PF(dst, first, last, extra, blk)                                                                              \
  do {                                                                                                                     \
    uint64_t pc_; int b_, e_;                                                                                              \
    asm volatile("s_getpc_b64 %0\n.Lpf%=:\n\ts_mov_b32 %1, " #first "-.Lpf%=\n\ts_mov_b32 %2, " #last "-.Lpf%=" : "=s"(pc_), "=s"(b_), "=s"(e_)); \
    const unsigned o_ = (blk) * 8192u + (threadIdx.x & 63) * 128u, n_ = (unsigned)(e_ - b_) + (extra) - 4u;                \
    dst = gld4((const char*)(pc_ + b_), o_ < n_ ? o_ : n_);                                                                \
  } while (0)
#define G_MFMA(d, a, b) asm volatile("v_mfma_f32_16x16x32_bf16 %0, %1, %2, %0" : "+v"(d) : "v"(a), "v"(b))
#define G_MFMA0(d, a, b) asm volatile("v_mfma_f32_16x16x32_bf16 %0, %1, %2, 0" : "=&v"(d) : "v"(a), "v"(b))
template <int AUX> DI void bst16(u32x4 v, __amdgpu_buffer_rsrc_t rs, unsigned voff, unsigned soff) {
  __builtin_amdgcn_raw_buffer_store_b128(v, rs, voff, soff, AUX);
  asm volatile("s_nop 1" ::"v"(v) : "memory");
}
DI __amdgpu_buffer_rsrc_t mkrsrc(const void* p, unsigned bytes) {
  return __builtin_amdgcn_make_buffer_rsrc((void*)p, 0, bytes, 0x00020000);
}

#define K1_DEPTH 2
constexpr int K1_PITCH = 224, K1_SLOT = 16384 + 64 * K1_PITCH  , K1_NS = K1_DEPTH + 1;
constexpr int K1_SP = 232;
constexpr int K1_STG = K1_NS * K1_SLOT, K1_CS = K1_STG + 128 * K1_SP + 64, K1_ST = K1_CS + 512 + 64, K1_LDS = K1_ST + 2048;
struct K1C {
  const char* xb; const char* wb; unsigned xo0, xo1, wo0, wo1, xd, wd0, wd1; int rot;
  LDSP const char *xa[2], *wl, *wc0, *wc1;
  u32x4 X[2][2], wf[4], ones;
  f32x4 acc[4][2];
  f32x4 sm[2]; float sq[2];
};
template <int Z, int I> DI void k1_issue(const K1C& c) {
  constexpr unsigned so = (Z % K1_NS) * K1_SLOT;
  int z = Z + c.rot; if (z >= 9) z -= 9;
  if (I == 0) dma16(c.xb + z * 128, c.xo0, c.xd + so);
  if (I == 1) dma16(c.wb + z * (64 * 3456), c.wo0, c.wd0 + so);
  if (I == 2) dma16(c.xb + z * 128, c.xo1, c.xd + so + 1024);
  if (I == 3) dma16(c.wb + z * (64 * 3456), c.wo1, c.wd1 + so);
}
DI u32x4 tr_frag(LDSP const char* p, int d) {
  s16x4 lo = __builtin_amdgcn_ds_read_tr16_b64_v4i16((LDSP s16x4*)p);
  s16x4 hi = __builtin_amdgcn_ds_read_tr16_b64_v4i16((LDSP s16x4*)(p + d));
  return __builtin_bit_cast(u32x4, __builtin_shufflevector(lo, hi, 0, 1, 2, 3, 4, 5, 6, 7));
}
template <int NHG, int F> DI void k1_read(K1C& c) {
  constexpr int NF = 4 + NHG, S = F / NF, j = F % NF, Z = S >> 1, so = (Z % K1_NS) * K1_SLOT;
  if constexpr (j == 0) {
    c.X[S & 1][0] = ldsr16(c.xa[S & 1] + so);
    c.X[S & 1][1] = ldsr16(c.xa[S & 1] + so + 2048);
  }
  constexpr int wo = so + (S & 1) * 7168;
  if constexpr (NHG == 0 || j < 3) c.wf[F & 3] = tr_frag(c.wl + wo + j * 32, 1792);
  else c.wf[F & 3] = tr_frag((j == 3 ? c.wc0 : c.wc1) + wo, 1792);
}
template <int NHG, int F> DI void k1_frag(K1C& c) {
  constexpr int NF = 4 + NHG, S = F / NF, j = F % NF, Z = S >> 1, N = F + 3, fz = F % (2 * NF);
  if constexpr (S == 0) {
    if constexpr (NHG == 0 || j < 3) { G_MFMA0(c.acc[j][0], c.wf[F & 3], c.X[0][0]); G_MFMA0(c.acc[j][1], c.wf[F & 3], c.X[0][1]); }
    else G_MFMA0(c.acc[3][j - 3], c.wf[F & 3], c.ones);
  } else {
    if constexpr (NHG == 0 || j < 3) { G_MFMA(c.acc[j][0], c.wf[F & 3], c.X[S & 1][0]); G_MFMA(c.acc[j][1], c.wf[F & 3], c.X[S & 1][1]); }
    else G_MFMA(c.acc[3][j - 3], c.wf[F & 3], c.ones);
  }
  if constexpr ((S & 1) == NHG && j < 4) {
#pragma unroll
    for (int t = 0; t < 2; ++t) { const unsigned v = c.X[S & 1][t][j]; c.sq[t] = dot2(v, v, c.sq[t]); PIN(c.sq[t]); }
    if constexpr (j < 2) { if constexpr (S < 2) G_MFMA0(c.sm[j], c.ones, c.X[S & 1][j]); else G_MFMA(c.sm[j], c.ones, c.X[S & 1][j]); }
  }
  if constexpr (N < 18 * NF) {
    if constexpr (N % (2 * NF) == 0) {
      constexpr int ZN = N / (2 * NF), rem = (8 - ZN < K1_DEPTH - 1 ? 8 - ZN : K1_DEPTH - 1) * 4;
      asm volatile("s_waitcnt vmcnt(%0) lgkmcnt(0)\n\ts_barrier" ::"i"(rem) : "memory");
    }
    k1_read<NHG, N>(c);
  }
  if constexpr (Z + K1_DEPTH < 9 && fz < 4) k1_issue<Z + K1_DEPTH, fz>(c);
  SBAR();
}
template <int NHG, int... Fs> DI void k1_run(K1C& c, std::integer_sequence<int, Fs...>) {
  k1_read<NHG, 0>(c); k1_read<NHG, 1>(c); k1_read<NHG, 2>(c);
  SBAR();
  (k1_frag<NHG, Fs>(c), ...);
  asm volatile("s_nop 7" : "+v"(c.acc[0][0]), "+v"(c.acc[0][1]), "+v"(c.acc[1][0]), "+v"(c.acc[1][1]), "+v"(c.acc[2][0]), "+v"(c.acc[2][1]),
               "+v"(c.acc[3][0]), "+v"(c.acc[3][1]), "+v"(c.sm[0]), "+v"(c.sm[1]));
  SBAR();
}
template <int... Is> DI void k1_prologue(const K1C& c, std::integer_sequence<int, Is...>) { (k1_issue<Is / 4, Is % 4>(c), ...); }
extern "C" __global__ __launch_bounds__(512, 2) void k_qkv(const u16* x, const u16* wqkv, char* ws) {
  extern __shared__ __attribute__((aligned(16))) char smem[];
  asm volatile("" : "+s"(x), "+s"(wqkv), "+s"(ws));
  LDSP char* lds = (LDSP char*)smem;
  const unsigned lds0 = (unsigned)(uintptr_t)lds;
  const int tid = threadIdx.x, lane = tid & 63, w = rfl(tid >> 6);
  const int xcd = blockIdx.x & 7, bi = blockIdx.x >> 3;
  const int rb = 4 * (xcd >> 1) + (bi & 3), cb = 8 * (xcd & 1) + (bi >> 2);
  const int c0 = 108 * cb - 4 * (cb & 1);
  const int m = lane & 15, g = lane >> 4, mg = w & 3, nh = w >> 2;
  STAMP_DECL; STAMP(0);
  unsigned pf;
  CODE_PF(pf, k_qkv, k_end, 0, w);
  K1C c;
  c.xb = (const char*)x + (128 * rb + 16 * w) * 1152;
  c.wb = (const char*)wqkv + c0 * 2;
  c.xo0 = (lane >> 3) * 1152 + (((lane & 7) ^ g) * 16);
  c.xo1 = (8 + (lane >> 3)) * 1152 + (((lane & 7) ^ (4 + g)) * 16);
  c.xd = lds0 + w * 2048;
  c.rot = K1_ROT == 1 ? ((bi >> 2) + 2 * (bi & 3)) % 9 : (K1_ROT == 2 ? (((bi >> 2) & 1) * 4 + (bi & 1) * 2) : (K1_ROT == 3 ? ((bi >> 2) + 2 * (bi & 3) + 3 * (xcd >> 1) + (xcd & 1)) % 9 : 0));
  {
    const int p1 = w < 6 ? w + 8 : 13;
    const unsigned u0 = 64 * w + lane, r0 = u0 / 14, u1 = 64 * p1 + lane, r1 = u1 / 14;
    c.wo0 = ((r0 & ~12u) | ((r0 & 4u) << 1) | ((r0 & 8u) >> 1)) * 3456 + (u0 - r0 * 14) * 16;
    c.wo1 = ((r1 & ~12u) | ((r1 & 4u) << 1) | ((r1 & 8u) >> 1)) * 3456 + (u1 - r1 * 14) * 16;
    c.wd0 = lds0 + 16384 + w * 1024;
    c.wd1 = lds0 + 16384 + p1 * 1024;
  }
  k1_prologue(c, std::make_integer_sequence<int, 4 * K1_DEPTH>{});
  c.ones = u32x4{0x3f803f80u, 0x3f803f80u, 0x3f803f80u, 0x3f803f80u};
  PIN(c.ones);
  c.xa[0] = lds + (32 * mg + m) * 128 + ((g ^ (m >> 1)) * 16);
  c.xa[1] = lds + (32 * mg + m) * 128 + (((4 + g) ^ (m >> 1)) * 16);
  LDSP const char* wbase = lds + 16384 + (16 * (g >> 1) + 4 * (g & 1) + (m >> 2)) * K1_PITCH + (m & 3) * 8;
  c.wl = wbase + nh * 128;
  c.wc0 = wbase + mg * 32;
  c.wc1 = wbase + (mg < 3 ? mg + 4 : 6) * 32;
  c.sq[0] = c.sq[1] = 0.f;
  const int off = 4 * (cb & 1), T0 = 128 * rb;
  LDSP char* stg = lds + K1_STG;
  LDSP char* a_st = lds + K1_ST + (32 * mg + m) * 8;                 PIN(a_st);
  LDSP char* a_cs = lds + K1_CS + (64 * nh + 4 * g) * 4;             PIN(a_cs);
  LDSP char* a_sw = stg + (32 * mg + m) * K1_SP + (64 * nh + 4 * g) * 2;   PIN(a_sw);
  LDSP char* a_tr = stg + (32 * mg + 4 * (g & 1) + (m >> 2)) * K1_SP + (64 * nh + 16 * (g >> 1) + 4 * (m & 3)) * 2;   PIN(a_tr);
  float qs[4];
#pragma unroll
  for (int j = 0; j < 4; ++j) { qs[j] = (c0 + 64 * nh + 16 * j + 4 * g < DM) ? QSCALE : 1.0f; PIN(qs[j]); }
  const __amdgpu_buffer_rsrc_t wrs = mkrsrc(ws, (unsigned)WS_END);
  constexpr unsigned DROP = 0x80000000u;
  const unsigned kvo = (unsigned)WS_KV + (T0 >> 9) * (unsigned)CHUNK_B;
  unsigned vo[2] = {DROP, DROP}, vone = DROP;
  if (cb >= 10) {
#pragma unroll
  for (int tt = 0; tt < 2; ++tt) {
    const int cc = 64 * nh + 32 * tt + 16 * (g >> 1) + m - off, hd = (cc + 18) / 18 - 1, G = 6 * cb + hd;
    vo[tt] = (cc >= 0 && cc < 108 && G >= 64) ? kvo + 18432 + (((T0 >> 4) & 31) + 2 * mg) * 608 + (G - 64) * (unsigned)KV_HEAD_B + (g & 1) * 304 + (cc - 18 * hd) * 16 : DROP;
  }
  vone = (tid < 96 && 6 * cb + (tid >> 4) >= 64) ? kvo + 18432 + ((T0 >> 4) & 31) * 608 + (6 * cb + (tid >> 4) - 64) * (unsigned)KV_HEAD_B + (tid & 15) * 304 + 288 : DROP;
  }
  PIN(vone); PIN(vo[0]); PIN(vo[1]);
  const int tok = tid & 127, T = T0 + tok;
  LDSP const char* a_g = stg + tok * K1_SP + off * 2;   PIN(a_g);
  unsigned um[2], ut[2];
  const unsigned tl = ((T >> 5) & 15) * 1024 + (T & 31) * 16;
#pragma unroll
  for (int it = 0; it < 2; ++it) {
    const int hd = (w >> 1) + 4 * it, G = 6 * cb + hd, head = G & 31;
    if (hd >= 6 || G >= 64) { um[it] = DROP; ut[it] = DROP; }
    else if (G < 32) { um[it] = (unsigned)WS_QM + head * 65536 + (T >> 9) * 16384 + tl; ut[it] = (unsigned)WS_QT + head * 8192 + T * 4; }
    else { um[it] = kvo + head * (unsigned)KV_HEAD_B + tl; ut[it] = kvo + head * (unsigned)KV_HEAD_B + 16384 + ((T >> 6) & 7) * 256 + (T & 31) * 8 + ((T >> 5) & 1) * 4; }
    PIN(um[it]); PIN(ut[it]);
  }
  STAMP(1);
  asm volatile("s_waitcnt vmcnt(%1)\n\ts_barrier" : "+v"(pf) : "i"(4 * (K1_DEPTH - 1)) : "memory");
  STAMP(2);
  if (nh) k1_run<1>(c, std::make_integer_sequence<int, 90>{});
  else k1_run<0>(c, std::make_integer_sequence<int, 72>{});
  STAMP(3);
  if (blockIdx.x < 4 && w == 0) bst16<16>(u32x4{0, 0, 0, 0}, wrs, (unsigned)WS_CNT + (blockIdx.x * 64 + lane) * 128, 0);
#pragma unroll
  for (int t = 0; t < 2; ++t) {
    const f32x2 st = {c.sm[t][0], xor32_sum(xor16_sum(c.sq[t]))};
    *(LDSP f32x2*)(a_st + nh * 1024 + t * 128) = st;
  }
  if (nh) {
    *(LDSP f32x4*)(lds + K1_CS + (16 * mg + 4 * g) * 4) = c.acc[3][0];
    *(LDSP f32x4*)(lds + K1_CS + (16 * (mg + 4) + 4 * g) * 4) = c.acc[3][1];
  }
  asm volatile("s_waitcnt lgkmcnt(0)\n\ts_barrier" ::: "memory");
  {
    f32x2 p[2][2]; f32x4 cc[4];
#pragma unroll
    for (int t = 0; t < 2; ++t) { p[t][0] = *(LDSP const f32x2*)(a_st + t * 128); p[t][1] = *(LDSP const f32x2*)(a_st + 1024 + t * 128); }
#pragma unroll
    for (int j = 0; j < 4; ++j) cc[j] = *(LDSP const f32x4*)(a_cs + j * 64);
    SBAR();
#pragma unroll
    for (int t = 0; t < 2; ++t) {
      const float mean = (p[t][0][0] + p[t][1][0]) * (1.0f / DM);
      const float var = fmaxf((p[t][0][1] + p[t][1][1]) * (1.0f / DM) - mean * mean, 0.f);
      const float rstd = __builtin_amdgcn_rsqf(var + LN_EPS);
      if (cb == 0 && nh == 0) __builtin_amdgcn_raw_buffer_store_b32(__float_as_uint(mean), wrs, (unsigned)WS_XMEAN + (T0 + 32 * mg + 16 * t + m) * 4, 0, QKV_AUX);
#pragma unroll
      for (int j = 0; j < 4; ++j) {
        if (j < 3 || nh == 0) {
          const f32x4 a = (c.acc[j][t] - mean * cc[j]) * (rstd * qs[j]);
          *(LDSP u32x2*)(a_sw + t * (16 * K1_SP) + j * 32) = u32x2{cvtpk(a[0], a[1]), cvtpk(a[2], a[3])};
        }
      }
    }
  }
  if (cb >= 10) {
    asm volatile("s_waitcnt lgkmcnt(0)" ::: "memory");
    u32x4 v[2][2];
#pragma unroll
    for (int tt = 0; tt < 2; ++tt)
#pragma unroll
      for (int t = 0; t < 2; ++t) v[tt][t] = tr_frag(a_tr + t * (16 * K1_SP) + tt * 64, 8 * K1_SP);
    bst16<V_AUX>(c.ones, wrs, vone, 0);
#pragma unroll
    for (int tt = 0; tt < 2; ++tt)
#pragma unroll
      for (int t = 0; t < 2; ++t) bst16<V_AUX>(v[tt][t], wrs, vo[tt] + t * 608, 0);
  }
  STAMP(4);
  if (cb <= 10) {
    asm volatile("s_waitcnt lgkmcnt(0)\n\ts_barrier" ::: "memory");
    unsigned d[2][9];
#pragma unroll
    for (int it = 0; it < 2; ++it) {
      const int hd = (w >> 1) + 4 * it;
      LDSP const unsigned* sp = (LDSP const unsigned*)(a_g + 36 * (hd < 6 ? hd : 0));
#pragma unroll
      for (int i = 0; i < 9; ++i) d[it][i] = sp[i];
    }
    SBAR();
#pragma unroll
    for (int it = 0; it < 2; ++it) {
      bst16<QKV_AUX>(u32x4{d[it][0], d[it][1], d[it][2], d[it][3]}, wrs, um[it], 0);
      bst16<QKV_AUX>(u32x4{d[it][4], d[it][5], d[it][6], d[it][7]}, wrs, um[it] + 512, 0);
      __builtin_amdgcn_raw_buffer_store_b32(d[it][8], wrs, ut[it], 0, QKT_AUX);
    }
#pragma unroll
    for (int it = 0; it < 2; ++it) {
      const int hd = (w >> 1) + 4 * it, G = 6 * cb + hd;
      if (hd < 6 && G >= 32 && G < 64) {
        float n2 = 0.f;
#pragma unroll
        for (int i = 0; i < 9; ++i) n2 = dot2(d[it][i], d[it][i], n2);
        unsigned nb = __float_as_uint(n2);
        { auto r = __builtin_amdgcn_permlane32_swap(nb, nb, false, false); nb = max(r[0], r[1]); }
        { auto r = __builtin_amdgcn_permlane16_swap(nb, nb, false, false); nb = max(r[0], r[1]); }
        __builtin_amdgcn_raw_buffer_store_b32(nb, wrs, (unsigned)WS_KMAX + (((2 * rb + (w & 1)) * 32 + (G & 31)) * 16 + m) * 4, 0, QKV_AUX);
      }
    }
  }
  STAMP(5); STAMP_OUT(1, 6);
}

constexpr int K2_LDS = (int)KV_HEAD_B;
constexpr int NJ_W1 = 72 * 36, NJ_WD = 9 * 144, NJ_WO = 9 * 18, NJ_ALL = NJ_W1 + NJ_WD + NJ_WO;
struct PrepJob { const char* src; unsigned rs, c1, c2, voff, dst[4]; };
DI void prep_setup(PrepJob& j, int J, int lane, const u16* wo, const u16* wg, const u16* wu, const u16* wd) {
  if (J >= NJ_ALL) J = NJ_ALL - 1;
  const int g = (lane >> 2) & 3;
  unsigned lrow = 8 * g;
  j.c1 = 0; j.c2 = 0;
  if (J < NJ_W1) {
    const int tg = J / 36, sm = J - tg * 36;
    j.src = (const char*)((sm & 1) ? wu : wg) + (size_t)(sm >> 1) * (32 * FF * 2) + tg * 128;
    j.rs = FF * 2;
#pragma unroll
    for (int t = 0; t < 4; ++t) j.dst[t] = (unsigned)WS_W1 + (4 * tg + t) * 36864 + sm * 1024;
  } else if (J < NJ_W1 + NJ_WD) {
    const int q = J - NJ_W1, cc = q / 9, tg = q - cc * 9;
    j.src = (const char*)wd + (size_t)cc * (32 * DM * 2) + tg * 128;
    j.rs = DM * 2;
#pragma unroll
    for (int t = 0; t < 4; ++t) { const int nt = 4 * tg + t, nb = nt / 9; j.dst[t] = (unsigned)WS_WD + ((nb * 144 + cc) * 9 + (nt - nb * 9)) * 1024; }
  } else {
    const int q = J - NJ_W1 - NJ_WD, step = q / 9, tg = q - step * 9;
    j.rs = DM * 2;
    if (step < 16) { j.src = (const char*)wo + (size_t)step * (36 * DM * 2) + tg * 128; lrow = 18 * (g >> 1) + 4 * (g & 1); j.c2 = 4; }
    else { j.src = (const char*)wo + (size_t)(288 * (step - 16) + 16) * (DM * 2) + tg * 128; lrow = 72 * g; j.c1 = 16; }
#pragma unroll
    for (int t = 0; t < 4; ++t) { const int nt = 4 * tg + t, nb = nt / 3; j.dst[t] = (unsigned)WS_WO + ((nb * 18 + step) * 3 + (nt - nb * 3)) * 1024; }
  }
#pragma unroll
  for (int t = 0; t < 4; ++t) j.dst[t] = rfl(j.dst[t]);
  j.voff = lrow * j.rs + (lane >> 4) * 32 + (lane & 3) * 8;
}
template <int JJ>
DI void prep_load(const PrepJob& j, u32x2 (&r)[8]) {
  const char* b = j.src + (JJ + j.c1 * (JJ >> 1) + j.c2 * (JJ >> 2)) * j.rs;
  asm volatile("global_load_dwordx2 %0, %1, %2" : "=v"(r[JJ]) : "v"(j.voff), "s"(b));
}
DI void prep_store(const PrepJob& j, const u32x2 (&r)[8], __amdgpu_buffer_rsrc_t rs, unsigned so) {
  u32x4 u[4];
#pragma unroll
  for (int i = 0; i < 4; ++i) {
    u[0][i] = __builtin_amdgcn_perm(r[2 * i + 1][0], r[2 * i][0], 0x05040100u);
    u[1][i] = __builtin_amdgcn_perm(r[2 * i + 1][0], r[2 * i][0], 0x07060302u);
    u[2][i] = __builtin_amdgcn_perm(r[2 * i + 1][1], r[2 * i][1], 0x05040100u);
    u[3][i] = __builtin_amdgcn_perm(r[2 * i + 1][1], r[2 * i][1], 0x07060302u);
  }
#pragma unroll
  for (int i = 0; i < 4; ++i) {
    const auto a0 = __builtin_amdgcn_permlane32_swap(u[0][i], u[2][i], false, false);
    const auto a1 = __builtin_amdgcn_permlane32_swap(u[1][i], u[3][i], false, false);
    const auto b0 = __builtin_amdgcn_permlane16_swap(a0[0], a1[0], false, false);
    const auto b1 = __builtin_amdgcn_permlane16_swap(a0[1], a1[1], false, false);
    u[0][i] = b0[0]; u[1][i] = b0[1]; u[2][i] = b1[0]; u[3][i] = b1[1];
  }
#pragma unroll
  for (int t = 0; t < 4; ++t) bst16<PREP_AUX>(u[t], rs, so, j.dst[t]);
}
#define PIN8(r) "+v"(r[0]), "+v"(r[1]), "+v"(r[2]), "+v"(r[3]), "+v"(r[4]), "+v"(r[5]), "+v"(r[6]), "+v"(r[7])

#define A_MFMA_ACC(d, a, b) asm volatile("v_mfma_f32_32x32x16_bf16 %0, %1, %2, %0" : "+v"(d) : "v"(a), "v"(b))
#define AT_M(m)
#define AT_U
struct AttnC {
  LDSP const char *kmb, *ktb, *vb;
  unsigned kma[3], kta[3], va[3]; unsigned long long mk, mv;
  u32x4 qm, qt, kf, v0, v1, p1; u32x2 ktl;
  f32x16 negm, o, sa, sb;
};
constexpr int k_off(int T) { return (T >> 4) * (int)CHUNK_B + (T & 15) * 1024; }
constexpr int kt_off(int T) { return (T >> 4) * (int)CHUNK_B + ((T >> 1) & 7) * 256; }
constexpr int v_off(int T) { return (T >> 4) * (int)CHUNK_B + (T & 15) * 1216; }
#define AT_STMT(TXT)                                                                                                              \
  asm volatile(TXT : "+{v[2:17]}"(c.o), "+{v[34:49]}"(c.sa), "+{v[50:65]}"(c.sb), "+{v[66:69]}"(c.kf), "+{v[70:73]}"(c.p1),        \
               "+{v[84:87]}"(c.v1), "+{v[88:91]}"(c.v0), "+{v[92:93]}"(c.ktl)                                                      \
               : "{v[18:33]}"(c.negm), "{v[98:101]}"(c.qm), "{v[102:105]}"(c.qt), [ka] "v"(c.kma[KO >> 16]), [kt] "v"(c.kta[KT >> 16]), \
                 [va] "v"(c.va[V1 >> 16]), [vb] "v"(c.va[V0 >> 16]), [ko] "i"(KO & 0xffff), [kto] "i"(KT & 0xffff), [vo1] "i"(V1 & 0xffff),  \
                 [vo0] "i"(V0 & 0xffff), [mk] "s"(c.mk), [mv] "s"(c.mv)                                                            \
               : "v74", "v75", "v76", "v77")
template <int T> DI void attn_tile(AttnC& c) {
  constexpr int KO = k_off(T < 62 ? T + 2 : 0), KT = kt_off(T < 62 ? T + 2 : 0), V1 = v_off(T) + 608, V0 = v_off(T < 63 ? T + 1 : 0);
  if constexpr (T == 0)
    AT_STMT("v_mfma_f32_32x32x8_bf16 v[50:65], v[92:93], v[104:105], v[18:33]\n\t" "v_exp_f32 v34, v34\n\t" "v_exp_f32 v35, v35\n\t" "v_exp_f32 v36, v36\n\t" "v_exp_f32 v37, v37\n\t" AT_M("mk") "ds_read_b64 v[92:93], %[kt] offset:%[kto]\n\t" AT_U AT_M("mv") "ds_read_b128 v[84:87], %[va] offset:%[vo1]\n\t" AT_U "v_cvt_pk_bf16_f32 v74, v34, v35\n\t" "v_exp_f32 v38, v38\n\t" "v_cvt_pk_bf16_f32 v75, v36, v37\n\t" "v_exp_f32 v39, v39\n\t" "v_exp_f32 v40, v40\n\t" "v_exp_f32 v41, v41\n\t" "s_waitcnt lgkmcnt(2)\n\t" "v_mfma_f32_32x32x16_bf16 v[50:65], v[66:69], v[98:101], v[50:65]\n\t" "ds_read_b128 v[66:69], %[ka] offset:%[ko]\n\t" "v_cvt_pk_bf16_f32 v76, v38, v39\n\t" "v_exp_f32 v42, v42\n\t" "v_cvt_pk_bf16_f32 v77, v40, v41\n\t" "v_exp_f32 v43, v43\n\t" "v_exp_f32 v44, v44\n\t" "v_exp_f32 v45, v45\n\t" "s_waitcnt lgkmcnt(2)\n\t" "v_mfma_f32_32x32x16_bf16 v[2:17], v[88:91], v[74:77], v[2:17]\n\t" AT_M("mv") "ds_read_b128 v[88:91], %[vb] offset:%[vo0]\n\t" AT_U "v_cvt_pk_bf16_f32 v70, v42, v43\n\t" "v_exp_f32 v46, v46\n\t" "v_cvt_pk_bf16_f32 v71, v44, v45\n\t" "v_exp_f32 v47, v47\n\t" "v_exp_f32 v48, v48\n\t" "v_exp_f32 v49, v49\n\t" "v_cvt_pk_bf16_f32 v72, v46, v47\n\t" "v_cvt_pk_bf16_f32 v73, v48, v49\n\t");
  else if constexpr (T == 62)
    AT_STMT("v_mfma_f32_32x32x8_bf16 v[50:65], v[92:93], v[104:105], v[18:33]\n\t" "v_exp_f32 v34, v34\n\t" "v_exp_f32 v35, v35\n\t" "v_exp_f32 v36, v36\n\t" "v_exp_f32 v37, v37\n\t" "s_waitcnt lgkmcnt(2)\n\t" "v_mfma_f32_32x32x16_bf16 v[2:17], v[84:87], v[70:73], v[2:17]\n\t" AT_M("mv") "ds_read_b128 v[84:87], %[va] offset:%[vo1]\n\t" AT_U "v_cvt_pk_bf16_f32 v74, v34, v35\n\t" "v_exp_f32 v38, v38\n\t" "v_cvt_pk_bf16_f32 v75, v36, v37\n\t" "v_exp_f32 v39, v39\n\t" "v_exp_f32 v40, v40\n\t" "v_exp_f32 v41, v41\n\t" "s_waitcnt lgkmcnt(2)\n\t" "v_mfma_f32_32x32x16_bf16 v[50:65], v[66:69], v[98:101], v[50:65]\n\t" "v_cvt_pk_bf16_f32 v76, v38, v39\n\t" "v_exp_f32 v42, v42\n\t" "v_cvt_pk_bf16_f32 v77, v40, v41\n\t" "v_exp_f32 v43, v43\n\t" "v_exp_f32 v44, v44\n\t" "v_exp_f32 v45, v45\n\t" "s_waitcnt lgkmcnt(1)\n\t" "v_mfma_f32_32x32x16_bf16 v[2:17], v[88:91], v[74:77], v[2:17]\n\t" AT_M("mv") "ds_read_b128 v[88:91], %[vb] offset:%[vo0]\n\t" AT_U "v_cvt_pk_bf16_f32 v70, v42, v43\n\t" "v_exp_f32 v46, v46\n\t" "v_cvt_pk_bf16_f32 v71, v44, v45\n\t" "v_exp_f32 v47, v47\n\t" "v_exp_f32 v48, v48\n\t" "v_exp_f32 v49, v49\n\t" "v_cvt_pk_bf16_f32 v72, v46, v47\n\t" "v_cvt_pk_bf16_f32 v73, v48, v49\n\t");
  else if constexpr (T == 63)
    AT_STMT("v_exp_f32 v50, v50\n\t" "v_exp_f32 v51, v51\n\t" "v_exp_f32 v52, v52\n\t" "v_exp_f32 v53, v53\n\t" "s_waitcnt lgkmcnt(0)\n\t" "v_mfma_f32_32x32x16_bf16 v[2:17], v[84:87], v[70:73], v[2:17]\n\t" AT_M("mv") "ds_read_b128 v[84:87], %[va] offset:%[vo1]\n\t" AT_U "v_cvt_pk_bf16_f32 v74, v50, v51\n\t" "v_exp_f32 v54, v54\n\t" "v_cvt_pk_bf16_f32 v75, v52, v53\n\t" "v_exp_f32 v55, v55\n\t" "v_exp_f32 v56, v56\n\t" "v_exp_f32 v57, v57\n\t" "v_cvt_pk_bf16_f32 v76, v54, v55\n\t" "v_exp_f32 v58, v58\n\t" "v_cvt_pk_bf16_f32 v77, v56, v57\n\t" "v_exp_f32 v59, v59\n\t" "v_exp_f32 v60, v60\n\t" "v_exp_f32 v61, v61\n\t" "s_waitcnt lgkmcnt(1)\n\t" "v_mfma_f32_32x32x16_bf16 v[2:17], v[88:91], v[74:77], v[2:17]\n\t" "v_cvt_pk_bf16_f32 v70, v58, v59\n\t" "v_exp_f32 v62, v62\n\t" "v_cvt_pk_bf16_f32 v71, v60, v61\n\t" "v_exp_f32 v63, v63\n\t" "v_exp_f32 v64, v64\n\t" "v_exp_f32 v65, v65\n\t" "v_cvt_pk_bf16_f32 v72, v62, v63\n\t" "v_cvt_pk_bf16_f32 v73, v64, v65\n\t");
  else if constexpr ((T & 3) == 0)
    AT_STMT("v_mfma_f32_32x32x8_bf16 v[50:65], v[92:93], v[104:105], v[18:33]\n\t" "v_exp_f32 v34, v34\n\t" "v_exp_f32 v35, v35\n\t" "v_exp_f32 v36, v36\n\t" "v_exp_f32 v37, v37\n\t" "s_waitcnt lgkmcnt(2)\n\t" "v_mfma_f32_32x32x16_bf16 v[2:17], v[84:87], v[70:73], v[2:17]\n\t" AT_M("mk") "ds_read_b64 v[92:93], %[kt] offset:%[kto]\n\t" AT_U AT_M("mv") "ds_read_b128 v[84:87], %[va] offset:%[vo1]\n\t" AT_U "v_cvt_pk_bf16_f32 v74, v34, v35\n\t" "v_exp_f32 v38, v38\n\t" "v_cvt_pk_bf16_f32 v75, v36, v37\n\t" "v_exp_f32 v39, v39\n\t" "v_exp_f32 v40, v40\n\t" "v_exp_f32 v41, v41\n\t" "s_waitcnt lgkmcnt(2)\n\t" "v_mfma_f32_32x32x16_bf16 v[50:65], v[66:69], v[98:101], v[50:65]\n\t" "ds_read_b128 v[66:69], %[ka] offset:%[ko]\n\t" "v_cvt_pk_bf16_f32 v76, v38, v39\n\t" "v_exp_f32 v42, v42\n\t" "v_cvt_pk_bf16_f32 v77, v40, v41\n\t" "v_exp_f32 v43, v43\n\t" "v_exp_f32 v44, v44\n\t" "v_exp_f32 v45, v45\n\t" "s_waitcnt lgkmcnt(2)\n\t" "v_mfma_f32_32x32x16_bf16 v[2:17], v[88:91], v[74:77], v[2:17]\n\t" AT_M("mv") "ds_read_b128 v[88:91], %[vb] offset:%[vo0]\n\t" AT_U "v_cvt_pk_bf16_f32 v70, v42, v43\n\t" "v_exp_f32 v46, v46\n\t" "v_cvt_pk_bf16_f32 v71, v44, v45\n\t" "v_exp_f32 v47, v47\n\t" "v_exp_f32 v48, v48\n\t" "v_exp_f32 v49, v49\n\t" "v_cvt_pk_bf16_f32 v72, v46, v47\n\t" "v_cvt_pk_bf16_f32 v73, v48, v49\n\t");
  else if constexpr ((T & 3) == 1)
    AT_STMT("v_mfma_f32_32x32x8_bf16 v[34:49], v[92:93], v[102:103], v[18:33]\n\t" "v_exp_f32 v50, v50\n\t" "v_exp_f32 v51, v51\n\t" "v_exp_f32 v52, v52\n\t" "v_exp_f32 v53, v53\n\t" "s_waitcnt lgkmcnt(2)\n\t" "v_mfma_f32_32x32x16_bf16 v[2:17], v[84:87], v[70:73], v[2:17]\n\t" AT_M("mv") "ds_read_b128 v[84:87], %[va] offset:%[vo1]\n\t" AT_U "v_cvt_pk_bf16_f32 v74, v50, v51\n\t" "v_exp_f32 v54, v54\n\t" "v_cvt_pk_bf16_f32 v75, v52, v53\n\t" "v_exp_f32 v55, v55\n\t" "v_exp_f32 v56, v56\n\t" "v_exp_f32 v57, v57\n\t" "s_waitcnt lgkmcnt(2)\n\t" "v_mfma_f32_32x32x16_bf16 v[34:49], v[66:69], v[98:101], v[34:49]\n\t" "ds_read_b128 v[66:69], %[ka] offset:%[ko]\n\t" "v_cvt_pk_bf16_f32 v76, v54, v55\n\t" "v_exp_f32 v58, v58\n\t" "v_cvt_pk_bf16_f32 v77, v56, v57\n\t" "v_exp_f32 v59, v59\n\t" "v_exp_f32 v60, v60\n\t" "v_exp_f32 v61, v61\n\t" "s_waitcnt lgkmcnt(2)\n\t" "v_mfma_f32_32x32x16_bf16 v[2:17], v[88:91], v[74:77], v[2:17]\n\t" AT_M("mv") "ds_read_b128 v[88:91], %[vb] offset:%[vo0]\n\t" AT_U "v_cvt_pk_bf16_f32 v70, v58, v59\n\t" "v_exp_f32 v62, v62\n\t" "v_cvt_pk_bf16_f32 v71, v60, v61\n\t" "v_exp_f32 v63, v63\n\t" "v_exp_f32 v64, v64\n\t" "v_exp_f32 v65, v65\n\t" "v_cvt_pk_bf16_f32 v72, v62, v63\n\t" "v_cvt_pk_bf16_f32 v73, v64, v65\n\t");
  else if constexpr ((T & 3) == 2)
    AT_STMT("v_mfma_f32_32x32x8_bf16 v[50:65], v[92:93], v[104:105], v[18:33]\n\t" "v_exp_f32 v34, v34\n\t" "v_exp_f32 v35, v35\n\t" "v_exp_f32 v36, v36\n\t" "v_exp_f32 v37, v37\n\t" "s_waitcnt lgkmcnt(2)\n\t" "v_mfma_f32_32x32x16_bf16 v[2:17], v[84:87], v[70:73], v[2:17]\n\t" AT_M("mk") "ds_read_b64 v[92:93], %[kt] offset:%[kto]\n\t" AT_U AT_M("mv") "ds_read_b128 v[84:87], %[va] offset:%[vo1]\n\t" AT_U "v_cvt_pk_bf16_f32 v74, v34, v35\n\t" "v_exp_f32 v38, v38\n\t" "v_cvt_pk_bf16_f32 v75, v36, v37\n\t" "v_exp_f32 v39, v39\n\t" "v_exp_f32 v40, v40\n\t" "v_exp_f32 v41, v41\n\t" "s_waitcnt lgkmcnt(2)\n\t" "v_mfma_f32_32x32x16_bf16 v[50:65], v[66:69], v[98:101], v[50:65]\n\t" "ds_read_b128 v[66:69], %[ka] offset:%[ko]\n\t" "v_cvt_pk_bf16_f32 v76, v38, v39\n\t" "v_exp_f32 v42, v42\n\t" "v_cvt_pk_bf16_f32 v77, v40, v41\n\t" "v_exp_f32 v43, v43\n\t" "v_exp_f32 v44, v44\n\t" "v_exp_f32 v45, v45\n\t" "s_waitcnt lgkmcnt(2)\n\t" "v_mfma_f32_32x32x16_bf16 v[2:17], v[88:91], v[74:77], v[2:17]\n\t" AT_M("mv") "ds_read_b128 v[88:91], %[vb] offset:%[vo0]\n\t" AT_U "v_cvt_pk_bf16_f32 v70, v42, v43\n\t" "v_exp_f32 v46, v46\n\t" "v_cvt_pk_bf16_f32 v71, v44, v45\n\t" "v_exp_f32 v47, v47\n\t" "v_exp_f32 v48, v48\n\t" "v_exp_f32 v49, v49\n\t" "v_cvt_pk_bf16_f32 v72, v46, v47\n\t" "v_cvt_pk_bf16_f32 v73, v48, v49\n\t");
  else if constexpr ((T & 3) == 3)
    AT_STMT("v_mfma_f32_32x32x8_bf16 v[34:49], v[92:93], v[102:103], v[18:33]\n\t" "v_exp_f32 v50, v50\n\t" "v_exp_f32 v51, v51\n\t" "v_exp_f32 v52, v52\n\t" "v_exp_f32 v53, v53\n\t" "s_waitcnt lgkmcnt(2)\n\t" "v_mfma_f32_32x32x16_bf16 v[2:17], v[84:87], v[70:73], v[2:17]\n\t" AT_M("mv") "ds_read_b128 v[84:87], %[va] offset:%[vo1]\n\t" AT_U "v_cvt_pk_bf16_f32 v74, v50, v51\n\t" "v_exp_f32 v54, v54\n\t" "v_cvt_pk_bf16_f32 v75, v52, v53\n\t" "v_exp_f32 v55, v55\n\t" "v_exp_f32 v56, v56\n\t" "v_exp_f32 v57, v57\n\t" "s_waitcnt lgkmcnt(2)\n\t" "v_mfma_f32_32x32x16_bf16 v[34:49], v[66:69], v[98:101], v[34:49]\n\t" "ds_read_b128 v[66:69], %[ka] offset:%[ko]\n\t" "v_cvt_pk_bf16_f32 v76, v54, v55\n\t" "v_exp_f32 v58, v58\n\t" "v_cvt_pk_bf16_f32 v77, v56, v57\n\t" "v_exp_f32 v59, v59\n\t" "v_exp_f32 v60, v60\n\t" "v_exp_f32 v61, v61\n\t" "s_waitcnt lgkmcnt(2)\n\t" "v_mfma_f32_32x32x16_bf16 v[2:17], v[88:91], v[74:77], v[2:17]\n\t" AT_M("mv") "ds_read_b128 v[88:91], %[vb] offset:%[vo0]\n\t" AT_U "v_cvt_pk_bf16_f32 v70, v58, v59\n\t" "v_exp_f32 v62, v62\n\t" "v_cvt_pk_bf16_f32 v71, v60, v61\n\t" "v_exp_f32 v63, v63\n\t" "v_exp_f32 v64, v64\n\t" "v_exp_f32 v65, v65\n\t" "v_cvt_pk_bf16_f32 v72, v62, v63\n\t" "v_cvt_pk_bf16_f32 v73, v64, v65\n\t");
}
DI void attn_group_slow(LDSP const char* km, LDSP const char* kt, LDSP const char* vp, const u32x4& qm, unsigned qtl, const f32x16& negm, f32x16& o) {
#pragma unroll
  for (int t = 0; t < 4; ++t) {
    const u32x2 k2 = *(LDSP const u32x2*)(kt + (t >> 1) * 256);
    const u32x4 ktl = {k2[0], k2[1], 0, 0};
    const u32x4 kf = ldsr16(km + t * 1024);
    const u32x4 v0 = ldsr16(vp + t * 1216), v1 = ldsr16(vp + t * 1216 + 608);
    u32x4 qq = {0, 0, 0, 0}; qq[t & 1] = qtl;
    f32x16 s = mfma32(ktl, qq, negm);
    s = mfma32(kf, qm, s);
    u32x4 p0, p1;
#pragma unroll
    for (int i = 0; i < 4; ++i) {
      p0[i] = cvtpk(__builtin_amdgcn_exp2f(s[2 * i]), __builtin_amdgcn_exp2f(s[2 * i + 1]));
      p1[i] = cvtpk(__builtin_amdgcn_exp2f(s[8 + 2 * i]), __builtin_amdgcn_exp2f(s[8 + 2 * i + 1]));
    }
    o = mfma32(v0, p0, o);
    o = mfma32(v1, p1, o);
  }
}
struct AttnSide { const char* kv; unsigned lane16, lds0; int w; PrepJob ja, jb; u32x2 ra[8], rb[8]; };
DI void attn_dma(const AttnSide& s, int chunk, int i) {
  int p = 8 * i + s.w; if (p > 36) p = 36;
  const unsigned o = chunk * (unsigned)CHUNK_B + p * 1024;
  dma16(s.kv + o, s.lane16, s.lds0 + o);
}
template <int G, int I> DI void attn_hook(AttnSide& s) {
  if constexpr (G < 3) { attn_dma(s, G + 1, I); if constexpr (I == 3) attn_dma(s, G + 1, 4); }
  else if constexpr (G < 5) prep_load<4 * (G - 3) + I>(s.ja, s.ra);
  else if constexpr (G < 7) prep_load<4 * (G - 5) + I>(s.jb, s.rb);
}
template <int G> DI void attn_group(AttnC& c, AttnSide& s) {
#define AT_PM 2
  if constexpr (AT_PM == 2) { if constexpr (G == 8 || G == 12) asm volatile("s_barrier"); __builtin_amdgcn_s_setprio(3 - (G & 3)); }
  if constexpr (AT_PM == 3) { __builtin_amdgcn_s_setprio(3 - (G >> 2)); }
  if constexpr (AT_PM == 4) { if constexpr (G >= 7 && (G & 1)) asm volatile("s_barrier"); __builtin_amdgcn_s_setprio((G & 1) ? 3 : 1); }
  if constexpr (AT_PM == 5) { if constexpr (G == 9 || G == 13) asm volatile("s_barrier"); __builtin_amdgcn_s_setprio(3 - ((G + 3) & 3)); }
  if constexpr (AT_PM == 6) { if constexpr (G == 8) asm volatile("s_barrier"); __builtin_amdgcn_s_setprio(G < 8 ? 3 - (G & 3) : (G < 12 ? 3 : (G < 14 ? 2 : 15 - G))); }
  attn_tile<4 * G + 0>(c); attn_hook<G, 0>(s);
  attn_tile<4 * G + 1>(c); attn_hook<G, 1>(s);
  if constexpr (AT_PM == 4) __builtin_amdgcn_s_setprio((G & 1) ? 2 : 0);
  attn_tile<4 * G + 2>(c); attn_hook<G, 2>(s);
  attn_tile<4 * G + 3>(c); attn_hook<G, 3>(s);
}
extern "C" __global__ __launch_bounds__(512, 2) void k_attn(const u16* x, const u16* wo, const u16* wg, const u16* wu, const u16* wd, char* ws) {
  extern __shared__ __attribute__((aligned(16))) char smem[];
  asm volatile("" : "+s"(x), "+s"(wo), "+s"(wg), "+s"(wu), "+s"(wd), "+s"(ws));
  LDSP char* lds = (LDSP char*)smem;
  const int tid = threadIdx.x, lane = tid & 63, w = rfl(tid >> 6);
  const int xcd = blockIdx.x & 7, bi = blockIdx.x >> 3;
  const int head = 4 * xcd + (bi & 3), qb = bi >> 2;
  const int r = lane & 31, h = lane >> 5;
  const int qt32 = 8 * qb + w;
  STAMP_DECL; STAMP(0);
  AttnSide s;
  s.kv = ws + WS_KV + (size_t)head * KV_HEAD_B; s.lane16 = lane * 16; s.lds0 = (unsigned)(uintptr_t)lds; s.w = w;
  AttnC c;
  c.qm = gld16(ws + WS_QM, head * 65536 + qt32 * 1024 + lane * 16);
  unsigned qtl = gld4(ws + WS_QT, head * 8192 + (32 * qt32 + r) * 4);
  u32x4 km0 = gld16(ws + WS_KMAX, ((lane >> 1) * 32 + head) * 64 + (lane & 1) * 32);
  u32x4 km1 = gld16(ws + WS_KMAX, ((lane >> 1) * 32 + head) * 64 + (lane & 1) * 32 + 16);
  attn_dma(s, 0, 0); attn_dma(s, 0, 2);
  attn_dma(s, 0, 1); attn_dma(s, 0, 3); attn_dma(s, 0, 4);
  const int vrow = r < 19 ? r : ((r >= 20 && r < 28) ? 0 : 18);
  c.kmb = lds + lane * 16;
  c.ktb = lds + 16384 + r * 8;
  c.vb = lds + 18432 + h * 304 + vrow * 16;
#pragma unroll
  for (int i = 0; i < 3; ++i) {
    c.kma[i] = (unsigned)(uintptr_t)c.kmb + i * 65536; c.kta[i] = (unsigned)(uintptr_t)c.ktb + i * 65536; c.va[i] = (unsigned)(uintptr_t)c.vb + i * 65536;
    PIN(c.kma[i]); PIN(c.kta[i]); PIN(c.va[i]);
  }
  c.mk = 0x00000000ffffffffull; c.mv = 0x0007ffff0007ffffull;
  c.ktl = u32x2{0, 0}; c.v0 = u32x4{0, 0, 0, 0}; c.v1 = c.v0; PIN(c.ktl); PIN(c.v0); PIN(c.v1);
  const int gw = blockIdx.x * 8 + w;
  prep_setup(s.ja, 2 * gw, lane, wo, wg, wu, wd);
  prep_setup(s.jb, 2 * gw + 1, lane, wo, wg, wu, wd);
  PIN(s.ja.voff); PIN(s.jb.voff);
#pragma unroll
  for (int i = 0; i < 16; ++i) c.o[i] = 0.f;
  PIN(c.o);
  SBAR();
  asm volatile("s_waitcnt vmcnt(5)" : "+v"(c.qm), "+v"(qtl), "+v"(km0), "+v"(km1)::"memory");
  if (h) qtl = 0;
  float q2 = dot2(qtl, qtl, 0.f);
#pragma unroll
  for (int i = 0; i < 4; ++i) q2 = dot2(c.qm[i], c.qm[i], q2);
  unsigned k2 = max(max(max(km0[0], km0[1]), max(km0[2], km0[3])), max(max(km1[0], km1[1]), max(km1[2], km1[3])));
  { auto rr = __builtin_amdgcn_permlane32_swap(k2, k2, false, false); k2 = max(rr[0], rr[1]); }
  { auto rr = __builtin_amdgcn_permlane16_swap(k2, k2, false, false); k2 = max(rr[0], rr[1]); }
  k2 = max(k2, (unsigned)__builtin_amdgcn_update_dpp(0u, k2, 0x128, 0xf, 0xf, true));
  k2 = max(k2, (unsigned)__builtin_amdgcn_update_dpp(0u, k2, 0x124, 0xf, 0xf, true));
  k2 = max(k2, (unsigned)__builtin_amdgcn_update_dpp(0u, k2, 0x122, 0xf, 0xf, true));
  k2 = max(k2, (unsigned)__builtin_amdgcn_update_dpp(0u, k2, 0x121, 0xf, 0xf, true));
  q2 = xor32_sum(q2);
  const float mshift = __builtin_amdgcn_sqrtf(q2 * __uint_as_float(k2)) * 1.001f;
  c.qt = u32x4{qtl, 0, 0, qtl}; PIN(c.qt);
#pragma unroll
  for (int i = 0; i < 16; ++i) c.negm[i] = -mshift;
  PIN(c.negm);
  STAMP(1);
  asm volatile("s_waitcnt vmcnt(3)\n\ts_barrier" ::: "memory");
  STAMP(2);
  asm volatile(AT_M("mk") "ds_read_b64 v[92:93], %[kt]\n\t" AT_U "ds_read_b128 v[70:73], %[ka]\n\tds_read_b128 v[66:69], %[ka] offset:1024\n\t"
               AT_M("mv") "ds_read_b128 v[88:91], %[va]\n\t" AT_U "s_waitcnt lgkmcnt(2)\n\t"
               "v_mfma_f32_32x32x8_bf16 v[34:49], v[92:93], v[102:103], v[18:33]\n\t"
               "v_mfma_f32_32x32x16_bf16 v[34:49], v[70:73], v[98:101], v[34:49]\n\ts_nop 11"
               : "={v[34:49]}"(c.sa), "={v[50:65]}"(c.sb), "={v[66:69]}"(c.kf), "={v[70:73]}"(c.p1), "+{v[88:91]}"(c.v0), "+{v[92:93]}"(c.ktl)
               : "{v[18:33]}"(c.negm), "{v[98:101]}"(c.qm), "{v[102:105]}"(c.qt), [ka] "v"(c.kma[0]), [kt] "v"(c.kta[0]), [va] "v"(c.va[0]),
                 [mk] "s"(c.mk), [mv] "s"(c.mv));
  attn_group<0>(c, s);
  asm volatile("s_waitcnt vmcnt(5)\n\ts_barrier" ::: "memory");
  attn_group<1>(c, s); attn_group<2>(c, s);
  asm volatile("s_waitcnt vmcnt(10)\n\ts_barrier" ::: "memory");
  attn_group<3>(c, s); attn_group<4>(c, s);
  asm volatile("s_waitcnt vmcnt(8)\n\ts_barrier" ::: "memory");
  STAMP(3);
  u32x4 xu0, xu1;
  const int xj1 = gw < 256 ? 2048 + gw : gw;
  const unsigned xl = (lane >> 3) * 1152 + (lane & 7) * 16, xs = (lane & 7) * 256 + (lane >> 3) * 16;
  xu0 = gld16((const char*)x + ((gw / 9) * (8 * 1152) + (gw % 9) * 128), xl);
  xu1 = gld16((const char*)x + ((xj1 / 9) * (8 * 1152) + (xj1 % 9) * 128), xl);
  unsigned wr[5];
  const bool dobar = gw < DM;
  if (dobar) {
    const char* b = (const char*)wo + (size_t)wo_row(gw >> 5, (gw >> 3) & 3, gw & 7) * (DM * 2);
#pragma unroll
    for (int i = 0; i < 5; ++i) wr[i] = gld4(b, (i < 4 ? lane + 64 * i : 256 + r) * 4);
  }
  attn_group<5>(c, s); attn_group<6>(c, s); attn_group<7>(c, s); attn_group<8>(c, s);
  const __amdgpu_buffer_rsrc_t wrs = mkrsrc(ws, (unsigned)WS_END);
  const unsigned pso = ((lane >> 2) & 3) * 256 + ((lane & 3) * 4 + (lane >> 4)) * 16;
  asm volatile("s_waitcnt vmcnt(8)" : PIN8(s.ra), "+v"(wr[0]), "+v"(wr[1]), "+v"(wr[2]), "+v"(wr[3]), "+v"(wr[4]), "+v"(xu0), "+v"(xu1)::"memory");
  prep_store(s.ja, s.ra, wrs, pso);
  bst16<PREP_AUX>(xu0, wrs, xs, (unsigned)WS_XU + (gw / 18) * 18432 + (gw % 9) * 2048 + ((gw / 9) & 1) * 128);
  bst16<PREP_AUX>(xu1, wrs, xs, (unsigned)WS_XU + (xj1 / 18) * 18432 + (xj1 % 9) * 2048 + ((xj1 / 9) & 1) * 128);
  if (dobar) {
    float a = 0.f;
#pragma unroll
    for (int i = 0; i < 4; ++i) a = dot2(wr[i], 0x3f803f80u, a);
    a = dot2(h ? 0u : wr[4], 0x3f803f80u, a);
    a = xor16_sum(xor32_sum(a));
    a += __uint_as_float(__builtin_amdgcn_update_dpp(0u, __float_as_uint(a), 0x128, 0xf, 0xf, false));
    a += __uint_as_float(__builtin_amdgcn_update_dpp(0u, __float_as_uint(a), 0x124, 0xf, 0xf, false));
    a += __uint_as_float(__builtin_amdgcn_update_dpp(0u, __float_as_uint(a), 0x122, 0xf, 0xf, false));
    a += __uint_as_float(__builtin_amdgcn_update_dpp(0u, __float_as_uint(a), 0x121, 0xf, 0xf, false));
    a *= (1.0f / DM);
    const unsigned hi = cvtpk(a, 0.f) & 0xffffu;
    const unsigned lo = cvtpk(a - bflo(hi), 0.f) & 0xffffu;
    __builtin_amdgcn_raw_buffer_store_b16((u16)(lane ? lo : hi), wrs, lane < 2 ? (unsigned)WS_WBAR + (lane * DM + gw) * 2 : 0x80000000u, 0, 0);
  }
  attn_group<9>(c, s); attn_group<10>(c, s);
  asm volatile("s_waitcnt vmcnt(6)" : PIN8(s.rb)::"memory");
  prep_store(s.jb, s.rb, wrs, pso);
  unsigned pf;
  CODE_PF(pf, k_oproj, k_down, 0, w & 3);
  attn_group<11>(c, s); attn_group<12>(c, s);
  attn_group<13>(c, s); attn_group<14>(c, s); attn_group<15>(c, s);
  asm volatile("s_waitcnt vmcnt(0) lgkmcnt(0)\n\tv_mfma_f32_32x32x16_bf16 v[2:17], v[84:87], v[70:73], v[2:17]\n\ts_nop 11"
               : "+{v[2:17]}"(c.o), "+v"(pf) : "{v[84:87]}"(c.v1), "{v[70:73]}"(c.p1), "{v[18:33]}"(c.negm));
  STAMP(4);
  f32x16 o = c.o;
  float l;
  { auto rr = __builtin_amdgcn_permlane32_swap(__float_as_uint(o[10]), __float_as_uint(o[10]), false, false); l = __uint_as_float(rr[0]); }
  if (__builtin_expect(__any(!(l >= 9.094947e-13f)), 0)) {
    float mx = -INFINITY;
    f32x16 z, negm;
#pragma unroll
    for (int i = 0; i < 16; ++i) z[i] = 0.f;
#pragma unroll 1
    for (int tt = 0; tt < 64; ++tt) {
      const int ck = tt >> 4, tl = tt & 15;
      const u32x2 k2 = *(LDSP const u32x2*)(c.ktb + ck * CHUNK_B + (tl >> 1) * 256);
      const u32x4 ktl = {k2[0], k2[1], 0, 0};
      const u32x4 kf = ldsr16(c.kmb + ck * CHUNK_B + tl * 1024);
      const u32x4 qq = {(tl & 1) ? 0u : qtl, (tl & 1) ? qtl : 0u, 0, 0};
      f32x16 sc = mfma32(ktl, qq, z);
      sc = mfma32(kf, c.qm, sc);
#pragma unroll
      for (int i = 0; i < 16; ++i) mx = fmaxf(mx, sc[i]);
    }
    mx = xor32_max(mx);
#pragma unroll
    for (int i = 0; i < 16; ++i) { negm[i] = -mx; o[i] = 0.f; }
#pragma unroll 1
    for (int gq = 0; gq < 16; ++gq) {
      const int ck = gq >> 2, gg = gq & 3;
      attn_group_slow(c.kmb + ck * CHUNK_B + gg * 4096, c.ktb + ck * CHUNK_B + gg * 512, c.vb + ck * CHUNK_B + gg * 4864, c.qm, qtl, negm, o);
    }
    auto rr = __builtin_amdgcn_permlane32_swap(__float_as_uint(o[10]), __float_as_uint(o[10]), false, false);
    l = __uint_as_float(rr[0]);
  }
  const float rl = 1.0f / l;
  u32x4 ou;
#pragma unroll
  for (int i = 0; i < 4; ++i) ou[i] = cvtpk(o[2 * i] * rl, o[2 * i + 1] * rl);
  const int q = 32 * qt32 + r;
  const unsigned ob = (unsigned)WS_OFR + (q >> 4) * 18432 + (q & 15) * 16;
  bst16<O_AUX>(ou, wrs, ob + (head >> 1) * 1024 + (2 * (head & 1) + h) * 256, 0);
  __builtin_amdgcn_raw_buffer_store_b32(cvtpk(o[8] * rl, o[9] * rl), wrs, h ? 0x80000000u : ob + (16 + (head >> 4)) * 1024 + ((head >> 2) & 3) * 256 + (head & 3) * 4, 0, O_AUX);
  STAMP(5); STAMP_OUT(2, 6);
}

constexpr int K3_WBAR = 54 * 1024, K3_LDS = K3_WBAR + 3072;
extern "C" __global__ __launch_bounds__(512, 2) void k_oproj(char* __restrict__ ws) {
  extern __shared__ __attribute__((aligned(16))) char smem[];
  LDSP char* lds = (LDSP char*)smem;
  const unsigned lds0 = (unsigned)(uintptr_t)lds;
  const int tid = threadIdx.x, lane = tid & 63, w = rfl(tid >> 6);
  const int xcd = blockIdx.x & 7, bi = blockIdx.x >> 3;
  if (bi >= 24) {
    const char* b = ws + WS_W1 + (size_t)(8 * (xcd >> 1) + bi - 24) * (9 * 36864);
    __builtin_amdgcn_s_sleep(K3_SLEEP);
    unsigned t0 = gld4(b, min(w * 128 + lane, 863) * 128), t1 = gld4(b, min(w * 128 + 64 + lane, 863) * 128);
    asm volatile("s_waitcnt vmcnt(0)" : "+v"(t0), "+v"(t1));
    return;
  }
  const int rb = 2 * xcd + (bi & 1), nb = bi >> 1;
  const int m = lane & 15, g = lane >> 4;
  const int mt = 8 * rb + w, row = 16 * mt + m;
  STAMP_DECL; STAMP(0);
  const unsigned uo = mt * 18432 + (6 * nb + (g >> 1)) * 256 + m * 16 + (g & 1) * 8;
  u32x4 O[18];
  u32x2 xr[3];
  unsigned xm;
  {
    const char* wb = ws + WS_WO + (size_t)nb * 54 * 1024;
    const char* ob = ws + WS_OFR + (size_t)mt * 18432;
    if (w < 3) dma16(ws + WS_WBAR, w * 1024 + lane * 16, lds0 + K3_WBAR + w * 1024);
#pragma unroll
    for (int i = 0; i < 18; ++i) {
      if (i < 7) { int p = 8 * i + w; if (p > 53) p = 53; dma16(wb, p * 1024 + lane * 16, lds0 + p * 1024); }
      O[i] = gld16(ob, i * 1024 + lane * 16);
      if (i == 8) {
#pragma unroll
        for (int t = 0; t < 3; ++t)
          asm volatile("global_load_dwordx2 %0, %1, %2 offset:%3" : "=v"(xr[t]) : "v"(uo), "s"(ws + WS_XU), "i"(t * 512));
        xm = gld4(ws + WS_XMEAN, row * 4);
      }
    }
  }
  f32x4 acc[4];
  LDSP const char* wl = lds + lane * 16;
  LDSP const char* wbl = lds + K3_WBAR + (m & 1) * 1152 + g * 16;
  const __amdgpu_buffer_rsrc_t wrs = mkrsrc(ws, (unsigned)WS_END);
  unsigned so = ((g & 1) ? (unsigned)WS_HC : (unsigned)WS_HU) + (uo & ~8u); PIN(so);
  STAMP(1);
  asm volatile("s_waitcnt vmcnt(15)\n\ts_barrier" : "+v"(O[0]), "+v"(O[1]), "+v"(O[2]), "+v"(O[3]), "+v"(O[4]), "+v"(O[5]), "+v"(O[6])::"memory");
  u32x4 wf[6];
#define K3_RD(f) (((f) & 3) == 3 ? ldsr16(wbl + ((f) >> 2) * 64) : ldsr16(wl + (3 * ((f) >> 2) + ((f) & 3)) * 1024))
#pragma unroll
  for (int f = 0; f < 5; ++f) wf[f] = K3_RD(f);
#pragma unroll
  for (int f = 0; f < 72; ++f) {
    if (f == 28) asm volatile("s_waitcnt vmcnt(5)" : "+v"(O[7]), "+v"(O[8]), "+v"(xr[0]), "+v"(xr[1]), "+v"(xr[2]), "+v"(xm), "+v"(O[9]), "+v"(O[10]), "+v"(O[11]), "+v"(O[12]));
    if (f == 52) asm volatile("s_waitcnt vmcnt(0)" : "+v"(O[13]), "+v"(O[14]), "+v"(O[15]), "+v"(O[16]), "+v"(O[17]));
    if (f + 5 < 72) wf[(f + 5) % 6] = K3_RD(f + 5);
    if (f < 4) G_MFMA0(acc[f & 3], wf[f % 6], O[f >> 2]);
    else G_MFMA(acc[f & 3], wf[f % 6], O[f >> 2]);
    SBAR();
  }
  asm volatile("s_nop 7" : "+v"(acc[0]), "+v"(acc[1]), "+v"(acc[2]), "+v"(acc[3]));
#undef K3_RD
  STAMP(2);
  const float mean = __uint_as_float(xm) + acc[3][0] + acc[3][1];
  float ssq = 0.f;
#pragma unroll
  for (int t = 0; t < 3; ++t) {
    const unsigned h0 = cvtpk(bflo(xr[t][0]) + acc[t][0], bfhi(xr[t][0]) + acc[t][1]);
    const unsigned h1 = cvtpk(bflo(xr[t][1]) + acc[t][2], bfhi(xr[t][1]) + acc[t][3]);
    const unsigned c0 = cvtpk(bflo(h0) - mean, bfhi(h0) - mean);
    const unsigned c1 = cvtpk(bflo(h1) - mean, bfhi(h1) - mean);
    ssq = dot2(c0, c0, ssq); ssq = dot2(c1, c1, ssq);
    const auto r0 = __builtin_amdgcn_permlane16_swap(h0, c0, false, false);
    const auto r1 = __builtin_amdgcn_permlane16_swap(h1, c1, false, false);
    bst16<H_AUX>(u32x4{r0[0], r1[0], r0[1], r1[1]}, wrs, so + t * 512, 0);
  }
  ssq = xor32_sum(xor16_sum(ssq));
  __builtin_amdgcn_raw_buffer_store_b32(__float_as_uint(ssq), wrs, (unsigned)WS_SSQ + (nb * 2048 + row) * 4, 0, H_AUX);
  STAMP(3); STAMP_OUT(3, 4);
}

constexpr int K4_SLOT = 36864, K4_LDS = 4 * K4_SLOT;
#define K4_PFD 4
constexpr int K4_PF = K4_PFD;
struct K4C {
  const char *wb, *xb; unsigned lane16, lds0; int w, rot;
  const char* pfb; unsigned pfo, pf0, pf1;
  LDSP const char* wl[4];
  u32x4 X[2][18], wf[K4_PF + 1];
  f32x4 acc[4][4];
  float c1[2], c2[2];
  __amdgpu_buffer_rsrc_t ars; unsigned avo; int T0;
  float ea[2][2], et[2][2]; unsigned pk[2][4];
};
DI void k4_piece(const K4C& c, int tile, int p) {
  int q = p + 4 * c.rot; if (q >= 36) q -= 36;
  dma16(c.wb + (tile * K4_SLOT + q * 1024), c.lane16, c.lds0 + (tile & 3) * K4_SLOT + p * 1024);
}
template <int ZW> DI void k4_issue_w(K4C& c, int i) {
  if (ZW < 9) {
    if (i == 0) k4_piece(c, c.w >> 2, 4 * ZW + (c.w & 3));
    else if ((c.w >> 2) == (ZW & 1)) k4_piece(c, 2, 4 * ZW + (c.w & 3));
  } else if (ZW == 9) { k4_piece(c, 3, 8 * i + c.w); }
}
template <int Z, int I> DI void k4_issue1(K4C& c) {
  if (I == 0) k4_issue_w<Z + K4_WD>(c, 0);
  else if (I < 5) {
    int z = Z + c.rot; if (z >= 9) z -= 9;
    c.X[(I - 1) >> 1][2 * Z + ((I - 1) & 1)] = gld16(c.xb + (((I - 1) >> 1) * 18432 + (2 * z + ((I - 1) & 1)) * 1024), c.lane16);
  }
  else k4_issue_w<Z + K4_WD>(c, 1);
}
template <int TL, int I> DI void k4_issue2(K4C& c) {
  if (I < 4) k4_piece(c, TL, 8 * I + c.w);
  else if ((c.w >> 2) == (TL & 1)) k4_piece(c, TL, 32 + (c.w & 3));
}
template <int TL, int E, int U> DI void k4_epi(K4C& c) {
  constexpr int e = U >> 2, u = U & 3, mt = e >> 2, i = e & 3, q = e & 1;
  if constexpr (U < 32) {
    if (u == 0) { const float a = c.acc[TL & 3][mt][i] * c.c1[mt]; c.ea[E][q] = a; c.et[E][q] = a * c.acc[TL & 3][2 + mt][i]; PIN(c.ea[E][q]); PIN(c.et[E][q]); }
    if (u == 1) { c.ea[E][q] = __builtin_amdgcn_exp2f(c.ea[E][q]); PIN(c.ea[E][q]); }
    if (u == 2) { c.ea[E][q] = __builtin_amdgcn_rcpf(__builtin_fmaf(c.ea[E][q], c.c2[mt], c.c2[mt])); PIN(c.ea[E][q]); }
    if (u == 3) { c.et[E][q] *= c.ea[E][q]; PIN(c.et[E][q]); if (q) { c.pk[E][e >> 1] = cvtpk(c.et[E][0], c.et[E][1]); PIN(c.pk[E][e >> 1]); } }
  } else if constexpr (U == 32) {
    auto r0 = __builtin_amdgcn_permlane16_swap(c.pk[E][0], c.pk[E][2], false, false);
    auto r1 = __builtin_amdgcn_permlane16_swap(c.pk[E][1], c.pk[E][3], false, false);
    c.pk[E][0] = r0[0]; c.pk[E][1] = r1[0]; c.pk[E][2] = r0[1]; c.pk[E][3] = r1[1];
  } else if constexpr (U == 33) {
    const int T = c.T0 + TL;
    bst16<ACT_AUX>(u32x4{c.pk[E][0], c.pk[E][1], c.pk[E][2], c.pk[E][3]}, c.ars, c.avo, (T >> 1) * 1024 + (T & 1) * 512);
  }
}
constexpr int k4_tile(int F) { return F < 108 ? (F % 6) >> 1 : 3 + (F - 108) / 36; }
constexpr int k4_step(int F) { return F < 108 ? F / 6 : ((F - 108) % 36) >> 1; }
constexpr int k4_addr(int F) { return k4_step(F) * 2048 + (F & 1) * 1024; }
template <int F> DI void k4_frag(K4C& c) {
  constexpr int TL = k4_tile(F), S = k4_step(F), M = F & 1, N = F + K4_PF;
  const u32x4 a = c.wf[F % (K4_PF + 1)];
  if constexpr (F < 108) { if constexpr ((F + 4) % 3 == 0) __builtin_amdgcn_s_setprio(3 - ((F + 4) % 12) / 3); }
  else if constexpr ((F - 108 + 4) % 9 == 0) __builtin_amdgcn_s_setprio(3 - ((F - 108 + 4) % 36) / 9);
  if (S == 0) {
    G_MFMA0(c.acc[TL & 3][2 * M], a, c.X[0][S]);
    G_MFMA0(c.acc[TL & 3][2 * M + 1], a, c.X[1][S]);
  } else {
    G_MFMA(c.acc[TL & 3][2 * M], a, c.X[0][S]);
    G_MFMA(c.acc[TL & 3][2 * M + 1], a, c.X[1][S]);
  }
  if constexpr (N < 108 && N % 12 == 0) {
    constexpr int Z = N / 12;
    if (Z < 8) asm volatile("s_waitcnt vmcnt(5)\n\ts_barrier" : "+v"(c.X[0][2 * Z]), "+v"(c.X[0][2 * Z + 1]), "+v"(c.X[1][2 * Z]), "+v"(c.X[1][2 * Z + 1])::"memory");
    else asm volatile("s_waitcnt vmcnt(2)\n\ts_barrier" : "+v"(c.X[0][2 * Z]), "+v"(c.X[0][2 * Z + 1]), "+v"(c.X[1][2 * Z]), "+v"(c.X[1][2 * Z + 1])::"memory");
  } else if constexpr (N >= 108 && N < 324 && (N - 108) % 36 == 0) {
    constexpr int NT = k4_tile(N);
    if (NT == 3) asm volatile("s_waitcnt vmcnt(0) lgkmcnt(0)\n\ts_barrier" ::: "memory");
    else if (NT == 8) asm volatile("s_waitcnt vmcnt(%0) lgkmcnt(0)\n\ts_barrier" ::"i"(2 + K4_PFWD) : "memory");
    else asm volatile("s_waitcnt vmcnt(4) lgkmcnt(0)\n\ts_barrier" ::: "memory");
  }
  if constexpr (N < 324) c.wf[N % (K4_PF + 1)] = ldsr16(c.wl[k4_tile(N) & 3] + k4_addr(N));
  if constexpr (F < 108) {
    constexpr int Z = F / 12, I = F % 12;
    if constexpr (Z == 0 && I < 4 && !(I & 1)) k4_issue1<2, I / 2>(c);
    if constexpr (Z < 7 && I < 8 && (I & 1)) k4_issue1<Z + 2, 2 + I / 2>(c);
    if constexpr (Z < 6 && I > 8 && (I & 1)) k4_issue1<Z + 3, (I - 9) / 2>(c);
    if constexpr (K4_WD == 0) {
      if constexpr (Z == 6 && I == 10) k4_issue2<3, 0>(c);
      if constexpr (Z == 7 && I < 8 && (I & 1)) k4_issue2<3, 1 + I / 2>(c);
    } else {
      if constexpr (Z == 6 && I == 10) k4_issue2<3, 2>(c);
      if constexpr (Z == 7 && (I == 1 || I == 3)) k4_issue2<3, 3 + I / 2>(c);
    }
  } else {
    constexpr int P = (F - 108) % 36;
    if constexpr (TL == 3) { if constexpr (P % 3 == 0 && P < 15) k4_issue2<4, P / 3>(c); if constexpr (P % 3 == 0 && P >= 15 && P < 30) k4_issue2<5, (P - 15) / 3>(c); }
    else if constexpr (TL + 2 < 9 && P % 4 == 0 && P < 20) k4_issue2<TL + 2, P / 4>(c);
    if constexpr (TL == 7 && P == 0) {
      if (K4_PFWD) c.pf0 = gld4(c.pfb, c.pfo);
      CODE_PF(c.pf1, k_down, k_end, 0, c.w & 1);
    }
    if constexpr (P >= 2) {
      if constexpr (TL == 3) { k4_epi<0, 0, P - 2>(c); k4_epi<1, 1, P - 2>(c); }
      else if constexpr (TL == 4) { k4_epi<2, 0, P - 2>(c); k4_epi<3, 1, P - 2>(c); }
      else k4_epi<TL - 1, 0, P - 2>(c);
    }
  }
  SBAR();
}
template <int B, int... Fs> DI void k4_run(K4C& c, std::integer_sequence<int, Fs...>) { (k4_frag<B + Fs>(c), ...); }
DI void k4_last(K4C& c) {
  float a[8], t[8];
#pragma unroll
  for (int e = 0; e < 8; ++e) { a[e] = c.acc[0][e >> 2][e & 3] * c.c1[e >> 2]; t[e] = a[e] * c.acc[0][2 + (e >> 2)][e & 3]; }
#pragma unroll
  for (int e = 0; e < 8; ++e) a[e] = __builtin_amdgcn_exp2f(a[e]);
#pragma unroll
  for (int e = 0; e < 8; ++e) a[e] = __builtin_amdgcn_rcpf(__builtin_fmaf(a[e], c.c2[e >> 2], c.c2[e >> 2]));
#pragma unroll
  for (int e = 0; e < 4; ++e) c.pk[0][e] = cvtpk(t[2 * e] * a[2 * e], t[2 * e + 1] * a[2 * e + 1]);
  k4_epi<8, 0, 32>(c); k4_epi<8, 0, 33>(c);
}
extern "C" __global__ __launch_bounds__(512, 2) void k_gateup(char* __restrict__ ws) {
  extern __shared__ __attribute__((aligned(16))) char smem[];
  LDSP char* lds = (LDSP char*)smem;
  const int tid = threadIdx.x, lane = tid & 63, w = rfl(tid >> 6);
  const int xcd = blockIdx.x & 7, bi = blockIdx.x >> 3;
  const int rb = 4 * (xcd & 1) + (bi & 3), fs = 8 * (xcd >> 1) + (bi >> 2);
  const int m = lane & 15, g = lane >> 4;
  const int mt = 16 * rb + 2 * w;
  STAMP_DECL; STAMP(0);
  K4C c;
  c.lds0 = (unsigned)(uintptr_t)lds; c.lane16 = lane * 16; c.w = w;
  c.wb = ws + WS_W1 + (size_t)fs * 9 * K4_SLOT;
  c.xb = ws + WS_HC + (size_t)mt * 18432;
  c.rot = K4_ROT ? ((bi >> 2) + 2 * (bi & 3)) % 9 : 0;
  if (K4_WD) { k4_issue_w<0>(c, 0); k4_issue_w<0>(c, 1); }
  k4_issue1<0, 0>(c); k4_issue1<0, 1>(c); k4_issue1<0, 2>(c); k4_issue1<0, 3>(c); k4_issue1<0, 4>(c); k4_issue1<0, 5>(c);
  unsigned sq[6];
#pragma unroll
  for (int i = 0; i < 6; ++i) sq[i] = gld4(ws + WS_SSQ, (3 * g + (i >> 1)) * 8192 + (16 * mt + 16 * (i & 1) + m) * 4);
  k4_issue1<1, 0>(c); k4_issue1<1, 1>(c); k4_issue1<1, 2>(c); k4_issue1<1, 3>(c); k4_issue1<1, 4>(c); k4_issue1<1, 5>(c);
#pragma unroll
  for (int i = 0; i < 4; ++i) { c.wl[i] = lds + c.lane16 + i * K4_SLOT; PIN(c.wl[i]); }
  c.ars = mkrsrc(ws + WS_ACT, 128u * 144 * 1024);
  c.avo = (mt + (g & 1)) * (144 * 1024) + (g >> 1) * 256 + m * 16;
  c.T0 = 9 * fs;
  {
    const unsigned ln = (8 * bi + w) * 41 + (lane < 41 ? lane : 40), lc = ln < 10368 ? ln : 10367, nbq = lc / 2592;
    c.pfb = ws + WS_WD + (size_t)(xcd >> 1) * 331776;
    c.pfo = nbq * 1327104 + (lc - nbq * 2592) * 128;
    PIN(c.pfo);
  }
  STAMP(1);
  asm volatile("s_waitcnt vmcnt(5)\n\ts_barrier" : "+v"(sq[0]), "+v"(sq[1]), "+v"(sq[2]), "+v"(sq[3]), "+v"(sq[4]), "+v"(sq[5]),
               "+v"(c.X[0][0]), "+v"(c.X[0][1]), "+v"(c.X[1][0]), "+v"(c.X[1][1])::"memory");
  STAMP(2);
#pragma unroll
  for (int i = 0; i < K4_PF; ++i) c.wf[i] = ldsr16(c.wl[k4_tile(i)] + k4_addr(i));
#pragma unroll
  for (int t = 0; t < 2; ++t) {
    float ss = __uint_as_float(sq[t]) + __uint_as_float(sq[2 + t]) + __uint_as_float(sq[4 + t]);
    ss = xor32_sum(xor16_sum(ss));
    const float v = ss * (1.0f / DM) + LN_EPS, rs = __builtin_amdgcn_rsqf(v);
    c.c1[t] = -LOG2E * rs; c.c2[t] = -LOG2E * (v * rs);
    PIN(c.c1[t]); PIN(c.c2[t]);
  }
  SBAR();
  k4_run<0>(c, std::make_integer_sequence<int, 108>{});
  STAMP(3);
  k4_run<108>(c, std::make_integer_sequence<int, 72>{});
  STAMP(4);
  k4_run<180>(c, std::make_integer_sequence<int, 72>{});
  STAMP(5);
  k4_run<252>(c, std::make_integer_sequence<int, 72>{});
  asm volatile("s_nop 7" : "+v"(c.acc[0][0]), "+v"(c.acc[0][1]), "+v"(c.acc[0][2]), "+v"(c.acc[0][3]));
  STAMP(6);
  k4_last(c);
  asm volatile("s_waitcnt vmcnt(3)" : "+v"(c.pf0), "+v"(c.pf1));
  STAMP(7); STAMP_OUT(4, 8);
}

constexpr int K5_SLOT = 36864, K5_LDS = 4 * K5_SLOT;
struct K5C {
  const char *wb, *ab, *ab2; unsigned lane16, lds0; int w; unsigned q[5];
  LDSP const char* wl[4];
  LDSP char* xw;
  u32x4 A[3][4], wf[5];
  f32x4 acc[2][9];
  const char* hb; unsigned ho; int kq; u32x2 hr[3];
};
template <int Z, int I> DI void k5_issue(K5C& c) {
  if (I < 8) {
    if (I & 1) c.A[Z % 3][I >> 1] = gld16(((I >> 2) ? c.ab2 : c.ab) + (2 * Z + ((I >> 1) & 1)) * 1024, c.lane16);
    else dma16(c.wb + (Z * 18432 + c.q[I >> 1]), c.lane16, c.lds0 + (Z & 3) * K5_SLOT + (4 * I + c.w) * 1024);
  } else if ((c.w >> 2) == (Z & 1)) {
    dma16(c.wb + (Z * 18432 + c.q[4]), c.lane16, c.lds0 + (Z & 3) * K5_SLOT + (32 + (c.w & 3)) * 1024);
  }
}
template <int F> DI void k5_frag(K5C& c) {
  constexpr int Z = F / 18, f = F % 18, N = F + 4;
  const u32x4 a = c.wf[F % 5];
  if (F < 9) {
    G_MFMA0(c.acc[0][f % 9], a, c.A[Z % 3][f / 9]);
    G_MFMA0(c.acc[1][f % 9], a, c.A[Z % 3][2 + f / 9]);
  } else {
    G_MFMA(c.acc[1][f % 9], a, c.A[Z % 3][2 + f / 9]);
    G_MFMA(c.acc[0][f % 9], a, c.A[Z % 3][f / 9]);
  }
  if constexpr (N < 162 && N % 18 == 0) {
    u32x4(&A)[4] = c.A[(Z + 1) % 3];
    if (Z < 7) asm volatile("s_waitcnt vmcnt(8) lgkmcnt(0)\n\ts_barrier" : "+v"(A[0]), "+v"(A[1]), "+v"(A[2]), "+v"(A[3])::"memory");
    else asm volatile("s_waitcnt vmcnt(3) lgkmcnt(0)\n\ts_barrier" : "+v"(A[0]), "+v"(A[1]), "+v"(A[2]), "+v"(A[3])::"memory");
  }
  if constexpr (Z == 7 && f == 1) asm volatile("global_load_dwordx2 %0, %1, %2" : "=v"(c.hr[0]) : "v"(c.ho), "s"(c.hb + c.kq * 1024));
  if constexpr (Z == 7 && f == 3) asm volatile("global_load_dwordx2 %0, %1, %2 offset:512" : "=v"(c.hr[1]) : "v"(c.ho), "s"(c.hb + c.kq * 1024));
  if constexpr (Z == 7 && f == 5) asm volatile("global_load_dwordx2 %0, %1, %2" : "=v"(c.hr[2]) : "v"(c.ho), "s"(c.hb + 4096));
  if constexpr (N < 162) c.wf[N % 5] = ldsr16(c.wl[(N / 18) & 3] + (N % 18) * 1024);
  if constexpr (Z == 0 && f < 4 && !(f & 1)) k5_issue<2, f / 2>(c);
  if constexpr (Z + 2 < 9 && f < 14 && (f & 1)) k5_issue<Z + 2, 2 + f / 2>(c);
  if constexpr (Z + 3 < 9 && f > 14 && (f & 1)) k5_issue<Z + 3, (f - 15) / 2>(c);
  if constexpr (F >= 157) { PIN(c.acc[1][F - 157]); *(LDSP f32x4*)(c.xw + (F - 157) * 1024) = c.acc[1][F - 157]; }
  SBAR();
}
template <int... Fs> DI void k5_run(K5C& c, std::integer_sequence<int, Fs...>) { (k5_frag<Fs>(c), ...); }
template <int Z, int... Is> DI void k5_issue_all(K5C& c, std::integer_sequence<int, Is...>) { (k5_issue<Z, Is>(c), ...); }
DI f32x4 addbf(f32x4 a, unsigned lo, unsigned hi) { return a + f32x4{bflo(lo), bfhi(lo), bflo(hi), bfhi(hi)}; }
extern "C" __global__ __launch_bounds__(512, 2) void k_down(char* out, char* ws) {
  extern __shared__ __attribute__((aligned(16))) char smem[];
  asm volatile("" : "+s"(out), "+s"(ws));
  LDSP char* lds = (LDSP char*)smem;
  const int tid = threadIdx.x, lane = tid & 63, w = rfl(tid >> 6);
  const int xcd = blockIdx.x & 7, bi = blockIdx.x >> 3;
  const int kq = xcd >> 1, rb = 8 * (xcd & 1) + (bi & 7), nb = bi >> 3;
  const int mg = w & 3, kh = w >> 2, mu = 8 * rb + 2 * mg + kh;
  STAMP_DECL; STAMP(0);
  K5C c;
  c.lds0 = (unsigned)(uintptr_t)lds; c.lane16 = lane * 16; c.w = w;
  c.wb = ws + WS_WD + ((size_t)nb * 144 + 36 * kq) * 9 * 1024;
  c.ab = ws + WS_ACT + ((size_t)mu * 144 + 36 * kq + 18 * kh) * 1024;
  c.ab2 = ws + WS_ACT + ((size_t)(mu ^ 1) * 144 + 36 * kq + 18 * kh) * 1024;
  k5_issue<0, 1>(c); k5_issue<0, 5>(c);
  SBAR();
#pragma unroll
  for (int i = 0; i < 5; ++i) {
    const int p = i < 4 ? 8 * i + w : 32 + (w & 3), st = (p * 57) >> 9, t = p - 9 * st;
    c.q[i] = ((st >> 1) * 144 + p + (t < 8 ? ((t + 2 * kq) & 7) - t : 0)) * 1024;
  }
  k5_issue<0, 0>(c); k5_issue<0, 2>(c); k5_issue<0, 4>(c); k5_issue<0, 6>(c); k5_issue<0, 8>(c); k5_issue<0, 3>(c); k5_issue<0, 7>(c);
  const int m = lane & 15, g = lane >> 4;
  c.hb = ws + WS_HU + (size_t)mu * 18432 + nb * 4608; c.ho = (g >> 1) * 256 + m * 16 + (g & 1) * 8; c.kq = kq;
  k5_issue_all<1>(c, std::make_integer_sequence<int, 9>{});
#pragma unroll
  for (int i = 0; i < 4; ++i) { c.wl[i] = lds + kh * 18432 + c.lane16 + i * K5_SLOT; PIN(c.wl[i]); }
  c.xw = lds + K5_SLOT + w * 9216 + c.lane16; PIN(c.xw);
  LDSP const char* xr = lds + K5_SLOT + (w ^ 4) * 9216 + c.lane16; PIN(xr);
  const __amdgpu_buffer_rsrc_t wrs = mkrsrc(ws, (unsigned)WS_END), ors = mkrsrc(out, SEQ * DM * 2);
  const int tile = rb * 4 + nb;
  const bool own8 = (mu & 3) == kq;
  const unsigned tw = tile * 8 + w;
  unsigned oo = (16 * mu + m) * 1152 + nb * 288;
  unsigned oo8 = own8 ? oo + 256 + g * 8 : 0x80000000u;
  oo += kq * 64 + (g & 1) * 32 + (g >> 1) * 16;
  unsigned p8 = (unsigned)WS_PART8 + tw * 2048 + lane * 8;
  PIN(oo); PIN(oo8); PIN(p8);
  STAMP(1);
  asm volatile("s_waitcnt vmcnt(8)\n\ts_barrier" : "+v"(c.A[0][0]), "+v"(c.A[0][1]), "+v"(c.A[0][2]), "+v"(c.A[0][3])::"memory");
  STAMP(2);
#pragma unroll
  for (int i = 0; i < 4; ++i) c.wf[i] = ldsr16(c.wl[0] + i * 1024);
  SBAR();
  k5_run(c, std::make_integer_sequence<int, 162>{});
  asm volatile("s_waitcnt vmcnt(0)\n\ts_nop 7" : "+v"(c.acc[0][8]), "+v"(c.acc[1][8]), "+v"(c.acc[0][7]), "+v"(c.acc[1][7]), "+v"(c.acc[1][5]), "+v"(c.acc[1][6]),
               "+v"(c.hr[0]), "+v"(c.hr[1]), "+v"(c.hr[2]));
  STAMP(3);
#pragma unroll
  for (int t = 5; t < 9; ++t) *(LDSP f32x4*)(c.xw + t * 1024) = c.acc[1][t];
  asm volatile("s_waitcnt lgkmcnt(0)\n\ts_barrier" ::: "memory");
  f32x4 v[9];
#pragma unroll
  for (int t = 0; t < 9; ++t) v[(t + 2) % 9] = *(LDSP const f32x4*)(xr + ((t + 2) % 9) * 1024);
  SBAR();
#pragma unroll
  for (int j = 1; j < 4; ++j) {
    const f32x4 a = c.acc[0][2 * j] + v[2 * j], b = c.acc[0][2 * j + 1] + v[2 * j + 1];
    bst16<16>(u32x4{cvtpk(a[0], a[1]), cvtpk(a[2], a[3]), cvtpk(b[0], b[1]), cvtpk(b[2], b[3])}, wrs, c.lane16,
              (unsigned)WS_PART + ((((kq + j) & 3) * 512 + tw) * 3 + j - 1) * 1024);
  }
  f32x4 e8 = c.acc[0][8] + v[8];
  __builtin_amdgcn_raw_buffer_store_b64(u32x2{cvtpk(e8[0], e8[1]), cvtpk(e8[2], e8[3])}, wrs, own8 ? 0x80000000u : p8, kq * 512, 16);
  f32x4 e0 = addbf(c.acc[0][0] + v[0], c.hr[0][0], c.hr[0][1]), e1 = addbf(c.acc[0][1] + v[1], c.hr[1][0], c.hr[1][1]);
  e8 = addbf(e8, c.hr[2][0], c.hr[2][1]);
  PIN(e0); PIN(e1); PIN(e8);
  STAMP(4);
  asm volatile("s_waitcnt vmcnt(0)\n\ts_barrier" ::: "memory");
  STAMP(5);
  if (w == 0) {
    __attribute__((address_space(1))) unsigned* cnt = (__attribute__((address_space(1))) unsigned*)(ws + WS_CNT + tile * 128);
    if (lane == 0) __hip_atomic_fetch_add(cnt, 1u, __ATOMIC_RELAXED, __HIP_MEMORY_SCOPE_AGENT);
    for (unsigned spin = 0; spin < (1u << 16); ++spin) {
      if (rfl(__hip_atomic_load(cnt, __ATOMIC_RELAXED, __HIP_MEMORY_SCOPE_AGENT)) >= 4u) break;
      __builtin_amdgcn_s_sleep(1);
    }
  }
  asm volatile("s_barrier" ::: "memory");
  STAMP(6);
  u32x4 r[3]; u32x2 r8[3];
#pragma unroll
  for (int j = 0; j < 3; ++j) {
    r[j] = __builtin_amdgcn_raw_buffer_load_b128(wrs, c.lane16, (unsigned)WS_PART + ((kq * 512 + tw) * 3 + j) * 1024, 16);
    r8[j] = __builtin_amdgcn_raw_buffer_load_b64(wrs, own8 ? p8 : 0x80000000u, ((kq + 1 + j) & 3) * 512, 16);
  }
#pragma unroll
  for (int j = 0; j < 3; ++j) { e0 = addbf(e0, r[j][0], r[j][1]); e1 = addbf(e1, r[j][2], r[j][3]); e8 = addbf(e8, r8[j][0], r8[j][1]); }
  {
    const auto s0 = __builtin_amdgcn_permlane16_swap(cvtpk(e0[0], e0[1]), cvtpk(e1[0], e1[1]), false, false);
    const auto s1 = __builtin_amdgcn_permlane16_swap(cvtpk(e0[2], e0[3]), cvtpk(e1[2], e1[3]), false, false);
    bst16<OUT_AUX>(u32x4{s0[0], s1[0], s0[1], s1[1]}, ors, oo, 0);
    __builtin_amdgcn_raw_buffer_store_b64(u32x2{cvtpk(e8[0], e8[1]), cvtpk(e8[2], e8[3])}, ors, oo8, 0, OUT_AUX);
  }
  STAMP(7); STAMP_OUT(5, 8);
}
extern "C" __global__ void k_end() {}

extern "C" void kernel_launch(void* const* d_in, const int* in_sizes, int n_in, void* d_out, int out_size, void* d_ws, size_t ws_size,
                              hipStream_t stream) {
  const u16* x = (const u16*)d_in[0];
  const u16* wqkv = (const u16*)d_in[1];
  const u16* wo = (const u16*)d_in[2];
  const u16* wg = (const u16*)d_in[4];
  const u16* wu = (const u16*)d_in[5];
  const u16* wd = (const u16*)d_in[6];
  char* ws = (char*)d_ws;
  (void)hipFuncSetAttribute((const void*)k_qkv, hipFuncAttributeMaxDynamicSharedMemorySize, K1_LDS);
  (void)hipFuncSetAttribute((const void*)k_attn, hipFuncAttributeMaxDynamicSharedMemorySize, K2_LDS);
  (void)hipFuncSetAttribute((const void*)k_oproj, hipFuncAttributeMaxDynamicSharedMemorySize, K3_LDS);
  (void)hipFuncSetAttribute((const void*)k_gateup, hipFuncAttributeMaxDynamicSharedMemorySize, K4_LDS);
  (void)hipFuncSetAttribute((const void*)k_down, hipFuncAttributeMaxDynamicSharedMemorySize, K5_LDS);
  k_qkv<<<256, 512, K1_LDS, stream>>>(x, wqkv, ws);
  k_attn<<<256, 512, K2_LDS, stream>>>(x, wo, wg, wu, wd, ws);
  k_oproj<<<K3_PF ? 256 : 192, 512, K3_LDS, stream>>>(ws);
  k_gateup<<<256, 512, K4_LDS, stream>>>(ws);
  k_down<<<256, 512, K5_LDS, stream>>>((char*)d_out, ws);
}
```

```cpp
#include <hip/hip_runtime.h>
#include <hip/hip_bf16.h>
#include <stdint.h>
#include <utility>
#include <type_traits>

typedef __bf16 bf16x8 __attribute__((ext_vector_type(8)));
typedef __bf16 bf16x2 __attribute__((ext_vector_type(2)));
typedef short s16x4 __attribute__((ext_vector_type(4)));
typedef float f32x16 __attribute__((ext_vector_type(16)));
typedef float f32x4 __attribute__((ext_vector_type(4)));
typedef float f32x2 __attribute__((ext_vector_type(2)));
typedef unsigned u32x4 __attribute__((ext_vector_type(4)));
typedef unsigned u32x2 __attribute__((ext_vector_type(2)));
typedef unsigned short u16;
#define LDSP __attribute__((address_space(3)))
#define DI __device__ __forceinline__
#define SBAR() __builtin_amdgcn_sched_barrier(0)

constexpr int SEQ = 2048, DM = 576, NH = 32, DH = 18, FF = 4608;
constexpr float LOG2E = 1.4426950408889634f;
constexpr float QSCALE = 0.23570226039551584f * LOG2E;
constexpr float LN_EPS = 1e-5f;

constexpr size_t CHUNK_B = 37888;
constexpr size_t KV_HEAD_B = 4 * CHUNK_B;
constexpr size_t WS_QM = 0;
constexpr size_t WS_QT = WS_QM + (size_t)NH * 65536;
constexpr size_t WS_KV = WS_QT + (size_t)NH * 8192;
constexpr size_t WS_KMAX = WS_KV + (size_t)NH * KV_HEAD_B;
constexpr size_t WS_XMEAN = WS_KMAX + 65536;
constexpr size_t WS_OFR = WS_XMEAN + 8192;
constexpr size_t WS_WO = WS_OFR + (size_t)128 * 18 * 1024;
constexpr size_t WS_WBAR = WS_WO + (size_t)12 * 54 * 1024;
constexpr size_t WS_HC = WS_WBAR + 3072;
constexpr size_t WS_SSQ = WS_HC + (size_t)64 * 36 * 1024;
constexpr size_t WS_W1 = WS_SSQ + (size_t)12 * 8192;
constexpr size_t WS_WD = WS_W1 + (size_t)288 * 36 * 1024;
constexpr size_t WS_ACT = WS_WD + (size_t)4 * 144 * 9 * 1024;
constexpr size_t UNIT_B = (size_t)128 * 18432;
constexpr size_t WS_PART = WS_ACT + (size_t)128 * 144 * 1024;
constexpr size_t WS_PART8 = WS_PART + (size_t)4 * 64 * 8 * 3 * 1024;
constexpr size_t WS_CNT = WS_PART8 + (size_t)64 * 8 * 4 * 512;
constexpr size_t WS_XU = WS_CNT + 32768;
constexpr size_t WS_HU = WS_XU + UNIT_B;
constexpr size_t WS_END = WS_HU + UNIT_B;

DI unsigned cvtpk(float lo, float hi) {
  f32x2 v = {lo, hi};
  bf16x2 b = __builtin_convertvector(v, bf16x2);
  return __builtin_bit_cast(unsigned, b);
}
DI float bflo(unsigned u) { return __uint_as_float(u << 16); }
DI float bfhi(unsigned u) { return __uint_as_float(u & 0xffff0000u); }
DI float dot2(unsigned a, unsigned b, float c) {
  return __builtin_amdgcn_fdot2_f32_bf16(__builtin_bit_cast(bf16x2, a), __builtin_bit_cast(bf16x2, b), c, false);
}
DI f32x16 mfma32(u32x4 a, u32x4 b, f32x16 c) {
  return __builtin_amdgcn_mfma_f32_32x32x16_bf16(__builtin_bit_cast(bf16x8, a), __builtin_bit_cast(bf16x8, b), c, 0, 0, 0);
}
DI f32x4 mfma16(u32x4 a, u32x4 b, f32x4 c) {
  return __builtin_amdgcn_mfma_f32_16x16x32_bf16(__builtin_bit_cast(bf16x8, a), __builtin_bit_cast(bf16x8, b), c, 0, 0, 0);
}
DI unsigned rfl(unsigned x) { return __builtin_amdgcn_readfirstlane(x); }
DI const char* uniptr(const char* p) {
  uint64_t v = (uint64_t)p;
  unsigned lo = rfl((unsigned)v), hi = rfl((unsigned)(v >> 32));
  return (const char*)(((uint64_t)hi << 32) | lo);
}
DI void dma16(const char* sbase, unsigned voff, unsigned lds) {
  asm volatile("s_mov_b32 m0, %2\n\ts_nop 0\n\tglobal_load_lds_dwordx4 %0, %1" ::"v"(voff), "s"(sbase), "s"(lds));
}
DI u32x4 gld16(const char* sbase, unsigned voff) {
  u32x4 r;
  asm volatile("global_load_dwordx4 %0, %1, %2" : "=v"(r) : "v"(voff), "s"(sbase));
  return r;
}
DI unsigned gld4(const char* sbase, unsigned voff) {
  unsigned r;
  asm volatile("global_load_dword %0, %1, %2" : "=v"(r) : "v"(voff), "s"(sbase));
  return r;
}
#define WAITV(n) asm volatile("s_waitcnt vmcnt(" #n ")" ::: "memory")
#define BARRIER() asm volatile("s_barrier" ::: "memory")
#define PIN(x) asm volatile("" : "+v"(x))
DI u32x4 ldsr16(LDSP const char* p) { return *(LDSP const u32x4*)p; }
DI float xor32_sum(float v) {
  auto r = __builtin_amdgcn_permlane32_swap(__float_as_uint(v), __float_as_uint(v), false, false);
  return __uint_as_float(r[0]) + __uint_as_float(r[1]);
}
DI float xor32_max(float v) {
  auto r = __builtin_amdgcn_permlane32_swap(__float_as_uint(v), __float_as_uint(v), false, false);
  return fmaxf(__uint_as_float(r[0]), __uint_as_float(r[1]));
}
DI float xor16_sum(float v) {
  auto r = __builtin_amdgcn_permlane16_swap(__float_as_uint(v), __float_as_uint(v), false, false);
  return __uint_as_float(r[0]) + __uint_as_float(r[1]);
}

#define STAMP_DECL
#define STAMP(i)
#define STAMP_OUT(kid, n)

DI int wo_row(int step, int g, int j) {
  if (step < 16) return 18 * (2 * step + (g >> 1)) + 4 * (g & 1) + (j & 3) + 8 * (j >> 2);
  return 18 * (4 * (4 * (step - 16) + g) + (j >> 1)) + 16 + (j & 1);
}

#define K4_SPREAD 1
#define K4_PM 1
#define K4_WD 1
#define K3_PF 0
#define K3_SLEEP 32
#define K1_ROT 0
#define AT_PM 9
#define HELP_PRIO 3
#define HELP_EARLY 0
#define PREP_ORD 1
#define GRAY 1
#define K4_POLL 10
#define K4_ROT 0
#define K5_FLAG 0
#define K5_SPREAD 1
#define K5_PM 0
#define OUT_AUX 16
#define K4_PFWD 1
#define QKV_AUX 16
#define QKT_AUX 16
#define V_AUX 16
#define O_AUX 16
#define H_AUX 16
#define PART_AUX 16
#define ACT_AUX 16
#define PREP_AUX 16
#define CODE_PF(dst, first, last, extra, blk) CODE_PF2(dst, first, last, extra, blk, 128u)
#define CODE_PF2(dst, first, last, extra, blk, stride)                                                                              \
  do {                                                                                                                     \
    uint64_t pc_; int b_, e_;                                                                                              \
    asm volatile("s_getpc_b64 %0\n.Lpf%=:\n\ts_mov_b32 %1, " #first "-.Lpf%=\n\ts_mov_b32 %2, " #last "-.Lpf%=" : "=s"(pc_), "=s"(b_), "=s"(e_)); \
    const unsigned o_ = (blk) * 8192u + (threadIdx.x & 63) * (stride), n_ = (unsigned)(e_ - b_) + (extra) - 4u;                \
    dst = gld4((const char*)(pc_ + b_), o_ < n_ ? o_ : n_);                                                                \
  } while (0)
#define G_MFMA(d, a, b) asm volatile("v_mfma_f32_16x16x32_bf16 %0, %1, %2, %0" : "+v"(d) : "v"(a), "v"(b))
#define G_MFMA0(d, a, b) asm volatile("v_mfma_f32_16x16x32_bf16 %0, %1, %2, 0" : "=&v"(d) : "v"(a), "v"(b))
template <int AUX> DI void bst16(u32x4 v, __amdgpu_buffer_rsrc_t rs, unsigned voff, unsigned soff) {
  __builtin_amdgcn_raw_buffer_store_b128(v, rs, voff, soff, AUX);
  asm volatile("s_nop 1" ::"v"(v) : "memory");
}
DI __amdgpu_buffer_rsrc_t mkrsrc(const void* p, unsigned bytes) {
  return __builtin_amdgcn_make_buffer_rsrc((void*)p, 0, bytes, 0x00020000);
}

#define K1_PF 4
#define K1_TOUCH 1
#define K1_TSLEEP 16
#define K1_DEPTH 2
constexpr int K1_PITCH = 224, K1_SLOT = 16384 + 64 * K1_PITCH  , K1_NS = K1_DEPTH + 1;
constexpr int K1_SP = 232;
constexpr int K1_STG = K1_NS * K1_SLOT, K1_CS = K1_STG + 128 * K1_SP + 64, K1_ST = K1_CS + 512 + 64, K1_LDS = K1_ST + 2048;
struct K1C {
  LDSP const char *xa[2], *wl, *wc0, *wc1;
  u32x4 X[3][2], wf[K1_PF + 1], ones;
  f32x4 acc[4][2];
  f32x4 sm[2]; float sq[2];
};
DI void k1_loader(const char* xb, const char* wb, char* ws, int j, int lane, unsigned lds0) {
  __builtin_amdgcn_s_setprio(3);
  unsigned xo[2], wo[4];
  const int g = lane >> 4;
  xo[0] = (lane >> 3) * 1152 + (((lane & 7) ^ g) * 16);
  xo[1] = (8 + (lane >> 3)) * 1152 + (((lane & 7) ^ (4 + g)) * 16);
  xb += j * (32 * 1152);
  const unsigned xd = lds0 + j * 4096, wd = lds0 + 16384 + j * 1024;
#define K1_X(z, so) do { dma16(xb + (z) * 128, xo[0], xd + (so)); dma16(xb + (z) * 128, xo[1], xd + (so) + 1024);                    \
    dma16(xb + (z) * 128 + 16 * 1152, xo[0], xd + (so) + 2048); dma16(xb + (z) * 128 + 16 * 1152, xo[1], xd + (so) + 3072); } while (0)
#define K1_W(z, so) do { _Pragma("unroll") for (int i = 0; i < 3; ++i) dma16(wb + (z) * (64 * 3456), wo[i], wd + (so) + i * 4096);     \
    if (j < 2) dma16(wb + (z) * (64 * 3456), wo[3], wd + (so) + 3 * 4096); } while (0)
  K1_X(0, 0);
  SBAR();
#pragma unroll
  for (int i = 0; i < 4; ++i) {
    const unsigned u = 64 * (j + 4 * i) + lane, r = u / 14;
    wo[i] = ((r & ~12u) | ((r & 4u) << 1) | ((r & 8u) >> 1)) * 3456 + (u - r * 14) * 16;
  }
  K1_W(0, 0);
#pragma unroll
  for (int z = 1; z < K1_DEPTH; ++z) { K1_X(z, z * K1_SLOT); K1_W(z, z * K1_SLOT); }
  unsigned so = K1_DEPTH * K1_SLOT;
#pragma unroll 1
  for (int z = K1_DEPTH; z < 9; ++z) {
    asm volatile("s_waitcnt vmcnt(%0)\n\ts_barrier" ::"i"(7 * (K1_DEPTH - 1)) : "memory");
    K1_X(z, so); K1_W(z, so);
    so = so == (K1_NS - 1) * K1_SLOT ? 0 : so + K1_SLOT;
  }
  if (K1_DEPTH > 2) asm volatile("s_waitcnt vmcnt(14)\n\ts_barrier" ::: "memory");
  asm volatile("s_waitcnt vmcnt(7)\n\ts_barrier" ::: "memory");
  asm volatile("s_waitcnt vmcnt(0)\n\ts_barrier" ::: "memory");
  if (blockIdx.x < 4 && j == 0) bst16<16>(u32x4{0, 0, 0, 0}, mkrsrc(ws, (unsigned)WS_END), (unsigned)WS_CNT + (blockIdx.x * 64 + lane) * 128, 0);
#undef K1_X
#undef K1_W
}
DI u32x4 tr_frag(LDSP const char* p, int d) {
  s16x4 lo = __builtin_amdgcn_ds_read_tr16_b64_v4i16((LDSP s16x4*)p);
  s16x4 hi = __builtin_amdgcn_ds_read_tr16_b64_v4i16((LDSP s16x4*)(p + d));
  return __builtin_bit_cast(u32x4, __builtin_shufflevector(lo, hi, 0, 1, 2, 3, 4, 5, 6, 7));
}
template <int NHG, int F> DI void k1_read(K1C& c) {
  constexpr int NF = 4 + NHG, S = F / NF, j = F % NF, Z = S >> 1, so = (Z % K1_NS) * K1_SLOT;
  if constexpr (j == 0) {
    c.X[S % 3][0] = ldsr16(c.xa[S & 1] + so);
    c.X[S % 3][1] = ldsr16(c.xa[S & 1] + so + 2048);
  }
  constexpr int wo = so + (S & 1) * 7168;
  if constexpr (NHG == 0 || j < 3) c.wf[F % (K1_PF + 1)] = tr_frag(c.wl + wo + j * 32, 1792);
  else c.wf[F % (K1_PF + 1)] = tr_frag((j == 3 ? c.wc0 : c.wc1) + wo, 1792);
}
template <int NHG, int F> DI void k1_frag(K1C& c) {
  constexpr int NF = 4 + NHG, S = F / NF, j = F % NF, Z = S >> 1, N = F + K1_PF, fz = F % (2 * NF);
  if constexpr (S == 0) {
    if constexpr (NHG == 0 || j < 3) { G_MFMA0(c.acc[j][0], c.wf[F % (K1_PF + 1)], c.X[0][0]); G_MFMA0(c.acc[j][1], c.wf[F % (K1_PF + 1)], c.X[0][1]); }
    else G_MFMA0(c.acc[3][j - 3], c.wf[F % (K1_PF + 1)], c.ones);
  } else {
    if constexpr (NHG == 0 || j < 3) { G_MFMA(c.acc[j][0], c.wf[F % (K1_PF + 1)], c.X[S % 3][0]); G_MFMA(c.acc[j][1], c.wf[F % (K1_PF + 1)], c.X[S % 3][1]); }
    else G_MFMA(c.acc[3][j - 3], c.wf[F % (K1_PF + 1)], c.ones);
  }
  if constexpr ((S & 1) == NHG && j < 4) {
#pragma unroll
    for (int t = 0; t < 2; ++t) { const unsigned v = c.X[S % 3][t][j]; c.sq[t] = dot2(v, v, c.sq[t]); PIN(c.sq[t]); }
    if constexpr (j < 2) { if constexpr (S < 2) G_MFMA0(c.sm[j], c.ones, c.X[S % 3][j]); else G_MFMA(c.sm[j], c.ones, c.X[S % 3][j]); }
  }
  if constexpr (N < 18 * NF) {
    if constexpr (N % (2 * NF) == 0) {
      asm volatile("s_waitcnt lgkmcnt(0)\n\ts_barrier" ::: "memory");
    }
    k1_read<NHG, N>(c);
  }
  SBAR();
}
template <int NHG, int... Fs> DI void k1_run(K1C& c, std::integer_sequence<int, Fs...>) {
  k1_read<NHG, 0>(c); k1_read<NHG, 1>(c); k1_read<NHG, 2>(c);
  if (K1_PF > 3) k1_read<NHG, 3>(c);
  if (K1_PF > 4) k1_read<NHG, 4>(c);
  if (K1_PF > 5) k1_read<NHG, 5>(c);
  SBAR();
  (k1_frag<NHG, Fs>(c), ...);
  asm volatile("s_nop 7" : "+v"(c.acc[0][0]), "+v"(c.acc[0][1]), "+v"(c.acc[1][0]), "+v"(c.acc[1][1]), "+v"(c.acc[2][0]), "+v"(c.acc[2][1]),
               "+v"(c.acc[3][0]), "+v"(c.acc[3][1]), "+v"(c.sm[0]), "+v"(c.sm[1]));
  SBAR();
}
extern "C" __global__ __launch_bounds__(768, 3) void k_qkv(const u16* x, const u16* wqkv, char* ws) {
  extern __shared__ __attribute__((aligned(16))) char smem[];
  asm volatile("" : "+s"(x), "+s"(wqkv), "+s"(ws));
  LDSP char* lds = (LDSP char*)smem;
  const unsigned lds0 = (unsigned)(uintptr_t)lds;
  const int tid = threadIdx.x, lane = tid & 63, w = rfl(tid >> 6);
  const int xcd = blockIdx.x & 7, bi = blockIdx.x >> 3;
  const int rb = 4 * (xcd >> 1) + (bi & 3), cb = 8 * (xcd & 1) + (bi >> 2);
  const int c0 = 108 * cb - 4 * (cb & 1);
  if (w >= 8) { k1_loader((const char*)x + rb * (128 * 1152), (const char*)wqkv + c0 * 2, ws, w - 8, lane, lds0); return; }
  const int m = lane & 15, g = lane >> 4, mg = w & 3, nh = w >> 2;
  STAMP_DECL; STAMP(0);
  unsigned pf = 0;
  if (w == 0 && bi < 9) CODE_PF(pf, k_qkv, k_end, 0, bi);
  K1C c;
  c.ones = u32x4{0x3f803f80u, 0x3f803f80u, 0x3f803f80u, 0x3f803f80u};
  PIN(c.ones);
  c.xa[0] = lds + (32 * mg + m) * 128 + ((g ^ (m >> 1)) * 16);
  c.xa[1] = lds + (32 * mg + m) * 128 + (((4 + g) ^ (m >> 1)) * 16);
  LDSP const char* wbase = lds + 16384 + (16 * (g >> 1) + 4 * (g & 1) + (m >> 2)) * K1_PITCH + (m & 3) * 8;
  c.wl = wbase + nh * 128;
  c.wc0 = wbase + mg * 32;
  c.wc1 = wbase + (mg < 3 ? mg + 4 : 6) * 32;
  c.sq[0] = c.sq[1] = 0.f;
  const int off = 4 * (cb & 1), T0 = 128 * rb;
  LDSP char* stg = lds + K1_STG;
  LDSP char* a_st = lds + K1_ST + (32 * mg + m) * 8;                 PIN(a_st);
  LDSP char* a_cs = lds + K1_CS + (64 * nh + 4 * g) * 4;             PIN(a_cs);
  LDSP char* a_sw = stg + (32 * mg + m) * K1_SP + (64 * nh + 4 * g) * 2;   PIN(a_sw);
  LDSP char* a_tr = stg + (32 * mg + 4 * (g & 1) + (m >> 2)) * K1_SP + (64 * nh + 16 * (g >> 1) + 4 * (m & 3)) * 2;   PIN(a_tr);
  float qs[4];
#pragma unroll
  for (int j = 0; j < 4; ++j) { qs[j] = (c0 + 64 * nh + 16 * j + 4 * g < DM) ? QSCALE : 1.0f; PIN(qs[j]); }
  const __amdgpu_buffer_rsrc_t wrs = mkrsrc(ws, (unsigned)WS_END);
  constexpr unsigned DROP = 0x80000000u;
  const unsigned kvo = (unsigned)WS_KV + (T0 >> 9) * (unsigned)CHUNK_B;
  unsigned vo[2] = {0, 0}, vone = 0, vd[4] = {DROP, DROP, DROP, DROP};
  if (cb >= 10) {
#pragma unroll
  for (int tt = 0; tt < 2; ++tt) {
    const int cc = 64 * nh + 32 * tt + 16 * (g >> 1) + m - off, hd = (cc + 18) / 18 - 1, G = 6 * cb + hd;
    vo[tt] = (cc >= 0 && cc < 108) ? hd * 4864 + 2 * mg * 608 + (g & 1) * 304 + (cc - 18 * hd) * 16 : 29184;
  }
  vone = tid < 96 ? (tid >> 4) * 4864 + (tid & 15) * 304 + 288 : 29184;
#pragma unroll
  for (int i = 0; i < 4; ++i) {
    const unsigned u = i * 512 + tid, hd = u / 304;
    vd[i] = (u < 1824 && 6 * cb + hd >= 64) ? kvo + 18432 + ((T0 >> 4) & 31) * 608 + (6 * cb + hd - 64) * (unsigned)KV_HEAD_B + (u - hd * 304) * 16 : DROP;
  }
  }
  PIN(vone); PIN(vo[0]); PIN(vo[1]); PIN(vd[0]); PIN(vd[1]); PIN(vd[2]); PIN(vd[3]);
  const int tok = tid & 127, T = T0 + tok;
  LDSP const char* a_g = stg + tok * K1_SP + off * 2;   PIN(a_g);
  unsigned um[2], ut[2];
  const unsigned tl = ((T >> 5) & 15) * 1024 + (T & 31) * 16;
#pragma unroll
  for (int it = 0; it < 2; ++it) {
    const int hd = (w >> 1) + 4 * it, G = 6 * cb + hd, head = G & 31;
    if (hd >= 6 || G >= 64) { um[it] = DROP; ut[it] = DROP; }
    else if (G < 32) { um[it] = (unsigned)WS_QM + head * 65536 + (T >> 9) * 16384 + tl; ut[it] = (unsigned)WS_QT + head * 8192 + T * 4; }
    else { um[it] = kvo + head * (unsigned)KV_HEAD_B + tl; ut[it] = kvo + head * (unsigned)KV_HEAD_B + 16384 + ((T >> 6) & 7) * 256 + (T & 31) * 8 + ((T >> 5) & 1) * 4; }
    PIN(um[it]); PIN(ut[it]);
  }
  STAMP(1);
  asm volatile("s_barrier" ::: "memory");
  STAMP(2);
  if (nh) k1_run<1>(c, std::make_integer_sequence<int, 90>{});
  else k1_run<0>(c, std::make_integer_sequence<int, 72>{});
  asm volatile("s_waitcnt vmcnt(0)" : "+v"(pf));
  STAMP(3);
#pragma unroll
  for (int t = 0; t < 2; ++t) {
    const f32x2 st = {c.sm[t][0], xor32_sum(xor16_sum(c.sq[t]))};
    *(LDSP f32x2*)(a_st + nh * 1024 + t * 128) = st;
  }
  if (nh) {
    *(LDSP f32x4*)(lds + K1_CS + (16 * mg + 4 * g) * 4) = c.acc[3][0];
    *(LDSP f32x4*)(lds + K1_CS + (16 * (mg + 4) + 4 * g) * 4) = c.acc[3][1];
  }
  asm volatile("s_waitcnt lgkmcnt(0)\n\ts_barrier" ::: "memory");
  {
    f32x2 p[2][2]; f32x4 cc[4];
#pragma unroll
    for (int t = 0; t < 2; ++t) { p[t][0] = *(LDSP const f32x2*)(a_st + t * 128); p[t][1] = *(LDSP const f32x2*)(a_st + 1024 + t * 128); }
#pragma unroll
    for (int j = 0; j < 4; ++j) cc[j] = *(LDSP const f32x4*)(a_cs + j * 64);
    SBAR();
#pragma unroll
    for (int t = 0; t < 2; ++t) {
      const float mean = (p[t][0][0] + p[t][1][0]) * (1.0f / DM);
      const float var = fmaxf((p[t][0][1] + p[t][1][1]) * (1.0f / DM) - mean * mean, 0.f);
      const float rstd = __builtin_amdgcn_rsqf(var + LN_EPS);
      if (cb == 0 && nh == 0) __builtin_amdgcn_raw_buffer_store_b32(__float_as_uint(mean), wrs, (unsigned)WS_XMEAN + (T0 + 32 * mg + 16 * t + m) * 4, 0, QKV_AUX);
#pragma unroll
      for (int j = 0; j < 4; ++j) {
        if (j < 3 || nh == 0) {
          const f32x4 a = (c.acc[j][t] - mean * cc[j]) * (rstd * qs[j]);
          *(LDSP u32x2*)(a_sw + t * (16 * K1_SP) + j * 32) = u32x2{cvtpk(a[0], a[1]), cvtpk(a[2], a[3])};
        }
      }
    }
  }
  if (cb >= 10) {
    asm volatile("s_waitcnt lgkmcnt(0)" ::: "memory");
    u32x4 v[2][2];
#pragma unroll
    for (int tt = 0; tt < 2; ++tt)
#pragma unroll
      for (int t = 0; t < 2; ++t) v[tt][t] = tr_frag(a_tr + t * (16 * K1_SP) + tt * 64, 8 * K1_SP);
    *(LDSP u32x4*)(lds + vone) = c.ones;
#pragma unroll
    for (int tt = 0; tt < 2; ++tt)
#pragma unroll
      for (int t = 0; t < 2; ++t) *(LDSP u32x4*)(lds + vo[tt] + t * 608) = v[tt][t];
  }
  STAMP(4);
  asm volatile("s_waitcnt lgkmcnt(0)\n\ts_barrier" ::: "memory");
  if (cb >= 10) {
    u32x4 v[4];
#pragma unroll
    for (int i = 0; i < 4; ++i) v[i] = ldsr16(lds + i * 8192 + tid * 16);
#pragma unroll
    for (int i = 0; i < 4; ++i) bst16<V_AUX>(v[i], wrs, vd[i], 0);
  }
  if (cb <= 10) {
    unsigned d[2][9];
#pragma unroll
    for (int it = 0; it < 2; ++it) {
      const int hd = (w >> 1) + 4 * it;
      LDSP const unsigned* sp = (LDSP const unsigned*)(a_g + 36 * (hd < 6 ? hd : 0));
#pragma unroll
      for (int i = 0; i < 9; ++i) d[it][i] = sp[i];
    }
    SBAR();
#pragma unroll
    for (int it = 0; it < 2; ++it) {
      bst16<QKV_AUX>(u32x4{d[it][0], d[it][1], d[it][2], d[it][3]}, wrs, um[it], 0);
      bst16<QKV_AUX>(u32x4{d[it][4], d[it][5], d[it][6], d[it][7]}, wrs, um[it] + 512, 0);
      __builtin_amdgcn_raw_buffer_store_b32(d[it][8], wrs, ut[it], 0, QKT_AUX);
    }
#pragma unroll
    for (int it = 0; it < 2; ++it) {
      const int hd = (w >> 1) + 4 * it, G = 6 * cb + hd;
      if (hd < 6 && G >= 32 && G < 64) {
        float n2 = 0.f;
#pragma unroll
        for (int i = 0; i < 9; ++i) n2 = dot2(d[it][i], d[it][i], n2);
        unsigned nb = __float_as_uint(n2);
        { auto r = __builtin_amdgcn_permlane32_swap(nb, nb, false, false); nb = max(r[0], r[1]); }
        { auto r = __builtin_amdgcn_permlane16_swap(nb, nb, false, false); nb = max(r[0], r[1]); }
        __builtin_amdgcn_raw_buffer_store_b32(nb, wrs, (unsigned)WS_KMAX + (((2 * rb + (w & 1)) * 32 + (G & 31)) * 16 + m) * 4, 0, QKV_AUX);
      }
    }
  }
  STAMP(5); STAMP_OUT(1, 6);
}

constexpr int K2_LDS = (int)KV_HEAD_B;
constexpr int NJ_W1 = 72 * 36, NJ_WD = 9 * 144, NJ_WO = 9 * 18, NJ_ALL = NJ_W1 + NJ_WD + NJ_WO;
struct PrepJob { const char* src; unsigned rs, c1, c2, voff, dst[4]; };
DI void prep_setup(PrepJob& j, int J, int lane, const u16* wo, const u16* wg, const u16* wu, const u16* wd) {
  if (J >= NJ_ALL) J = NJ_ALL - 1;
  const int g = (lane >> 2) & 3;
  unsigned lrow = 8 * g;
  j.c1 = 0; j.c2 = 0;
  if (J < NJ_W1) {
    const int sm = J / 72, tg = J - sm * 72;
    j.src = (const char*)((sm & 1) ? wu : wg) + (size_t)(sm >> 1) * (32 * FF * 2) + tg * 128;
    j.rs = FF * 2;
#pragma unroll
    for (int t = 0; t < 4; ++t) j.dst[t] = (unsigned)WS_W1 + (4 * tg + t) * 36864 + sm * 1024;
  } else if (J < NJ_W1 + NJ_WD) {
    const int q = J - NJ_W1, cc = q / 9, tg = q - cc * 9;
    j.src = (const char*)wd + (size_t)cc * (32 * DM * 2) + tg * 128;
    j.rs = DM * 2;
#pragma unroll
    for (int t = 0; t < 4; ++t) { const int nt = 4 * tg + t, nb = nt / 9; j.dst[t] = (unsigned)WS_WD + ((nb * 144 + cc) * 9 + (nt - nb * 9)) * 1024; }
  } else {
    const int q = J - NJ_W1 - NJ_WD, step = q / 9, tg = q - step * 9;
    j.rs = DM * 2;
    if (step < 16) { j.src = (const char*)wo + (size_t)step * (36 * DM * 2) + tg * 128; lrow = 18 * (g >> 1) + 4 * (g & 1); j.c2 = 4; }
    else { j.src = (const char*)wo + (size_t)(288 * (step - 16) + 16) * (DM * 2) + tg * 128; lrow = 72 * g; j.c1 = 16; }
#pragma unroll
    for (int t = 0; t < 4; ++t) { const int nt = 4 * tg + t, nb = nt / 3; j.dst[t] = (unsigned)WS_WO + ((nb * 18 + step) * 3 + (nt - nb * 3)) * 1024; }
  }
#pragma unroll
  for (int t = 0; t < 4; ++t) j.dst[t] = rfl(j.dst[t]);
  j.voff = lrow * j.rs + (lane >> 4) * 32 + (lane & 3) * 8;
}
template <int JJ>
DI void prep_load(const PrepJob& j, u32x2 (&r)[8]) {
  const char* b = j.src + (JJ + j.c1 * (JJ >> 1) + j.c2 * (JJ >> 2)) * j.rs;
  asm volatile("global_load_dwordx2 %0, %1, %2" : "=v"(r[JJ]) : "v"(j.voff), "s"(b));
}
DI void prep_load_all(const PrepJob& j, u32x2 (&r)[8]) {
  prep_load<0>(j, r); prep_load<1>(j, r); prep_load<2>(j, r); prep_load<3>(j, r); prep_load<4>(j, r); prep_load<5>(j, r); prep_load<6>(j, r); prep_load<7>(j, r);
}
DI void prep_store(const PrepJob& j, const u32x2 (&r)[8], __amdgpu_buffer_rsrc_t rs, unsigned so) {
  u32x4 u[4];
#pragma unroll
  for (int i = 0; i < 4; ++i) {
    u[0][i] = __builtin_amdgcn_perm(r[2 * i + 1][0], r[2 * i][0], 0x05040100u);
    u[1][i] = __builtin_amdgcn_perm(r[2 * i + 1][0], r[2 * i][0], 0x07060302u);
    u[2][i] = __builtin_amdgcn_perm(r[2 * i + 1][1], r[2 * i][1], 0x05040100u);
    u[3][i] = __builtin_amdgcn_perm(r[2 * i + 1][1], r[2 * i][1], 0x07060302u);
  }
#pragma unroll
  for (int i = 0; i < 4; ++i) {
    const auto a0 = __builtin_amdgcn_permlane32_swap(u[0][i], u[2][i], false, false);
    const auto a1 = __builtin_amdgcn_permlane32_swap(u[1][i], u[3][i], false, false);
    const auto b0 = __builtin_amdgcn_permlane16_swap(a0[0], a1[0], false, false);
    const auto b1 = __builtin_amdgcn_permlane16_swap(a0[1], a1[1], false, false);
    u[0][i] = b0[0]; u[1][i] = b0[1]; u[2][i] = b1[0]; u[3][i] = b1[1];
  }
#pragma unroll
  for (int t = 0; t < 4; ++t) bst16<PREP_AUX>(u[t], rs, so, j.dst[t]);
}
#define PIN8(r) "+v"(r[0]), "+v"(r[1]), "+v"(r[2]), "+v"(r[3]), "+v"(r[4]), "+v"(r[5]), "+v"(r[6]), "+v"(r[7])

#define A_MFMA_ACC(d, a, b) asm volatile("v_mfma_f32_32x32x16_bf16 %0, %1, %2, %0" : "+v"(d) : "v"(a), "v"(b))
#define AT_M(m)
#define AT_U
struct AttnC {
  LDSP const char *kmb, *ktb, *vb;
  unsigned kma[3], kta[3], va[3]; unsigned long long mk, mv;
  u32x4 qm, qt, kf, v0, v1, p1; u32x2 ktl;
  f32x16 negm, o, sa, sb;
};
constexpr int k_off(int T) { return (T >> 4) * (int)CHUNK_B + (T & 15) * 1024; }
constexpr int kt_off(int T) { return (T >> 4) * (int)CHUNK_B + ((T >> 1) & 7) * 256; }
constexpr int v_off(int T) { return (T >> 4) * (int)CHUNK_B + (T & 15) * 1216; }
#define AT_STMT(TXT)                                                                                                              \
  asm volatile(TXT : "+{v[2:17]}"(c.o), "+{v[34:49]}"(c.sa), "+{v[50:65]}"(c.sb), "+{v[66:69]}"(c.kf), "+{v[70:73]}"(c.p1),        \
               "+{v[84:87]}"(c.v1), "+{v[88:91]}"(c.v0), "+{v[92:93]}"(c.ktl)                                                      \
               : "{v[18:33]}"(c.negm), "{v[98:101]}"(c.qm), "{v[102:105]}"(c.qt), [ka] "v"(c.kma[KO >> 16]), [kt] "v"(c.kta[KT >> 16]), \
                 [va] "v"(c.va[V1 >> 16]), [vb] "v"(c.va[V0 >> 16]), [ko] "i"(KO & 0xffff), [kto] "i"(KT & 0xffff), [vo1] "i"(V1 & 0xffff),  \
                 [vo0] "i"(V0 & 0xffff), [mk] "s"(c.mk), [mv] "s"(c.mv)                                                            \
               : "v74", "v75", "v76", "v77")
template <int T> DI void attn_tile(AttnC& c) {
  constexpr int KO = k_off(T < 62 ? T + 2 : 0), KT = kt_off(T < 62 ? T + 2 : 0), V1 = v_off(T) + 608, V0 = v_off(T < 63 ? T + 1 : 0);
  if constexpr (T == 0)
    AT_STMT("v_mfma_f32_32x32x8_bf16 v[50:65], v[92:93], v[104:105], v[18:33]\n\t" "v_exp_f32 v34, v34\n\t" "v_exp_f32 v35, v35\n\t" "v_exp_f32 v36, v36\n\t" "v_exp_f32 v37, v37\n\t" AT_M("mk") "ds_read_b64 v[92:93], %[kt] offset:%[kto]\n\t" AT_U AT_M("mv") "ds_read_b128 v[84:87], %[va] offset:%[vo1]\n\t" AT_U "v_cvt_pk_bf16_f32 v74, v34, v35\n\t" "v_exp_f32 v38, v38\n\t" "v_cvt_pk_bf16_f32 v75, v36, v37\n\t" "v_exp_f32 v39, v39\n\t" "v_exp_f32 v40, v40\n\t" "v_exp_f32 v41, v41\n\t" "s_waitcnt lgkmcnt(2)\n\t" "v_mfma_f32_32x32x16_bf16 v[50:65], v[66:69], v[98:101], v[50:65]\n\t" "ds_read_b128 v[66:69], %[ka] offset:%[ko]\n\t" "v_cvt_pk_bf16_f32 v76, v38, v39\n\t" "v_exp_f32 v42, v42\n\t" "v_cvt_pk_bf16_f32 v77, v40, v41\n\t" "v_exp_f32 v43, v43\n\t" "v_exp_f32 v44, v44\n\t" "v_exp_f32 v45, v45\n\t" "s_waitcnt lgkmcnt(2)\n\t" "v_mfma_f32_32x32x16_bf16 v[2:17], v[88:91], v[74:77], v[2:17]\n\t" AT_M("mv") "ds_read_b128 v[88:91], %[vb] offset:%[vo0]\n\t" AT_U "v_cvt_pk_bf16_f32 v70, v42, v43\n\t" "v_exp_f32 v46, v46\n\t" "v_cvt_pk_bf16_f32 v71, v44, v45\n\t" "v_exp_f32 v47, v47\n\t" "v_exp_f32 v48, v48\n\t" "v_exp_f32 v49, v49\n\t" "v_cvt_pk_bf16_f32 v72, v46, v47\n\t" "v_cvt_pk_bf16_f32 v73, v48, v49\n\t");
  else if constexpr (T == 62)
    AT_STMT("v_mfma_f32_32x32x8_bf16 v[50:65], v[92:93], v[104:105], v[18:33]\n\t" "v_exp_f32 v34, v34\n\t" "v_exp_f32 v35, v35\n\t" "v_exp_f32 v36, v36\n\t" "v_exp_f32 v37, v37\n\t" "s_waitcnt lgkmcnt(2)\n\t" "v_mfma_f32_32x32x16_bf16 v[2:17], v[84:87], v[70:73], v[2:17]\n\t" AT_M("mv") "ds_read_b128 v[84:87], %[va] offset:%[vo1]\n\t" AT_U "v_cvt_pk_bf16_f32 v74, v34, v35\n\t" "v_exp_f32 v38, v38\n\t" "v_cvt_pk_bf16_f32 v75, v36, v37\n\t" "v_exp_f32 v39, v39\n\t" "v_exp_f32 v40, v40\n\t" "v_exp_f32 v41, v41\n\t" "s_waitcnt lgkmcnt(2)\n\t" "v_mfma_f32_32x32x16_bf16 v[50:65], v[66:69], v[98:101], v[50:65]\n\t" "v_cvt_pk_bf16_f32 v76, v38, v39\n\t" "v_exp_f32 v42, v42\n\t" "v_cvt_pk_bf16_f32 v77, v40, v41\n\t" "v_exp_f32 v43, v43\n\t" "v_exp_f32 v44, v44\n\t" "v_exp_f32 v45, v45\n\t" "s_waitcnt lgkmcnt(1)\n\t" "v_mfma_f32_32x32x16_bf16 v[2:17], v[88:91], v[74:77], v[2:17]\n\t" AT_M("mv") "ds_read_b128 v[88:91], %[vb] offset:%[vo0]\n\t" AT_U "v_cvt_pk_bf16_f32 v70, v42, v43\n\t" "v_exp_f32 v46, v46\n\t" "v_cvt_pk_bf16_f32 v71, v44, v45\n\t" "v_exp_f32 v47, v47\n\t" "v_exp_f32 v48, v48\n\t" "v_exp_f32 v49, v49\n\t" "v_cvt_pk_bf16_f32 v72, v46, v47\n\t" "v_cvt_pk_bf16_f32 v73, v48, v49\n\t");
  else if constexpr (T == 63)
    AT_STMT("v_exp_f32 v50, v50\n\t" "v_exp_f32 v51, v51\n\t" "v_exp_f32 v52, v52\n\t" "v_exp_f32 v53, v53\n\t" "s_waitcnt lgkmcnt(0)\n\t" "v_mfma_f32_32x32x16_bf16 v[2:17], v[84:87], v[70:73], v[2:17]\n\t" AT_M("mv") "ds_read_b128 v[84:87], %[va] offset:%[vo1]\n\t" AT_U "v_cvt_pk_bf16_f32 v74, v50, v51\n\t" "v_exp_f32 v54, v54\n\t" "v_cvt_pk_bf16_f32 v75, v52, v53\n\t" "v_exp_f32 v55, v55\n\t" "v_exp_f32 v56, v56\n\t" "v_exp_f32 v57, v57\n\t" "v_cvt_pk_bf16_f32 v76, v54, v55\n\t" "v_exp_f32 v58, v58\n\t" "v_cvt_pk_bf16_f32 v77, v56, v57\n\t" "v_exp_f32 v59, v59\n\t" "v_exp_f32 v60, v60\n\t" "v_exp_f32 v61, v61\n\t" "s_waitcnt lgkmcnt(1)\n\t" "v_mfma_f32_32x32x16_bf16 v[2:17], v[88:91], v[74:77], v[2:17]\n\t" "v_cvt_pk_bf16_f32 v70, v58, v59\n\t" "v_exp_f32 v62, v62\n\t" "v_cvt_pk_bf16_f32 v71, v60, v61\n\t" "v_exp_f32 v63, v63\n\t" "v_exp_f32 v64, v64\n\t" "v_exp_f32 v65, v65\n\t" "v_cvt_pk_bf16_f32 v72, v62, v63\n\t" "v_cvt_pk_bf16_f32 v73, v64, v65\n\t");
  else if constexpr ((T & 3) == 0)
    AT_STMT("v_mfma_f32_32x32x8_bf16 v[50:65], v[92:93], v[104:105], v[18:33]\n\t" "v_exp_f32 v34, v34\n\t" "v_exp_f32 v35, v35\n\t" "v_exp_f32 v36, v36\n\t" "v_exp_f32 v37, v37\n\t" "s_waitcnt lgkmcnt(2)\n\t" "v_mfma_f32_32x32x16_bf16 v[2:17], v[84:87], v[70:73], v[2:17]\n\t" AT_M("mk") "ds_read_b64 v[92:93], %[kt] offset:%[kto]\n\t" AT_U AT_M("mv") "ds_read_b128 v[84:87], %[va] offset:%[vo1]\n\t" AT_U "v_cvt_pk_bf16_f32 v74, v34, v35\n\t" "v_exp_f32 v38, v38\n\t" "v_cvt_pk_bf16_f32 v75, v36, v37\n\t" "v_exp_f32 v39, v39\n\t" "v_exp_f32 v40, v40\n\t" "v_exp_f32 v41, v41\n\t" "s_waitcnt lgkmcnt(2)\n\t" "v_mfma_f32_32x32x16_bf16 v[50:65], v[66:69], v[98:101], v[50:65]\n\t" "ds_read_b128 v[66:69], %[ka] offset:%[ko]\n\t" "v_cvt_pk_bf16_f32 v76, v38, v39\n\t" "v_exp_f32 v42, v42\n\t" "v_cvt_pk_bf16_f32 v77, v40, v41\n\t" "v_exp_f32 v43, v43\n\t" "v_exp_f32 v44, v44\n\t" "v_exp_f32 v45, v45\n\t" "s_waitcnt lgkmcnt(2)\n\t" "v_mfma_f32_32x32x16_bf16 v[2:17], v[88:91], v[74:77], v[2:17]\n\t" AT_M("mv") "ds_read_b128 v[88:91], %[vb] offset:%[vo0]\n\t" AT_U "v_cvt_pk_bf16_f32 v70, v42, v43\n\t" "v_exp_f32 v46, v46\n\t" "v_cvt_pk_bf16_f32 v71, v44, v45\n\t" "v_exp_f32 v47, v47\n\t" "v_exp_f32 v48, v48\n\t" "v_exp_f32 v49, v49\n\t" "v_cvt_pk_bf16_f32 v72, v46, v47\n\t" "v_cvt_pk_bf16_f32 v73, v48, v49\n\t");
  else if constexpr ((T & 3) == 1)
    AT_STMT("v_mfma_f32_32x32x8_bf16 v[34:49], v[92:93], v[102:103], v[18:33]\n\t" "v_exp_f32 v50, v50\n\t" "v_exp_f32 v51, v51\n\t" "v_exp_f32 v52, v52\n\t" "v_exp_f32 v53, v53\n\t" "s_waitcnt lgkmcnt(2)\n\t" "v_mfma_f32_32x32x16_bf16 v[2:17], v[84:87], v[70:73], v[2:17]\n\t" AT_M("mv") "ds_read_b128 v[84:87], %[va] offset:%[vo1]\n\t" AT_U "v_cvt_pk_bf16_f32 v74, v50, v51\n\t" "v_exp_f32 v54, v54\n\t" "v_cvt_pk_bf16_f32 v75, v52, v53\n\t" "v_exp_f32 v55, v55\n\t" "v_exp_f32 v56, v56\n\t" "v_exp_f32 v57, v57\n\t" "s_waitcnt lgkmcnt(2)\n\t" "v_mfma_f32_32x32x16_bf16 v[34:49], v[66:69], v[98:101], v[34:49]\n\t" "ds_read_b128 v[66:69], %[ka] offset:%[ko]\n\t" "v_cvt_pk_bf16_f32 v76, v54, v55\n\t" "v_exp_f32 v58, v58\n\t" "v_cvt_pk_bf16_f32 v77, v56, v57\n\t" "v_exp_f32 v59, v59\n\t" "v_exp_f32 v60, v60\n\t" "v_exp_f32 v61, v61\n\t" "s_waitcnt lgkmcnt(2)\n\t" "v_mfma_f32_32x32x16_bf16 v[2:17], v[88:91], v[74:77], v[2:17]\n\t" AT_M("mv") "ds_read_b128 v[88:91], %[vb] offset:%[vo0]\n\t" AT_U "v_cvt_pk_bf16_f32 v70, v58, v59\n\t" "v_exp_f32 v62, v62\n\t" "v_cvt_pk_bf16_f32 v71, v60, v61\n\t" "v_exp_f32 v63, v63\n\t" "v_exp_f32 v64, v64\n\t" "v_exp_f32 v65, v65\n\t" "v_cvt_pk_bf16_f32 v72, v62, v63\n\t" "v_cvt_pk_bf16_f32 v73, v64, v65\n\t");
  else if constexpr ((T & 3) == 2)
    AT_STMT("v_mfma_f32_32x32x8_bf16 v[50:65], v[92:93], v[104:105], v[18:33]\n\t" "v_exp_f32 v34, v34\n\t" "v_exp_f32 v35, v35\n\t" "v_exp_f32 v36, v36\n\t" "v_exp_f32 v37, v37\n\t" "s_waitcnt lgkmcnt(2)\n\t" "v_mfma_f32_32x32x16_bf16 v[2:17], v[84:87], v[70:73], v[2:17]\n\t" AT_M("mk") "ds_read_b64 v[92:93], %[kt] offset:%[kto]\n\t" AT_U AT_M("mv") "ds_read_b128 v[84:87], %[va] offset:%[vo1]\n\t" AT_U "v_cvt_pk_bf16_f32 v74, v34, v35\n\t" "v_exp_f32 v38, v38\n\t" "v_cvt_pk_bf16_f32 v75, v36, v37\n\t" "v_exp_f32 v39, v39\n\t" "v_exp_f32 v40, v40\n\t" "v_exp_f32 v41, v41\n\t" "s_waitcnt lgkmcnt(2)\n\t" "v_mfma_f32_32x32x16_bf16 v[50:65], v[66:69], v[98:101], v[50:65]\n\t" "ds_read_b128 v[66:69], %[ka] offset:%[ko]\n\t" "v_cvt_pk_bf16_f32 v76, v38, v39\n\t" "v_exp_f32 v42, v42\n\t" "v_cvt_pk_bf16_f32 v77, v40, v41\n\t" "v_exp_f32 v43, v43\n\t" "v_exp_f32 v44, v44\n\t" "v_exp_f32 v45, v45\n\t" "s_waitcnt lgkmcnt(2)\n\t" "v_mfma_f32_32x32x16_bf16 v[2:17], v[88:91], v[74:77], v[2:17]\n\t" AT_M("mv") "ds_read_b128 v[88:91], %[vb] offset:%[vo0]\n\t" AT_U "v_cvt_pk_bf16_f32 v70, v42, v43\n\t" "v_exp_f32 v46, v46\n\t" "v_cvt_pk_bf16_f32 v71, v44, v45\n\t" "v_exp_f32 v47, v47\n\t" "v_exp_f32 v48, v48\n\t" "v_exp_f32 v49, v49\n\t" "v_cvt_pk_bf16_f32 v72, v46, v47\n\t" "v_cvt_pk_bf16_f32 v73, v48, v49\n\t");
  else if constexpr ((T & 3) == 3)
    AT_STMT("v_mfma_f32_32x32x8_bf16 v[34:49], v[92:93], v[102:103], v[18:33]\n\t" "v_exp_f32 v50, v50\n\t" "v_exp_f32 v51, v51\n\t" "v_exp_f32 v52, v52\n\t" "v_exp_f32 v53, v53\n\t" "s_waitcnt lgkmcnt(2)\n\t" "v_mfma_f32_32x32x16_bf16 v[2:17], v[84:87], v[70:73], v[2:17]\n\t" AT_M("mv") "ds_read_b128 v[84:87], %[va] offset:%[vo1]\n\t" AT_U "v_cvt_pk_bf16_f32 v74, v50, v51\n\t" "v_exp_f32 v54, v54\n\t" "v_cvt_pk_bf16_f32 v75, v52, v53\n\t" "v_exp_f32 v55, v55\n\t" "v_exp_f32 v56, v56\n\t" "v_exp_f32 v57, v57\n\t" "s_waitcnt lgkmcnt(2)\n\t" "v_mfma_f32_32x32x16_bf16 v[34:49], v[66:69], v[98:101], v[34:49]\n\t" "ds_read_b128 v[66:69], %[ka] offset:%[ko]\n\t" "v_cvt_pk_bf16_f32 v76, v54, v55\n\t" "v_exp_f32 v58, v58\n\t" "v_cvt_pk_bf16_f32 v77, v56, v57\n\t" "v_exp_f32 v59, v59\n\t" "v_exp_f32 v60, v60\n\t" "v_exp_f32 v61, v61\n\t" "s_waitcnt lgkmcnt(2)\n\t" "v_mfma_f32_32x32x16_bf16 v[2:17], v[88:91], v[74:77], v[2:17]\n\t" AT_M("mv") "ds_read_b128 v[88:91], %[vb] offset:%[vo0]\n\t" AT_U "v_cvt_pk_bf16_f32 v70, v58, v59\n\t" "v_exp_f32 v62, v62\n\t" "v_cvt_pk_bf16_f32 v71, v60, v61\n\t" "v_exp_f32 v63, v63\n\t" "v_exp_f32 v64, v64\n\t" "v_exp_f32 v65, v65\n\t" "v_cvt_pk_bf16_f32 v72, v62, v63\n\t" "v_cvt_pk_bf16_f32 v73, v64, v65\n\t");
}
DI void attn_group_slow(LDSP const char* km, LDSP const char* kt, LDSP const char* vp, const u32x4& qm, unsigned qtl, const f32x16& negm, f32x16& o) {
#pragma unroll
  for (int t = 0; t < 4; ++t) {
    const u32x2 k2 = *(LDSP const u32x2*)(kt + (t >> 1) * 256);
    const u32x4 ktl = {k2[0], k2[1], 0, 0};
    const u32x4 kf = ldsr16(km + t * 1024);
    const u32x4 v0 = ldsr16(vp + t * 1216), v1 = ldsr16(vp + t * 1216 + 608);
    u32x4 qq = {0, 0, 0, 0}; qq[t & 1] = qtl;
    f32x16 s = mfma32(ktl, qq, negm);
    s = mfma32(kf, qm, s);
    u32x4 p0, p1;
#pragma unroll
    for (int i = 0; i < 4; ++i) {
      p0[i] = cvtpk(__builtin_amdgcn_exp2f(s[2 * i]), __builtin_amdgcn_exp2f(s[2 * i + 1]));
      p1[i] = cvtpk(__builtin_amdgcn_exp2f(s[8 + 2 * i]), __builtin_amdgcn_exp2f(s[8 + 2 * i + 1]));
    }
    o = mfma32(v0, p0, o);
    o = mfma32(v1, p1, o);
  }
}
template <int G> DI void attn_group(AttnC& c) {
  __builtin_amdgcn_s_setprio(G < 5 ? 3 - (G & 3) : (G < 8 ? 3 : (G < 11 ? 2 : (G < 14 ? 1 : 0))));
  attn_tile<4 * G + 0>(c); attn_tile<4 * G + 1>(c); attn_tile<4 * G + 2>(c); attn_tile<4 * G + 3>(c);
}
DI void attn_helper(const u16* x, const u16* wo, const u16* wg, const u16* wu, const u16* wd, char* ws, int head, int j, int lane, unsigned lds0) {
  __builtin_amdgcn_s_setprio(HELP_PRIO);
  const char* kv = ws + WS_KV + (size_t)head * KV_HEAD_B;
  const unsigned lane16 = lane * 16;
#define KV_DMA(o) dma16(kv + (o), lane16, lds0 + (o))
#pragma unroll
  for (int i = 0; i < 4; ++i) KV_DMA(((i >> 1) * 16 + (i & 1) * 4 + j) * 1024);
#pragma unroll
  for (int i = 0; i < 6; ++i) { const int p = i < 2 ? 8 + 4 * i + j : min(16 + 4 * i + j, 36); KV_DMA(p * 1024); }
  const int hw = blockIdx.x * 4 + j;
  PrepJob jb[4];
#pragma unroll
  for (int i = 0; i < 4; ++i) { prep_setup(jb[i], 4 * hw + i, lane, wo, wg, wu, wd); PIN(jb[i].voff); }
  SBAR();
  asm volatile("s_waitcnt vmcnt(6)\n\ts_barrier" ::: "memory");
#pragma unroll
  for (int i = 0; i < 10; ++i) KV_DMA((unsigned)CHUNK_B + min(4 * i + j, 36) * 1024);
  asm volatile("s_waitcnt vmcnt(10)\n\ts_barrier" ::: "memory");
#pragma unroll
  for (int i = 0; i < 19; ++i) KV_DMA(2 * (unsigned)CHUNK_B + min(4 * i + j, 73) * 1024);
#undef KV_DMA
  u32x2 r[4][8];
  u32x4 xu[3];
  unsigned wr[5], pf;
  asm volatile("s_waitcnt vmcnt(19)\n\ts_barrier" ::: "memory");
  asm volatile("s_waitcnt vmcnt(0)\n\ts_barrier" ::: "memory");
  prep_load_all(jb[0], r[0]); prep_load_all(jb[1], r[1]);
  const unsigned xl = (lane >> 3) * 1152 + (lane & 7) * 16, xs = (lane & 7) * 256 + (lane >> 3) * 16;
  int xj[3];
#pragma unroll
  for (int i = 0; i < 3; ++i) { xj[i] = i < 2 ? 2 * hw + i : (hw < 256 ? 2048 + hw : 2 * hw); xu[i] = gld16((const char*)x + ((xj[i] / 9) * (8 * 1152) + (xj[i] % 9) * 128), xl); }
  const int br = hw < DM ? hw : DM - 1;
  {
    const char* b = (const char*)wo + (size_t)wo_row(br >> 5, (br >> 3) & 3, br & 7) * (DM * 2);
#pragma unroll
    for (int i = 0; i < 5; ++i) wr[i] = gld4(b, (i < 4 ? lane + 64 * i : 256 + (lane & 31)) * 4);
  }
  prep_load_all(jb[2], r[2]); prep_load_all(jb[3], r[3]);
  CODE_PF2(pf, k_oproj, k_end, 0, j, (blockIdx.x >> 3) == 0 ? 128u : 0u);
  const __amdgpu_buffer_rsrc_t wrs = mkrsrc(ws, (unsigned)WS_END);
  const unsigned pso = ((lane >> 2) & 3) * 256 + ((lane & 3) * 4 + (lane >> 4)) * 16;
  asm volatile("s_waitcnt vmcnt(33)" : PIN8(r[0])::"memory");
  prep_store(jb[0], r[0], wrs, pso);
  asm volatile("s_waitcnt vmcnt(29)" : PIN8(r[1])::"memory");
  prep_store(jb[1], r[1], wrs, pso);
  asm volatile("s_waitcnt vmcnt(25)" : "+v"(xu[0]), "+v"(xu[1]), "+v"(xu[2]), "+v"(wr[0]), "+v"(wr[1]), "+v"(wr[2]), "+v"(wr[3]), "+v"(wr[4])::"memory");
#pragma unroll
  for (int i = 0; i < 3; ++i) bst16<PREP_AUX>(xu[i], wrs, xs, (unsigned)WS_XU + (xj[i] / 18) * 18432 + (xj[i] % 9) * 2048 + ((xj[i] / 9) & 1) * 128);
  {
    float a = 0.f;
#pragma unroll
    for (int i = 0; i < 4; ++i) a = dot2(wr[i], 0x3f803f80u, a);
    a = dot2(lane >= 32 ? 0u : wr[4], 0x3f803f80u, a);
    a = xor16_sum(xor32_sum(a));
    a += __uint_as_float(__builtin_amdgcn_update_dpp(0u, __float_as_uint(a), 0x128, 0xf, 0xf, false));
    a += __uint_as_float(__builtin_amdgcn_update_dpp(0u, __float_as_uint(a), 0x124, 0xf, 0xf, false));
    a += __uint_as_float(__builtin_amdgcn_update_dpp(0u, __float_as_uint(a), 0x122, 0xf, 0xf, false));
    a += __uint_as_float(__builtin_amdgcn_update_dpp(0u, __float_as_uint(a), 0x121, 0xf, 0xf, false));
    a *= (1.0f / DM);
    const unsigned hi = cvtpk(a, 0.f) & 0xffffu;
    const unsigned lo = cvtpk(a - bflo(hi), 0.f) & 0xffffu;
    __builtin_amdgcn_raw_buffer_store_b16((u16)(lane ? lo : hi), wrs, lane < 2 ? (unsigned)WS_WBAR + (lane * DM + br) * 2 : 0x80000000u, 0, 0);
  }
  asm volatile("s_waitcnt vmcnt(21)" : PIN8(r[2])::"memory");
  prep_store(jb[2], r[2], wrs, pso);
  asm volatile("s_waitcnt vmcnt(16)" : PIN8(r[3]), "+v"(pf)::"memory");
  prep_store(jb[3], r[3], wrs, pso);
}
extern "C" __global__ __launch_bounds__(768, 3) void k_attn(const u16* x, const u16* wo, const u16* wg, const u16* wu, const u16* wd, char* ws) {
  extern __shared__ __attribute__((aligned(16))) char smem[];
  asm volatile("" : "+s"(x), "+s"(wo), "+s"(wg), "+s"(wu), "+s"(wd), "+s"(ws));
  LDSP char* lds = (LDSP char*)smem;
  const int tid = threadIdx.x, lane = tid & 63, w = rfl(tid >> 6);
  const int xcd = blockIdx.x & 7, bi = blockIdx.x >> 3;
  const int head = 4 * xcd + (bi & 3), qb = bi >> 2;
  if (w >= 8) { attn_helper(x, wo, wg, wu, wd, ws, head, w - 8, lane, (unsigned)(uintptr_t)lds); return; }
  const int r = lane & 31, h = lane >> 5;
  const int qt32 = 8 * qb + w;
  STAMP_DECL; STAMP(0);
  AttnC c;
  c.qm = gld16(ws + WS_QM, head * 65536 + qt32 * 1024 + lane * 16);
  unsigned qtl = gld4(ws + WS_QT, head * 8192 + (32 * qt32 + r) * 4);
  u32x4 km0 = gld16(ws + WS_KMAX, ((lane >> 1) * 32 + head) * 64 + (lane & 1) * 32);
  u32x4 km1 = gld16(ws + WS_KMAX, ((lane >> 1) * 32 + head) * 64 + (lane & 1) * 32 + 16);
  const int vrow = r < 19 ? r : ((r >= 20 && r < 28) ? 0 : 18);
  c.kmb = lds + lane * 16;
  c.ktb = lds + 16384 + r * 8;
  c.vb = lds + 18432 + h * 304 + vrow * 16;
#pragma unroll
  for (int i = 0; i < 3; ++i) {
    c.kma[i] = (unsigned)(uintptr_t)c.kmb + i * 65536; c.kta[i] = (unsigned)(uintptr_t)c.ktb + i * 65536; c.va[i] = (unsigned)(uintptr_t)c.vb + i * 65536;
    PIN(c.kma[i]); PIN(c.kta[i]); PIN(c.va[i]);
  }
  c.mk = 0x00000000ffffffffull; c.mv = 0x0007ffff0007ffffull;
  c.ktl = u32x2{0, 0}; c.v0 = u32x4{0, 0, 0, 0}; c.v1 = c.v0; PIN(c.ktl); PIN(c.v0); PIN(c.v1);
#pragma unroll
  for (int i = 0; i < 16; ++i) c.o[i] = 0.f;
  PIN(c.o);
  const __amdgpu_buffer_rsrc_t wrs = mkrsrc(ws, (unsigned)WS_END);
  SBAR();
  asm volatile("s_waitcnt vmcnt(0)" : "+v"(c.qm), "+v"(qtl), "+v"(km0), "+v"(km1)::"memory");
  if (h) qtl = 0;
  float q2 = dot2(qtl, qtl, 0.f);
#pragma unroll
  for (int i = 0; i < 4; ++i) q2 = dot2(c.qm[i], c.qm[i], q2);
  unsigned k2 = max(max(max(km0[0], km0[1]), max(km0[2], km0[3])), max(max(km1[0], km1[1]), max(km1[2], km1[3])));
  { auto rr = __builtin_amdgcn_permlane32_swap(k2, k2, false, false); k2 = max(rr[0], rr[1]); }
  { auto rr = __builtin_amdgcn_permlane16_swap(k2, k2, false, false); k2 = max(rr[0], rr[1]); }
  k2 = max(k2, (unsigned)__builtin_amdgcn_update_dpp(0u, k2, 0x128, 0xf, 0xf, true));
  k2 = max(k2, (unsigned)__builtin_amdgcn_update_dpp(0u, k2, 0x124, 0xf, 0xf, true));
  k2 = max(k2, (unsigned)__builtin_amdgcn_update_dpp(0u, k2, 0x122, 0xf, 0xf, true));
  k2 = max(k2, (unsigned)__builtin_amdgcn_update_dpp(0u, k2, 0x121, 0xf, 0xf, true));
  q2 = xor32_sum(q2);
  const float mshift = __builtin_amdgcn_sqrtf(q2 * __uint_as_float(k2)) * 1.001f;
  c.qt = u32x4{qtl, 0, 0, qtl}; PIN(c.qt);
#pragma unroll
  for (int i = 0; i < 16; ++i) c.negm[i] = -mshift;
  PIN(c.negm);
  STAMP(1);
  asm volatile("s_barrier" ::: "memory");
  STAMP(2);
  asm volatile(AT_M("mk") "ds_read_b64 v[92:93], %[kt]\n\t" AT_U "ds_read_b128 v[70:73], %[ka]\n\tds_read_b128 v[66:69], %[ka] offset:1024\n\t"
               AT_M("mv") "ds_read_b128 v[88:91], %[va]\n\t" AT_U "s_waitcnt lgkmcnt(2)\n\t"
               "v_mfma_f32_32x32x8_bf16 v[34:49], v[92:93], v[102:103], v[18:33]\n\t"
               "v_mfma_f32_32x32x16_bf16 v[34:49], v[70:73], v[98:101], v[34:49]\n\ts_nop 11"
               : "={v[34:49]}"(c.sa), "={v[50:65]}"(c.sb), "={v[66:69]}"(c.kf), "={v[70:73]}"(c.p1), "+{v[88:91]}"(c.v0), "+{v[92:93]}"(c.ktl)
               : "{v[18:33]}"(c.negm), "{v[98:101]}"(c.qm), "{v[102:105]}"(c.qt), [ka] "v"(c.kma[0]), [kt] "v"(c.kta[0]), [va] "v"(c.va[0]),
                 [mk] "s"(c.mk), [mv] "s"(c.mv));
  attn_group<0>(c);
  asm volatile("s_barrier" ::: "memory");
  attn_group<1>(c); attn_group<2>(c);
  asm volatile("s_barrier" ::: "memory");
  attn_group<3>(c); attn_group<4>(c);
  asm volatile("s_barrier" ::: "memory");
  STAMP(3);
  attn_group<5>(c); attn_group<6>(c); attn_group<7>(c); attn_group<8>(c); attn_group<9>(c); attn_group<10>(c);
  attn_group<11>(c); attn_group<12>(c); attn_group<13>(c); attn_group<14>(c); attn_group<15>(c);
  asm volatile("s_waitcnt lgkmcnt(0)\n\tv_mfma_f32_32x32x16_bf16 v[2:17], v[84:87], v[70:73], v[2:17]\n\ts_nop 11"
               : "+{v[2:17]}"(c.o) : "{v[84:87]}"(c.v1), "{v[70:73]}"(c.p1), "{v[18:33]}"(c.negm));
  STAMP(4);
  f32x16 o = c.o;
  float l;
  { auto rr = __builtin_amdgcn_permlane32_swap(__float_as_uint(o[10]), __float_as_uint(o[10]), false, false); l = __uint_as_float(rr[0]); }
  if (__builtin_expect(__any(!(l >= 5.421011e-20f)), 0)) {
    float mx = -INFINITY;
    f32x16 z, negm;
#pragma unroll
    for (int i = 0; i < 16; ++i) z[i] = 0.f;
#pragma unroll 1
    for (int tt = 0; tt < 64; ++tt) {
      const int ck = tt >> 4, tl = tt & 15;
      const u32x2 k2 = *(LDSP const u32x2*)(c.ktb + ck * CHUNK_B + (tl >> 1) * 256);
      const u32x4 ktl = {k2[0], k2[1], 0, 0};
      const u32x4 kf = ldsr16(c.kmb + ck * CHUNK_B + tl * 1024);
      const u32x4 qq = {(tl & 1) ? 0u : qtl, (tl & 1) ? qtl : 0u, 0, 0};
      f32x16 sc = mfma32(ktl, qq, z);
      sc = mfma32(kf, c.qm, sc);
#pragma unroll
      for (int i = 0; i < 16; ++i) mx = fmaxf(mx, sc[i]);
    }
    mx = xor32_max(mx);
#pragma unroll
    for (int i = 0; i < 16; ++i) { negm[i] = -mx; o[i] = 0.f; }
#pragma unroll 1
    for (int gq = 0; gq < 16; ++gq) {
      const int ck = gq >> 2, gg = gq & 3;
      attn_group_slow(c.kmb + ck * CHUNK_B + gg * 4096, c.ktb + ck * CHUNK_B + gg * 512, c.vb + ck * CHUNK_B + gg * 4864, c.qm, qtl, negm, o);
    }
    auto rr = __builtin_amdgcn_permlane32_swap(__float_as_uint(o[10]), __float_as_uint(o[10]), false, false);
    l = __uint_as_float(rr[0]);
  }
  const float rl = 1.0f / l;
  u32x4 ou;
#pragma unroll
  for (int i = 0; i < 4; ++i) ou[i] = cvtpk(o[2 * i] * rl, o[2 * i + 1] * rl);
  const int q = 32 * qt32 + r;
  const unsigned ob = (unsigned)WS_OFR + (q >> 4) * 18432 + (q & 15) * 16;
  bst16<O_AUX>(ou, wrs, ob + (head >> 1) * 1024 + (2 * (head & 1) + h) * 256, 0);
  __builtin_amdgcn_raw_buffer_store_b32(cvtpk(o[8] * rl, o[9] * rl), wrs, h ? 0x80000000u : ob + (16 + (head >> 4)) * 1024 + ((head >> 2) & 3) * 256 + (head & 3) * 4, 0, O_AUX);
  STAMP(5); STAMP_OUT(2, 6);
}

constexpr int K3_WBAR = 54 * 1024, K3_O = K3_WBAR + 3072, K3_LDS = K3_O + 96 * 1024;
extern "C" __global__ __launch_bounds__(768, 3) void k_oproj(char* __restrict__ ws) {
  extern __shared__ __attribute__((aligned(16))) char smem[];
  LDSP char* lds = (LDSP char*)smem;
  const unsigned lds0 = (unsigned)(uintptr_t)lds;
  const int tid = threadIdx.x, lane = tid & 63, w = rfl(tid >> 6);
  const int xcd = blockIdx.x & 7, bi = blockIdx.x >> 3;
  const int rb = 2 * xcd + (bi & 1), nb = bi >> 1;
  const unsigned lane16 = lane * 16;
  if (w >= 8) {
    const int j = w - 8;
    const char* wb = ws + WS_WO + (size_t)nb * 54 * 1024;
    const char* ob = ws + WS_OFR + (size_t)(8 * rb + 2 * j) * 18432;
    const unsigned od = lds0 + K3_O + j * 24576;
#define K3_W(z) _Pragma("unroll") for (int i = 0; i < 5; ++i) { const int p = 18 * (z) + min(4 * i + j, 17); dma16(wb + p * 1024, lane16, lds0 + p * 1024); }
#define K3_OS(z) _Pragma("unroll") for (int i = 0; i < 12; ++i) dma16(ob + ((i & 1) * 18 + 6 * (z) + (i >> 1)) * 1024, lane16, od + ((i & 1) * 12 + 6 * (z) - 6 + (i >> 1)) * 1024)
    K3_W(0);
    dma16(ws + WS_WBAR + min(j, 2) * 1024, lane16, lds0 + K3_WBAR + min(j, 2) * 1024);
    K3_W(1); K3_OS(1);
    asm volatile("s_waitcnt vmcnt(17)\n\ts_barrier" ::: "memory");
    K3_W(2); K3_OS(2);
    asm volatile("s_waitcnt vmcnt(17)\n\ts_barrier" ::: "memory");
    asm volatile("s_waitcnt vmcnt(0)\n\ts_barrier" ::: "memory");
#undef K3_W
#undef K3_OS
    return;
  }
  const int m = lane & 15, g = lane >> 4;
  const int mt = 8 * rb + w, row = 16 * mt + m;
  STAMP_DECL; STAMP(0);
  u32x4 O[18];
  u32x2 xr[3];
  {
    const char* ob = ws + WS_OFR + (size_t)mt * 18432;
#pragma unroll
    for (int i = 0; i < 6; ++i) O[i] = gld16(ob + i * 1024, lane16);
  }
  const unsigned uo = mt * 18432 + (6 * nb + (g >> 1)) * 256 + m * 16 + (g & 1) * 8;
  f32x4 acc[4];
  LDSP const char* wl = lds + lane16;
  LDSP const char* ol = lds + K3_O + w * 12288 + lane16; PIN(ol);
  LDSP const char* wbl = lds + K3_WBAR + (m & 1) * 1152 + g * 16;
  const __amdgpu_buffer_rsrc_t wrs = mkrsrc(ws, (unsigned)WS_END);
  unsigned so = ((g & 1) ? (unsigned)WS_HC : (unsigned)WS_HU) + (uo & ~8u); PIN(so);
  STAMP(1);
  asm volatile("s_waitcnt vmcnt(0)\n\ts_barrier" : "+v"(O[0]), "+v"(O[1]), "+v"(O[2]), "+v"(O[3]), "+v"(O[4]), "+v"(O[5])::"memory");
#pragma unroll
  for (int t = 0; t < 3; ++t)
    asm volatile("global_load_dwordx2 %0, %1, %2 offset:%3" : "=v"(xr[t]) : "v"(uo), "s"(ws + WS_XU), "i"(t * 512));
  unsigned xm = gld4(ws + WS_XMEAN, row * 4);
  u32x4 wf[6];
#define K3_RD(f) (((f) & 3) == 3 ? ldsr16(wbl + ((f) >> 2) * 64) : ldsr16(wl + (3 * ((f) >> 2) + ((f) & 3)) * 1024))
#pragma unroll
  for (int f = 0; f < 5; ++f) wf[f] = K3_RD(f);
#pragma unroll
  for (int f = 0; f < 72; ++f) {
    if (f == 19 || f == 43) {
      asm volatile("s_barrier" ::: "memory");
#pragma unroll
      for (int i = 0; i < 6; ++i) O[(f + 5) / 4 + i] = ldsr16(ol + ((f + 5) / 4 + i - 6) * 1024);
    }
    if (f + 5 < 72) wf[(f + 5) % 6] = K3_RD(f + 5);
    if (f < 4) G_MFMA0(acc[f & 3], wf[f % 6], O[f >> 2]);
    else G_MFMA(acc[f & 3], wf[f % 6], O[f >> 2]);
    SBAR();
  }
  asm volatile("s_waitcnt vmcnt(0)" : "+v"(xr[0]), "+v"(xr[1]), "+v"(xr[2]), "+v"(xm));
  asm volatile("s_nop 7" : "+v"(acc[0]), "+v"(acc[1]), "+v"(acc[2]), "+v"(acc[3]));
#undef K3_RD
  STAMP(2);
  const float mean = __uint_as_float(xm) + acc[3][0] + acc[3][1];
  float ssq = 0.f;
#pragma unroll
  for (int t = 0; t < 3; ++t) {
    const unsigned h0 = cvtpk(bflo(xr[t][0]) + acc[t][0], bfhi(xr[t][0]) + acc[t][1]);
    const unsigned h1 = cvtpk(bflo(xr[t][1]) + acc[t][2], bfhi(xr[t][1]) + acc[t][3]);
    const unsigned c0 = cvtpk(bflo(h0) - mean, bfhi(h0) - mean);
    const unsigned c1 = cvtpk(bflo(h1) - mean, bfhi(h1) - mean);
    ssq = dot2(c0, c0, ssq); ssq = dot2(c1, c1, ssq);
    const auto r0 = __builtin_amdgcn_permlane16_swap(h0, c0, false, false);
    const auto r1 = __builtin_amdgcn_permlane16_swap(h1, c1, false, false);
    bst16<H_AUX>(u32x4{r0[0], r1[0], r0[1], r1[1]}, wrs, so + t * 512, 0);
  }
  ssq = xor32_sum(xor16_sum(ssq));
  __builtin_amdgcn_raw_buffer_store_b32(__float_as_uint(ssq), wrs, (unsigned)WS_SSQ + (nb * 2048 + row) * 4, 0, H_AUX);
  STAMP(3); STAMP_OUT(3, 4);
}

constexpr int K4_SLOT = 36864, K4_LDS = 4 * K4_SLOT;
#define K4_PFD 4
constexpr int K4_PF = K4_PFD;
typedef __attribute__((address_space(1))) unsigned gu32;
constexpr int K5_PERM[18] = {0, 1, 2, 5, 6, 9, 10, 11, 14, 15, 3, 4, 7, 8, 12, 13, 16, 17};
template <int Z> DI void k5_piece(const char* wb, const unsigned (&q)[5], const unsigned (&qs)[5], int i, int w, unsigned lane16, unsigned lds0) {
  constexpr int P0 = K5_PERM[2 * Z] * 9216, D = (K5_PERM[2 * Z + 1] - K5_PERM[2 * Z] - 1) * 9216;
  if (i < 4 || (w >> 2) == (Z & 1))
    dma16(wb + (P0 + q[i] + (D ? qs[i] * D : 0)), lane16, lds0 + ((Z + 1) & 3) * 36864 + (i < 4 ? 8 * i + w : 32 + (w & 3)) * 1024);
}
DI void wait_count(gu32* cnt, unsigned first, unsigned want) {
  if (rfl(first) >= want) return;
  for (unsigned spin = 0; spin < (1u << 16); ++spin) {
    if (rfl(__hip_atomic_load(cnt, __ATOMIC_RELAXED, __HIP_MEMORY_SCOPE_AGENT)) >= want) break;
    __builtin_amdgcn_s_sleep(1);
  }
}
#define POLL(dst, p) asm volatile("global_load_dword %0, %1, %2 sc1" : "=v"(dst) : "v"(0u), "s"(p))
struct K4C {
  const char *wb, *xb; unsigned lane16, lds0; int w, rot;
  unsigned pf1, poll;
  const char* wdb; unsigned q[5], qs[5];
  gu32 *cntA, *cntB;
  const char *ab, *ab2; u32x4 A0[2][4];
  LDSP const char* wl[4];
  u32x4 X[2][18], wf[K4_PF + 1];
  f32x4 acc[4][4];
  float c1[2], c2[2];
  __amdgpu_buffer_rsrc_t ars; unsigned avo; int T0;
  float ea[2][2], et[2][2]; unsigned pk[2][4];
};
DI void k4_piece(const K4C& c, int tile, int p) {
  int q = p + 4 * c.rot; if (q >= 36) q -= 36;
  dma16(c.wb + (tile * K4_SLOT + q * 1024), c.lane16, c.lds0 + (tile & 3) * K4_SLOT + p * 1024);
}
template <int ZW> DI void k4_issue_w(K4C& c, int i) {
  if (ZW < 9) {
    if (i == 0) k4_piece(c, c.w >> 2, 4 * ZW + (c.w & 3));
    else if ((c.w >> 2) == (ZW & 1)) k4_piece(c, 2, 4 * ZW + (c.w & 3));
  } else if (ZW == 9) { k4_piece(c, 3, 8 * i + c.w); }
}
template <int Z, int I> DI void k4_issue1(K4C& c) {
  if (I == 0) k4_issue_w<Z + K4_WD>(c, 0);
  else if (I < 5) {
    int z = Z + c.rot; if (z >= 9) z -= 9;
    c.X[(I - 1) >> 1][2 * Z + ((I - 1) & 1)] = gld16(c.xb + (((I - 1) >> 1) * 18432 + (2 * z + ((I - 1) & 1)) * 1024), c.lane16);
  }
  else k4_issue_w<Z + K4_WD>(c, 1);
}
template <int TL, int I> DI void k4_issue2(K4C& c) {
  if (I < 4) k4_piece(c, TL, 8 * I + c.w);
  else if ((c.w >> 2) == (TL & 1)) k4_piece(c, TL, 32 + (c.w & 3));
}
template <int TL, int E, int U> DI void k4_epi(K4C& c) {
  constexpr int e = U >> 2, u = U & 3, mt = e >> 2, i = e & 3, q = e & 1;
  if constexpr (U < 32) {
    if (u == 0) { const float a = c.acc[TL & 3][mt][i] * c.c1[mt]; c.ea[E][q] = a; c.et[E][q] = a * c.acc[TL & 3][2 + mt][i]; PIN(c.ea[E][q]); PIN(c.et[E][q]); }
    if (u == 1) { c.ea[E][q] = __builtin_amdgcn_exp2f(c.ea[E][q]); PIN(c.ea[E][q]); }
    if (u == 2) { c.ea[E][q] = __builtin_amdgcn_rcpf(__builtin_fmaf(c.ea[E][q], c.c2[mt], c.c2[mt])); PIN(c.ea[E][q]); }
    if (u == 3) { c.et[E][q] *= c.ea[E][q]; PIN(c.et[E][q]); if (q) { c.pk[E][e >> 1] = cvtpk(c.et[E][0], c.et[E][1]); PIN(c.pk[E][e >> 1]); } }
  } else if constexpr (U == 32) {
    auto r0 = __builtin_amdgcn_permlane16_swap(c.pk[E][0], c.pk[E][2], false, false);
    auto r1 = __builtin_amdgcn_permlane16_swap(c.pk[E][1], c.pk[E][3], false, false);
    c.pk[E][0] = r0[0]; c.pk[E][1] = r1[0]; c.pk[E][2] = r0[1]; c.pk[E][3] = r1[1];
  } else if constexpr (U == 33) {
    const int T = c.T0 + TL;
    bst16<ACT_AUX>(u32x4{c.pk[E][0], c.pk[E][1], c.pk[E][2], c.pk[E][3]}, c.ars, c.avo, (T >> 1) * 1024 + (T & 1) * 512);
  }
}
constexpr int k4_tile(int F) { return F < 108 ? (F % 6) >> 1 : 3 + (F - 108) / 36; }
constexpr int k4_step(int F) { return F < 108 ? F / 6 : ((F - 108) % 36) >> 1; }
constexpr int k4_addr(int F) { return k4_step(F) * 2048 + (F & 1) * 1024; }
template <int F> DI void k4_frag(K4C& c) {
  constexpr int TL = k4_tile(F), S = k4_step(F), M = F & 1, N = F + K4_PF;
  const u32x4 a = c.wf[F % (K4_PF + 1)];
  if constexpr (F < 108) { if constexpr ((F + 4) % 3 == 0) __builtin_amdgcn_s_setprio(3 - ((F + 4) % 12) / 3); }
  else if constexpr ((F - 108 + 4) % 9 == 0) __builtin_amdgcn_s_setprio(3 - ((F - 108 + 4) % 36) / 9);
  if (S == 0) {
    G_MFMA0(c.acc[TL & 3][2 * M], a, c.X[0][S]);
    G_MFMA0(c.acc[TL & 3][2 * M + 1], a, c.X[1][S]);
  } else {
    constexpr int o = GRAY ? M : 0;
    G_MFMA(c.acc[TL & 3][2 * M + o], a, c.X[o][S]);
    G_MFMA(c.acc[TL & 3][2 * M + 1 - o], a, c.X[1 - o][S]);
  }
  if constexpr (N < 108 && N % 12 == 0) {
    constexpr int Z = N / 12;
    if (Z < 8) asm volatile("s_waitcnt vmcnt(5)\n\ts_barrier" : "+v"(c.X[0][2 * Z]), "+v"(c.X[0][2 * Z + 1]), "+v"(c.X[1][2 * Z]), "+v"(c.X[1][2 * Z + 1])::"memory");
    else asm volatile("s_waitcnt vmcnt(2)\n\ts_barrier" : "+v"(c.X[0][2 * Z]), "+v"(c.X[0][2 * Z + 1]), "+v"(c.X[1][2 * Z]), "+v"(c.X[1][2 * Z + 1])::"memory");
  } else if constexpr (N >= 108 && N < 324 && (N - 108) % 36 == 0) {
    constexpr int NT = k4_tile(N);
    if (NT == 3) asm volatile("s_waitcnt vmcnt(0) lgkmcnt(0)\n\ts_barrier" ::: "memory");
    else if (NT == 8) {
      asm volatile("s_waitcnt vmcnt(0) lgkmcnt(0)\n\ts_barrier" ::: "memory");
      if (c.w == 0 && c.lane16 == 0) __hip_atomic_fetch_add(c.cntA, 1u, __ATOMIC_RELAXED, __HIP_MEMORY_SCOPE_AGENT);
    }
    else asm volatile("s_waitcnt vmcnt(4) lgkmcnt(0)\n\ts_barrier" ::: "memory");
  }
  if constexpr (N < 324) c.wf[N % (K4_PF + 1)] = ldsr16(c.wl[k4_tile(N) & 3] + k4_addr(N));
  if constexpr (F < 108) {
    constexpr int Z = F / 12, I = F % 12;
    if constexpr (Z == 0 && I < 4 && !(I & 1)) k4_issue1<2, I / 2>(c);
    if constexpr (Z < 7 && I < 8 && (I & 1)) k4_issue1<Z + 2, 2 + I / 2>(c);
    if constexpr (Z < 6 && I > 8 && (I & 1)) k4_issue1<Z + 3, (I - 9) / 2>(c);
    if constexpr (K4_WD == 0) {
      if constexpr (Z == 6 && I == 10) k4_issue2<3, 0>(c);
      if constexpr (Z == 7 && I < 8 && (I & 1)) k4_issue2<3, 1 + I / 2>(c);
    } else {
      if constexpr (Z == 6 && I == 10) k4_issue2<3, 2>(c);
      if constexpr (Z == 7 && (I == 1 || I == 3)) k4_issue2<3, 3 + I / 2>(c);
    }
  } else {
    constexpr int P = (F - 108) % 36;
    if constexpr (TL == 3) { if constexpr (P % 3 == 0 && P < 15) k4_issue2<4, P / 3>(c); if constexpr (P % 3 == 0 && P >= 15 && P < 30) k4_issue2<5, (P - 15) / 3>(c); }
    else if constexpr (TL + 2 < 9 && P % 4 == 0 && P < 20) k4_issue2<TL + 2, P / 4>(c);
    if constexpr (TL == 7 && P == 0) CODE_PF2(c.pf1, k_ffn, k_end, 0, 2 + (c.w & 1), ((blockIdx.x >> 3) == 0 && c.w < 2) ? 128u : 0u);
    if constexpr (TL == 7 && P % 3 == 2 && P <= 14) k5_piece<0>(c.wdb, c.q, c.qs, P / 3, c.w, c.lane16, c.lds0);
    if constexpr (TL == 8 && P % 2 == 0 && P <= 8) k5_piece<1>(c.wdb, c.q, c.qs, P / 2, c.w, c.lane16, c.lds0);
    if constexpr (TL == 8 && P == K4_POLL) POLL(c.poll, c.cntA);
    if constexpr (TL == 8 && P == K4_POLL + 12) { asm volatile("s_waitcnt vmcnt(0)" : "+v"(c.poll), "+v"(c.pf1)); wait_count(c.cntA, c.poll, 8u); }
    if constexpr (TL == 8 && P > K4_POLL + 12 && P <= K4_POLL + 20) {
      constexpr int i = P - K4_POLL - 13, z = i >> 2, j = (i & 1) * 2 + ((i >> 1) & 1);
      asm volatile("global_load_dwordx4 %0, %1, %2 sc1" : "=v"(c.A0[z][j]) : "v"(c.lane16), "s"(((j >> 1) ? c.ab2 : c.ab) + K5_PERM[2 * z + (j & 1)] * 1024));
    }
    if constexpr (P >= 2) {
      if constexpr (TL == 3) { k4_epi<0, 0, P - 2>(c); k4_epi<1, 1, P - 2>(c); }
      else if constexpr (TL == 4) { k4_epi<2, 0, P - 2>(c); k4_epi<3, 1, P - 2>(c); }
      else k4_epi<TL - 1, 0, P - 2>(c);
    }
  }
  SBAR();
}
template <int B, int... Fs> DI void k4_run(K4C& c, std::integer_sequence<int, Fs...>) { (k4_frag<B + Fs>(c), ...); }
DI void k4_last(K4C& c) {
  float a[8], t[8];
#pragma unroll
  for (int e = 0; e < 8; ++e) { a[e] = c.acc[0][e >> 2][e & 3] * c.c1[e >> 2]; t[e] = a[e] * c.acc[0][2 + (e >> 2)][e & 3]; }
#pragma unroll
  for (int e = 0; e < 8; ++e) a[e] = __builtin_amdgcn_exp2f(a[e]);
#pragma unroll
  for (int e = 0; e < 8; ++e) a[e] = __builtin_amdgcn_rcpf(__builtin_fmaf(a[e], c.c2[e >> 2], c.c2[e >> 2]));
#pragma unroll
  for (int e = 0; e < 4; ++e) c.pk[0][e] = cvtpk(t[2 * e] * a[2 * e], t[2 * e + 1] * a[2 * e + 1]);
  k4_epi<8, 0, 32>(c); k4_epi<8, 0, 33>(c);
}
constexpr int K5_SLOT = 36864;
struct K5C {
  const char *ab, *ab2;
  LDSP const char* wl[4];
  LDSP char* xw;
  u32x4 A[3][4], wf[5];
  f32x4 acc[2][9];
  const char* hb; unsigned ho; int kq; u32x2 hr[3];
};
template <int Z, int I> DI void k5_issue(K5C& c, const K4C& k) {
  if (I & 1) asm volatile("global_load_dwordx4 %0, %1, %2 sc1" : "=v"(c.A[Z % 3][I >> 1]) : "v"(k.lane16), "s"(((I >> 2) ? c.ab2 : c.ab) + K5_PERM[2 * Z + ((I >> 1) & 1)] * 1024));
  else k5_piece<Z>(k.wdb, k.q, k.qs, I >> 1, k.w, k.lane16, k.lds0);
}
template <int F> DI void k5_frag(K5C& c, K4C& k) {
  constexpr int Z = F / 18, f = F % 18, N = F + 4;
  const u32x4 a = c.wf[F % 5];
  if (F < 9) {
    G_MFMA0(c.acc[0][f % 9], a, c.A[Z % 3][f / 9]);
    G_MFMA0(c.acc[1][f % 9], a, c.A[Z % 3][2 + f / 9]);
  } else {
    G_MFMA(c.acc[1][f % 9], a, c.A[Z % 3][2 + f / 9]);
    G_MFMA(c.acc[0][f % 9], a, c.A[Z % 3][f / 9]);
  }
  if constexpr (N < 162 && N % 18 == 0) {
    u32x4(&A)[4] = c.A[(Z + 1) % 3];
    if (Z == 2) asm volatile("s_waitcnt vmcnt(6) lgkmcnt(0)\n\ts_barrier" : "+v"(A[0]), "+v"(A[1]), "+v"(A[2]), "+v"(A[3]), "+v"(k.poll)::"memory");
    else if (Z < 7) asm volatile("s_waitcnt vmcnt(8) lgkmcnt(0)\n\ts_barrier" : "+v"(A[0]), "+v"(A[1]), "+v"(A[2]), "+v"(A[3])::"memory");
    else asm volatile("s_waitcnt vmcnt(3) lgkmcnt(0)\n\ts_barrier" : "+v"(A[0]), "+v"(A[1]), "+v"(A[2]), "+v"(A[3])::"memory");
    if (Z == 0 && k.w == 0 && k.lane16 == 0) __hip_atomic_fetch_add(k.cntB, 1u, __ATOMIC_RELAXED, __HIP_MEMORY_SCOPE_AGENT);
    if (Z == 2) wait_count(k.cntB, k.poll, 8u);
  }
  if constexpr (Z == 2 && f == 0) POLL(k.poll, k.cntB);
  if constexpr (Z == 7 && f == 1) asm volatile("global_load_dwordx2 %0, %1, %2" : "=v"(c.hr[0]) : "v"(c.ho), "s"(c.hb + c.kq * 1024));
  if constexpr (Z == 7 && f == 3) asm volatile("global_load_dwordx2 %0, %1, %2 offset:512" : "=v"(c.hr[1]) : "v"(c.ho), "s"(c.hb + c.kq * 1024));
  if constexpr (Z == 7 && f == 5) asm volatile("global_load_dwordx2 %0, %1, %2" : "=v"(c.hr[2]) : "v"(c.ho), "s"(c.hb + 4096));
  if constexpr (N < 162) c.wf[N % 5] = ldsr16(c.wl[(N / 18 + 1) & 3] + (N % 18) * 1024);
  if constexpr (Z == 0 && f < 4 && !(f & 1)) k5_issue<2, f / 2>(c, k);
  if constexpr (Z + 2 < 9 && f < 14 && (f & 1)) k5_issue<Z + 2, 2 + f / 2>(c, k);
  if constexpr (Z + 3 < 9 && f > 14 && (f & 1)) k5_issue<Z + 3, (f - 15) / 2>(c, k);
  if constexpr (F >= 157) { PIN(c.acc[1][F - 157]); *(LDSP f32x4*)(c.xw + (F - 157) * 1024) = c.acc[1][F - 157]; }
  SBAR();
}
template <int... Fs> DI void k5_run(K5C& c, K4C& k, std::integer_sequence<int, Fs...>) { (k5_frag<Fs>(c, k), ...); }
template <int B, int... Fs> DI void k5_run2(K5C& c, K4C& k, std::integer_sequence<int, Fs...>) { (k5_frag<B + Fs>(c, k), ...); }
DI f32x4 addbf(f32x4 a, unsigned lo, unsigned hi) { return a + f32x4{bflo(lo), bfhi(lo), bflo(hi), bfhi(hi)}; }

extern "C" __global__ __launch_bounds__(512, 2) void k_ffn(char* out, char* ws) {
  extern __shared__ __attribute__((aligned(16))) char smem[];
  asm volatile("" : "+s"(out), "+s"(ws));
  LDSP char* lds = (LDSP char*)smem;
  const int tid = threadIdx.x, lane = tid & 63, w = rfl(tid >> 6);
  const int xcd = blockIdx.x & 7, bi = blockIdx.x >> 3;
  const int m = lane & 15, g = lane >> 4, kq = xcd >> 1;
  STAMP_DECL; STAMP(0);
  K4C c;
  {
    const int rb = 4 * (xcd & 1) + (bi & 3), fs = 8 * kq + (bi >> 2), mt = 16 * rb + 2 * w;
    c.lds0 = (unsigned)(uintptr_t)lds; c.lane16 = lane * 16; c.w = w;
    c.wb = ws + WS_W1 + (size_t)fs * 9 * K4_SLOT;
    c.xb = ws + WS_HC + (size_t)mt * 18432;
    c.rot = 0;
    if (K4_WD) { k4_issue_w<0>(c, 0); k4_issue_w<0>(c, 1); }
    k4_issue1<0, 0>(c); k4_issue1<0, 1>(c); k4_issue1<0, 2>(c); k4_issue1<0, 3>(c); k4_issue1<0, 4>(c); k4_issue1<0, 5>(c);
    unsigned sq[6];
#pragma unroll
    for (int i = 0; i < 6; ++i) sq[i] = gld4(ws + WS_SSQ, (3 * g + (i >> 1)) * 8192 + (16 * mt + 16 * (i & 1) + m) * 4);
    k4_issue1<1, 0>(c); k4_issue1<1, 1>(c); k4_issue1<1, 2>(c); k4_issue1<1, 3>(c); k4_issue1<1, 4>(c); k4_issue1<1, 5>(c);
    SBAR();
#pragma unroll
    for (int i = 0; i < 4; ++i) { c.wl[i] = lds + c.lane16 + i * K4_SLOT; PIN(c.wl[i]); }
    c.ars = mkrsrc(ws + WS_ACT, 128u * 144 * 1024);
    c.avo = (mt + (g & 1)) * (144 * 1024) + (g >> 1) * 256 + m * 16;
    c.T0 = 9 * fs;
    c.wdb = ws + WS_WD + ((size_t)(bi >> 3) * 144 + 36 * kq) * 9 * 1024;
#pragma unroll
    for (int i = 0; i < 5; ++i) {
      const int p = i < 4 ? 8 * i + w : 32 + (w & 3), st = (p * 57) >> 9, t = p - 9 * st;
      c.q[i] = ((st >> 1) * 144 + p + (t < 8 ? ((t + 2 * kq) & 7) - t : 0)) * 1024;
      c.qs[i] = st & 1;
      asm volatile("" : "+s"(c.q[i]), "+s"(c.qs[i]));
    }
    c.cntA = (gu32*)(ws + WS_CNT + (64 + rb * 4 + kq) * 128);
    STAMP(1);
    asm volatile("s_waitcnt vmcnt(5)\n\ts_barrier" : "+v"(sq[0]), "+v"(sq[1]), "+v"(sq[2]), "+v"(sq[3]), "+v"(sq[4]), "+v"(sq[5]),
                 "+v"(c.X[0][0]), "+v"(c.X[0][1]), "+v"(c.X[1][0]), "+v"(c.X[1][1])::"memory");
#pragma unroll
    for (int i = 0; i < K4_PF; ++i) c.wf[i] = ldsr16(c.wl[k4_tile(i)] + k4_addr(i));
#pragma unroll
    for (int t = 0; t < 2; ++t) {
      float ss = __uint_as_float(sq[t]) + __uint_as_float(sq[2 + t]) + __uint_as_float(sq[4 + t]);
      ss = xor32_sum(xor16_sum(ss));
      const float v = ss * (1.0f / DM) + LN_EPS, rs = __builtin_amdgcn_rsqf(v);
      c.c1[t] = -LOG2E * rs; c.c2[t] = -LOG2E * (v * rs);
      PIN(c.c1[t]); PIN(c.c2[t]);
    }
    SBAR();
    k4_run<0>(c, std::make_integer_sequence<int, 288>{});
    c.cntB = c.cntA + 32 * 32;
  }
  const int rb = 8 * (xcd & 1) + (bi & 7), nb = bi >> 3, mg = w & 3, kh = w >> 2, mu = 8 * rb + 2 * mg + kh;
  gu32* prodB = c.cntB;
  c.cntA = (gu32*)(ws + WS_CNT + (64 + (rb >> 1) * 4 + kq) * 128);
  c.ab = ws + WS_ACT + ((size_t)mu * 144 + 36 * kq + 18 * kh) * 1024;
  c.ab2 = ws + WS_ACT + ((size_t)(mu ^ 1) * 144 + 36 * kq + 18 * kh) * 1024;
  k4_run<288>(c, std::make_integer_sequence<int, 36>{});
  STAMP(2);
  K5C d;
  d.ab = c.ab; d.ab2 = c.ab2;
  asm volatile("s_nop 7" : "+v"(c.acc[0][0]), "+v"(c.acc[0][1]), "+v"(c.acc[0][2]), "+v"(c.acc[0][3]));
  k4_last(c);
  SBAR();
  gu32* consB = c.cntA + 32 * 32;
  d.hb = ws + WS_HU + (size_t)mu * 18432 + nb * 4608; d.ho = (g >> 1) * 256 + m * 16 + (g & 1) * 8; d.kq = kq;
#pragma unroll
  for (int i = 0; i < 4; ++i) { d.wl[i] = lds + kh * 18432 + c.lane16 + i * K5_SLOT; PIN(d.wl[i]); }
  d.xw = lds + 2 * K5_SLOT + w * 9216 + c.lane16; PIN(d.xw);
  LDSP const char* xr = lds + 2 * K5_SLOT + (w ^ 4) * 9216 + c.lane16; PIN(xr);
  const __amdgpu_buffer_rsrc_t wrs = mkrsrc(ws, (unsigned)WS_END), ors = mkrsrc(out, SEQ * DM * 2);
  const int tile = rb * 4 + nb;
  const bool own8 = (mu & 3) == kq;
  const unsigned tw = tile * 8 + w;
  unsigned oo = (16 * mu + m) * 1152 + nb * 288;
  unsigned oo8 = own8 ? oo + 256 + g * 8 : 0x80000000u;
  oo += kq * 64 + (g & 1) * 32 + (g >> 1) * 16;
  unsigned p8 = (unsigned)WS_PART8 + tw * 2048 + lane * 8;
  PIN(oo); PIN(oo8); PIN(p8);
#pragma unroll
  for (int i = 0; i < 4; ++i) { d.A[0][i] = c.A0[0][i]; d.A[1][i] = c.A0[1][i]; }
  asm volatile("s_waitcnt vmcnt(6) lgkmcnt(0)\n\ts_barrier" : "+v"(d.A[0][0]), "+v"(d.A[0][1]), "+v"(d.A[0][2]), "+v"(d.A[0][3])::"memory");
  STAMP(3);
#pragma unroll
  for (int i = 0; i < 4; ++i) d.wf[i] = ldsr16(d.wl[1] + i * 1024);
  SBAR();
  c.cntB = prodB;
  k5_run(d, c, std::make_integer_sequence<int, 18>{});
  c.cntB = consB;
  k5_run2<18>(d, c, std::make_integer_sequence<int, 144>{});
  asm volatile("s_waitcnt vmcnt(0)\n\ts_nop 7" : "+v"(d.acc[0][8]), "+v"(d.acc[1][8]), "+v"(d.acc[0][7]), "+v"(d.acc[1][7]), "+v"(d.acc[1][5]), "+v"(d.acc[1][6]),
               "+v"(d.hr[0]), "+v"(d.hr[1]), "+v"(d.hr[2]));
  STAMP(4);
#pragma unroll
  for (int t = 5; t < 9; ++t) *(LDSP f32x4*)(d.xw + t * 1024) = d.acc[1][t];
  asm volatile("s_waitcnt lgkmcnt(0)\n\ts_barrier" ::: "memory");
  f32x4 v[9];
#pragma unroll
  for (int t = 0; t < 9; ++t) v[(t + 2) % 9] = *(LDSP const f32x4*)(xr + ((t + 2) % 9) * 1024);
  SBAR();
#pragma unroll
  for (int j = 1; j < 4; ++j) {
    const f32x4 a = d.acc[0][2 * j] + v[2 * j], b = d.acc[0][2 * j + 1] + v[2 * j + 1];
    bst16<16>(u32x4{cvtpk(a[0], a[1]), cvtpk(a[2], a[3]), cvtpk(b[0], b[1]), cvtpk(b[2], b[3])}, wrs, c.lane16,
              (unsigned)WS_PART + ((((kq + j) & 3) * 512 + tw) * 3 + j - 1) * 1024);
  }
  f32x4 e8 = d.acc[0][8] + v[8];
  __builtin_amdgcn_raw_buffer_store_b64(u32x2{cvtpk(e8[0], e8[1]), cvtpk(e8[2], e8[3])}, wrs, own8 ? 0x80000000u : p8, kq * 512, 16);
  f32x4 e0 = addbf(d.acc[0][0] + v[0], d.hr[0][0], d.hr[0][1]), e1 = addbf(d.acc[0][1] + v[1], d.hr[1][0], d.hr[1][1]);
  e8 = addbf(e8, d.hr[2][0], d.hr[2][1]);
  PIN(e0); PIN(e1); PIN(e8);
  asm volatile("s_waitcnt vmcnt(0)\n\ts_barrier" ::: "memory");
  STAMP(5);
  if (w == 0) {
    gu32* cnt = (gu32*)(ws + WS_CNT + tile * 128);
    if (lane == 0) __hip_atomic_fetch_add(cnt, 1u, __ATOMIC_RELAXED, __HIP_MEMORY_SCOPE_AGENT);
    wait_count(cnt, 0u, 4u);
  }
  asm volatile("s_barrier" ::: "memory");
  STAMP(6);
  u32x4 r[3]; u32x2 r8[3];
#pragma unroll
  for (int j = 0; j < 3; ++j) {
    r[j] = __builtin_amdgcn_raw_buffer_load_b128(wrs, c.lane16, (unsigned)WS_PART + ((kq * 512 + tw) * 3 + j) * 1024, 16);
    r8[j] = __builtin_amdgcn_raw_buffer_load_b64(wrs, own8 ? p8 : 0x80000000u, ((kq + 1 + j) & 3) * 512, 16);
  }
#pragma unroll
  for (int j = 0; j < 3; ++j) { e0 = addbf(e0, r[j][0], r[j][1]); e1 = addbf(e1, r[j][2], r[j][3]); e8 = addbf(e8, r8[j][0], r8[j][1]); }
  {
    const auto s0 = __builtin_amdgcn_permlane16_swap(cvtpk(e0[0], e0[1]), cvtpk(e1[0], e1[1]), false, false);
    const auto s1 = __builtin_amdgcn_permlane16_swap(cvtpk(e0[2], e0[3]), cvtpk(e1[2], e1[3]), false, false);
    bst16<OUT_AUX>(u32x4{s0[0], s1[0], s0[1], s1[1]}, ors, oo, 0);
    __builtin_amdgcn_raw_buffer_store_b64(u32x2{cvtpk(e8[0], e8[1]), cvtpk(e8[2], e8[3])}, ors, oo8, 0, OUT_AUX);
  }
  STAMP(7); STAMP_OUT(4, 8);
}
extern "C" __global__ void k_end() {}

extern "C" void kernel_launch(void* const* d_in, const int* in_sizes, int n_in, void* d_out, int out_size, void* d_ws, size_t ws_size,
                              hipStream_t stream) {
  const u16* x = (const u16*)d_in[0];
  const u16* wqkv = (const u16*)d_in[1];
  const u16* wo = (const u16*)d_in[2];
  const u16* wg = (const u16*)d_in[4];
  const u16* wu = (const u16*)d_in[5];
  const u16* wd = (const u16*)d_in[6];
  char* ws = (char*)d_ws;
  (void)hipFuncSetAttribute((const void*)k_qkv, hipFuncAttributeMaxDynamicSharedMemorySize, K1_LDS);
  (void)hipFuncSetAttribute((const void*)k_attn, hipFuncAttributeMaxDynamicSharedMemorySize, K2_LDS);
  (void)hipFuncSetAttribute((const void*)k_oproj, hipFuncAttributeMaxDynamicSharedMemorySize, K3_LDS);
  (void)hipFuncSetAttribute((const void*)k_ffn, hipFuncAttributeMaxDynamicSharedMemorySize, K4_LDS);
  k_qkv<<<256, 768, K1_LDS, stream>>>(x, wqkv, ws);
  k_attn<<<256, 768, K2_LDS, stream>>>(x, wo, wg, wu, wd, ws);
  k_oproj<<<192, 768, K3_LDS, stream>>>(ws);
  k_ffn<<<256, 512, K4_LDS, stream>>>((char*)d_out, ws);
}
```

```cpp
#include <hip/hip_runtime.h>
#include <hip/hip_bf16.h>
#include <stdint.h>
#include <utility>
#include <type_traits>

typedef __bf16 bf16x8 __attribute__((ext_vector_type(8)));
typedef __bf16 bf16x2 __attribute__((ext_vector_type(2)));
typedef short s16x4 __attribute__((ext_vector_type(4)));
typedef float f32x16 __attribute__((ext_vector_type(16)));
typedef float f32x4 __attribute__((ext_vector_type(4)));
typedef float f32x2 __attribute__((ext_vector_type(2)));
typedef unsigned u32x4 __attribute__((ext_vector_type(4)));
typedef unsigned u32x2 __attribute__((ext_vector_type(2)));
typedef unsigned short u16;
#define LDSP __attribute__((address_space(3)))
#define DI __device__ __forceinline__
#define SBAR() __builtin_amdgcn_sched_barrier(0)

constexpr int SEQ = 2048, DM = 576, NH = 32, DH = 18, FF = 4608;
constexpr float LOG2E = 1.4426950408889634f;
constexpr float QSCALE = 0.23570226039551584f * LOG2E;
constexpr float LN_EPS = 1e-5f;

constexpr size_t CHUNK_B = 37888;
constexpr size_t KV_HEAD_B = 4 * CHUNK_B;
constexpr size_t WS_QM = 0;
constexpr size_t WS_QT = WS_QM + (size_t)NH * 65536;
constexpr size_t WS_KV = WS_QT + (size_t)NH * 8192;
constexpr size_t WS_KMAX = WS_KV + (size_t)NH * KV_HEAD_B;
constexpr size_t WS_XMEAN = WS_KMAX + 65536;
constexpr size_t WS_OFR = WS_XMEAN + 8192;
constexpr size_t WS_WO = WS_OFR + (size_t)128 * 18 * 1024;
constexpr size_t WS_WBAR = WS_WO + (size_t)12 * 54 * 1024;
constexpr size_t WS_HC = WS_WBAR + 3072;
constexpr size_t WS_SSQ = WS_HC + (size_t)64 * 36 * 1024;
constexpr size_t WS_W1 = WS_SSQ + (size_t)12 * 8192;
constexpr size_t WS_WD = WS_W1 + (size_t)288 * 36 * 1024;
constexpr size_t WS_ACT = WS_WD + (size_t)4 * 144 * 9 * 1024;
constexpr size_t UNIT_B = (size_t)128 * 18432;
constexpr size_t WS_PART = WS_ACT + (size_t)128 * 144 * 1024;
constexpr size_t WS_PART8 = WS_PART + (size_t)4 * 64 * 8 * 3 * 1024;
constexpr size_t WS_CNT = WS_PART8 + (size_t)64 * 8 * 4 * 512;
constexpr size_t WS_XU = WS_CNT + 32768;
constexpr size_t WS_HU = WS_XU + UNIT_B;
constexpr size_t WS_END = WS_HU + UNIT_B;

DI unsigned cvtpk(float lo, float hi) {
  f32x2 v = {lo, hi};
  bf16x2 b = __builtin_convertvector(v, bf16x2);
  return __builtin_bit_cast(unsigned, b);
}
DI float bflo(unsigned u) { return __uint_as_float(u << 16); }
DI float bfhi(unsigned u) { return __uint_as_float(u & 0xffff0000u); }
DI float dot2(unsigned a, unsigned b, float c) {
  return __builtin_amdgcn_fdot2_f32_bf16(__builtin_bit_cast(bf16x2, a), __builtin_bit_cast(bf16x2, b), c, false);
}
DI f32x16 mfma32(u32x4 a, u32x4 b, f32x16 c) {
  return __builtin_amdgcn_mfma_f32_32x32x16_bf16(__builtin_bit_cast(bf16x8, a), __builtin_bit_cast(bf16x8, b), c, 0, 0, 0);
}
DI f32x4 mfma16(u32x4 a, u32x4 b, f32x4 c) {
  return __builtin_amdgcn_mfma_f32_16x16x32_bf16(__builtin_bit_cast(bf16x8, a), __builtin_bit_cast(bf16x8, b), c, 0, 0, 0);
}
DI unsigned rfl(unsigned x) { return __builtin_amdgcn_readfirstlane(x); }
DI const char* uniptr(const char* p) {
  uint64_t v = (uint64_t)p;
  unsigned lo = rfl((unsigned)v), hi = rfl((unsigned)(v >> 32));
  return (const char*)(((uint64_t)hi << 32) | lo);
}
DI void dma16(const char* sbase, unsigned voff, unsigned lds) {
  asm volatile("s_mov_b32 m0, %2\n\ts_nop 0\n\tglobal_load_lds_dwordx4 %0, %1" ::"v"(voff), "s"(sbase), "s"(lds));
}
DI u32x4 gld16(const char* sbase, unsigned voff) {
  u32x4 r;
  asm volatile("global_load_dwordx4 %0, %1, %2" : "=v"(r) : "v"(voff), "s"(sbase));
  return r;
}
DI unsigned gld4(const char* sbase, unsigned voff) {
  unsigned r;
  asm volatile("global_load_dword %0, %1, %2" : "=v"(r) : "v"(voff), "s"(sbase));
  return r;
}
#define WAITV(n) asm volatile("s_waitcnt vmcnt(" #n ")" ::: "memory")
#define BARRIER() asm volatile("s_barrier" ::: "memory")
#define PIN(x) asm volatile("" : "+v"(x))
DI u32x4 ldsr16(LDSP const char* p) { return *(LDSP const u32x4*)p; }
DI float xor32_sum(float v) {
  auto r = __builtin_amdgcn_permlane32_swap(__float_as_uint(v), __float_as_uint(v), false, false);
  return __uint_as_float(r[0]) + __uint_as_float(r[1]);
}
DI float xor32_max(float v) {
  auto r = __builtin_amdgcn_permlane32_swap(__float_as_uint(v), __float_as_uint(v), false, false);
  return fmaxf(__uint_as_float(r[0]), __uint_as_float(r[1]));
}
DI float xor16_sum(float v) {
  auto r = __builtin_amdgcn_permlane16_swap(__float_as_uint(v), __float_as_uint(v), false, false);
  return __uint_as_float(r[0]) + __uint_as_float(r[1]);
}

#define STAMP_DECL
#define STAMP(i)
#define STAMP_OUT(kid, n)

DI int wo_row(int step, int g, int j) {
  if (step < 16) return 18 * (2 * step + (g >> 1)) + 4 * (g & 1) + (j & 3) + 8 * (j >> 2);
  return 18 * (4 * (4 * (step - 16) + g) + (j >> 1)) + 16 + (j & 1);
}

#define K4_SPREAD 1
#define K4_PM 1
#define K4_WD 1
#define K3_PF 0
#define K3_SLEEP 32
#define K1_ROT 0
#define AT_PM 9
#define HELP_PRIO 3
#define HELP_EARLY 0
#define PREP_ORD 1
#define GRAY 1
#define K4_POLL 10
#define K4_PK 0
#define K5_ORD 1
#define K5_SIG 154
#define K4_MID 1
#define K4_ROT 0
#define K5_FLAG 0
#define K5_SPREAD 1
#define K5_PM 0
#define OUT_AUX 16
#define K4_PFWD 1
#define QKV_AUX 16
#define QKT_AUX 16
#define V_AUX 16
#define O_AUX 16
#define H_AUX 16
#define PART_AUX 16
#define ACT_AUX 16
#define PREP_AUX 16
#define CODE_PF(dst, first, last, extra, blk) CODE_PF2(dst, first, last, extra, blk, 128u)
#define CODE_PF2(dst, first, last, extra, blk, stride)                                                                              \
  do {                                                                                                                     \
    uint64_t pc_; int b_, e_;                                                                                              \
    asm volatile("s_getpc_b64 %0\n.Lpf%=:\n\ts_mov_b32 %1, " #first "-.Lpf%=\n\ts_mov_b32 %2, " #last "-.Lpf%=" : "=s"(pc_), "=s"(b_), "=s"(e_)); \
    const unsigned o_ = (blk) * 8192u + (threadIdx.x & 63) * (stride), n_ = (unsigned)(e_ - b_) + (extra) - 4u;                \
    dst = gld4((const char*)(pc_ + b_), o_ < n_ ? o_ : n_);                                                                \
  } while (0)
#define G_MFMA(d, a, b) asm volatile("v_mfma_f32_16x16x32_bf16 %0, %1, %2, %0" : "+v"(d) : "v"(a), "v"(b))
#define G_MFMA0(d, a, b) asm volatile("v_mfma_f32_16x16x32_bf16 %0, %1, %2, 0" : "=&v"(d) : "v"(a), "v"(b))
template <int AUX> DI void bst16(u32x4 v, __amdgpu_buffer_rsrc_t rs, unsigned voff, unsigned soff) {
  __builtin_amdgcn_raw_buffer_store_b128(v, rs, voff, soff, AUX);
  asm volatile("s_nop 1" ::"v"(v) : "memory");
}
DI __amdgpu_buffer_rsrc_t mkrsrc(const void* p, unsigned bytes) {
  return __builtin_amdgcn_make_buffer_rsrc((void*)p, 0, bytes, 0x00020000);
}

#define K1_PF 4
#define K1_TOUCH 1
#define K1_TSLEEP 16
#define K1_DEPTH 2
constexpr int K1_PITCH = 224, K1_SLOT = 16384 + 64 * K1_PITCH  , K1_NS = K1_DEPTH + 1;
constexpr int K1_SP = 232;
constexpr int K1_STG = K1_NS * K1_SLOT, K1_CS = K1_STG + 128 * K1_SP + 64, K1_ST = K1_CS + 512 + 64, K1_LDS = K1_ST + 2048;
struct K1C {
  LDSP const char *xa[2], *wl, *wc0, *wc1;
  u32x4 X[3][2], wf[K1_PF + 1], ones;
  f32x4 acc[4][2];
  f32x4 sm[2]; float sq[2];
};
DI void k1_loader(const char* xb, const char* wb, char* ws, int j, int lane, unsigned lds0) {
  __builtin_amdgcn_s_setprio(3);
  unsigned xo[2], wo[4];
  const int g = lane >> 4;
  xo[0] = (lane >> 3) * 1152 + (((lane & 7) ^ g) * 16);
  xo[1] = (8 + (lane >> 3)) * 1152 + (((lane & 7) ^ (4 + g)) * 16);
  xb += j * (32 * 1152);
  const unsigned xd = lds0 + j * 4096, wd = lds0 + 16384 + j * 1024;
#define K1_X(z, so) do { dma16(xb + (z) * 128, xo[0], xd + (so)); dma16(xb + (z) * 128, xo[1], xd + (so) + 1024);                    \
    dma16(xb + (z) * 128 + 16 * 1152, xo[0], xd + (so) + 2048); dma16(xb + (z) * 128 + 16 * 1152, xo[1], xd + (so) + 3072); } while (0)
#define K1_W(z, so) do { _Pragma("unroll") for (int i = 0; i < 3; ++i) dma16(wb + (z) * (64 * 3456), wo[i], wd + (so) + i * 4096);     \
    if (j < 2) dma16(wb + (z) * (64 * 3456), wo[3], wd + (so) + 3 * 4096); } while (0)
  K1_X(0, 0);
  SBAR();
#pragma unroll
  for (int i = 0; i < 4; ++i) {
    const unsigned u = 64 * (j + 4 * i) + lane, r = u / 14;
    wo[i] = ((r & ~12u) | ((r & 4u) << 1) | ((r & 8u) >> 1)) * 3456 + (u - r * 14) * 16;
  }
  K1_W(0, 0);
#pragma unroll
  for (int z = 1; z < K1_DEPTH; ++z) { K1_X(z, z * K1_SLOT); K1_W(z, z * K1_SLOT); }
  unsigned so = K1_DEPTH * K1_SLOT;
#pragma unroll 1
  for (int z = K1_DEPTH; z < 9; ++z) {
    asm volatile("s_waitcnt vmcnt(%0)\n\ts_barrier" ::"i"(7 * (K1_DEPTH - 1)) : "memory");
    K1_X(z, so); K1_W(z, so);
    so = so == (K1_NS - 1) * K1_SLOT ? 0 : so + K1_SLOT;
  }
  if (K1_DEPTH > 2) asm volatile("s_waitcnt vmcnt(14)\n\ts_barrier" ::: "memory");
  asm volatile("s_waitcnt vmcnt(7)\n\ts_barrier" ::: "memory");
  asm volatile("s_waitcnt vmcnt(0)\n\ts_barrier" ::: "memory");
  if (blockIdx.x < 4 && j == 0) bst16<16>(u32x4{0, 0, 0, 0}, mkrsrc(ws, (unsigned)WS_END), (unsigned)WS_CNT + (blockIdx.x * 64 + lane) * 128, 0);
#undef K1_X
#undef K1_W
}
DI u32x4 tr_frag(LDSP const char* p, int d) {
  s16x4 lo = __builtin_amdgcn_ds_read_tr16_b64_v4i16((LDSP s16x4*)p);
  s16x4 hi = __builtin_amdgcn_ds_read_tr16_b64_v4i16((LDSP s16x4*)(p + d));
  return __builtin_bit_cast(u32x4, __builtin_shufflevector(lo, hi, 0, 1, 2, 3, 4, 5, 6, 7));
}
template <int NHG, int F> DI void k1_read(K1C& c) {
  constexpr int NF = 4 + NHG, S = F / NF, j = F % NF, Z = S >> 1, so = (Z % K1_NS) * K1_SLOT;
  if constexpr (j == 0) {
    c.X[S % 3][0] = ldsr16(c.xa[S & 1] + so);
    c.X[S % 3][1] = ldsr16(c.xa[S & 1] + so + 2048);
  }
  constexpr int wo = so + (S & 1) * 7168;
  if constexpr (NHG == 0 || j < 3) c.wf[F % (K1_PF + 1)] = tr_frag(c.wl + wo + j * 32, 1792);
  else c.wf[F % (K1_PF + 1)] = tr_frag((j == 3 ? c.wc0 : c.wc1) + wo, 1792);
}
template <int NHG, int F> DI void k1_frag(K1C& c) {
  constexpr int NF = 4 + NHG, S = F / NF, j = F % NF, Z = S >> 1, N = F + K1_PF, fz = F % (2 * NF);
  if constexpr (S == 0) {
    if constexpr (NHG == 0 || j < 3) { G_MFMA0(c.acc[j][0], c.wf[F % (K1_PF + 1)], c.X[0][0]); G_MFMA0(c.acc[j][1], c.wf[F % (K1_PF + 1)], c.X[0][1]); }
    else G_MFMA0(c.acc[3][j - 3], c.wf[F % (K1_PF + 1)], c.ones);
  } else {
    if constexpr (NHG == 0 || j < 3) { G_MFMA(c.acc[j][0], c.wf[F % (K1_PF + 1)], c.X[S % 3][0]); G_MFMA(c.acc[j][1], c.wf[F % (K1_PF + 1)], c.X[S % 3][1]); }
    else G_MFMA(c.acc[3][j - 3], c.wf[F % (K1_PF + 1)], c.ones);
  }
  if constexpr ((S & 1) == NHG && j < 4) {
#pragma unroll
    for (int t = 0; t < 2; ++t) { const unsigned v = c.X[S % 3][t][j]; c.sq[t] = dot2(v, v, c.sq[t]); PIN(c.sq[t]); }
    if constexpr (j < 2) { if constexpr (S < 2) G_MFMA0(c.sm[j], c.ones, c.X[S % 3][j]); else G_MFMA(c.sm[j], c.ones, c.X[S % 3][j]); }
  }
  if constexpr (N < 18 * NF) {
    if constexpr (N % (2 * NF) == 0) {
      asm volatile("s_waitcnt lgkmcnt(0)\n\ts_barrier" ::: "memory");
    }
    k1_read<NHG, N>(c);
  }
  SBAR();
}
template <int NHG, int... Fs> DI void k1_run(K1C& c, std::integer_sequence<int, Fs...>) {
  k1_read<NHG, 0>(c); k1_read<NHG, 1>(c); k1_read<NHG, 2>(c);
  if (K1_PF > 3) k1_read<NHG, 3>(c);
  if (K1_PF > 4) k1_read<NHG, 4>(c);
  if (K1_PF > 5) k1_read<NHG, 5>(c);
  SBAR();
  (k1_frag<NHG, Fs>(c), ...);
  asm volatile("s_nop 7" : "+v"(c.acc[0][0]), "+v"(c.acc[0][1]), "+v"(c.acc[1][0]), "+v"(c.acc[1][1]), "+v"(c.acc[2][0]), "+v"(c.acc[2][1]),
               "+v"(c.acc[3][0]), "+v"(c.acc[3][1]), "+v"(c.sm[0]), "+v"(c.sm[1]));
  SBAR();
}
extern "C" __global__ __launch_bounds__(768, 3) void k_qkv(const u16* x, const u16* wqkv, char* ws) {
  extern __shared__ __attribute__((aligned(16))) char smem[];
  asm volatile("" : "+s"(x), "+s"(wqkv), "+s"(ws));
  LDSP char* lds = (LDSP char*)smem;
  const unsigned lds0 = (unsigned)(uintptr_t)lds;
  const int tid = threadIdx.x, lane = tid & 63, w = rfl(tid >> 6);
  const int xcd = blockIdx.x & 7, bi = blockIdx.x >> 3;
  const int rb = 4 * (xcd >> 1) + (bi & 3), cb = 8 * (xcd & 1) + (bi >> 2);
  const int c0 = 108 * cb - 4 * (cb & 1);
  if (w >= 8) { k1_loader((const char*)x + rb * (128 * 1152), (const char*)wqkv + c0 * 2, ws, w - 8, lane, lds0); return; }
  const int m = lane & 15, g = lane >> 4, mg = w & 3, nh = w >> 2;
  STAMP_DECL; STAMP(0);
  unsigned pf = 0;
  if (w == 0 && bi < 9) CODE_PF(pf, k_qkv, k_end, 0, bi);
  K1C c;
  c.ones = u32x4{0x3f803f80u, 0x3f803f80u, 0x3f803f80u, 0x3f803f80u};
  PIN(c.ones);
  c.xa[0] = lds + (32 * mg + m) * 128 + ((g ^ (m >> 1)) * 16);
  c.xa[1] = lds + (32 * mg + m) * 128 + (((4 + g) ^ (m >> 1)) * 16);
  LDSP const char* wbase = lds + 16384 + (16 * (g >> 1) + 4 * (g & 1) + (m >> 2)) * K1_PITCH + (m & 3) * 8;
  c.wl = wbase + nh * 128;
  c.wc0 = wbase + mg * 32;
  c.wc1 = wbase + (mg < 3 ? mg + 4 : 6) * 32;
  c.sq[0] = c.sq[1] = 0.f;
  const int off = 4 * (cb & 1), T0 = 128 * rb;
  LDSP char* stg = lds + K1_STG;
  LDSP char* a_st = lds + K1_ST + (32 * mg + m) * 8;                 PIN(a_st);
  LDSP char* a_cs = lds + K1_CS + (64 * nh + 4 * g) * 4;             PIN(a_cs);
  LDSP char* a_sw = stg + (32 * mg + m) * K1_SP + (64 * nh + 4 * g) * 2;   PIN(a_sw);
  LDSP char* a_tr = stg + (32 * mg + 4 * (g & 1) + (m >> 2)) * K1_SP + (64 * nh + 16 * (g >> 1) + 4 * (m & 3)) * 2;   PIN(a_tr);
  float qs[4];
#pragma unroll
  for (int j = 0; j < 4; ++j) { qs[j] = (c0 + 64 * nh + 16 * j + 4 * g < DM) ? QSCALE : 1.0f; PIN(qs[j]); }
  const __amdgpu_buffer_rsrc_t wrs = mkrsrc(ws, (unsigned)WS_END);
  constexpr unsigned DROP = 0x80000000u;
  const unsigned kvo = (unsigned)WS_KV + (T0 >> 9) * (unsigned)CHUNK_B;
  unsigned vo[2] = {0, 0}, vone = 0, vd[4] = {DROP, DROP, DROP, DROP};
  if (cb >= 10) {
#pragma unroll
  for (int tt = 0; tt < 2; ++tt) {
    const int cc = 64 * nh + 32 * tt + 16 * (g >> 1) + m - off, hd = (cc + 18) / 18 - 1, G = 6 * cb + hd;
    vo[tt] = (cc >= 0 && cc < 108) ? hd * 4864 + 2 * mg * 608 + (g & 1) * 304 + (cc - 18 * hd) * 16 : 29184;
  }
  vone = tid < 96 ? (tid >> 4) * 4864 + (tid & 15) * 304 + 288 : 29184;
#pragma unroll
  for (int i = 0; i < 4; ++i) {
    const unsigned u = i * 512 + tid, hd = u / 304;
    vd[i] = (u < 1824 && 6 * cb + hd >= 64) ? kvo + 18432 + ((T0 >> 4) & 31) * 608 + (6 * cb + hd - 64) * (unsigned)KV_HEAD_B + (u - hd * 304) * 16 : DROP;
  }
  }
  PIN(vone); PIN(vo[0]); PIN(vo[1]); PIN(vd[0]); PIN(vd[1]); PIN(vd[2]); PIN(vd[3]);
  const int tok = tid & 127, T = T0 + tok;
  LDSP const char* a_g = stg + tok * K1_SP + off * 2;   PIN(a_g);
  unsigned um[2], ut[2];
  const unsigned tl = ((T >> 5) & 15) * 1024 + (T & 31) * 16;
#pragma unroll
  for (int it = 0; it < 2; ++it) {
    const int hd = (w >> 1) + 4 * it, G = 6 * cb + hd, head = G & 31;
    if (hd >= 6 || G >= 64) { um[it] = DROP; ut[it] = DROP; }
    else if (G < 32) { um[it] = (unsigned)WS_QM + head * 65536 + (T >> 9) * 16384 + tl; ut[it] = (unsigned)WS_QT + head * 8192 + T * 4; }
    else { um[it] = kvo + head * (unsigned)KV_HEAD_B + tl; ut[it] = kvo + head * (unsigned)KV_HEAD_B + 16384 + ((T >> 6) & 7) * 256 + (T & 31) * 8 + ((T >> 5) & 1) * 4; }
    PIN(um[it]); PIN(ut[it]);
  }
  STAMP(1);
  asm volatile("s_barrier" ::: "memory");
  STAMP(2);
  if (nh) k1_run<1>(c, std::make_integer_sequence<int, 90>{});
  else k1_run<0>(c, std::make_integer_sequence<int, 72>{});
  asm volatile("s_waitcnt vmcnt(0)" : "+v"(pf));
  STAMP(3);
#pragma unroll
  for (int t = 0; t < 2; ++t) {
    const f32x2 st = {c.sm[t][0], xor32_sum(xor16_sum(c.sq[t]))};
    *(LDSP f32x2*)(a_st + nh * 1024 + t * 128) = st;
  }
  if (nh) {
    *(LDSP f32x4*)(lds + K1_CS + (16 * mg + 4 * g) * 4) = c.acc[3][0];
    *(LDSP f32x4*)(lds + K1_CS + (16 * (mg + 4) + 4 * g) * 4) = c.acc[3][1];
  }
  asm volatile("s_waitcnt lgkmcnt(0)\n\ts_barrier" ::: "memory");
  {
    f32x2 p[2][2]; f32x4 cc[4];
#pragma unroll
    for (int t = 0; t < 2; ++t) { p[t][0] = *(LDSP const f32x2*)(a_st + t * 128); p[t][1] = *(LDSP const f32x2*)(a_st + 1024 + t * 128); }
#pragma unroll
    for (int j = 0; j < 4; ++j) cc[j] = *(LDSP const f32x4*)(a_cs + j * 64);
    SBAR();
#pragma unroll
    for (int t = 0; t < 2; ++t) {
      const float mean = (p[t][0][0] + p[t][1][0]) * (1.0f / DM);
      const float var = fmaxf((p[t][0][1] + p[t][1][1]) * (1.0f / DM) - mean * mean, 0.f);
      const float rstd = __builtin_amdgcn_rsqf(var + LN_EPS);
      if (cb == 0 && nh == 0) __builtin_amdgcn_raw_buffer_store_b32(__float_as_uint(mean), wrs, (unsigned)WS_XMEAN + (T0 + 32 * mg + 16 * t + m) * 4, 0, QKV_AUX);
#pragma unroll
      for (int j = 0; j < 4; ++j) {
        if (j < 3 || nh == 0) {
          const f32x4 a = (c.acc[j][t] - mean * cc[j]) * (rstd * qs[j]);
          *(LDSP u32x2*)(a_sw + t * (16 * K1_SP) + j * 32) = u32x2{cvtpk(a[0], a[1]), cvtpk(a[2], a[3])};
        }
      }
    }
  }
  if (cb >= 10) {
    asm volatile("s_waitcnt lgkmcnt(0)" ::: "memory");
    u32x4 v[2][2];
#pragma unroll
    for (int tt = 0; tt < 2; ++tt)
#pragma unroll
      for (int t = 0; t < 2; ++t) v[tt][t] = tr_frag(a_tr + t * (16 * K1_SP) + tt * 64, 8 * K1_SP);
    *(LDSP u32x4*)(lds + vone) = c.ones;
#pragma unroll
    for (int tt = 0; tt < 2; ++tt)
#pragma unroll
      for (int t = 0; t < 2; ++t) *(LDSP u32x4*)(lds + vo[tt] + t * 608) = v[tt][t];
  }
  STAMP(4);
  asm volatile("s_waitcnt lgkmcnt(0)\n\ts_barrier" ::: "memory");
  if (cb >= 10) {
    u32x4 v[4];
#pragma unroll
    for (int i = 0; i < 4; ++i) v[i] = ldsr16(lds + i * 8192 + tid * 16);
#pragma unroll
    for (int i = 0; i < 4; ++i) bst16<V_AUX>(v[i], wrs, vd[i], 0);
  }
  if (cb <= 10) {
    unsigned d[2][9];
#pragma unroll
    for (int it = 0; it < 2; ++it) {
      const int hd = (w >> 1) + 4 * it;
      LDSP const unsigned* sp = (LDSP const unsigned*)(a_g + 36 * (hd < 6 ? hd : 0));
#pragma unroll
      for (int i = 0; i < 9; ++i) d[it][i] = sp[i];
    }
    SBAR();
#pragma unroll
    for (int it = 0; it < 2; ++it) {
      bst16<QKV_AUX>(u32x4{d[it][0], d[it][1], d[it][2], d[it][3]}, wrs, um[it], 0);
      bst16<QKV_AUX>(u32x4{d[it][4], d[it][5], d[it][6], d[it][7]}, wrs, um[it] + 512, 0);
      __builtin_amdgcn_raw_buffer_store_b32(d[it][8], wrs, ut[it], 0, QKT_AUX);
    }
#pragma unroll
    for (int it = 0; it < 2; ++it) {
      const int hd = (w >> 1) + 4 * it, G = 6 * cb + hd;
      if (hd < 6 && G >= 32 && G < 64) {
        float n2 = 0.f;
#pragma unroll
        for (int i = 0; i < 9; ++i) n2 = dot2(d[it][i], d[it][i], n2);
        unsigned nb = __float_as_uint(n2);
        { auto r = __builtin_amdgcn_permlane32_swap(nb, nb, false, false); nb = max(r[0], r[1]); }
        { auto r = __builtin_amdgcn_permlane16_swap(nb, nb, false, false); nb = max(r[0], r[1]); }
        __builtin_amdgcn_raw_buffer_store_b32(nb, wrs, (unsigned)WS_KMAX + (((2 * rb + (w & 1)) * 32 + (G & 31)) * 16 + m) * 4, 0, QKV_AUX);
      }
    }
  }
  STAMP(5); STAMP_OUT(1, 6);
}

constexpr int K2_LDS = (int)KV_HEAD_B;
constexpr int NJ_W1 = 72 * 36, NJ_WD = 9 * 144, NJ_WO = 9 * 18, NJ_ALL = NJ_W1 + NJ_WD + NJ_WO;
struct PrepJob { const char* src; unsigned rs, c1, c2, voff, dst[4]; };
DI void prep_setup(PrepJob& j, int J, int lane, const u16* wo, const u16* wg, const u16* wu, const u16* wd) {
  if (J >= NJ_ALL) J = NJ_ALL - 1;
  const int g = (lane >> 2) & 3;
  unsigned lrow = 8 * g;
  j.c1 = 0; j.c2 = 0;
  if (J < NJ_W1) {
    const int sm = J / 72, tg = J - sm * 72;
    j.src = (const char*)((sm & 1) ? wu : wg) + (size_t)(sm >> 1) * (32 * FF * 2) + tg * 128;
    j.rs = FF * 2;
#pragma unroll
    for (int t = 0; t < 4; ++t) j.dst[t] = (unsigned)WS_W1 + (4 * tg + t) * 36864 + sm * 1024;
  } else if (J < NJ_W1 + NJ_WD) {
    const int q = J - NJ_W1, cc = q / 9, tg = q - cc * 9;
    j.src = (const char*)wd + (size_t)cc * (32 * DM * 2) + tg * 128;
    j.rs = DM * 2;
#pragma unroll
    for (int t = 0; t < 4; ++t) { const int nt = 4 * tg + t, nb = nt / 9; j.dst[t] = (unsigned)WS_WD + ((nb * 144 + cc) * 9 + (nt - nb * 9)) * 1024; }
  } else {
    const int q = J - NJ_W1 - NJ_WD, step = q / 9, tg = q - step * 9;
    j.rs = DM * 2;
    if (step < 16) { j.src = (const char*)wo + (size_t)step * (36 * DM * 2) + tg * 128; lrow = 18 * (g >> 1) + 4 * (g & 1); j.c2 = 4; }
    else { j.src = (const char*)wo + (size_t)(288 * (step - 16) + 16) * (DM * 2) + tg * 128; lrow = 72 * g; j.c1 = 16; }
#pragma unroll
    for (int t = 0; t < 4; ++t) { const int nt = 4 * tg + t, nb = nt / 3; j.dst[t] = (unsigned)WS_WO + ((nb * 18 + step) * 3 + (nt - nb * 3)) * 1024; }
  }
#pragma unroll
  for (int t = 0; t < 4; ++t) j.dst[t] = rfl(j.dst[t]);
  j.voff = lrow * j.rs + (lane >> 4) * 32 + (lane & 3) * 8;
}
template <int JJ>
DI void prep_load(const PrepJob& j, u32x2 (&r)[8]) {
  const char* b = j.src + (JJ + j.c1 * (JJ >> 1) + j.c2 * (JJ >> 2)) * j.rs;
  asm volatile("global_load_dwordx2 %0, %1, %2" : "=v"(r[JJ]) : "v"(j.voff), "s"(b));
}
DI void prep_load_all(const PrepJob& j, u32x2 (&r)[8]) {
  prep_load<0>(j, r); prep_load<1>(j, r); prep_load<2>(j, r); prep_load<3>(j, r); prep_load<4>(j, r); prep_load<5>(j, r); prep_load<6>(j, r); prep_load<7>(j, r);
}
DI void prep_store(const PrepJob& j, const u32x2 (&r)[8], __amdgpu_buffer_rsrc_t rs, unsigned so) {
  u32x4 u[4];
#pragma unroll
  for (int i = 0; i < 4; ++i) {
    u[0][i] = __builtin_amdgcn_perm(r[2 * i + 1][0], r[2 * i][0], 0x05040100u);
    u[1][i] = __builtin_amdgcn_perm(r[2 * i + 1][0], r[2 * i][0], 0x07060302u);
    u[2][i] = __builtin_amdgcn_perm(r[2 * i + 1][1], r[2 * i][1], 0x05040100u);
    u[3][i] = __builtin_amdgcn_perm(r[2 * i + 1][1], r[2 * i][1], 0x07060302u);
  }
#pragma unroll
  for (int i = 0; i < 4; ++i) {
    const auto a0 = __builtin_amdgcn_permlane32_swap(u[0][i], u[2][i], false, false);
    const auto a1 = __builtin_amdgcn_permlane32_swap(u[1][i], u[3][i], false, false);
    const auto b0 = __builtin_amdgcn_permlane16_swap(a0[0], a1[0], false, false);
    const auto b1 = __builtin_amdgcn_permlane16_swap(a0[1], a1[1], false, false);
    u[0][i] = b0[0]; u[1][i] = b0[1]; u[2][i] = b1[0]; u[3][i] = b1[1];
  }
#pragma unroll
  for (int t = 0; t < 4; ++t) bst16<PREP_AUX>(u[t], rs, so, j.dst[t]);
}
#define PIN8(r) "+v"(r[0]), "+v"(r[1]), "+v"(r[2]), "+v"(r[3]), "+v"(r[4]), "+v"(r[5]), "+v"(r[6]), "+v"(r[7])

#define A_MFMA_ACC(d, a, b) asm volatile("v_mfma_f32_32x32x16_bf16 %0, %1, %2, %0" : "+v"(d) : "v"(a), "v"(b))
#define AT_M(m)
#define AT_U
struct AttnC {
  LDSP const char *kmb, *ktb, *vb;
  unsigned kma[3], kta[3], va[3]; unsigned long long mk, mv;
  u32x4 qm, qt, kf, v0, v1, p1; u32x2 ktl;
  f32x16 negm, o, sa, sb;
};
constexpr int k_off(int T) { return (T >> 4) * (int)CHUNK_B + (T & 15) * 1024; }
constexpr int kt_off(int T) { return (T >> 4) * (int)CHUNK_B + ((T >> 1) & 7) * 256; }
constexpr int v_off(int T) { return (T >> 4) * (int)CHUNK_B + (T & 15) * 1216; }
#define AT_STMT(TXT)                                                                                                              \
  asm volatile(TXT : "+{v[2:17]}"(c.o), "+{v[34:49]}"(c.sa), "+{v[50:65]}"(c.sb), "+{v[66:69]}"(c.kf), "+{v[70:73]}"(c.p1),        \
               "+{v[84:87]}"(c.v1), "+{v[88:91]}"(c.v0), "+{v[92:93]}"(c.ktl)                                                      \
               : "{v[18:33]}"(c.negm), "{v[98:101]}"(c.qm), "{v[102:105]}"(c.qt), [ka] "v"(c.kma[KO >> 16]), [kt] "v"(c.kta[KT >> 16]), \
                 [va] "v"(c.va[V1 >> 16]), [vb] "v"(c.va[V0 >> 16]), [ko] "i"(KO & 0xffff), [kto] "i"(KT & 0xffff), [vo1] "i"(V1 & 0xffff),  \
                 [vo0] "i"(V0 & 0xffff), [mk] "s"(c.mk), [mv] "s"(c.mv)                                                            \
               : "v74", "v75", "v76", "v77")
template <int T> DI void attn_tile(AttnC& c) {
  constexpr int KO = k_off(T < 62 ? T + 2 : 0), KT = kt_off(T < 62 ? T + 2 : 0), V1 = v_off(T) + 608, V0 = v_off(T < 63 ? T + 1 : 0);
  if constexpr (T == 0)
    AT_STMT("v_mfma_f32_32x32x8_bf16 v[50:65], v[92:93], v[104:105], v[18:33]\n\t" "v_exp_f32 v34, v34\n\t" "v_exp_f32 v35, v35\n\t" "v_exp_f32 v36, v36\n\t" "v_exp_f32 v37, v37\n\t" AT_M("mk") "ds_read_b64 v[92:93], %[kt] offset:%[kto]\n\t" AT_U AT_M("mv") "ds_read_b128 v[84:87], %[va] offset:%[vo1]\n\t" AT_U "v_cvt_pk_bf16_f32 v74, v34, v35\n\t" "v_exp_f32 v38, v38\n\t" "v_cvt_pk_bf16_f32 v75, v36, v37\n\t" "v_exp_f32 v39, v39\n\t" "v_exp_f32 v40, v40\n\t" "v_exp_f32 v41, v41\n\t" "s_waitcnt lgkmcnt(2)\n\t" "v_mfma_f32_32x32x16_bf16 v[50:65], v[66:69], v[98:101], v[50:65]\n\t" "ds_read_b128 v[66:69], %[ka] offset:%[ko]\n\t" "v_cvt_pk_bf16_f32 v76, v38, v39\n\t" "v_exp_f32 v42, v42\n\t" "v_cvt_pk_bf16_f32 v77, v40, v41\n\t" "v_exp_f32 v43, v43\n\t" "v_exp_f32 v44, v44\n\t" "v_exp_f32 v45, v45\n\t" "s_waitcnt lgkmcnt(2)\n\t" "v_mfma_f32_32x32x16_bf16 v[2:17], v[88:91], v[74:77], v[2:17]\n\t" AT_M("mv") "ds_read_b128 v[88:91], %[vb] offset:%[vo0]\n\t" AT_U "v_cvt_pk_bf16_f32 v70, v42, v43\n\t" "v_exp_f32 v46, v46\n\t" "v_cvt_pk_bf16_f32 v71, v44, v45\n\t" "v_exp_f32 v47, v47\n\t" "v_exp_f32 v48, v48\n\t" "v_exp_f32 v49, v49\n\t" "v_cvt_pk_bf16_f32 v72, v46, v47\n\t" "v_cvt_pk_bf16_f32 v73, v48, v49\n\t");
  else if constexpr (T == 62)
    AT_STMT("v_mfma_f32_32x32x8_bf16 v[50:65], v[92:93], v[104:105], v[18:33]\n\t" "v_exp_f32 v34, v34\n\t" "v_exp_f32 v35, v35\n\t" "v_exp_f32 v36, v36\n\t" "v_exp_f32 v37, v37\n\t" "s_waitcnt lgkmcnt(2)\n\t" "v_mfma_f32_32x32x16_bf16 v[2:17], v[84:87], v[70:73], v[2:17]\n\t" AT_M("mv") "ds_read_b128 v[84:87], %[va] offset:%[vo1]\n\t" AT_U "v_cvt_pk_bf16_f32 v74, v34, v35\n\t" "v_exp_f32 v38, v38\n\t" "v_cvt_pk_bf16_f32 v75, v36, v37\n\t" "v_exp_f32 v39, v39\n\t" "v_exp_f32 v40, v40\n\t" "v_exp_f32 v41, v41\n\t" "s_waitcnt lgkmcnt(2)\n\t" "v_mfma_f32_32x32x16_bf16 v[50:65], v[66:69], v[98:101], v[50:65]\n\t" "v_cvt_pk_bf16_f32 v76, v38, v39\n\t" "v_exp_f32 v42, v42\n\t" "v_cvt_pk_bf16_f32 v77, v40, v41\n\t" "v_exp_f32 v43, v43\n\t" "v_exp_f32 v44, v44\n\t" "v_exp_f32 v45, v45\n\t" "s_waitcnt lgkmcnt(1)\n\t" "v_mfma_f32_32x32x16_bf16 v[2:17], v[88:91], v[74:77], v[2:17]\n\t" AT_M("mv") "ds_read_b128 v[88:91], %[vb] offset:%[vo0]\n\t" AT_U "v_cvt_pk_bf16_f32 v70, v42, v43\n\t" "v_exp_f32 v46, v46\n\t" "v_cvt_pk_bf16_f32 v71, v44, v45\n\t" "v_exp_f32 v47, v47\n\t" "v_exp_f32 v48, v48\n\t" "v_exp_f32 v49, v49\n\t" "v_cvt_pk_bf16_f32 v72, v46, v47\n\t" "v_cvt_pk_bf16_f32 v73, v48, v49\n\t");
  else if constexpr (T == 63)
    AT_STMT("v_exp_f32 v50, v50\n\t" "v_exp_f32 v51, v51\n\t" "v_exp_f32 v52, v52\n\t" "v_exp_f32 v53, v53\n\t" "s_waitcnt lgkmcnt(0)\n\t" "v_mfma_f32_32x32x16_bf16 v[2:17], v[84:87], v[70:73], v[2:17]\n\t" AT_M("mv") "ds_read_b128 v[84:87], %[va] offset:%[vo1]\n\t" AT_U "v_cvt_pk_bf16_f32 v74, v50, v51\n\t" "v_exp_f32 v54, v54\n\t" "v_cvt_pk_bf16_f32 v75, v52, v53\n\t" "v_exp_f32 v55, v55\n\t" "v_exp_f32 v56, v56\n\t" "v_exp_f32 v57, v57\n\t" "v_cvt_pk_bf16_f32 v76, v54, v55\n\t" "v_exp_f32 v58, v58\n\t" "v_cvt_pk_bf16_f32 v77, v56, v57\n\t" "v_exp_f32 v59, v59\n\t" "v_exp_f32 v60, v60\n\t" "v_exp_f32 v61, v61\n\t" "s_waitcnt lgkmcnt(1)\n\t" "v_mfma_f32_32x32x16_bf16 v[2:17], v[88:91], v[74:77], v[2:17]\n\t" "v_cvt_pk_bf16_f32 v70, v58, v59\n\t" "v_exp_f32 v62, v62\n\t" "v_cvt_pk_bf16_f32 v71, v60, v61\n\t" "v_exp_f32 v63, v63\n\t" "v_exp_f32 v64, v64\n\t" "v_exp_f32 v65, v65\n\t" "v_cvt_pk_bf16_f32 v72, v62, v63\n\t" "v_cvt_pk_bf16_f32 v73, v64, v65\n\t");
  else if constexpr ((T & 3) == 0)
    AT_STMT("v_mfma_f32_32x32x8_bf16 v[50:65], v[92:93], v[104:105], v[18:33]\n\t" "v_exp_f32 v34, v34\n\t" "v_exp_f32 v35, v35\n\t" "v_exp_f32 v36, v36\n\t" "v_exp_f32 v37, v37\n\t" "s_waitcnt lgkmcnt(2)\n\t" "v_mfma_f32_32x32x16_bf16 v[2:17], v[84:87], v[70:73], v[2:17]\n\t" AT_M("mk") "ds_read_b64 v[92:93], %[kt] offset:%[kto]\n\t" AT_U AT_M("mv") "ds_read_b128 v[84:87], %[va] offset:%[vo1]\n\t" AT_U "v_cvt_pk_bf16_f32 v74, v34, v35\n\t" "v_exp_f32 v38, v38\n\t" "v_cvt_pk_bf16_f32 v75, v36, v37\n\t" "v_exp_f32 v39, v39\n\t" "v_exp_f32 v40, v40\n\t" "v_exp_f32 v41, v41\n\t" "s_waitcnt lgkmcnt(2)\n\t" "v_mfma_f32_32x32x16_bf16 v[50:65], v[66:69], v[98:101], v[50:65]\n\t" "ds_read_b128 v[66:69], %[ka] offset:%[ko]\n\t" "v_cvt_pk_bf16_f32 v76, v38, v39\n\t" "v_exp_f32 v42, v42\n\t" "v_cvt_pk_bf16_f32 v77, v40, v41\n\t" "v_exp_f32 v43, v43\n\t" "v_exp_f32 v44, v44\n\t" "v_exp_f32 v45, v45\n\t" "s_waitcnt lgkmcnt(2)\n\t" "v_mfma_f32_32x32x16_bf16 v[2:17], v[88:91], v[74:77], v[2:17]\n\t" AT_M("mv") "ds_read_b128 v[88:91], %[vb] offset:%[vo0]\n\t" AT_U "v_cvt_pk_bf16_f32 v70, v42, v43\n\t" "v_exp_f32 v46, v46\n\t" "v_cvt_pk_bf16_f32 v71, v44, v45\n\t" "v_exp_f32 v47, v47\n\t" "v_exp_f32 v48, v48\n\t" "v_exp_f32 v49, v49\n\t" "v_cvt_pk_bf16_f32 v72, v46, v47\n\t" "v_cvt_pk_bf16_f32 v73, v48, v49\n\t");
  else if constexpr ((T & 3) == 1)
    AT_STMT("v_mfma_f32_32x32x8_bf16 v[34:49], v[92:93], v[102:103], v[18:33]\n\t" "v_exp_f32 v50, v50\n\t" "v_exp_f32 v51, v51\n\t" "v_exp_f32 v52, v52\n\t" "v_exp_f32 v53, v53\n\t" "s_waitcnt lgkmcnt(2)\n\t" "v_mfma_f32_32x32x16_bf16 v[2:17], v[84:87], v[70:73], v[2:17]\n\t" AT_M("mv") "ds_read_b128 v[84:87], %[va] offset:%[vo1]\n\t" AT_U "v_cvt_pk_bf16_f32 v74, v50, v51\n\t" "v_exp_f32 v54, v54\n\t" "v_cvt_pk_bf16_f32 v75, v52, v53\n\t" "v_exp_f32 v55, v55\n\t" "v_exp_f32 v56, v56\n\t" "v_exp_f32 v57, v57\n\t" "s_waitcnt lgkmcnt(2)\n\t" "v_mfma_f32_32x32x16_bf16 v[34:49], v[66:69], v[98:101], v[34:49]\n\t" "ds_read_b128 v[66:69], %[ka] offset:%[ko]\n\t" "v_cvt_pk_bf16_f32 v76, v54, v55\n\t" "v_exp_f32 v58, v58\n\t" "v_cvt_pk_bf16_f32 v77, v56, v57\n\t" "v_exp_f32 v59, v59\n\t" "v_exp_f32 v60, v60\n\t" "v_exp_f32 v61, v61\n\t" "s_waitcnt lgkmcnt(2)\n\t" "v_mfma_f32_32x32x16_bf16 v[2:17], v[88:91], v[74:77], v[2:17]\n\t" AT_M("mv") "ds_read_b128 v[88:91], %[vb] offset:%[vo0]\n\t" AT_U "v_cvt_pk_bf16_f32 v70, v58, v59\n\t" "v_exp_f32 v62, v62\n\t" "v_cvt_pk_bf16_f32 v71, v60, v61\n\t" "v_exp_f32 v63, v63\n\t" "v_exp_f32 v64, v64\n\t" "v_exp_f32 v65, v65\n\t" "v_cvt_pk_bf16_f32 v72, v62, v63\n\t" "v_cvt_pk_bf16_f32 v73, v64, v65\n\t");
  else if constexpr ((T & 3) == 2)
    AT_STMT("v_mfma_f32_32x32x8_bf16 v[50:65], v[92:93], v[104:105], v[18:33]\n\t" "v_exp_f32 v34, v34\n\t" "v_exp_f32 v35, v35\n\t" "v_exp_f32 v36, v36\n\t" "v_exp_f32 v37, v37\n\t" "s_waitcnt lgkmcnt(2)\n\t" "v_mfma_f32_32x32x16_bf16 v[2:17], v[84:87], v[70:73], v[2:17]\n\t" AT_M("mk") "ds_read_b64 v[92:93], %[kt] offset:%[kto]\n\t" AT_U AT_M("mv") "ds_read_b128 v[84:87], %[va] offset:%[vo1]\n\t" AT_U "v_cvt_pk_bf16_f32 v74, v34, v35\n\t" "v_exp_f32 v38, v38\n\t" "v_cvt_pk_bf16_f32 v75, v36, v37\n\t" "v_exp_f32 v39, v39\n\t" "v_exp_f32 v40, v40\n\t" "v_exp_f32 v41, v41\n\t" "s_waitcnt lgkmcnt(2)\n\t" "v_mfma_f32_32x32x16_bf16 v[50:65], v[66:69], v[98:101], v[50:65]\n\t" "ds_read_b128 v[66:69], %[ka] offset:%[ko]\n\t" "v_cvt_pk_bf16_f32 v76, v38, v39\n\t" "v_exp_f32 v42, v42\n\t" "v_cvt_pk_bf16_f32 v77, v40, v41\n\t" "v_exp_f32 v43, v43\n\t" "v_exp_f32 v44, v44\n\t" "v_exp_f32 v45, v45\n\t" "s_waitcnt lgkmcnt(2)\n\t" "v_mfma_f32_32x32x16_bf16 v[2:17], v[88:91], v[74:77], v[2:17]\n\t" AT_M("mv") "ds_read_b128 v[88:91], %[vb] offset:%[vo0]\n\t" AT_U "v_cvt_pk_bf16_f32 v70, v42, v43\n\t" "v_exp_f32 v46, v46\n\t" "v_cvt_pk_bf16_f32 v71, v44, v45\n\t" "v_exp_f32 v47, v47\n\t" "v_exp_f32 v48, v48\n\t" "v_exp_f32 v49, v49\n\t" "v_cvt_pk_bf16_f32 v72, v46, v47\n\t" "v_cvt_pk_bf16_f32 v73, v48, v49\n\t");
  else if constexpr ((T & 3) == 3)
    AT_STMT("v_mfma_f32_32x32x8_bf16 v[34:49], v[92:93], v[102:103], v[18:33]\n\t" "v_exp_f32 v50, v50\n\t" "v_exp_f32 v51, v51\n\t" "v_exp_f32 v52, v52\n\t" "v_exp_f32 v53, v53\n\t" "s_waitcnt lgkmcnt(2)\n\t" "v_mfma_f32_32x32x16_bf16 v[2:17], v[84:87], v[70:73], v[2:17]\n\t" AT_M("mv") "ds_read_b128 v[84:87], %[va] offset:%[vo1]\n\t" AT_U "v_cvt_pk_bf16_f32 v74, v50, v51\n\t" "v_exp_f32 v54, v54\n\t" "v_cvt_pk_bf16_f32 v75, v52, v53\n\t" "v_exp_f32 v55, v55\n\t" "v_exp_f32 v56, v56\n\t" "v_exp_f32 v57, v57\n\t" "s_waitcnt lgkmcnt(2)\n\t" "v_mfma_f32_32x32x16_bf16 v[34:49], v[66:69], v[98:101], v[34:49]\n\t" "ds_read_b128 v[66:69], %[ka] offset:%[ko]\n\t" "v_cvt_pk_bf16_f32 v76, v54, v55\n\t" "v_exp_f32 v58, v58\n\t" "v_cvt_pk_bf16_f32 v77, v56, v57\n\t" "v_exp_f32 v59, v59\n\t" "v_exp_f32 v60, v60\n\t" "v_exp_f32 v61, v61\n\t" "s_waitcnt lgkmcnt(2)\n\t" "v_mfma_f32_32x32x16_bf16 v[2:17], v[88:91], v[74:77], v[2:17]\n\t" AT_M("mv") "ds_read_b128 v[88:91], %[vb] offset:%[vo0]\n\t" AT_U "v_cvt_pk_bf16_f32 v70, v58, v59\n\t" "v_exp_f32 v62, v62\n\t" "v_cvt_pk_bf16_f32 v71, v60, v61\n\t" "v_exp_f32 v63, v63\n\t" "v_exp_f32 v64, v64\n\t" "v_exp_f32 v65, v65\n\t" "v_cvt_pk_bf16_f32 v72, v62, v63\n\t" "v_cvt_pk_bf16_f32 v73, v64, v65\n\t");
}
DI void attn_group_slow(LDSP const char* km, LDSP const char* kt, LDSP const char* vp, const u32x4& qm, unsigned qtl, const f32x16& negm, f32x16& o) {
#pragma unroll
  for (int t = 0; t < 4; ++t) {
    const u32x2 k2 = *(LDSP const u32x2*)(kt + (t >> 1) * 256);
    const u32x4 ktl = {k2[0], k2[1], 0, 0};
    const u32x4 kf = ldsr16(km + t * 1024);
    const u32x4 v0 = ldsr16(vp + t * 1216), v1 = ldsr16(vp + t * 1216 + 608);
    u32x4 qq = {0, 0, 0, 0}; qq[t & 1] = qtl;
    f32x16 s = mfma32(ktl, qq, negm);
    s = mfma32(kf, qm, s);
    u32x4 p0, p1;
#pragma unroll
    for (int i = 0; i < 4; ++i) {
      p0[i] = cvtpk(__builtin_amdgcn_exp2f(s[2 * i]), __builtin_amdgcn_exp2f(s[2 * i + 1]));
      p1[i] = cvtpk(__builtin_amdgcn_exp2f(s[8 + 2 * i]), __builtin_amdgcn_exp2f(s[8 + 2 * i + 1]));
    }
    o = mfma32(v0, p0, o);
    o = mfma32(v1, p1, o);
  }
}
template <int G> DI void attn_group(AttnC& c) {
  __builtin_amdgcn_s_setprio(G < 5 ? 3 - (G & 3) : (G < 8 ? 3 : (G < 11 ? 2 : (G < 14 ? 1 : 0))));
  attn_tile<4 * G + 0>(c); attn_tile<4 * G + 1>(c); attn_tile<4 * G + 2>(c); attn_tile<4 * G + 3>(c);
}
DI void attn_helper(const u16* x, const u16* wo, const u16* wg, const u16* wu, const u16* wd, char* ws, int head, int j, int lane, unsigned lds0) {
  __builtin_amdgcn_s_setprio(HELP_PRIO);
  const char* kv = ws + WS_KV + (size_t)head * KV_HEAD_B;
  const unsigned lane16 = lane * 16;
#define KV_DMA(o) dma16(kv + (o), lane16, lds0 + (o))
#pragma unroll
  for (int i = 0; i < 4; ++i) KV_DMA(((i >> 1) * 16 + (i & 1) * 4 + j) * 1024);
#pragma unroll
  for (int i = 0; i < 6; ++i) { const int p = i < 2 ? 8 + 4 * i + j : min(16 + 4 * i + j, 36); KV_DMA(p * 1024); }
  const int hw = blockIdx.x * 4 + j;
  PrepJob jb[4];
#pragma unroll
  for (int i = 0; i < 4; ++i) { prep_setup(jb[i], 4 * hw + i, lane, wo, wg, wu, wd); PIN(jb[i].voff); }
  SBAR();
  asm volatile("s_waitcnt vmcnt(6)\n\ts_barrier" ::: "memory");
#pragma unroll
  for (int i = 0; i < 10; ++i) KV_DMA((unsigned)CHUNK_B + min(4 * i + j, 36) * 1024);
  asm volatile("s_waitcnt vmcnt(10)\n\ts_barrier" ::: "memory");
#pragma unroll
  for (int i = 0; i < 19; ++i) KV_DMA(2 * (unsigned)CHUNK_B + min(4 * i + j, 73) * 1024);
#undef KV_DMA
  u32x2 r[4][8];
  u32x4 xu[3];
  unsigned wr[5], pf;
  asm volatile("s_waitcnt vmcnt(19)\n\ts_barrier" ::: "memory");
  asm volatile("s_waitcnt vmcnt(0)\n\ts_barrier" ::: "memory");
  prep_load_all(jb[0], r[0]); prep_load_all(jb[1], r[1]);
  const unsigned xl = (lane >> 3) * 1152 + (lane & 7) * 16, xs = (lane & 7) * 256 + (lane >> 3) * 16;
  int xj[3];
#pragma unroll
  for (int i = 0; i < 3; ++i) { xj[i] = i < 2 ? 2 * hw + i : (hw < 256 ? 2048 + hw : 2 * hw); xu[i] = gld16((const char*)x + ((xj[i] / 9) * (8 * 1152) + (xj[i] % 9) * 128), xl); }
  const int br = hw < DM ? hw : DM - 1;
  {
    const char* b = (const char*)wo + (size_t)wo_row(br >> 5, (br >> 3) & 3, br & 7) * (DM * 2);
#pragma unroll
    for (int i = 0; i < 5; ++i) wr[i] = gld4(b, (i < 4 ? lane + 64 * i : 256 + (lane & 31)) * 4);
  }
  prep_load_all(jb[2], r[2]); prep_load_all(jb[3], r[3]);
  CODE_PF2(pf, k_oproj, k_end, 0, j, (blockIdx.x >> 3) == 0 ? 128u : 0u);
  const __amdgpu_buffer_rsrc_t wrs = mkrsrc(ws, (unsigned)WS_END);
  const unsigned pso = ((lane >> 2) & 3) * 256 + ((lane & 3) * 4 + (lane >> 4)) * 16;
  asm volatile("s_waitcnt vmcnt(33)" : PIN8(r[0])::"memory");
  prep_store(jb[0], r[0], wrs, pso);
  asm volatile("s_waitcnt vmcnt(29)" : PIN8(r[1])::"memory");
  prep_store(jb[1], r[1], wrs, pso);
  asm volatile("s_waitcnt vmcnt(25)" : "+v"(xu[0]), "+v"(xu[1]), "+v"(xu[2]), "+v"(wr[0]), "+v"(wr[1]), "+v"(wr[2]), "+v"(wr[3]), "+v"(wr[4])::"memory");
#pragma unroll
  for (int i = 0; i < 3; ++i) bst16<PREP_AUX>(xu[i], wrs, xs, (unsigned)WS_XU + (xj[i] / 18) * 18432 + (xj[i] % 9) * 2048 + ((xj[i] / 9) & 1) * 128);
  {
    float a = 0.f;
#pragma unroll
    for (int i = 0; i < 4; ++i) a = dot2(wr[i], 0x3f803f80u, a);
    a = dot2(lane >= 32 ? 0u : wr[4], 0x3f803f80u, a);
    a = xor16_sum(xor32_sum(a));
    a += __uint_as_float(__builtin_amdgcn_update_dpp(0u, __float_as_uint(a), 0x128, 0xf, 0xf, false));
    a += __uint_as_float(__builtin_amdgcn_update_dpp(0u, __float_as_uint(a), 0x124, 0xf, 0xf, false));
    a += __uint_as_float(__builtin_amdgcn_update_dpp(0u, __float_as_uint(a), 0x122, 0xf, 0xf, false));
    a += __uint_as_float(__builtin_amdgcn_update_dpp(0u, __float_as_uint(a), 0x121, 0xf, 0xf, false));
    a *= (1.0f / DM);
    const unsigned hi = cvtpk(a, 0.f) & 0xffffu;
    const unsigned lo = cvtpk(a - bflo(hi), 0.f) & 0xffffu;
    __builtin_amdgcn_raw_buffer_store_b16((u16)(lane ? lo : hi), wrs, lane < 2 ? (unsigned)WS_WBAR + (lane * DM + br) * 2 : 0x80000000u, 0, 0);
  }
  asm volatile("s_waitcnt vmcnt(21)" : PIN8(r[2])::"memory");
  prep_store(jb[2], r[2], wrs, pso);
  asm volatile("s_waitcnt vmcnt(16)" : PIN8(r[3]), "+v"(pf)::"memory");
  prep_store(jb[3], r[3], wrs, pso);
}
extern "C" __global__ __launch_bounds__(768, 3) void k_attn(const u16* x, const u16* wo, const u16* wg, const u16* wu, const u16* wd, char* ws) {
  extern __shared__ __attribute__((aligned(16))) char smem[];
  asm volatile("" : "+s"(x), "+s"(wo), "+s"(wg), "+s"(wu), "+s"(wd), "+s"(ws));
  LDSP char* lds = (LDSP char*)smem;
  const int tid = threadIdx.x, lane = tid & 63, w = rfl(tid >> 6);
  const int xcd = blockIdx.x & 7, bi = blockIdx.x >> 3;
  const int head = 4 * xcd + (bi & 3), qb = bi >> 2;
  if (w >= 8) { attn_helper(x, wo, wg, wu, wd, ws, head, w - 8, lane, (unsigned)(uintptr_t)lds); return; }
  const int r = lane & 31, h = lane >> 5;
  const int qt32 = 8 * qb + w;
  STAMP_DECL; STAMP(0);
  AttnC c;
  c.qm = gld16(ws + WS_QM, head * 65536 + qt32 * 1024 + lane * 16);
  unsigned qtl = gld4(ws + WS_QT, head * 8192 + (32 * qt32 + r) * 4);
  u32x4 km0 = gld16(ws + WS_KMAX, ((lane >> 1) * 32 + head) * 64 + (lane & 1) * 32);
  u32x4 km1 = gld16(ws + WS_KMAX, ((lane >> 1) * 32 + head) * 64 + (lane & 1) * 32 + 16);
  const int vrow = r < 19 ? r : ((r >= 20 && r < 28) ? 0 : 18);
  c.kmb = lds + lane * 16;
  c.ktb = lds + 16384 + r * 8;
  c.vb = lds + 18432 + h * 304 + vrow * 16;
#pragma unroll
  for (int i = 0; i < 3; ++i) {
    c.kma[i] = (unsigned)(uintptr_t)c.kmb + i * 65536; c.kta[i] = (unsigned)(uintptr_t)c.ktb + i * 65536; c.va[i] = (unsigned)(uintptr_t)c.vb + i * 65536;
    PIN(c.kma[i]); PIN(c.kta[i]); PIN(c.va[i]);
  }
  c.mk = 0x00000000ffffffffull; c.mv = 0x0007ffff0007ffffull;
  c.ktl = u32x2{0, 0}; c.v0 = u32x4{0, 0, 0, 0}; c.v1 = c.v0; PIN(c.ktl); PIN(c.v0); PIN(c.v1);
#pragma unroll
  for (int i = 0; i < 16; ++i) c.o[i] = 0.f;
  PIN(c.o);
  const __amdgpu_buffer_rsrc_t wrs = mkrsrc(ws, (unsigned)WS_END);
  SBAR();
  asm volatile("s_waitcnt vmcnt(0)" : "+v"(c.qm), "+v"(qtl), "+v"(km0), "+v"(km1)::"memory");
  if (h) qtl = 0;
  float q2 = dot2(qtl, qtl, 0.f);
#pragma unroll
  for (int i = 0; i < 4; ++i) q2 = dot2(c.qm[i], c.qm[i], q2);
  unsigned k2 = max(max(max(km0[0], km0[1]), max(km0[2], km0[3])), max(max(km1[0], km1[1]), max(km1[2], km1[3])));
  { auto rr = __builtin_amdgcn_permlane32_swap(k2, k2, false, false); k2 = max(rr[0], rr[1]); }
  { auto rr = __builtin_amdgcn_permlane16_swap(k2, k2, false, false); k2 = max(rr[0], rr[1]); }
  k2 = max(k2, (unsigned)__builtin_amdgcn_update_dpp(0u, k2, 0x128, 0xf, 0xf, true));
  k2 = max(k2, (unsigned)__builtin_amdgcn_update_dpp(0u, k2, 0x124, 0xf, 0xf, true));
  k2 = max(k2, (unsigned)__builtin_amdgcn_update_dpp(0u, k2, 0x122, 0xf, 0xf, true));
  k2 = max(k2, (unsigned)__builtin_amdgcn_update_dpp(0u, k2, 0x121, 0xf, 0xf, true));
  q2 = xor32_sum(q2);
  const float mshift = __builtin_amdgcn_sqrtf(q2 * __uint_as_float(k2)) * 1.001f;
  c.qt = u32x4{qtl, 0, 0, qtl}; PIN(c.qt);
#pragma unroll
  for (int i = 0; i < 16; ++i) c.negm[i] = -mshift;
  PIN(c.negm);
  STAMP(1);
  asm volatile("s_barrier" ::: "memory");
  STAMP(2);
  asm volatile(AT_M("mk") "ds_read_b64 v[92:93], %[kt]\n\t" AT_U "ds_read_b128 v[70:73], %[ka]\n\tds_read_b128 v[66:69], %[ka] offset:1024\n\t"
               AT_M("mv") "ds_read_b128 v[88:91], %[va]\n\t" AT_U "s_waitcnt lgkmcnt(2)\n\t"
               "v_mfma_f32_32x32x8_bf16 v[34:49], v[92:93], v[102:103], v[18:33]\n\t"
               "v_mfma_f32_32x32x16_bf16 v[34:49], v[70:73], v[98:101], v[34:49]\n\ts_nop 11"
               : "={v[34:49]}"(c.sa), "={v[50:65]}"(c.sb), "={v[66:69]}"(c.kf), "={v[70:73]}"(c.p1), "+{v[88:91]}"(c.v0), "+{v[92:93]}"(c.ktl)
               : "{v[18:33]}"(c.negm), "{v[98:101]}"(c.qm), "{v[102:105]}"(c.qt), [ka] "v"(c.kma[0]), [kt] "v"(c.kta[0]), [va] "v"(c.va[0]),
                 [mk] "s"(c.mk), [mv] "s"(c.mv));
  attn_group<0>(c);
  asm volatile("s_barrier" ::: "memory");
  attn_group<1>(c); attn_group<2>(c);
  asm volatile("s_barrier" ::: "memory");
  attn_group<3>(c); attn_group<4>(c);
  asm volatile("s_barrier" ::: "memory");
  STAMP(3);
  attn_group<5>(c); attn_group<6>(c); attn_group<7>(c); attn_group<8>(c); attn_group<9>(c); attn_group<10>(c);
  attn_group<11>(c); attn_group<12>(c); attn_group<13>(c); attn_group<14>(c); attn_group<15>(c);
  asm volatile("s_waitcnt lgkmcnt(0)\n\tv_mfma_f32_32x32x16_bf16 v[2:17], v[84:87], v[70:73], v[2:17]\n\ts_nop 11"
               : "+{v[2:17]}"(c.o) : "{v[84:87]}"(c.v1), "{v[70:73]}"(c.p1), "{v[18:33]}"(c.negm));
  STAMP(4);
  f32x16 o = c.o;
  float l;
  { auto rr = __builtin_amdgcn_permlane32_swap(__float_as_uint(o[10]), __float_as_uint(o[10]), false, false); l = __uint_as_float(rr[0]); }
  if (__builtin_expect(__any(!(l >= 5.421011e-20f)), 0)) {
    float mx = -INFINITY;
    f32x16 z, negm;
#pragma unroll
    for (int i = 0; i < 16; ++i) z[i] = 0.f;
#pragma unroll 1
    for (int tt = 0; tt < 64; ++tt) {
      const int ck = tt >> 4, tl = tt & 15;
      const u32x2 k2 = *(LDSP const u32x2*)(c.ktb + ck * CHUNK_B + (tl >> 1) * 256);
      const u32x4 ktl = {k2[0], k2[1], 0, 0};
      const u32x4 kf = ldsr16(c.kmb + ck * CHUNK_B + tl * 1024);
      const u32x4 qq = {(tl & 1) ? 0u : qtl, (tl & 1) ? qtl : 0u, 0, 0};
      f32x16 sc = mfma32(ktl, qq, z);
      sc = mfma32(kf, c.qm, sc);
#pragma unroll
      for (int i = 0; i < 16; ++i) mx = fmaxf(mx, sc[i]);
    }
    mx = xor32_max(mx);
#pragma unroll
    for (int i = 0; i < 16; ++i) { negm[i] = -mx; o[i] = 0.f; }
#pragma unroll 1
    for (int gq = 0; gq < 16; ++gq) {
      const int ck = gq >> 2, gg = gq & 3;
      attn_group_slow(c.kmb + ck * CHUNK_B + gg * 4096, c.ktb + ck * CHUNK_B + gg * 512, c.vb + ck * CHUNK_B + gg * 4864, c.qm, qtl, negm, o);
    }
    auto rr = __builtin_amdgcn_permlane32_swap(__float_as_uint(o[10]), __float_as_uint(o[10]), false, false);
    l = __uint_as_float(rr[0]);
  }
  const float rl = 1.0f / l;
  u32x4 ou;
#pragma unroll
  for (int i = 0; i < 4; ++i) ou[i] = cvtpk(o[2 * i] * rl, o[2 * i + 1] * rl);
  const int q = 32 * qt32 + r;
  const unsigned ob = (unsigned)WS_OFR + (q >> 4) * 18432 + (q & 15) * 16;
  bst16<O_AUX>(ou, wrs, ob + (head >> 1) * 1024 + (2 * (head & 1) + h) * 256, 0);
  __builtin_amdgcn_raw_buffer_store_b32(cvtpk(o[8] * rl, o[9] * rl), wrs, h ? 0x80000000u : ob + (16 + (head >> 4)) * 1024 + ((head >> 2) & 3) * 256 + (head & 3) * 4, 0, O_AUX);
  STAMP(5); STAMP_OUT(2, 6);
}

constexpr int K3_WBAR = 54 * 1024, K3_O = K3_WBAR + 3072, K3_LDS = K3_O + 96 * 1024;
extern "C" __global__ __launch_bounds__(768, 3) void k_oproj(char* __restrict__ ws) {
  extern __shared__ __attribute__((aligned(16))) char smem[];
  LDSP char* lds = (LDSP char*)smem;
  const unsigned lds0 = (unsigned)(uintptr_t)lds;
  const int tid = threadIdx.x, lane = tid & 63, w = rfl(tid >> 6);
  const int xcd = blockIdx.x & 7, bi = blockIdx.x >> 3;
  const int rb = 2 * xcd + (bi & 1), nb = bi >> 1;
  const unsigned lane16 = lane * 16;
  if (w >= 8) {
    const int j = w - 8;
    const char* wb = ws + WS_WO + (size_t)nb * 54 * 1024;
    const char* ob = ws + WS_OFR + (size_t)(8 * rb + 2 * j) * 18432;
    const unsigned od = lds0 + K3_O + j * 24576;
#define K3_W(z) _Pragma("unroll") for (int i = 0; i < 5; ++i) { const int p = 18 * (z) + min(4 * i + j, 17); dma16(wb + p * 1024, lane16, lds0 + p * 1024); }
#define K3_OS(z) _Pragma("unroll") for (int i = 0; i < 12; ++i) dma16(ob + ((i & 1) * 18 + 6 * (z) + (i >> 1)) * 1024, lane16, od + ((i & 1) * 12 + 6 * (z) - 6 + (i >> 1)) * 1024)
    K3_W(0);
    dma16(ws + WS_WBAR + min(j, 2) * 1024, lane16, lds0 + K3_WBAR + min(j, 2) * 1024);
    K3_W(1); K3_OS(1);
    asm volatile("s_waitcnt vmcnt(17)\n\ts_barrier" ::: "memory");
    K3_W(2); K3_OS(2);
    asm volatile("s_waitcnt vmcnt(17)\n\ts_barrier" ::: "memory");
    asm volatile("s_waitcnt vmcnt(0)\n\ts_barrier" ::: "memory");
#undef K3_W
#undef K3_OS
    return;
  }
  const int m = lane & 15, g = lane >> 4;
  const int mt = 8 * rb + w, row = 16 * mt + m;
  STAMP_DECL; STAMP(0);
  u32x4 O[18];
  u32x2 xr[3];
  {
    const char* ob = ws + WS_OFR + (size_t)mt * 18432;
#pragma unroll
    for (int i = 0; i < 6; ++i) O[i] = gld16(ob + i * 1024, lane16);
  }
  const unsigned uo = mt * 18432 + (6 * nb + (g >> 1)) * 256 + m * 16 + (g & 1) * 8;
  f32x4 acc[4];
  LDSP const char* wl = lds + lane16;
  LDSP const char* ol = lds + K3_O + w * 12288 + lane16; PIN(ol);
  LDSP const char* wbl = lds + K3_WBAR + (m & 1) * 1152 + g * 16;
  const __amdgpu_buffer_rsrc_t wrs = mkrsrc(ws, (unsigned)WS_END);
  unsigned so = ((g & 1) ? (unsigned)WS_HC : (unsigned)WS_HU) + (uo & ~8u); PIN(so);
  STAMP(1);
  asm volatile("s_waitcnt vmcnt(0)\n\ts_barrier" : "+v"(O[0]), "+v"(O[1]), "+v"(O[2]), "+v"(O[3]), "+v"(O[4]), "+v"(O[5])::"memory");
#pragma unroll
  for (int t = 0; t < 3; ++t)
    asm volatile("global_load_dwordx2 %0, %1, %2 offset:%3" : "=v"(xr[t]) : "v"(uo), "s"(ws + WS_XU), "i"(t * 512));
  unsigned xm = gld4(ws + WS_XMEAN, row * 4);
  u32x4 wf[6];
#define K3_RD(f) (((f) & 3) == 3 ? ldsr16(wbl + ((f) >> 2) * 64) : ldsr16(wl + (3 * ((f) >> 2) + ((f) & 3)) * 1024))
#pragma unroll
  for (int f = 0; f < 5; ++f) wf[f] = K3_RD(f);
#pragma unroll
  for (int f = 0; f < 72; ++f) {
    if (f == 19 || f == 43) {
      asm volatile("s_barrier" ::: "memory");
#pragma unroll
      for (int i = 0; i < 6; ++i) O[(f + 5) / 4 + i] = ldsr16(ol + ((f + 5) / 4 + i - 6) * 1024);
    }
    if (f + 5 < 72) wf[(f + 5) % 6] = K3_RD(f + 5);
    if (f < 4) G_MFMA0(acc[f & 3], wf[f % 6], O[f >> 2]);
    else G_MFMA(acc[f & 3], wf[f % 6], O[f >> 2]);
    SBAR();
  }
  asm volatile("s_waitcnt vmcnt(0)" : "+v"(xr[0]), "+v"(xr[1]), "+v"(xr[2]), "+v"(xm));
  asm volatile("s_nop 7" : "+v"(acc[0]), "+v"(acc[1]), "+v"(acc[2]), "+v"(acc[3]));
#undef K3_RD
  STAMP(2);
  const float mean = __uint_as_float(xm) + acc[3][0] + acc[3][1];
  float ssq = 0.f;
#pragma unroll
  for (int t = 0; t < 3; ++t) {
    const unsigned h0 = cvtpk(bflo(xr[t][0]) + acc[t][0], bfhi(xr[t][0]) + acc[t][1]);
    const unsigned h1 = cvtpk(bflo(xr[t][1]) + acc[t][2], bfhi(xr[t][1]) + acc[t][3]);
    const unsigned c0 = cvtpk(bflo(h0) - mean, bfhi(h0) - mean);
    const unsigned c1 = cvtpk(bflo(h1) - mean, bfhi(h1) - mean);
    ssq = dot2(c0, c0, ssq); ssq = dot2(c1, c1, ssq);
    const auto r0 = __builtin_amdgcn_permlane16_swap(h0, c0, false, false);
    const auto r1 = __builtin_amdgcn_permlane16_swap(h1, c1, false, false);
    bst16<H_AUX>(u32x4{r0[0], r1[0], r0[1], r1[1]}, wrs, so + t * 512, 0);
  }
  ssq = xor32_sum(xor16_sum(ssq));
  __builtin_amdgcn_raw_buffer_store_b32(__float_as_uint(ssq), wrs, (unsigned)WS_SSQ + (nb * 2048 + row) * 4, 0, H_AUX);
  STAMP(3); STAMP_OUT(3, 4);
}

constexpr int K4_SLOT = 36864, K4_LDS = 163840;
#define K4_PFD 4
constexpr int K4_PF = K4_PFD;
typedef __attribute__((address_space(1))) unsigned gu32;
constexpr int K5_PERM[18] = {0, 1, 2, 6, 7, 8, 9, 10, 11, 15, 16, 17, 3, 4, 5, 12, 13, 14};
constexpr int K5_R[4] = {36864, 73728, 110592, 0};
constexpr int K5_OC[9] = {65536, 102400, 139264, 147456, 155648, 0, 8192, 16384, 24576};
struct K5P { const char* wb; unsigned q[4], qs[4], qo, qos, lds0; };
template <int Z> DI void k5_piece(const K5P& k, int i, int w, unsigned lane16) {
  constexpr int P0 = K5_PERM[2 * Z] * 9216, D = (K5_PERM[2 * Z + 1] - K5_PERM[2 * Z]) * 9216;
  if (i < 3 || (w >> 2) == (Z & 1))
    dma16(k.wb + (P0 + k.q[i] + k.qs[i] * D), lane16, k.lds0 + K5_R[Z & 3] + (i < 3 ? 8 * i + w : 24 + (w & 3)) * 1024);
}
template <int Z> DI void k5_own(const K5P& k, int w, unsigned lane16) {
  constexpr int P0 = K5_PERM[2 * Z] * 9216, D = (K5_PERM[2 * Z + 1] - K5_PERM[2 * Z]) * 9216;
  dma16(k.wb + (P0 + k.qo + k.qos * D), lane16, k.lds0 + K5_OC[Z] + w * 1024);
}
DI void wait_count(gu32* cnt, unsigned first, unsigned want) {
  if (rfl(first) >= want) return;
  for (unsigned spin = 0; spin < (1u << 16); ++spin) {
    if (rfl(__hip_atomic_load(cnt, __ATOMIC_RELAXED, __HIP_MEMORY_SCOPE_AGENT)) >= want) break;
    __builtin_amdgcn_s_sleep(1);
  }
}
#define POLL(dst, p) asm volatile("global_load_dword %0, %1, %2 sc1" : "=v"(dst) : "v"(0u), "s"(p))
struct K4C {
  const char *wb, *xb; unsigned lane16, lds0; int w, dir;
  unsigned pf0, pfo, pf1, poll; const char* pfb;
  K5P wd;
  gu32 *cntA, *cntB;
  const char *ab, *ab2; u32x4 A0[2][4];
  LDSP const char* wl[4];
  u32x4 X[2][18], wf[K4_PF + 1];
  f32x4 acc[4][4];
  float c1[2], c2[2];
  __amdgpu_buffer_rsrc_t ars; unsigned avo; int T0;
  float ea[2][2], et[2][2]; unsigned pk[2][4];
  f32x2 pa[2], pt[2], c1p[2], c2p[2];
};
DI void k4_piece(const K4C& c, int tile, int p) {
  dma16(c.wb + (tile * c.dir * K4_SLOT + p * 1024), c.lane16, c.lds0 + (tile & 3) * K4_SLOT + p * 1024);
}
template <int ZW> DI void k4_issue_w(K4C& c, int i) {
  if (ZW < 9) {
    if (i == 0) k4_piece(c, c.w >> 2, 4 * ZW + (c.w & 3));
    else if ((c.w >> 2) == (ZW & 1)) k4_piece(c, 2, 4 * ZW + (c.w & 3));
  } else if (ZW == 9) { k4_piece(c, 3, 8 * i + c.w); }
}
template <int Z, int I> DI void k4_issue1(K4C& c) {
  if (I == 0) k4_issue_w<Z + K4_WD>(c, 0);
  else if (I < 5) {
    c.X[(I - 1) >> 1][2 * Z + ((I - 1) & 1)] = gld16(c.xb + (((I - 1) >> 1) * 18432 + (2 * Z + ((I - 1) & 1)) * 1024), c.lane16);
  }
  else k4_issue_w<Z + K4_WD>(c, 1);
}
template <int TL, int I> DI void k4_issue2(K4C& c) {
  if (I < 4) k4_piece(c, TL, 8 * I + c.w);
  else if ((c.w >> 2) == (TL & 1)) k4_piece(c, TL, 32 + (c.w & 3));
}
template <int TL, int E, int U> DI void k4_epi(K4C& c) {
  constexpr int e = U >> 2, u = U & 3, mt = e >> 2, i = e & 3, q = e & 1;
  if constexpr (K4_PK && U < 32) {
    constexpr int pr = U >> 3, v = U & 7, m2 = pr >> 1, i0 = 2 * (pr & 1);
    if (v == 0) {
      const f32x2 G = {c.acc[TL & 3][m2][i0], c.acc[TL & 3][m2][i0 + 1]};
      asm volatile("v_pk_mul_f32 %0, %1, %2" : "=v"(c.pa[E]) : "v"(G), "v"(c.c1p[m2]));
    }
    if (v == 1) { const f32x2 Uu = {c.acc[TL & 3][2 + m2][i0], c.acc[TL & 3][2 + m2][i0 + 1]};
      asm volatile("v_pk_mul_f32 %0, %1, %2" : "=v"(c.pt[E]) : "v"(Uu), "v"(c.pa[E]));
      c.pa[E][0] = __builtin_amdgcn_exp2f(c.pa[E][0]); PIN(c.pa[E]); }
    if (v == 2) { c.pa[E][1] = __builtin_amdgcn_exp2f(c.pa[E][1]); PIN(c.pa[E]); }
    if (v == 3) asm volatile("v_pk_fma_f32 %0, %0, %1, %1" : "+v"(c.pa[E]) : "v"(c.c2p[m2]));
    if (v == 4) { c.pa[E][0] = __builtin_amdgcn_rcpf(c.pa[E][0]); PIN(c.pa[E]); }
    if (v == 5) { c.pa[E][1] = __builtin_amdgcn_rcpf(c.pa[E][1]); PIN(c.pa[E]); }
    if (v == 6) asm volatile("v_pk_mul_f32 %0, %0, %1" : "+v"(c.pt[E]) : "v"(c.pa[E]));
    if (v == 7) { c.pk[E][pr] = cvtpk(c.pt[E][0], c.pt[E][1]); PIN(c.pk[E][pr]); }
  } else if constexpr (U < 32) {
    if (u == 0) { const float a = c.acc[TL & 3][mt][i] * c.c1[mt]; c.ea[E][q] = a; c.et[E][q] = a * c.acc[TL & 3][2 + mt][i]; PIN(c.ea[E][q]); PIN(c.et[E][q]); }
    if (u == 1) { c.ea[E][q] = __builtin_amdgcn_exp2f(c.ea[E][q]); PIN(c.ea[E][q]); }
    if (u == 2) { c.ea[E][q] = __builtin_amdgcn_rcpf(__builtin_fmaf(c.ea[E][q], c.c2[mt], c.c2[mt])); PIN(c.ea[E][q]); }
    if (u == 3) { c.et[E][q] *= c.ea[E][q]; PIN(c.et[E][q]); if (q) { c.pk[E][e >> 1] = cvtpk(c.et[E][0], c.et[E][1]); PIN(c.pk[E][e >> 1]); } }
  } else if constexpr (U == 32) {
    auto r0 = __builtin_amdgcn_permlane16_swap(c.pk[E][0], c.pk[E][2], false, false);
    auto r1 = __builtin_amdgcn_permlane16_swap(c.pk[E][1], c.pk[E][3], false, false);
    c.pk[E][0] = r0[0]; c.pk[E][1] = r1[0]; c.pk[E][2] = r0[1]; c.pk[E][3] = r1[1];
  } else if constexpr (U == 33) {
    const int T = c.T0 + TL * c.dir;
    bst16<ACT_AUX>(u32x4{c.pk[E][0], c.pk[E][1], c.pk[E][2], c.pk[E][3]}, c.ars, c.avo, (T >> 1) * 1024 + (T & 1) * 512);
  }
}
constexpr int k4_tile(int F) { return F < 108 ? (F % 6) >> 1 : 3 + (F - 108) / 36; }
constexpr int k4_step(int F) { return F < 108 ? F / 6 : ((F - 108) % 36) >> 1; }
constexpr int k4_addr(int F) { return k4_step(F) * 2048 + (F & 1) * 1024; }
template <int F> DI void k4_frag(K4C& c) {
  constexpr int TL = k4_tile(F), S = k4_step(F), M = F & 1, N = F + K4_PF;
  const u32x4 a = c.wf[F % (K4_PF + 1)];
  if constexpr (F < 108) { if constexpr ((F + 4) % 3 == 0) __builtin_amdgcn_s_setprio(3 - ((F + 4) % 12) / 3); }
  else if constexpr ((F - 108 + 4) % 9 == 0) __builtin_amdgcn_s_setprio(3 - ((F - 108 + 4) % 36) / 9);
  if (S == 0) {
    G_MFMA0(c.acc[TL & 3][2 * M], a, c.X[0][S]);
    G_MFMA0(c.acc[TL & 3][2 * M + 1], a, c.X[1][S]);
  } else {
    constexpr int o = GRAY ? M : 0;
    G_MFMA(c.acc[TL & 3][2 * M + o], a, c.X[o][S]);
    if constexpr (K4_MID && F >= 108 && (F - 108) % 36 >= 2) {
      SBAR();
      k4_epi<(TL == 3 ? 0 : (TL == 4 ? 2 : TL - 1)), 0, (F - 108) % 36 - 2>(c);
      SBAR();
    }
    G_MFMA(c.acc[TL & 3][2 * M + 1 - o], a, c.X[1 - o][S]);
  }
  if constexpr (N < 108 && N % 12 == 0) {
    constexpr int Z = N / 12;
    if (Z < 8) asm volatile("s_waitcnt vmcnt(5)\n\ts_barrier" : "+v"(c.X[0][2 * Z]), "+v"(c.X[0][2 * Z + 1]), "+v"(c.X[1][2 * Z]), "+v"(c.X[1][2 * Z + 1])::"memory");
    else asm volatile("s_waitcnt vmcnt(2)\n\ts_barrier" : "+v"(c.X[0][2 * Z]), "+v"(c.X[0][2 * Z + 1]), "+v"(c.X[1][2 * Z]), "+v"(c.X[1][2 * Z + 1])::"memory");
  } else if constexpr (N >= 108 && N < 324 && (N - 108) % 36 == 0) {
    constexpr int NT = k4_tile(N);
    if (NT == 3) asm volatile("s_waitcnt vmcnt(0) lgkmcnt(0)\n\ts_barrier" ::: "memory");
    else if (NT == 8) {
      asm volatile("s_waitcnt vmcnt(0) lgkmcnt(0)\n\ts_barrier" ::: "memory");
      if (c.w == 0 && c.lane16 == 0) __hip_atomic_fetch_add(c.cntA, 1u, __ATOMIC_RELAXED, __HIP_MEMORY_SCOPE_AGENT);
    }
    else asm volatile("s_waitcnt vmcnt(4) lgkmcnt(0)\n\ts_barrier" ::: "memory");
  }
  if constexpr (N < 324) c.wf[N % (K4_PF + 1)] = ldsr16(c.wl[k4_tile(N) & 3] + k4_addr(N));
  if constexpr (F < 108) {
    constexpr int Z = F / 12, I = F % 12;
    if constexpr (Z == 0 && I < 4 && !(I & 1)) k4_issue1<2, I / 2>(c);
    if constexpr (Z < 7 && I < 8 && (I & 1)) k4_issue1<Z + 2, 2 + I / 2>(c);
    if constexpr (Z < 6 && I > 8 && (I & 1)) k4_issue1<Z + 3, (I - 9) / 2>(c);
    if constexpr (K4_WD == 0) {
      if constexpr (Z == 6 && I == 10) k4_issue2<3, 0>(c);
      if constexpr (Z == 7 && I < 8 && (I & 1)) k4_issue2<3, 1 + I / 2>(c);
    } else {
      if constexpr (Z == 6 && I == 10) k4_issue2<3, 2>(c);
      if constexpr (Z == 7 && (I == 1 || I == 3)) k4_issue2<3, 3 + I / 2>(c);
    }
  } else {
    constexpr int P = (F - 108) % 36;
    if constexpr (TL == 3) { if constexpr (P % 3 == 0 && P < 15) k4_issue2<4, P / 3>(c); if constexpr (P % 3 == 0 && P >= 15 && P < 30) k4_issue2<5, (P - 15) / 3>(c); }
    else if constexpr (TL + 2 < 9 && P % 4 == 0 && P < 20) k4_issue2<TL + 2, P / 4>(c);
    if constexpr (K4_PFWD && TL >= 4 && TL <= 7 && (P == 33 || (P == 34 && TL < 6))) {
      constexpr int tl = TL < 6 ? 2 * (TL - 4) + P - 33 : TL - 2;
      const int T = c.T0 + tl * c.dir;
      asm volatile("global_load_dword %0, %1, %2" : "+v"(c.pf0) : "v"(c.pfo), "s"(c.pfb + ((T >> 1) * 1024 + (T & 1) * 512)));
    }
    if constexpr (TL == 7 && P == 0) CODE_PF2(c.pf1, k_ffn, k_end, 0, 2 + (c.w & 1), ((blockIdx.x >> 3) == 0 && c.w < 2) ? 128u : 0u);
    if constexpr (TL == 7 && P % 3 == 2 && P <= 11) k5_piece<0>(c.wd, P / 3, c.w, c.lane16);
    if constexpr (TL == 8 && P < 4) k5_piece<1>(c.wd, P, c.w, c.lane16);
    if constexpr (TL == 8 && P >= 4 && P < 8) k5_piece<2>(c.wd, P - 4, c.w, c.lane16);
    if constexpr (TL == 8 && P == K4_POLL) POLL(c.poll, c.cntA);
    if constexpr (TL == 8 && P == K4_POLL + 12) { asm volatile("s_waitcnt vmcnt(0)" : "+v"(c.poll), "+v"(c.pf1), "+v"(c.pf0)); wait_count(c.cntA, c.poll, 8u); }
    if constexpr (TL == 8 && P > K4_POLL + 12 && P <= K4_POLL + 20) {
      constexpr int i = P - K4_POLL - 13, z = i >> 2, j = (i & 1) * 2 + ((i >> 1) & 1);
      asm volatile("global_load_dwordx4 %0, %1, %2 sc1" : "=v"(c.A0[z][j]) : "v"(c.lane16), "s"(((j >> 1) ? c.ab2 : c.ab) + K5_PERM[2 * z + (j & 1)] * 1024));
    }
    if constexpr (P >= 2) {
      if constexpr (TL == 3) { if (!K4_MID) k4_epi<0, 0, P - 2>(c); k4_epi<1, 1, P - 2>(c); }
      else if constexpr (TL == 4) { if (!K4_MID) k4_epi<2, 0, P - 2>(c); k4_epi<3, 1, P - 2>(c); }
      else if (!K4_MID) k4_epi<TL - 1, 0, P - 2>(c);
    }
  }
  SBAR();
}
template <int B, int... Fs> DI void k4_run(K4C& c, std::integer_sequence<int, Fs...>) { (k4_frag<B + Fs>(c), ...); }
DI void k4_last(K4C& c) {
  float a[8], t[8];
#pragma unroll
  for (int e = 0; e < 8; ++e) { a[e] = c.acc[0][e >> 2][e & 3] * c.c1[e >> 2]; t[e] = a[e] * c.acc[0][2 + (e >> 2)][e & 3]; }
#pragma unroll
  for (int e = 0; e < 8; ++e) a[e] = __builtin_amdgcn_exp2f(a[e]);
#pragma unroll
  for (int e = 0; e < 8; ++e) a[e] = __builtin_amdgcn_rcpf(__builtin_fmaf(a[e], c.c2[e >> 2], c.c2[e >> 2]));
#pragma unroll
  for (int e = 0; e < 4; ++e) c.pk[0][e] = cvtpk(t[2 * e] * a[2 * e], t[2 * e + 1] * a[2 * e + 1]);
  k4_epi<8, 0, 32>(c); k4_epi<8, 0, 33>(c);
}
DI f32x4 addbf(f32x4 a, unsigned lo, unsigned hi) { return a + f32x4{bflo(lo), bfhi(lo), bflo(hi), bfhi(hi)}; }
struct K5C {
  const char *ab, *ab2;
  LDSP const char *ra[4], *oa[3], *xr;
  LDSP char* xw;
  u32x4 A[9][4], wf[5];
  f32x4 acc[2][7], own[2][2], v[4];
  const char* hb; unsigned ho; int kq; u32x2 hr[3];
  __amdgpu_buffer_rsrc_t wrs; unsigned tw, p8s;
  gu32* cnt;
};
template <int Z, int J> DI void k5_issue(K5C& c, const K4C& k) {
  if constexpr (Z < 7 && J < 4) asm volatile("global_load_dwordx4 %0, %1, %2 sc1"
 : "=v"(c.A[Z + 2][J]) : "v"(k.lane16), "s"(((J >> 1) ? c.ab2 : c.ab) + K5_PERM[2 * (Z + 2) + (J & 1)] * 1024));
  if constexpr (Z < 6 && J >= 4) k5_piece<Z + 3>(k.wd, J - 4, k.w, k.lane16);
  if constexpr (Z == 6 && J >= 4) k5_own<J - 4>(k.wd, k.w, k.lane16);
  if constexpr (Z == 7 && J == 0) k5_own<4>(k.wd, k.w, k.lane16);
  if constexpr (Z == 7 && J == 1) asm volatile("global_load_dwordx2 %0, %1, %2" : "=v"(c.hr[0]) : "v"(c.ho), "s"(c.hb + c.kq * 1024));
  if constexpr (Z == 7 && J == 2) asm volatile("global_load_dwordx2 %0, %1, %2 offset:512" : "=v"(c.hr[1]) : "v"(c.ho), "s"(c.hb + c.kq * 1024));
  if constexpr (Z == 7 && J == 3) asm volatile("global_load_dwordx2 %0, %1, %2" : "=v"(c.hr[2]) : "v"(c.ho), "s"(c.hb + 4096));
  if constexpr (Z == 8 && J < 4) k5_own<5 + J>(k.wd, k.w, k.lane16);
}
template <int N> DI u32x4 k5_read(const K5C& c) {
  if constexpr (N < 126) return ldsr16(c.ra[(N / 14) & 3] + (N % 14) * 1024);
  else { constexpr int o = K5_OC[(N - 126) / 4] + ((N - 126) & 3) * 1024; return ldsr16(c.oa[o >> 16] + (o & 0xffff)); }
}
template <int F> DI void k5_frag(K5C& c, K4C& k) {
  constexpr int N = F + 4;
  const u32x4 a = c.wf[F % 5];
  if constexpr (F < 126) {
    constexpr int Z = F / 14, f = F % 14, st = f / 7, j = f % 7;
    if (F < 7) { G_MFMA0(c.acc[0][j], a, c.A[Z][st]); G_MFMA0(c.acc[1][j], a, c.A[Z][2 + st]); }
    else { G_MFMA(c.acc[1][j], a, c.A[Z][2 + st]); G_MFMA(c.acc[0][j], a, c.A[Z][st]); }
    if constexpr (f == 10 && Z == 8) asm volatile("s_waitcnt vmcnt(4)\n\ts_barrier" : "+v"(c.hr[0]), "+v"(c.hr[1]), "+v"(c.hr[2])::"memory");
    if constexpr (f == 10 && Z < 8) {
      u32x4(&A)[4] = c.A[Z + 1];
      if (Z == 0) asm volatile("s_waitcnt vmcnt(9) lgkmcnt(0)\n\ts_barrier" : "+v"(A[0]), "+v"(A[1]), "+v"(A[2]), "+v"(A[3])::"memory");
      else if (Z == 3) asm volatile("s_waitcnt vmcnt(4) lgkmcnt(0)\n\ts_barrier" : "+v"(A[0]), "+v"(A[1]), "+v"(A[2]), "+v"(A[3]), "+v"(k.poll)::"memory");
      else if (Z < 7) asm volatile("s_waitcnt vmcnt(7) lgkmcnt(0)\n\ts_barrier" : "+v"(A[0]), "+v"(A[1]), "+v"(A[2]), "+v"(A[3])::"memory");
      else asm volatile("s_waitcnt vmcnt(8) lgkmcnt(0)\n\ts_barrier" : "+v"(A[0]), "+v"(A[1]), "+v"(A[2]), "+v"(A[3])::"memory");
      if (Z == 1 && k.w == 0 && k.lane16 == 0) __hip_atomic_fetch_add(k.cntB, 1u, __ATOMIC_RELAXED, __HIP_MEMORY_SCOPE_AGENT);
      if (Z == 3) wait_count(k.cntB, k.poll, 8u);
    }
    c.wf[N % 5] = k5_read<N>(c);
    if constexpr (Z == 3 && f == 1) POLL(k.poll, k.cntB);
    constexpr int slot = f == 0 ? 2 : (f == 2 ? 3 : (f == 4 ? 4 : (f == 5 ? 5 : (f == 7 ? 6 : (f == 9 ? 7 : -1)))));
    if constexpr (F == 0) { k5_issue<0, 0>(c, k); k5_issue<0, 1>(c, k); }
    if constexpr (slot >= 0) k5_issue<Z, slot>(c, k);
    if constexpr (Z < 8 && f > 10 && (f & 1)) k5_issue<Z + 1, (f - 11) / 2>(c, k);
  } else {
    constexpr int B = F - 126, z = B / 4, st = (B >> 1) & 1, t = B & 1;
    if (B < 2) { G_MFMA0(c.own[0][t], a, c.A[z][st]); G_MFMA0(c.own[1][t], a, c.A[z][2 + st]); }
    else { G_MFMA(c.own[1][t], a, c.A[z][2 + st]); G_MFMA(c.own[0][t], a, c.A[z][st]); }
    if constexpr (F == 131) asm volatile("s_waitcnt vmcnt(0) lgkmcnt(0)\n\ts_barrier" ::: "memory");
    if constexpr (F == K5_SIG) {
      asm volatile("s_waitcnt vmcnt(0)\n\ts_barrier" ::: "memory");
      if (k.w == 0 && k.lane16 == 0) __hip_atomic_fetch_add(c.cnt, 1u, __ATOMIC_RELAXED, __HIP_MEMORY_SCOPE_AGENT);
    }
    if constexpr (N < 162) c.wf[N % 5] = k5_read<N>(c);
  }
  if constexpr (F >= 123 && F < 130) { PIN(c.acc[1][F - 123]); *(LDSP f32x4*)(c.xw + (F - 123) * 1024) = c.acc[1][F - 123]; }
  if constexpr (F == 131) { _Pragma("unroll") for (int i = 0; i < 4; ++i) c.v[i] = *(LDSP const f32x4*)(c.xr + i * 1024); }
  if constexpr (F == 133 || F == 135 || F == 138) {
    constexpr int j = F == 133 ? 0 : (F == 135 ? 1 : 2), o = 2 * (j & 1);
    const f32x4 x = c.acc[0][2 * j] + c.v[o], y = c.acc[0][2 * j + 1] + c.v[o + 1];
    bst16<16>(u32x4{cvtpk(x[0], x[1]), cvtpk(x[2], x[3]), cvtpk(y[0], y[1]), cvtpk(y[2], y[3])}, c.wrs, k.lane16,
              (unsigned)WS_PART + ((((c.kq + j + 1) & 3) * 512 + c.tw) * 3 + j) * 1024);
    if constexpr (F == 135) { _Pragma("unroll") for (int i = 0; i < 3; ++i) c.v[i] = *(LDSP const f32x4*)(c.xr + (4 + i) * 1024); }
  }
  if constexpr (F == 140) {
    c.acc[0][6] += c.v[2];
    __builtin_amdgcn_raw_buffer_store_b64(u32x2{cvtpk(c.acc[0][6][0], c.acc[0][6][1]), cvtpk(c.acc[0][6][2], c.acc[0][6][3])}, c.wrs, c.p8s, c.kq * 512, 16);
    PIN(c.acc[0][6]);
  }
  SBAR();
}
template <int B, int... Fs> DI void k5_run(K5C& c, K4C& k, std::integer_sequence<int, Fs...>) { (k5_frag<B + Fs>(c, k), ...); }

extern "C" __global__ __launch_bounds__(512, 2) void k_ffn(char* out, char* ws) {
  extern __shared__ __attribute__((aligned(16))) char smem[];
  asm volatile("" : "+s"(out), "+s"(ws));
  LDSP char* lds = (LDSP char*)smem;
  const int tid = threadIdx.x, lane = tid & 63, w = rfl(tid >> 6);
  const int xcd = blockIdx.x & 7, bi = blockIdx.x >> 3;
  const int m = lane & 15, g = lane >> 4, kq = xcd >> 1;
  STAMP_DECL; STAMP(0);
  K4C c;
  {
    const int rb = 4 * (xcd & 1) + (bi & 3), fs = 8 * kq + (bi >> 2), mt = 16 * rb + 2 * w;
    c.lds0 = (unsigned)(uintptr_t)lds; c.lane16 = lane * 16; c.w = w;
    c.T0 = 9 * fs + 8 * (fs & 1); c.dir = 1 - 2 * (fs & 1);
    c.wb = ws + WS_W1 + (size_t)c.T0 * K4_SLOT;
    c.xb = ws + WS_HC + (size_t)mt * 18432;
    if (K4_WD) { k4_issue_w<0>(c, 0); k4_issue_w<0>(c, 1); }
    k4_issue1<0, 0>(c); k4_issue1<0, 1>(c); k4_issue1<0, 2>(c); k4_issue1<0, 3>(c); k4_issue1<0, 4>(c); k4_issue1<0, 5>(c);
    unsigned sq[6];
#pragma unroll
    for (int i = 0; i < 6; ++i) sq[i] = gld4(ws + WS_SSQ, (3 * g + (i >> 1)) * 8192 + (16 * mt + 16 * (i & 1) + m) * 4);
    k4_issue1<1, 0>(c); k4_issue1<1, 1>(c); k4_issue1<1, 2>(c); k4_issue1<1, 3>(c); k4_issue1<1, 4>(c); k4_issue1<1, 5>(c);
    SBAR();
#pragma unroll
    for (int i = 0; i < 4; ++i) { c.wl[i] = lds + c.lane16 + i * K4_SLOT; PIN(c.wl[i]); }
    c.ars = mkrsrc(ws + WS_ACT, 128u * 144 * 1024);
    c.avo = (mt + (g & 1)) * (144 * 1024) + (g >> 1) * 256 + m * 16;
    c.wd.wb = ws + WS_WD + ((size_t)(bi >> 3) * 144 + 36 * kq) * 9 * 1024;
#pragma unroll
    for (int i = 0; i < 4; ++i) {
      const int p = i < 3 ? 8 * i + w : 24 + (w & 3), st = (p * 37) >> 8, t = p - 7 * st + 2;
      c.wd.q[i] = ((st >> 1) * 162 + (t < 8 ? (t + 2 * kq) & 7 : 8)) * 1024;
      c.wd.qs[i] = st & 1;
      asm volatile("" : "+s"(c.wd.q[i]), "+s"(c.wd.qs[i]));
    }
    c.wd.qo = ((w >> 2) * 162 + 2 * kq + (w & 1)) * 1024; c.wd.qos = (w >> 1) & 1; c.wd.lds0 = c.lds0;
    if (K4_PFWD) {
      c.pfb = ws + WS_ACT;
      c.pfo = (mt + ((lane >> 5) & 1)) * (144 * 1024) + ((lane >> 4) & 1) * 256 + ((lane >> 3) & 1) * 128;
      PIN(c.pfo);
    }
    c.pf0 = 0;
    c.cntA = (gu32*)(ws + WS_CNT + (64 + rb * 4 + kq) * 128);
    STAMP(1);
    asm volatile("s_waitcnt vmcnt(5)\n\ts_barrier" : "+v"(sq[0]), "+v"(sq[1]), "+v"(sq[2]), "+v"(sq[3]), "+v"(sq[4]), "+v"(sq[5]),
                 "+v"(c.X[0][0]), "+v"(c.X[0][1]), "+v"(c.X[1][0]), "+v"(c.X[1][1])::"memory");
#pragma unroll
    for (int i = 0; i < K4_PF; ++i) c.wf[i] = ldsr16(c.wl[k4_tile(i)] + k4_addr(i));
#pragma unroll
    for (int t = 0; t < 2; ++t) {
      float ss = __uint_as_float(sq[t]) + __uint_as_float(sq[2 + t]) + __uint_as_float(sq[4 + t]);
      ss = xor32_sum(xor16_sum(ss));
      const float v = ss * (1.0f / DM) + LN_EPS, rs = __builtin_amdgcn_rsqf(v);
      c.c1[t] = -LOG2E * rs; c.c2[t] = -LOG2E * (v * rs);
      PIN(c.c1[t]); PIN(c.c2[t]);
      if (K4_PK) { c.c1p[t] = f32x2{c.c1[t], c.c1[t]}; c.c2p[t] = f32x2{c.c2[t], c.c2[t]}; PIN(c.c1p[t]); PIN(c.c2p[t]); }
    }
    SBAR();
    k4_run<0>(c, std::make_integer_sequence<int, 288>{});
    c.cntB = c.cntA + 32 * 32;
  }
  const int rb = 8 * (xcd & 1) + (bi & 7), nb = bi >> 3, mg = w & 3, kh = w >> 2, mu = 8 * rb + 2 * mg + kh;
  gu32* prodB = c.cntB;
  c.cntA = (gu32*)(ws + WS_CNT + (64 + (rb >> 1) * 4 + kq) * 128);
  c.ab = ws + WS_ACT + ((size_t)mu * 144 + 36 * kq + 18 * kh) * 1024;
  c.ab2 = ws + WS_ACT + ((size_t)(mu ^ 1) * 144 + 36 * kq + 18 * kh) * 1024;
  k4_run<288>(c, std::make_integer_sequence<int, 36>{});
  STAMP(2);
  K5C d;
  d.ab = c.ab; d.ab2 = c.ab2;
  asm volatile("s_nop 7" : "+v"(c.acc[0][0]), "+v"(c.acc[0][1]), "+v"(c.acc[0][2]), "+v"(c.acc[0][3]));
  k4_last(c);
  SBAR();
  gu32* consB = c.cntA + 32 * 32;
  d.hb = ws + WS_HU + (size_t)mu * 18432 + nb * 4608; d.ho = (g >> 1) * 256 + m * 16 + (g & 1) * 8; d.kq = kq;
#pragma unroll
  for (int i = 0; i < 4; ++i) { d.ra[i] = lds + K5_R[i] + kh * 14336 + c.lane16; PIN(d.ra[i]); }
#pragma unroll
  for (int i = 0; i < 3; ++i) { d.oa[i] = lds + i * 65536 + kh * 4096 + c.lane16; PIN(d.oa[i]); }
  d.xw = lds + (kh ? K5_R[2] : K5_R[1]) + mg * 7168 + c.lane16; PIN(d.xw);
  d.xr = lds + (kh ? K5_R[1] : K5_R[2]) + mg * 7168 + c.lane16; PIN(d.xr);
  const __amdgpu_buffer_rsrc_t wrs = mkrsrc(ws, (unsigned)WS_END), ors = mkrsrc(out, SEQ * DM * 2);
  const int tile = rb * 4 + nb;
  const bool own8 = (mu & 3) == kq;
  const unsigned tw = tile * 8 + w;
  const unsigned p8 = (unsigned)WS_PART8 + tw * 2048 + lane * 8;
  d.cnt = (gu32*)(ws + WS_CNT + tile * 128);
  d.wrs = wrs; d.tw = tw; d.p8s = own8 ? 0x80000000u : p8; PIN(d.p8s);
#pragma unroll
  for (int i = 0; i < 4; ++i) { d.A[0][i] = c.A0[0][i]; d.A[1][i] = c.A0[1][i]; }
  asm volatile("s_waitcnt vmcnt(6) lgkmcnt(0)\n\ts_barrier" : "+v"(d.A[0][0]), "+v"(d.A[0][1]), "+v"(d.A[0][2]), "+v"(d.A[0][3])::"memory");
  STAMP(3);
  d.wf[0] = k5_read<0>(d); d.wf[1] = k5_read<1>(d); d.wf[2] = k5_read<2>(d); d.wf[3] = k5_read<3>(d);
  SBAR();
  c.cntB = prodB;
  k5_run<0>(d, c, std::make_integer_sequence<int, 28>{});
  c.cntB = consB;
  k5_run<28>(d, c, std::make_integer_sequence<int, 134>{});
  asm volatile("s_nop 7" : "+v"(d.own[0][0]), "+v"(d.own[0][1]), "+v"(d.own[1][0]), "+v"(d.own[1][1]));
  STAMP(4);
  LDSP char* xo = lds + K5_R[0] + c.lane16;
  *(LDSP f32x4*)(xo + w * 2048) = d.own[1][0];
  *(LDSP f32x4*)(xo + w * 2048 + 1024) = d.own[1][1];
  unsigned oo = (16 * mu + m) * 1152 + nb * 288;
  unsigned oo8 = own8 ? oo + 256 + g * 8 : 0x80000000u;
  oo += kq * 64 + (g & 1) * 32 + (g >> 1) * 16;
  PIN(oo); PIN(oo8);
  STAMP(5);
  if (w == 0) wait_count(d.cnt, 0u, 4u);
  asm volatile("s_waitcnt lgkmcnt(0)\n\ts_barrier" ::: "memory");
  STAMP(6);
  u32x4 r[3]; u32x2 r8[3];
#pragma unroll
  for (int j = 0; j < 3; ++j) {
    r[j] = __builtin_amdgcn_raw_buffer_load_b128(wrs, c.lane16, (unsigned)WS_PART + ((kq * 512 + tw) * 3 + j) * 1024, 16);
    r8[j] = __builtin_amdgcn_raw_buffer_load_b64(wrs, own8 ? p8 : 0x80000000u, ((kq + 1 + j) & 3) * 512, 16);
  }
  SBAR();
  f32x4 e0 = addbf(d.own[0][0] + *(LDSP const f32x4*)(xo + (w ^ 4) * 2048), d.hr[0][0], d.hr[0][1]);
  f32x4 e1 = addbf(d.own[0][1] + *(LDSP const f32x4*)(xo + (w ^ 4) * 2048 + 1024), d.hr[1][0], d.hr[1][1]);
  f32x4 e8 = addbf(d.acc[0][6], d.hr[2][0], d.hr[2][1]);
#pragma unroll
  for (int j = 0; j < 3; ++j) { e0 = addbf(e0, r[j][0], r[j][1]); e1 = addbf(e1, r[j][2], r[j][3]); e8 = addbf(e8, r8[j][0], r8[j][1]); }
  {
    const auto s0 = __builtin_amdgcn_permlane16_swap(cvtpk(e0[0], e0[1]), cvtpk(e1[0], e1[1]), false, false);
    const auto s1 = __builtin_amdgcn_permlane16_swap(cvtpk(e0[2], e0[3]), cvtpk(e1[2], e1[3]), false, false);
    bst16<OUT_AUX>(u32x4{s0[0], s1[0], s0[1], s1[1]}, ors, oo, 0);
    __builtin_amdgcn_raw_buffer_store_b64(u32x2{cvtpk(e8[0], e8[1]), cvtpk(e8[2], e8[3])}, ors, oo8, 0, OUT_AUX);
  }
  STAMP(7); STAMP_OUT(4, 8);
}
extern "C" __global__ void k_end() {}

extern "C" void kernel_launch(void* const* d_in, const int* in_sizes, int n_in, void* d_out, int out_size, void* d_ws, size_t ws_size,
                              hipStream_t stream) {
  const u16* x = (const u16*)d_in[0];
  const u16* wqkv = (const u16*)d_in[1];
  const u16* wo = (const u16*)d_in[2];
  const u16* wg = (const u16*)d_in[4];
  const u16* wu = (const u16*)d_in[5];
  const u16* wd = (const u16*)d_in[6];
  char* ws = (char*)d_ws;
  (void)hipFuncSetAttribute((const void*)k_qkv, hipFuncAttributeMaxDynamicSharedMemorySize, K1_LDS);
  (void)hipFuncSetAttribute((const void*)k_attn, hipFuncAttributeMaxDynamicSharedMemorySize, K2_LDS);
  (void)hipFuncSetAttribute((const void*)k_oproj, hipFuncAttributeMaxDynamicSharedMemorySize, K3_LDS);
  (void)hipFuncSetAttribute((const void*)k_ffn, hipFuncAttributeMaxDynamicSharedMemorySize, K4_LDS);
  k_qkv<<<256, 768, K1_LDS, stream>>>(x, wqkv, ws);
  k_attn<<<256, 768, K2_LDS, stream>>>(x, wo, wg, wu, wd, ws);
  k_oproj<<<192, 768, K3_LDS, stream>>>(ws);
  k_ffn<<<256, 512, K4_LDS, stream>>>((char*)d_out, ws);
}
```

```cpp
#include <hip/hip_runtime.h>
#include <hip/hip_bf16.h>
#include <stdint.h>
#include <utility>
#include <type_traits>

typedef __bf16 bf16x8 __attribute__((ext_vector_type(8)));
typedef __bf16 bf16x2 __attribute__((ext_vector_type(2)));
typedef short s16x4 __attribute__((ext_vector_type(4)));
typedef float f32x16 __attribute__((ext_vector_type(16)));
typedef float f32x4 __attribute__((ext_vector_type(4)));
typedef float f32x2 __attribute__((ext_vector_type(2)));
typedef unsigned u32x4 __attribute__((ext_vector_type(4)));
typedef unsigned u32x2 __attribute__((ext_vector_type(2)));
typedef unsigned short u16;
#define LDSP __attribute__((address_space(3)))
#define DI __device__ __forceinline__
#define SBAR() __builtin_amdgcn_sched_barrier(0)

constexpr int SEQ = 2048, DM = 576, NH = 32, DH = 18, FF = 4608;
constexpr float LOG2E = 1.4426950408889634f;
constexpr float QSCALE = 0.23570226039551584f * LOG2E;
constexpr float LN_EPS = 1e-5f;

constexpr size_t CHUNK_B = 37888;
constexpr size_t KV_HEAD_B = 4 * CHUNK_B;
constexpr size_t WS_QM = 0;
constexpr size_t WS_QT = WS_QM + (size_t)NH * 65536;
constexpr size_t WS_KV = WS_QT + (size_t)NH * 8192;
constexpr size_t WS_KMAX = WS_KV + (size_t)NH * KV_HEAD_B;
constexpr size_t WS_XMEAN = WS_KMAX + 65536;
constexpr size_t WS_OFR = WS_XMEAN + 8192;
constexpr size_t WS_WO = WS_OFR + (size_t)128 * 18 * 1024;
constexpr size_t WS_WBAR = WS_WO + (size_t)12 * 54 * 1024;
constexpr size_t WS_HC = WS_WBAR + 3072;
constexpr size_t WS_SSQ = WS_HC + (size_t)64 * 36 * 1024;
constexpr size_t WS_W1 = WS_SSQ + (size_t)12 * 8192;
constexpr size_t WS_WD = WS_W1 + (size_t)288 * 36 * 1024;
constexpr size_t WS_ACT = WS_WD + (size_t)4 * 144 * 9 * 1024;
constexpr size_t UNIT_B = (size_t)128 * 18432;
constexpr size_t WS_PART = WS_ACT + (size_t)128 * 144 * 1024;
constexpr size_t WS_PART8 = WS_PART + (size_t)4 * 64 * 8 * 3 * 1024;
constexpr size_t WS_CNT = WS_PART8 + (size_t)64 * 8 * 4 * 512;
constexpr size_t WS_XU = WS_CNT + 32768;
constexpr size_t WS_HU = WS_XU + UNIT_B;
constexpr size_t WS_END = WS_HU + UNIT_B;

static __device__ unsigned long long g_ptr[8];
typedef __attribute__((address_space(4))) const unsigned long long* cptr_t;
DI unsigned cvtpk(float lo, float hi) {
  f32x2 v = {lo, hi};
  bf16x2 b = __builtin_convertvector(v, bf16x2);
  return __builtin_bit_cast(unsigned, b);
}
DI float bflo(unsigned u) { return __uint_as_float(u << 16); }
DI float bfhi(unsigned u) { return __uint_as_float(u & 0xffff0000u); }
DI float dot2(unsigned a, unsigned b, float c) {
  return __builtin_amdgcn_fdot2_f32_bf16(__builtin_bit_cast(bf16x2, a), __builtin_bit_cast(bf16x2, b), c, false);
}
DI f32x16 mfma32(u32x4 a, u32x4 b, f32x16 c) {
  return __builtin_amdgcn_mfma_f32_32x32x16_bf16(__builtin_bit_cast(bf16x8, a), __builtin_bit_cast(bf16x8, b), c, 0, 0, 0);
}
DI f32x4 mfma16(u32x4 a, u32x4 b, f32x4 c) {
  return __builtin_amdgcn_mfma_f32_16x16x32_bf16(__builtin_bit_cast(bf16x8, a), __builtin_bit_cast(bf16x8, b), c, 0, 0, 0);
}
DI unsigned rfl(unsigned x) { return __builtin_amdgcn_readfirstlane(x); }
DI const char* uniptr(const char* p) {
  uint64_t v = (uint64_t)p;
  unsigned lo = rfl((unsigned)v), hi = rfl((unsigned)(v >> 32));
  return (const char*)(((uint64_t)hi << 32) | lo);
}
DI void dma16(const char* sbase, unsigned voff, unsigned lds) {
  asm volatile("s_mov_b32 m0, %2\n\ts_nop 0\n\tglobal_load_lds_dwordx4 %0, %1" ::"v"(voff), "s"(sbase), "s"(lds));
}
DI u32x4 gld16(const char* sbase, unsigned voff) {
  u32x4 r;
  asm volatile("global_load_dwordx4 %0, %1, %2" : "=v"(r) : "v"(voff), "s"(sbase));
  return r;
}
DI unsigned gld4(const char* sbase, unsigned voff) {
  unsigned r;
  asm volatile("global_load_dword %0, %1, %2" : "=v"(r) : "v"(voff), "s"(sbase));
  return r;
}
#define WAITV(n) asm volatile("s_waitcnt vmcnt(" #n ")" ::: "memory")
#define BARRIER() asm volatile("s_barrier" ::: "memory")
#define PIN(x) asm volatile("" : "+v"(x))
DI u32x4 ldsr16(LDSP const char* p) { return *(LDSP const u32x4*)p; }
DI float xor32_sum(float v) {
  auto r = __builtin_amdgcn_permlane32_swap(__float_as_uint(v), __float_as_uint(v), false, false);
  return __uint_as_float(r[0]) + __uint_as_float(r[1]);
}
DI float xor32_max(float v) {
  auto r = __builtin_amdgcn_permlane32_swap(__float_as_uint(v), __float_as_uint(v), false, false);
  return fmaxf(__uint_as_float(r[0]), __uint_as_float(r[1]));
}
DI float xor16_sum(float v) {
  auto r = __builtin_amdgcn_permlane16_swap(__float_as_uint(v), __float_as_uint(v), false, false);
  return __uint_as_float(r[0]) + __uint_as_float(r[1]);
}

#define STAMP_DECL
#define STAMP(i)
#define STAMP_OUT(kid, n)

DI int wo_row(int step, int g, int j) {
  if (step < 16) return 18 * (2 * step + (g >> 1)) + 4 * (g & 1) + (j & 3) + 8 * (j >> 2);
  return 18 * (4 * (4 * (step - 16) + g) + (j >> 1)) + 16 + (j & 1);
}

#define K4_SPREAD 1
#define K4_PM 1
#define K4_WD 1
#define K3_PF 0
#define K3_SLEEP 32
#define K1_ROT 0
#define AT_PM 9
#define HELP_PRIO 3
#define HELP_EARLY 0
#define PREP_ORD 1
#define GRAY 1
#define K4_POLL 10
#define K4_PK 0
#define K5_ORD 1
#define K5_SIG 154
#define K4_MID 0
#define AT_HFIRST 0
#define K4_ROT 0
#define K5_FLAG 0
#define K5_SPREAD 1
#define K5_PM 0
#define OUT_AUX 16
#define K4_PFWD 1
#define QKV_AUX 16
#define QKT_AUX 16
#define V_AUX 16
#define O_AUX 16
#define H_AUX 16
#define PART_AUX 16
#define ACT_AUX 16
#define PREP_AUX 16
#define CODE_PF(dst, first, last, extra, blk) CODE_PF2(dst, first, last, extra, blk, 128u)
#define CODE_PF2(dst, first, last, extra, blk, stride)                                                                              \
  do {                                                                                                                     \
    uint64_t pc_; int b_, e_;                                                                                              \
    asm volatile("s_getpc_b64 %0\n.Lpf%=:\n\ts_mov_b32 %1, " #first "-.Lpf%=\n\ts_mov_b32 %2, " #last "-.Lpf%=" : "=s"(pc_), "=s"(b_), "=s"(e_)); \
    const unsigned o_ = (blk) * 8192u + (threadIdx.x & 63) * (stride), n_ = (unsigned)(e_ - b_) + (extra) - 4u;                \
    dst = gld4((const char*)(pc_ + b_), o_ < n_ ? o_ : n_);                                                                \
  } while (0)
#define G_MFMA(d, a, b) asm volatile("v_mfma_f32_16x16x32_bf16 %0, %1, %2, %0" : "+v"(d) : "v"(a), "v"(b))
#define G_MFMA0(d, a, b) asm volatile("v_mfma_f32_16x16x32_bf16 %0, %1, %2, 0" : "=&v"(d) : "v"(a), "v"(b))
template <int AUX> DI void bst16(u32x4 v, __amdgpu_buffer_rsrc_t rs, unsigned voff, unsigned soff) {
  __builtin_amdgcn_raw_buffer_store_b128(v, rs, voff, soff, AUX);
  asm volatile("s_nop 1" ::"v"(v) : "memory");
}
DI __amdgpu_buffer_rsrc_t mkrsrc(const void* p, unsigned bytes) {
  return __builtin_amdgcn_make_buffer_rsrc((void*)p, 0, bytes, 0x00020000);
}

#define K1_PF 4
#define K1_TOUCH 1
#define K1_TSLEEP 16
#define K1_DEPTH 2
constexpr int K1_PITCH = 224, K1_SLOT = 16384 + 64 * K1_PITCH  , K1_NS = K1_DEPTH + 1;
constexpr int K1_SP = 232;
constexpr int K1_STG = K1_NS * K1_SLOT, K1_CS = K1_STG + 128 * K1_SP + 64, K1_ST = K1_CS + 512 + 64, K1_LDS = K1_ST + 2048;
struct K1C {
  LDSP const char *xa[2], *wl, *wc0, *wc1;
  u32x4 X[3][2], wf[K1_PF + 1], ones;
  f32x4 acc[4][2];
  f32x4 sm[2]; float sq[2];
};
DI void k1_loader(const char* xb, const char* wb, char* ws, int j, int lane, unsigned lds0) {
  __builtin_amdgcn_s_setprio(3);
  unsigned xo[2], wo[4];
  const int g = lane >> 4;
  xo[0] = (lane >> 3) * 1152 + (((lane & 7) ^ g) * 16);
  xo[1] = (8 + (lane >> 3)) * 1152 + (((lane & 7) ^ (4 + g)) * 16);
  xb += j * (32 * 1152);
  const unsigned xd = lds0 + j * 4096, wd = lds0 + 16384 + j * 1024;
#define K1_X(z, so) do { dma16(xb + (z) * 128, xo[0], xd + (so)); dma16(xb + (z) * 128, xo[1], xd + (so) + 1024);                    \
    dma16(xb + (z) * 128 + 16 * 1152, xo[0], xd + (so) + 2048); dma16(xb + (z) * 128 + 16 * 1152, xo[1], xd + (so) + 3072); } while (0)
#define K1_W(z, so) do { _Pragma("unroll") for (int i = 0; i < 3; ++i) dma16(wb + (z) * (64 * 3456), wo[i], wd + (so) + i * 4096);     \
    if (j < 2) dma16(wb + (z) * (64 * 3456), wo[3], wd + (so) + 3 * 4096); } while (0)
  K1_X(0, 0);
  SBAR();
#pragma unroll
  for (int i = 0; i < 4; ++i) {
    const unsigned u = 64 * (j + 4 * i) + lane, r = u / 14;
    wo[i] = ((r & ~12u) | ((r & 4u) << 1) | ((r & 8u) >> 1)) * 3456 + (u - r * 14) * 16;
  }
  K1_W(0, 0);
#pragma unroll
  for (int z = 1; z < K1_DEPTH; ++z) { K1_X(z, z * K1_SLOT); K1_W(z, z * K1_SLOT); }
  unsigned so = K1_DEPTH * K1_SLOT;
#pragma unroll 1
  for (int z = K1_DEPTH; z < 9; ++z) {
    asm volatile("s_waitcnt vmcnt(%0)\n\ts_barrier" ::"i"(7 * (K1_DEPTH - 1)) : "memory");
    K1_X(z, so); K1_W(z, so);
    so = so == (K1_NS - 1) * K1_SLOT ? 0 : so + K1_SLOT;
  }
  if (K1_DEPTH > 2) asm volatile("s_waitcnt vmcnt(14)\n\ts_barrier" ::: "memory");
  asm volatile("s_waitcnt vmcnt(7)\n\ts_barrier" ::: "memory");
  asm volatile("s_waitcnt vmcnt(0)\n\ts_barrier" ::: "memory");
  if (blockIdx.x < 4 && j == 0) bst16<16>(u32x4{0, 0, 0, 0}, mkrsrc(ws, (unsigned)WS_END), (unsigned)WS_CNT + (blockIdx.x * 64 + lane) * 128, 0);
#undef K1_X
#undef K1_W
}
DI u32x4 tr_frag(LDSP const char* p, int d) {
  s16x4 lo = __builtin_amdgcn_ds_read_tr16_b64_v4i16((LDSP s16x4*)p);
  s16x4 hi = __builtin_amdgcn_ds_read_tr16_b64_v4i16((LDSP s16x4*)(p + d));
  return __builtin_bit_cast(u32x4, __builtin_shufflevector(lo, hi, 0, 1, 2, 3, 4, 5, 6, 7));
}
template <int NHG, int F> DI void k1_read(K1C& c) {
  constexpr int NF = 4 + NHG, S = F / NF, j = F % NF, Z = S >> 1, so = (Z % K1_NS) * K1_SLOT;
  if constexpr (j == 0) {
    c.X[S % 3][0] = ldsr16(c.xa[S & 1] + so);
    c.X[S % 3][1] = ldsr16(c.xa[S & 1] + so + 2048);
  }
  constexpr int wo = so + (S & 1) * 7168;
  if constexpr (NHG == 0 || j < 3) c.wf[F % (K1_PF + 1)] = tr_frag(c.wl + wo + j * 32, 1792);
  else c.wf[F % (K1_PF + 1)] = tr_frag((j == 3 ? c.wc0 : c.wc1) + wo, 1792);
}
template <int NHG, int F> DI void k1_frag(K1C& c) {
  constexpr int NF = 4 + NHG, S = F / NF, j = F % NF, Z = S >> 1, N = F + K1_PF, fz = F % (2 * NF);
  if constexpr (S == 0) {
    if constexpr (NHG == 0 || j < 3) { G_MFMA0(c.acc[j][0], c.wf[F % (K1_PF + 1)], c.X[0][0]); G_MFMA0(c.acc[j][1], c.wf[F % (K1_PF + 1)], c.X[0][1]); }
    else G_MFMA0(c.acc[3][j - 3], c.wf[F % (K1_PF + 1)], c.ones);
  } else {
    if constexpr (NHG == 0 || j < 3) { G_MFMA(c.acc[j][0], c.wf[F % (K1_PF + 1)], c.X[S % 3][0]); G_MFMA(c.acc[j][1], c.wf[F % (K1_PF + 1)], c.X[S % 3][1]); }
    else G_MFMA(c.acc[3][j - 3], c.wf[F % (K1_PF + 1)], c.ones);
  }
  if constexpr ((S & 1) == NHG && j < 4) {
#pragma unroll
    for (int t = 0; t < 2; ++t) { const unsigned v = c.X[S % 3][t][j]; c.sq[t] = dot2(v, v, c.sq[t]); PIN(c.sq[t]); }
    if constexpr (j < 2) { if constexpr (S < 2) G_MFMA0(c.sm[j], c.ones, c.X[S % 3][j]); else G_MFMA(c.sm[j], c.ones, c.X[S % 3][j]); }
  }
  if constexpr (N < 18 * NF) {
    if constexpr (N % (2 * NF) == 0) {
      asm volatile("s_waitcnt lgkmcnt(0)\n\ts_barrier" ::: "memory");
    }
    k1_read<NHG, N>(c);
  }
  SBAR();
}
template <int NHG, int... Fs> DI void k1_run(K1C& c, std::integer_sequence<int, Fs...>) {
  k1_read<NHG, 0>(c); k1_read<NHG, 1>(c); k1_read<NHG, 2>(c);
  if (K1_PF > 3) k1_read<NHG, 3>(c);
  if (K1_PF > 4) k1_read<NHG, 4>(c);
  if (K1_PF > 5) k1_read<NHG, 5>(c);
  SBAR();
  (k1_frag<NHG, Fs>(c), ...);
  asm volatile("s_nop 7" : "+v"(c.acc[0][0]), "+v"(c.acc[0][1]), "+v"(c.acc[1][0]), "+v"(c.acc[1][1]), "+v"(c.acc[2][0]), "+v"(c.acc[2][1]),
               "+v"(c.acc[3][0]), "+v"(c.acc[3][1]), "+v"(c.sm[0]), "+v"(c.sm[1]));
  SBAR();
}
extern "C" __global__ __launch_bounds__(768, 3) void k_qkv(const u16* x, const u16* wqkv, char* ws, const u16* wo, const u16* wg, const u16* wu, const u16* wd, char* out) {
  extern __shared__ __attribute__((aligned(16))) char smem[];
  const int tid = threadIdx.x, lane = tid & 63, w = rfl(tid >> 6);
  const int xcd = blockIdx.x & 7, bi = blockIdx.x >> 3;
  unsigned pf;
  CODE_PF2(pf, k_qkv, k_end, 0, bi, (w == 8 && bi < 9) ? 128u : 0u);
  SBAR();
  asm volatile("" : "+s"(x), "+s"(wqkv), "+s"(ws));
  LDSP char* lds = (LDSP char*)smem;
  const unsigned lds0 = (unsigned)(uintptr_t)lds;
  const int rb = 4 * (xcd >> 1) + (bi & 3), cb = 8 * (xcd & 1) + (bi >> 2);
  const int c0 = 108 * cb - 4 * (cb & 1);
  if (__builtin_expect(w >= 8, 1)) {
    k1_loader((const char*)x + rb * (128 * 1152), (const char*)wqkv + c0 * 2, ws, w - 8, lane, lds0);
    asm volatile("" ::"v"(pf));
    if (blockIdx.x == 0 && tid == 512) {
      g_ptr[0] = (unsigned long long)x; g_ptr[1] = (unsigned long long)wo; g_ptr[2] = (unsigned long long)wg; g_ptr[3] = (unsigned long long)wu;
      g_ptr[4] = (unsigned long long)wd; g_ptr[5] = (unsigned long long)ws; g_ptr[6] = (unsigned long long)out;
    }
    return;
  }
  const int m = lane & 15, g = lane >> 4, mg = w & 3, nh = w >> 2;
  STAMP_DECL; STAMP(0);
  K1C c;
  c.ones = u32x4{0x3f803f80u, 0x3f803f80u, 0x3f803f80u, 0x3f803f80u};
  PIN(c.ones);
  c.xa[0] = lds + (32 * mg + m) * 128 + ((g ^ (m >> 1)) * 16);
  c.xa[1] = lds + (32 * mg + m) * 128 + (((4 + g) ^ (m >> 1)) * 16);
  LDSP const char* wbase = lds + 16384 + (16 * (g >> 1) + 4 * (g & 1) + (m >> 2)) * K1_PITCH + (m & 3) * 8;
  c.wl = wbase + nh * 128;
  c.wc0 = wbase + mg * 32;
  c.wc1 = wbase + (mg < 3 ? mg + 4 : 6) * 32;
  c.sq[0] = c.sq[1] = 0.f;
  const int off = 4 * (cb & 1), T0 = 128 * rb;
  LDSP char* stg = lds + K1_STG;
  LDSP char* a_st = lds + K1_ST + (32 * mg + m) * 8;                 PIN(a_st);
  LDSP char* a_cs = lds + K1_CS + (64 * nh + 4 * g) * 4;             PIN(a_cs);
  LDSP char* a_sw = stg + (32 * mg + m) * K1_SP + (64 * nh + 4 * g) * 2;   PIN(a_sw);
  LDSP char* a_tr = stg + (32 * mg + 4 * (g & 1) + (m >> 2)) * K1_SP + (64 * nh + 16 * (g >> 1) + 4 * (m & 3)) * 2;   PIN(a_tr);
  float qs[4];
#pragma unroll
  for (int j = 0; j < 4; ++j) { qs[j] = (c0 + 64 * nh + 16 * j + 4 * g < DM) ? QSCALE : 1.0f; PIN(qs[j]); }
  const __amdgpu_buffer_rsrc_t wrs = mkrsrc(ws, (unsigned)WS_END);
  constexpr unsigned DROP = 0x80000000u;
  const unsigned kvo = (unsigned)WS_KV + (T0 >> 9) * (unsigned)CHUNK_B;
  unsigned vo[2] = {0, 0}, vone = 0, vd[4] = {DROP, DROP, DROP, DROP};
  if (cb >= 10) {
#pragma unroll
  for (int tt = 0; tt < 2; ++tt) {
    const int cc = 64 * nh + 32 * tt + 16 * (g >> 1) + m - off, hd = (cc + 18) / 18 - 1, G = 6 * cb + hd;
    vo[tt] = (cc >= 0 && cc < 108) ? hd * 4864 + 2 * mg * 608 + (g & 1) * 304 + (cc - 18 * hd) * 16 : 29184;
  }
  vone = tid < 96 ? (tid >> 4) * 4864 + (tid & 15) * 304 + 288 : 29184;
#pragma unroll
  for (int i = 0; i < 4; ++i) {
    const unsigned u = i * 512 + tid, hd = u / 304;
    vd[i] = (u < 1824 && 6 * cb + hd >= 64) ? kvo + 18432 + ((T0 >> 4) & 31) * 608 + (6 * cb + hd - 64) * (unsigned)KV_HEAD_B + (u - hd * 304) * 16 : DROP;
  }
  }
  PIN(vone); PIN(vo[0]); PIN(vo[1]); PIN(vd[0]); PIN(vd[1]); PIN(vd[2]); PIN(vd[3]);
  const int tok = tid & 127, T = T0 + tok;
  LDSP const char* a_g = stg + tok * K1_SP + off * 2;   PIN(a_g);
  unsigned um[2], ut[2];
  const unsigned tl = ((T >> 5) & 15) * 1024 + (T & 31) * 16;
#pragma unroll
  for (int it = 0; it < 2; ++it) {
    const int hd = (w >> 1) + 4 * it, G = 6 * cb + hd, head = G & 31;
    if (hd >= 6 || G >= 64) { um[it] = DROP; ut[it] = DROP; }
    else if (G < 32) { um[it] = (unsigned)WS_QM + head * 65536 + (T >> 9) * 16384 + tl; ut[it] = (unsigned)WS_QT + head * 8192 + T * 4; }
    else { um[it] = kvo + head * (unsigned)KV_HEAD_B + tl; ut[it] = kvo + head * (unsigned)KV_HEAD_B + 16384 + ((T >> 6) & 7) * 256 + (T & 31) * 8 + ((T >> 5) & 1) * 4; }
    PIN(um[it]); PIN(ut[it]);
  }
  STAMP(1);
  asm volatile("s_waitcnt vmcnt(0)\n\ts_barrier" : "+v"(pf)::"memory");
  STAMP(2);
  if (nh) k1_run<1>(c, std::make_integer_sequence<int, 90>{});
  else k1_run<0>(c, std::make_integer_sequence<int, 72>{});
  STAMP(3);
#pragma unroll
  for (int t = 0; t < 2; ++t) {
    const f32x2 st = {c.sm[t][0], xor32_sum(xor16_sum(c.sq[t]))};
    *(LDSP f32x2*)(a_st + nh * 1024 + t * 128) = st;
  }
  if (nh) {
    *(LDSP f32x4*)(lds + K1_CS + (16 * mg + 4 * g) * 4) = c.acc[3][0];
    *(LDSP f32x4*)(lds + K1_CS + (16 * (mg + 4) + 4 * g) * 4) = c.acc[3][1];
  }
  asm volatile("s_waitcnt lgkmcnt(0)\n\ts_barrier" ::: "memory");
  {
    f32x2 p[2][2]; f32x4 cc[4];
#pragma unroll
    for (int t = 0; t < 2; ++t) { p[t][0] = *(LDSP const f32x2*)(a_st + t * 128); p[t][1] = *(LDSP const f32x2*)(a_st + 1024 + t * 128); }
#pragma unroll
    for (int j = 0; j < 4; ++j) cc[j] = *(LDSP const f32x4*)(a_cs + j * 64);
    SBAR();
#pragma unroll
    for (int t = 0; t < 2; ++t) {
      const float mean = (p[t][0][0] + p[t][1][0]) * (1.0f / DM);
      const float var = fmaxf((p[t][0][1] + p[t][1][1]) * (1.0f / DM) - mean * mean, 0.f);
      const float rstd = __builtin_amdgcn_rsqf(var + LN_EPS);
      if (cb == 0 && nh == 0) __builtin_amdgcn_raw_buffer_store_b32(__float_as_uint(mean), wrs, (unsigned)WS_XMEAN + (T0 + 32 * mg + 16 * t + m) * 4, 0, QKV_AUX);
#pragma unroll
      for (int j = 0; j < 4; ++j) {
        if (j < 3 || nh == 0) {
          const f32x4 a = (c.acc[j][t] - mean * cc[j]) * (rstd * qs[j]);
          *(LDSP u32x2*)(a_sw + t * (16 * K1_SP) + j * 32) = u32x2{cvtpk(a[0], a[1]), cvtpk(a[2], a[3])};
        }
      }
    }
  }
  if (cb >= 10) {
    asm volatile("s_waitcnt lgkmcnt(0)" ::: "memory");
    u32x4 v[2][2];
#pragma unroll
    for (int tt = 0; tt < 2; ++tt)
#pragma unroll
      for (int t = 0; t < 2; ++t) v[tt][t] = tr_frag(a_tr + t * (16 * K1_SP) + tt * 64, 8 * K1_SP);
    *(LDSP u32x4*)(lds + vone) = c.ones;
#pragma unroll
    for (int tt = 0; tt < 2; ++tt)
#pragma unroll
      for (int t = 0; t < 2; ++t) *(LDSP u32x4*)(lds + vo[tt] + t * 608) = v[tt][t];
  }
  STAMP(4);
  asm volatile("s_waitcnt lgkmcnt(0)\n\ts_barrier" ::: "memory");
  if (cb >= 10) {
    u32x4 v[4];
#pragma unroll
    for (int i = 0; i < 4; ++i) v[i] = ldsr16(lds + i * 8192 + tid * 16);
#pragma unroll
    for (int i = 0; i < 4; ++i) bst16<V_AUX>(v[i], wrs, vd[i], 0);
  }
  if (cb <= 10) {
    unsigned d[2][9];
#pragma unroll
    for (int it = 0; it < 2; ++it) {
      const int hd = (w >> 1) + 4 * it;
      LDSP const unsigned* sp = (LDSP const unsigned*)(a_g + 36 * (hd < 6 ? hd : 0));
#pragma unroll
      for (int i = 0; i < 9; ++i) d[it][i] = sp[i];
    }
    SBAR();
#pragma unroll
    for (int it = 0; it < 2; ++it) {
      bst16<QKV_AUX>(u32x4{d[it][0], d[it][1], d[it][2], d[it][3]}, wrs, um[it], 0);
      bst16<QKV_AUX>(u32x4{d[it][4], d[it][5], d[it][6], d[it][7]}, wrs, um[it] + 512, 0);
      __builtin_amdgcn_raw_buffer_store_b32(d[it][8], wrs, ut[it], 0, QKT_AUX);
    }
#pragma unroll
    for (int it = 0; it < 2; ++it) {
      const int hd = (w >> 1) + 4 * it, G = 6 * cb + hd;
      if (hd < 6 && G >= 32 && G < 64) {
        float n2 = 0.f;
#pragma unroll
        for (int i = 0; i < 9; ++i) n2 = dot2(d[it][i], d[it][i], n2);
        unsigned nb = __float_as_uint(n2);
        { auto r = __builtin_amdgcn_permlane32_swap(nb, nb, false, false); nb = max(r[0], r[1]); }
        { auto r = __builtin_amdgcn_permlane16_swap(nb, nb, false, false); nb = max(r[0], r[1]); }
        __builtin_amdgcn_raw_buffer_store_b32(nb, wrs, (unsigned)WS_KMAX + (((2 * rb + (w & 1)) * 32 + (G & 31)) * 16 + m) * 4, 0, QKV_AUX);
      }
    }
  }
  STAMP(5); STAMP_OUT(1, 6);
}

constexpr int K2_LDS = (int)KV_HEAD_B;
constexpr int NJ_W1 = 72 * 36, NJ_WD = 9 * 144, NJ_WO = 9 * 18, NJ_ALL = NJ_W1 + NJ_WD + NJ_WO;
struct PrepJob { const char* src; unsigned rs, c1, c2, voff, dst[4]; };
DI void prep_setup(PrepJob& j, int J, int lane, const u16* wo, const u16* wg, const u16* wu, const u16* wd) {
  if (J >= NJ_ALL) J = NJ_ALL - 1;
  const int g = (lane >> 2) & 3;
  unsigned lrow = 8 * g;
  j.c1 = 0; j.c2 = 0;
  if (J < NJ_W1) {
    const int sm = J / 72, tg = J - sm * 72;
    j.src = (const char*)((sm & 1) ? wu : wg) + (size_t)(sm >> 1) * (32 * FF * 2) + tg * 128;
    j.rs = FF * 2;
#pragma unroll
    for (int t = 0; t < 4; ++t) j.dst[t] = (unsigned)WS_W1 + (4 * tg + t) * 36864 + sm * 1024;
  } else if (J < NJ_W1 + NJ_WD) {
    const int q = J - NJ_W1, cc = q / 9, tg = q - cc * 9;
    j.src = (const char*)wd + (size_t)cc * (32 * DM * 2) + tg * 128;
    j.rs = DM * 2;
#pragma unroll
    for (int t = 0; t < 4; ++t) { const int nt = 4 * tg + t, nb = nt / 9; j.dst[t] = (unsigned)WS_WD + ((nb * 144 + cc) * 9 + (nt - nb * 9)) * 1024; }
  } else {
    const int q = J - NJ_W1 - NJ_WD, step = q / 9, tg = q - step * 9;
    j.rs = DM * 2;
    if (step < 16) { j.src = (const char*)wo + (size_t)step * (36 * DM * 2) + tg * 128; lrow = 18 * (g >> 1) + 4 * (g & 1); j.c2 = 4; }
    else { j.src = (const char*)wo + (size_t)(288 * (step - 16) + 16) * (DM * 2) + tg * 128; lrow = 72 * g; j.c1 = 16; }
#pragma unroll
    for (int t = 0; t < 4; ++t) { const int nt = 4 * tg + t, nb = nt / 3; j.dst[t] = (unsigned)WS_WO + ((nb * 18 + step) * 3 + (nt - nb * 3)) * 1024; }
  }
#pragma unroll
  for (int t = 0; t < 4; ++t) j.dst[t] = rfl(j.dst[t]);
  j.voff = lrow * j.rs + (lane >> 4) * 32 + (lane & 3) * 8;
}
template <int JJ>
DI void prep_load(const PrepJob& j, u32x2 (&r)[8]) {
  const char* b = j.src + (JJ + j.c1 * (JJ >> 1) + j.c2 * (JJ >> 2)) * j.rs;
  asm volatile("global_load_dwordx2 %0, %1, %2" : "=v"(r[JJ]) : "v"(j.voff), "s"(b));
}
DI void prep_load_all(const PrepJob& j, u32x2 (&r)[8]) {
  prep_load<0>(j, r); prep_load<1>(j, r); prep_load<2>(j, r); prep_load<3>(j, r); prep_load<4>(j, r); prep_load<5>(j, r); prep_load<6>(j, r); prep_load<7>(j, r);
}
DI void prep_store(const PrepJob& j, const u32x2 (&r)[8], __amdgpu_buffer_rsrc_t rs, unsigned so) {
  u32x4 u[4];
#pragma unroll
  for (int i = 0; i < 4; ++i) {
    u[0][i] = __builtin_amdgcn_perm(r[2 * i + 1][0], r[2 * i][0], 0x05040100u);
    u[1][i] = __builtin_amdgcn_perm(r[2 * i + 1][0], r[2 * i][0], 0x07060302u);
    u[2][i] = __builtin_amdgcn_perm(r[2 * i + 1][1], r[2 * i][1], 0x05040100u);
    u[3][i] = __builtin_amdgcn_perm(r[2 * i + 1][1], r[2 * i][1], 0x07060302u);
  }
#pragma unroll
  for (int i = 0; i < 4; ++i) {
    const auto a0 = __builtin_amdgcn_permlane32_swap(u[0][i], u[2][i], false, false);
    const auto a1 = __builtin_amdgcn_permlane32_swap(u[1][i], u[3][i], false, false);
    const auto b0 = __builtin_amdgcn_permlane16_swap(a0[0], a1[0], false, false);
    const auto b1 = __builtin_amdgcn_permlane16_swap(a0[1], a1[1], false, false);
    u[0][i] = b0[0]; u[1][i] = b0[1]; u[2][i] = b1[0]; u[3][i] = b1[1];
  }
#pragma unroll
  for (int t = 0; t < 4; ++t) bst16<PREP_AUX>(u[t], rs, so, j.dst[t]);
}
#define PIN8(r) "+v"(r[0]), "+v"(r[1]), "+v"(r[2]), "+v"(r[3]), "+v"(r[4]), "+v"(r[5]), "+v"(r[6]), "+v"(r[7])

#define A_MFMA_ACC(d, a, b) asm volatile("v_mfma_f32_32x32x16_bf16 %0, %1, %2, %0" : "+v"(d) : "v"(a), "v"(b))
#define AT_M(m)
#define AT_U
struct AttnC {
  LDSP const char *kmb, *ktb, *vb;
  unsigned kma[3], kta[3], va[3]; unsigned long long mk, mv;
  u32x4 qm, qt, kf, v0, v1, p1; u32x2 ktl;
  f32x16 negm, o, sa, sb;
};
constexpr int k_off(int T) { return (T >> 4) * (int)CHUNK_B + (T & 15) * 1024; }
constexpr int kt_off(int T) { return (T >> 4) * (int)CHUNK_B + ((T >> 1) & 7) * 256; }
constexpr int v_off(int T) { return (T >> 4) * (int)CHUNK_B + (T & 15) * 1216; }
#define AT_STMT(TXT)                                                                                                              \
  asm volatile(TXT : "+{v[2:17]}"(c.o), "+{v[34:49]}"(c.sa), "+{v[50:65]}"(c.sb), "+{v[66:69]}"(c.kf), "+{v[70:73]}"(c.p1),        \
               "+{v[84:87]}"(c.v1), "+{v[88:91]}"(c.v0), "+{v[92:93]}"(c.ktl)                                                      \
               : "{v[18:33]}"(c.negm), "{v[98:101]}"(c.qm), "{v[102:105]}"(c.qt), [ka] "v"(c.kma[KO >> 16]), [kt] "v"(c.kta[KT >> 16]), \
                 [va] "v"(c.va[V1 >> 16]), [vb] "v"(c.va[V0 >> 16]), [ko] "i"(KO & 0xffff), [kto] "i"(KT & 0xffff), [vo1] "i"(V1 & 0xffff),  \
                 [vo0] "i"(V0 & 0xffff), [mk] "s"(c.mk), [mv] "s"(c.mv)                                                            \
               : "v74", "v75", "v76", "v77")
template <int T> DI void attn_tile(AttnC& c) {
  constexpr int KO = k_off(T < 62 ? T + 2 : 0), KT = kt_off(T < 62 ? T + 2 : 0), V1 = v_off(T) + 608, V0 = v_off(T < 63 ? T + 1 : 0);
  if constexpr (T == 0)
    AT_STMT("v_mfma_f32_32x32x8_bf16 v[50:65], v[92:93], v[104:105], v[18:33]\n\t" "v_exp_f32 v34, v34\n\t" "v_exp_f32 v35, v35\n\t" "v_exp_f32 v36, v36\n\t" "v_exp_f32 v37, v37\n\t" AT_M("mk") "ds_read_b64 v[92:93], %[kt] offset:%[kto]\n\t" AT_U AT_M("mv") "ds_read_b128 v[84:87], %[va] offset:%[vo1]\n\t" AT_U "v_cvt_pk_bf16_f32 v74, v34, v35\n\t" "v_exp_f32 v38, v38\n\t" "v_cvt_pk_bf16_f32 v75, v36, v37\n\t" "v_exp_f32 v39, v39\n\t" "v_exp_f32 v40, v40\n\t" "v_exp_f32 v41, v41\n\t" "s_waitcnt lgkmcnt(2)\n\t" "v_mfma_f32_32x32x16_bf16 v[50:65], v[66:69], v[98:101], v[50:65]\n\t" "ds_read_b128 v[66:69], %[ka] offset:%[ko]\n\t" "v_cvt_pk_bf16_f32 v76, v38, v39\n\t" "v_exp_f32 v42, v42\n\t" "v_cvt_pk_bf16_f32 v77, v40, v41\n\t" "v_exp_f32 v43, v43\n\t" "v_exp_f32 v44, v44\n\t" "v_exp_f32 v45, v45\n\t" "s_waitcnt lgkmcnt(2)\n\t" "v_mfma_f32_32x32x16_bf16 v[2:17], v[88:91], v[74:77], v[2:17]\n\t" AT_M("mv") "ds_read_b128 v[88:91], %[vb] offset:%[vo0]\n\t" AT_U "v_cvt_pk_bf16_f32 v70, v42, v43\n\t" "v_exp_f32 v46, v46\n\t" "v_cvt_pk_bf16_f32 v71, v44, v45\n\t" "v_exp_f32 v47, v47\n\t" "v_exp_f32 v48, v48\n\t" "v_exp_f32 v49, v49\n\t" "v_cvt_pk_bf16_f32 v72, v46, v47\n\t" "v_cvt_pk_bf16_f32 v73, v48, v49\n\t");
  else if constexpr (T == 62)
    AT_STMT("v_mfma_f32_32x32x8_bf16 v[50:65], v[92:93], v[104:105], v[18:33]\n\t" "v_exp_f32 v34, v34\n\t" "v_exp_f32 v35, v35\n\t" "v_exp_f32 v36, v36\n\t" "v_exp_f32 v37, v37\n\t" "s_waitcnt lgkmcnt(2)\n\t" "v_mfma_f32_32x32x16_bf16 v[2:17], v[84:87], v[70:73], v[2:17]\n\t" AT_M("mv") "ds_read_b128 v[84:87], %[va] offset:%[vo1]\n\t" AT_U "v_cvt_pk_bf16_f32 v74, v34, v35\n\t" "v_exp_f32 v38, v38\n\t" "v_cvt_pk_bf16_f32 v75, v36, v37\n\t" "v_exp_f32 v39, v39\n\t" "v_exp_f32 v40, v40\n\t" "v_exp_f32 v41, v41\n\t" "s_waitcnt lgkmcnt(2)\n\t" "v_mfma_f32_32x32x16_bf16 v[50:65], v[66:69], v[98:101], v[50:65]\n\t" "v_cvt_pk_bf16_f32 v76, v38, v39\n\t" "v_exp_f32 v42, v42\n\t" "v_cvt_pk_bf16_f32 v77, v40, v41\n\t" "v_exp_f32 v43, v43\n\t" "v_exp_f32 v44, v44\n\t" "v_exp_f32 v45, v45\n\t" "s_waitcnt lgkmcnt(1)\n\t" "v_mfma_f32_32x32x16_bf16 v[2:17], v[88:91], v[74:77], v[2:17]\n\t" AT_M("mv") "ds_read_b128 v[88:91], %[vb] offset:%[vo0]\n\t" AT_U "v_cvt_pk_bf16_f32 v70, v42, v43\n\t" "v_exp_f32 v46, v46\n\t" "v_cvt_pk_bf16_f32 v71, v44, v45\n\t" "v_exp_f32 v47, v47\n\t" "v_exp_f32 v48, v48\n\t" "v_exp_f32 v49, v49\n\t" "v_cvt_pk_bf16_f32 v72, v46, v47\n\t" "v_cvt_pk_bf16_f32 v73, v48, v49\n\t");
  else if constexpr (T == 63)
    AT_STMT("v_exp_f32 v50, v50\n\t" "v_exp_f32 v51, v51\n\t" "v_exp_f32 v52, v52\n\t" "v_exp_f32 v53, v53\n\t" "s_waitcnt lgkmcnt(0)\n\t" "v_mfma_f32_32x32x16_bf16 v[2:17], v[84:87], v[70:73], v[2:17]\n\t" AT_M("mv") "ds_read_b128 v[84:87], %[va] offset:%[vo1]\n\t" AT_U "v_cvt_pk_bf16_f32 v74, v50, v51\n\t" "v_exp_f32 v54, v54\n\t" "v_cvt_pk_bf16_f32 v75, v52, v53\n\t" "v_exp_f32 v55, v55\n\t" "v_exp_f32 v56, v56\n\t" "v_exp_f32 v57, v57\n\t" "v_cvt_pk_bf16_f32 v76, v54, v55\n\t" "v_exp_f32 v58, v58\n\t" "v_cvt_pk_bf16_f32 v77, v56, v57\n\t" "v_exp_f32 v59, v59\n\t" "v_exp_f32 v60, v60\n\t" "v_exp_f32 v61, v61\n\t" "s_waitcnt lgkmcnt(1)\n\t" "v_mfma_f32_32x32x16_bf16 v[2:17], v[88:91], v[74:77], v[2:17]\n\t" "v_cvt_pk_bf16_f32 v70, v58, v59\n\t" "v_exp_f32 v62, v62\n\t" "v_cvt_pk_bf16_f32 v71, v60, v61\n\t" "v_exp_f32 v63, v63\n\t" "v_exp_f32 v64, v64\n\t" "v_exp_f32 v65, v65\n\t" "v_cvt_pk_bf16_f32 v72, v62, v63\n\t" "v_cvt_pk_bf16_f32 v73, v64, v65\n\t");
  else if constexpr ((T & 3) == 0)
    AT_STMT("v_mfma_f32_32x32x8_bf16 v[50:65], v[92:93], v[104:105], v[18:33]\n\t" "v_exp_f32 v34, v34\n\t" "v_exp_f32 v35, v35\n\t" "v_exp_f32 v36, v36\n\t" "v_exp_f32 v37, v37\n\t" "s_waitcnt lgkmcnt(2)\n\t" "v_mfma_f32_32x32x16_bf16 v[2:17], v[84:87], v[70:73], v[2:17]\n\t" AT_M("mk") "ds_read_b64 v[92:93], %[kt] offset:%[kto]\n\t" AT_U AT_M("mv") "ds_read_b128 v[84:87], %[va] offset:%[vo1]\n\t" AT_U "v_cvt_pk_bf16_f32 v74, v34, v35\n\t" "v_exp_f32 v38, v38\n\t" "v_cvt_pk_bf16_f32 v75, v36, v37\n\t" "v_exp_f32 v39, v39\n\t" "v_exp_f32 v40, v40\n\t" "v_exp_f32 v41, v41\n\t" "s_waitcnt lgkmcnt(2)\n\t" "v_mfma_f32_32x32x16_bf16 v[50:65], v[66:69], v[98:101], v[50:65]\n\t" "ds_read_b128 v[66:69], %[ka] offset:%[ko]\n\t" "v_cvt_pk_bf16_f32 v76, v38, v39\n\t" "v_exp_f32 v42, v42\n\t" "v_cvt_pk_bf16_f32 v77, v40, v41\n\t" "v_exp_f32 v43, v43\n\t" "v_exp_f32 v44, v44\n\t" "v_exp_f32 v45, v45\n\t" "s_waitcnt lgkmcnt(2)\n\t" "v_mfma_f32_32x32x16_bf16 v[2:17], v[88:91], v[74:77], v[2:17]\n\t" AT_M("mv") "ds_read_b128 v[88:91], %[vb] offset:%[vo0]\n\t" AT_U "v_cvt_pk_bf16_f32 v70, v42, v43\n\t" "v_exp_f32 v46, v46\n\t" "v_cvt_pk_bf16_f32 v71, v44, v45\n\t" "v_exp_f32 v47, v47\n\t" "v_exp_f32 v48, v48\n\t" "v_exp_f32 v49, v49\n\t" "v_cvt_pk_bf16_f32 v72, v46, v47\n\t" "v_cvt_pk_bf16_f32 v73, v48, v49\n\t");
  else if constexpr ((T & 3) == 1)
    AT_STMT("v_mfma_f32_32x32x8_bf16 v[34:49], v[92:93], v[102:103], v[18:33]\n\t" "v_exp_f32 v50, v50\n\t" "v_exp_f32 v51, v51\n\t" "v_exp_f32 v52, v52\n\t" "v_exp_f32 v53, v53\n\t" "s_waitcnt lgkmcnt(2)\n\t" "v_mfma_f32_32x32x16_bf16 v[2:17], v[84:87], v[70:73], v[2:17]\n\t" AT_M("mv") "ds_read_b128 v[84:87], %[va] offset:%[vo1]\n\t" AT_U "v_cvt_pk_bf16_f32 v74, v50, v51\n\t" "v_exp_f32 v54, v54\n\t" "v_cvt_pk_bf16_f32 v75, v52, v53\n\t" "v_exp_f32 v55, v55\n\t" "v_exp_f32 v56, v56\n\t" "v_exp_f32 v57, v57\n\t" "s_waitcnt lgkmcnt(2)\n\t" "v_mfma_f32_32x32x16_bf16 v[34:49], v[66:69], v[98:101], v[34:49]\n\t" "ds_read_b128 v[66:69], %[ka] offset:%[ko]\n\t" "v_cvt_pk_bf16_f32 v76, v54, v55\n\t" "v_exp_f32 v58, v58\n\t" "v_cvt_pk_bf16_f32 v77, v56, v57\n\t" "v_exp_f32 v59, v59\n\t" "v_exp_f32 v60, v60\n\t" "v_exp_f32 v61, v61\n\t" "s_waitcnt lgkmcnt(2)\n\t" "v_mfma_f32_32x32x16_bf16 v[2:17], v[88:91], v[74:77], v[2:17]\n\t" AT_M("mv") "ds_read_b128 v[88:91], %[vb] offset:%[vo0]\n\t" AT_U "v_cvt_pk_bf16_f32 v70, v58, v59\n\t" "v_exp_f32 v62, v62\n\t" "v_cvt_pk_bf16_f32 v71, v60, v61\n\t" "v_exp_f32 v63, v63\n\t" "v_exp_f32 v64, v64\n\t" "v_exp_f32 v65, v65\n\t" "v_cvt_pk_bf16_f32 v72, v62, v63\n\t" "v_cvt_pk_bf16_f32 v73, v64, v65\n\t");
  else if constexpr ((T & 3) == 2)
    AT_STMT("v_mfma_f32_32x32x8_bf16 v[50:65], v[92:93], v[104:105], v[18:33]\n\t" "v_exp_f32 v34, v34\n\t" "v_exp_f32 v35, v35\n\t" "v_exp_f32 v36, v36\n\t" "v_exp_f32 v37, v37\n\t" "s_waitcnt lgkmcnt(2)\n\t" "v_mfma_f32_32x32x16_bf16 v[2:17], v[84:87], v[70:73], v[2:17]\n\t" AT_M("mk") "ds_read_b64 v[92:93], %[kt] offset:%[kto]\n\t" AT_U AT_M("mv") "ds_read_b128 v[84:87], %[va] offset:%[vo1]\n\t" AT_U "v_cvt_pk_bf16_f32 v74, v34, v35\n\t" "v_exp_f32 v38, v38\n\t" "v_cvt_pk_bf16_f32 v75, v36, v37\n\t" "v_exp_f32 v39, v39\n\t" "v_exp_f32 v40, v40\n\t" "v_exp_f32 v41, v41\n\t" "s_waitcnt lgkmcnt(2)\n\t" "v_mfma_f32_32x32x16_bf16 v[50:65], v[66:69], v[98:101], v[50:65]\n\t" "ds_read_b128 v[66:69], %[ka] offset:%[ko]\n\t" "v_cvt_pk_bf16_f32 v76, v38, v39\n\t" "v_exp_f32 v42, v42\n\t" "v_cvt_pk_bf16_f32 v77, v40, v41\n\t" "v_exp_f32 v43, v43\n\t" "v_exp_f32 v44, v44\n\t" "v_exp_f32 v45, v45\n\t" "s_waitcnt lgkmcnt(2)\n\t" "v_mfma_f32_32x32x16_bf16 v[2:17], v[88:91], v[74:77], v[2:17]\n\t" AT_M("mv") "ds_read_b128 v[88:91], %[vb] offset:%[vo0]\n\t" AT_U "v_cvt_pk_bf16_f32 v70, v42, v43\n\t" "v_exp_f32 v46, v46\n\t" "v_cvt_pk_bf16_f32 v71, v44, v45\n\t" "v_exp_f32 v47, v47\n\t" "v_exp_f32 v48, v48\n\t" "v_exp_f32 v49, v49\n\t" "v_cvt_pk_bf16_f32 v72, v46, v47\n\t" "v_cvt_pk_bf16_f32 v73, v48, v49\n\t");
  else if constexpr ((T & 3) == 3)
    AT_STMT("v_mfma_f32_32x32x8_bf16 v[34:49], v[92:93], v[102:103], v[18:33]\n\t" "v_exp_f32 v50, v50\n\t" "v_exp_f32 v51, v51\n\t" "v_exp_f32 v52, v52\n\t" "v_exp_f32 v53, v53\n\t" "s_waitcnt lgkmcnt(2)\n\t" "v_mfma_f32_32x32x16_bf16 v[2:17], v[84:87], v[70:73], v[2:17]\n\t" AT_M("mv") "ds_read_b128 v[84:87], %[va] offset:%[vo1]\n\t" AT_U "v_cvt_pk_bf16_f32 v74, v50, v51\n\t" "v_exp_f32 v54, v54\n\t" "v_cvt_pk_bf16_f32 v75, v52, v53\n\t" "v_exp_f32 v55, v55\n\t" "v_exp_f32 v56, v56\n\t" "v_exp_f32 v57, v57\n\t" "s_waitcnt lgkmcnt(2)\n\t" "v_mfma_f32_32x32x16_bf16 v[34:49], v[66:69], v[98:101], v[34:49]\n\t" "ds_read_b128 v[66:69], %[ka] offset:%[ko]\n\t" "v_cvt_pk_bf16_f32 v76, v54, v55\n\t" "v_exp_f32 v58, v58\n\t" "v_cvt_pk_bf16_f32 v77, v56, v57\n\t" "v_exp_f32 v59, v59\n\t" "v_exp_f32 v60, v60\n\t" "v_exp_f32 v61, v61\n\t" "s_waitcnt lgkmcnt(2)\n\t" "v_mfma_f32_32x32x16_bf16 v[2:17], v[88:91], v[74:77], v[2:17]\n\t" AT_M("mv") "ds_read_b128 v[88:91], %[vb] offset:%[vo0]\n\t" AT_U "v_cvt_pk_bf16_f32 v70, v58, v59\n\t" "v_exp_f32 v62, v62\n\t" "v_cvt_pk_bf16_f32 v71, v60, v61\n\t" "v_exp_f32 v63, v63\n\t" "v_exp_f32 v64, v64\n\t" "v_exp_f32 v65, v65\n\t" "v_cvt_pk_bf16_f32 v72, v62, v63\n\t" "v_cvt_pk_bf16_f32 v73, v64, v65\n\t");
}
DI void attn_group_slow(LDSP const char* km, LDSP const char* kt, LDSP const char* vp, const u32x4& qm, unsigned qtl, const f32x16& negm, f32x16& o) {
#pragma unroll
  for (int t = 0; t < 4; ++t) {
    const u32x2 k2 = *(LDSP const u32x2*)(kt + (t >> 1) * 256);
    const u32x4 ktl = {k2[0], k2[1], 0, 0};
    const u32x4 kf = ldsr16(km + t * 1024);
    const u32x4 v0 = ldsr16(vp + t * 1216), v1 = ldsr16(vp + t * 1216 + 608);
    u32x4 qq = {0, 0, 0, 0}; qq[t & 1] = qtl;
    f32x16 s = mfma32(ktl, qq, negm);
    s = mfma32(kf, qm, s);
    u32x4 p0, p1;
#pragma unroll
    for (int i = 0; i < 4; ++i) {
      p0[i] = cvtpk(__builtin_amdgcn_exp2f(s[2 * i]), __builtin_amdgcn_exp2f(s[2 * i + 1]));
      p1[i] = cvtpk(__builtin_amdgcn_exp2f(s[8 + 2 * i]), __builtin_amdgcn_exp2f(s[8 + 2 * i + 1]));
    }
    o = mfma32(v0, p0, o);
    o = mfma32(v1, p1, o);
  }
}
template <int G> DI void attn_group(AttnC& c) {
  __builtin_amdgcn_s_setprio(G < 5 ? 3 - (G & 3) : (G < 8 ? 3 : (G < 11 ? 2 : (G < 14 ? 1 : 0))));
  attn_tile<4 * G + 0>(c); attn_tile<4 * G + 1>(c); attn_tile<4 * G + 2>(c); attn_tile<4 * G + 3>(c);
}
DI void attn_helper(const u16* x, const u16* wo, const u16* wg, const u16* wu, const u16* wd, char* ws, int head, int j, int lane, unsigned lds0) {
  __builtin_amdgcn_s_setprio(HELP_PRIO);
  const char* kv = ws + WS_KV + (size_t)head * KV_HEAD_B;
  const unsigned lane16 = lane * 16;
#define KV_DMA(o) dma16(kv + (o), lane16, lds0 + (o))
#pragma unroll
  for (int i = 0; i < 4; ++i) KV_DMA(((i >> 1) * 16 + (i & 1) * 4 + j) * 1024);
#pragma unroll
  for (int i = 0; i < 6; ++i) { const int p = i < 2 ? 8 + 4 * i + j : min(16 + 4 * i + j, 36); KV_DMA(p * 1024); }
  const int hw = blockIdx.x * 4 + j;
  PrepJob jb[4];
#pragma unroll
  for (int i = 0; i < 4; ++i) { prep_setup(jb[i], 4 * hw + i, lane, wo, wg, wu, wd); PIN(jb[i].voff); }
  SBAR();
  asm volatile("s_waitcnt vmcnt(6)\n\ts_barrier" ::: "memory");
#pragma unroll
  for (int i = 0; i < 10; ++i) KV_DMA((unsigned)CHUNK_B + min(4 * i + j, 36) * 1024);
  asm volatile("s_waitcnt vmcnt(10)\n\ts_barrier" ::: "memory");
#pragma unroll
  for (int i = 0; i < 19; ++i) KV_DMA(2 * (unsigned)CHUNK_B + min(4 * i + j, 73) * 1024);
#undef KV_DMA
  u32x2 r[4][8];
  u32x4 xu[3];
  unsigned wr[5], pf;
  asm volatile("s_waitcnt vmcnt(19)\n\ts_barrier" ::: "memory");
  asm volatile("s_waitcnt vmcnt(0)\n\ts_barrier" ::: "memory");
  prep_load_all(jb[0], r[0]); prep_load_all(jb[1], r[1]);
  const unsigned xl = (lane >> 3) * 1152 + (lane & 7) * 16, xs = (lane & 7) * 256 + (lane >> 3) * 16;
  int xj[3];
#pragma unroll
  for (int i = 0; i < 3; ++i) { xj[i] = i < 2 ? 2 * hw + i : (hw < 256 ? 2048 + hw : 2 * hw); xu[i] = gld16((const char*)x + ((xj[i] / 9) * (8 * 1152) + (xj[i] % 9) * 128), xl); }
  const int br = hw < DM ? hw : DM - 1;
  {
    const char* b = (const char*)wo + (size_t)wo_row(br >> 5, (br >> 3) & 3, br & 7) * (DM * 2);
#pragma unroll
    for (int i = 0; i < 5; ++i) wr[i] = gld4(b, (i < 4 ? lane + 64 * i : 256 + (lane & 31)) * 4);
  }
  prep_load_all(jb[2], r[2]); prep_load_all(jb[3], r[3]);
  CODE_PF2(pf, k_oproj, k_end, 0, j, (blockIdx.x >> 3) == 0 ? 128u : 0u);
  const __amdgpu_buffer_rsrc_t wrs = mkrsrc(ws, (unsigned)WS_END);
  const unsigned pso = ((lane >> 2) & 3) * 256 + ((lane & 3) * 4 + (lane >> 4)) * 16;
  asm volatile("s_waitcnt vmcnt(33)" : PIN8(r[0])::"memory");
  prep_store(jb[0], r[0], wrs, pso);
  asm volatile("s_waitcnt vmcnt(29)" : PIN8(r[1])::"memory");
  prep_store(jb[1], r[1], wrs, pso);
  asm volatile("s_waitcnt vmcnt(25)" : "+v"(xu[0]), "+v"(xu[1]), "+v"(xu[2]), "+v"(wr[0]), "+v"(wr[1]), "+v"(wr[2]), "+v"(wr[3]), "+v"(wr[4])::"memory");
#pragma unroll
  for (int i = 0; i < 3; ++i) bst16<PREP_AUX>(xu[i], wrs, xs, (unsigned)WS_XU + (xj[i] / 18) * 18432 + (xj[i] % 9) * 2048 + ((xj[i] / 9) & 1) * 128);
  {
    float a = 0.f;
#pragma unroll
    for (int i = 0; i < 4; ++i) a = dot2(wr[i], 0x3f803f80u, a);
    a = dot2(lane >= 32 ? 0u : wr[4], 0x3f803f80u, a);
    a = xor16_sum(xor32_sum(a));
    a += __uint_as_float(__builtin_amdgcn_update_dpp(0u, __float_as_uint(a), 0x128, 0xf, 0xf, false));
    a += __uint_as_float(__builtin_amdgcn_update_dpp(0u, __float_as_uint(a), 0x124, 0xf, 0xf, false));
    a += __uint_as_float(__builtin_amdgcn_update_dpp(0u, __float_as_uint(a), 0x122, 0xf, 0xf, false));
    a += __uint_as_float(__builtin_amdgcn_update_dpp(0u, __float_as_uint(a), 0x121, 0xf, 0xf, false));
    a *= (1.0f / DM);
    const unsigned hi = cvtpk(a, 0.f) & 0xffffu;
    const unsigned lo = cvtpk(a - bflo(hi), 0.f) & 0xffffu;
    __builtin_amdgcn_raw_buffer_store_b16((u16)(lane ? lo : hi), wrs, lane < 2 ? (unsigned)WS_WBAR + (lane * DM + br) * 2 : 0x80000000u, 0, 0);
  }
  asm volatile("s_waitcnt vmcnt(21)" : PIN8(r[2])::"memory");
  prep_store(jb[2], r[2], wrs, pso);
  asm volatile("s_waitcnt vmcnt(16)" : PIN8(r[3]), "+v"(pf)::"memory");
  prep_store(jb[3], r[3], wrs, pso);
}
extern "C" __global__ __launch_bounds__(768, 3) void k_attn() {
  extern __shared__ __attribute__((aligned(16))) char smem[];
  cptr_t gp = (cptr_t)(uintptr_t)g_ptr;
  const u16 *x = (const u16*)gp[0], *wo = (const u16*)gp[1], *wg = (const u16*)gp[2], *wu = (const u16*)gp[3], *wd = (const u16*)gp[4]; char* ws = (char*)gp[5];
  asm volatile("" : "+s"(x), "+s"(wo), "+s"(wg), "+s"(wu), "+s"(wd), "+s"(ws));
  LDSP char* lds = (LDSP char*)smem;
  const int tid = threadIdx.x, lane = tid & 63, w = rfl(tid >> 6);
  const int xcd = blockIdx.x & 7, bi = blockIdx.x >> 3;
  const int head = 4 * xcd + (bi & 3), qb = bi >> 2;
  if (__builtin_expect(w >= 8, AT_HFIRST)) { attn_helper(x, wo, wg, wu, wd, ws, head, w - 8, lane, (unsigned)(uintptr_t)lds); return; }
  const int r = lane & 31, h = lane >> 5;
  const int qt32 = 8 * qb + w;
  STAMP_DECL; STAMP(0);
  AttnC c;
  c.qm = gld16(ws + WS_QM, head * 65536 + qt32 * 1024 + lane * 16);
  unsigned qtl = gld4(ws + WS_QT, head * 8192 + (32 * qt32 + r) * 4);
  u32x4 km0 = gld16(ws + WS_KMAX, ((lane >> 1) * 32 + head) * 64 + (lane & 1) * 32);
  u32x4 km1 = gld16(ws + WS_KMAX, ((lane >> 1) * 32 + head) * 64 + (lane & 1) * 32 + 16);
  const int vrow = r < 19 ? r : ((r >= 20 && r < 28) ? 0 : 18);
  c.kmb = lds + lane * 16;
  c.ktb = lds + 16384 + r * 8;
  c.vb = lds + 18432 + h * 304 + vrow * 16;
#pragma unroll
  for (int i = 0; i < 3; ++i) {
    c.kma[i] = (unsigned)(uintptr_t)c.kmb + i * 65536; c.kta[i] = (unsigned)(uintptr_t)c.ktb + i * 65536; c.va[i] = (unsigned)(uintptr_t)c.vb + i * 65536;
    PIN(c.kma[i]); PIN(c.kta[i]); PIN(c.va[i]);
  }
  c.mk = 0x00000000ffffffffull; c.mv = 0x0007ffff0007ffffull;
  c.ktl = u32x2{0, 0}; c.v0 = u32x4{0, 0, 0, 0}; c.v1 = c.v0; PIN(c.ktl); PIN(c.v0); PIN(c.v1);
#pragma unroll
  for (int i = 0; i < 16; ++i) c.o[i] = 0.f;
  PIN(c.o);
  const __amdgpu_buffer_rsrc_t wrs = mkrsrc(ws, (unsigned)WS_END);
  SBAR();
  asm volatile("s_waitcnt vmcnt(0)" : "+v"(c.qm), "+v"(qtl), "+v"(km0), "+v"(km1)::"memory");
  if (h) qtl = 0;
  float q2 = dot2(qtl, qtl, 0.f);
#pragma unroll
  for (int i = 0; i < 4; ++i) q2 = dot2(c.qm[i], c.qm[i], q2);
  unsigned k2 = max(max(max(km0[0], km0[1]), max(km0[2], km0[3])), max(max(km1[0], km1[1]), max(km1[2], km1[3])));
  { auto rr = __builtin_amdgcn_permlane32_swap(k2, k2, false, false); k2 = max(rr[0], rr[1]); }
  { auto rr = __builtin_amdgcn_permlane16_swap(k2, k2, false, false); k2 = max(rr[0], rr[1]); }
  k2 = max(k2, (unsigned)__builtin_amdgcn_update_dpp(0u, k2, 0x128, 0xf, 0xf, true));
  k2 = max(k2, (unsigned)__builtin_amdgcn_update_dpp(0u, k2, 0x124, 0xf, 0xf, true));
  k2 = max(k2, (unsigned)__builtin_amdgcn_update_dpp(0u, k2, 0x122, 0xf, 0xf, true));
  k2 = max(k2, (unsigned)__builtin_amdgcn_update_dpp(0u, k2, 0x121, 0xf, 0xf, true));
  q2 = xor32_sum(q2);
  const float mshift = __builtin_amdgcn_sqrtf(q2 * __uint_as_float(k2)) * 1.001f;
  c.qt = u32x4{qtl, 0, 0, qtl}; PIN(c.qt);
#pragma unroll
  for (int i = 0; i < 16; ++i) c.negm[i] = -mshift;
  PIN(c.negm);
  STAMP(1);
  asm volatile("s_barrier" ::: "memory");
  STAMP(2);
  asm volatile(AT_M("mk") "ds_read_b64 v[92:93], %[kt]\n\t" AT_U "ds_read_b128 v[70:73], %[ka]\n\tds_read_b128 v[66:69], %[ka] offset:1024\n\t"
               AT_M("mv") "ds_read_b128 v[88:91], %[va]\n\t" AT_U "s_waitcnt lgkmcnt(2)\n\t"
               "v_mfma_f32_32x32x8_bf16 v[34:49], v[92:93], v[102:103], v[18:33]\n\t"
               "v_mfma_f32_32x32x16_bf16 v[34:49], v[70:73], v[98:101], v[34:49]\n\ts_nop 11"
               : "={v[34:49]}"(c.sa), "={v[50:65]}"(c.sb), "={v[66:69]}"(c.kf), "={v[70:73]}"(c.p1), "+{v[88:91]}"(c.v0), "+{v[92:93]}"(c.ktl)
               : "{v[18:33]}"(c.negm), "{v[98:101]}"(c.qm), "{v[102:105]}"(c.qt), [ka] "v"(c.kma[0]), [kt] "v"(c.kta[0]), [va] "v"(c.va[0]),
                 [mk] "s"(c.mk), [mv] "s"(c.mv));
  attn_group<0>(c);
  asm volatile("s_barrier" ::: "memory");
  attn_group<1>(c); attn_group<2>(c);
  asm volatile("s_barrier" ::: "memory");
  attn_group<3>(c); attn_group<4>(c);
  asm volatile("s_barrier" ::: "memory");
  STAMP(3);
  attn_group<5>(c); attn_group<6>(c); attn_group<7>(c); attn_group<8>(c); attn_group<9>(c); attn_group<10>(c);
  attn_group<11>(c); attn_group<12>(c); attn_group<13>(c); attn_group<14>(c); attn_group<15>(c);
  asm volatile("s_waitcnt lgkmcnt(0)\n\tv_mfma_f32_32x32x16_bf16 v[2:17], v[84:87], v[70:73], v[2:17]\n\ts_nop 11"
               : "+{v[2:17]}"(c.o) : "{v[84:87]}"(c.v1), "{v[70:73]}"(c.p1), "{v[18:33]}"(c.negm));
  STAMP(4);
  f32x16 o = c.o;
  float l;
  { auto rr = __builtin_amdgcn_permlane32_swap(__float_as_uint(o[10]), __float_as_uint(o[10]), false, false); l = __uint_as_float(rr[0]); }
  if (__builtin_expect(__any(!(l >= 5.421011e-20f)), 0)) {
    float mx = -INFINITY;
    f32x16 z, negm;
#pragma unroll
    for (int i = 0; i < 16; ++i) z[i] = 0.f;
#pragma unroll 1
    for (int tt = 0; tt < 64; ++tt) {
      const int ck = tt >> 4, tl = tt & 15;
      const u32x2 k2 = *(LDSP const u32x2*)(c.ktb + ck * CHUNK_B + (tl >> 1) * 256);
      const u32x4 ktl = {k2[0], k2[1], 0, 0};
      const u32x4 kf = ldsr16(c.kmb + ck * CHUNK_B + tl * 1024);
      const u32x4 qq = {(tl & 1) ? 0u : qtl, (tl & 1) ? qtl : 0u, 0, 0};
      f32x16 sc = mfma32(ktl, qq, z);
      sc = mfma32(kf, c.qm, sc);
#pragma unroll
      for (int i = 0; i < 16; ++i) mx = fmaxf(mx, sc[i]);
    }
    mx = xor32_max(mx);
#pragma unroll
    for (int i = 0; i < 16; ++i) { negm[i] = -mx; o[i] = 0.f; }
#pragma unroll 1
    for (int gq = 0; gq < 16; ++gq) {
      const int ck = gq >> 2, gg = gq & 3;
      attn_group_slow(c.kmb + ck * CHUNK_B + gg * 4096, c.ktb + ck * CHUNK_B + gg * 512, c.vb + ck * CHUNK_B + gg * 4864, c.qm, qtl, negm, o);
    }
    auto rr = __builtin_amdgcn_permlane32_swap(__float_as_uint(o[10]), __float_as_uint(o[10]), false, false);
    l = __uint_as_float(rr[0]);
  }
  const float rl = 1.0f / l;
  u32x4 ou;
#pragma unroll
  for (int i = 0; i < 4; ++i) ou[i] = cvtpk(o[2 * i] * rl, o[2 * i + 1] * rl);
  const int q = 32 * qt32 + r;
  const unsigned ob = (unsigned)WS_OFR + (q >> 4) * 18432 + (q & 15) * 16;
  bst16<O_AUX>(ou, wrs, ob + (head >> 1) * 1024 + (2 * (head & 1) + h) * 256, 0);
  __builtin_amdgcn_raw_buffer_store_b32(cvtpk(o[8] * rl, o[9] * rl), wrs, h ? 0x80000000u : ob + (16 + (head >> 4)) * 1024 + ((head >> 2) & 3) * 256 + (head & 3) * 4, 0, O_AUX);
  STAMP(5); STAMP_OUT(2, 6);
}

constexpr int K3_WBAR = 54 * 1024, K3_O = K3_WBAR + 3072, K3_LDS = K3_O + 96 * 1024;
extern "C" __global__ __launch_bounds__(768, 3) void k_oproj() {
  extern __shared__ __attribute__((aligned(16))) char smem[];
  char* ws = (char*)((cptr_t)(uintptr_t)g_ptr)[5];
  LDSP char* lds = (LDSP char*)smem;
  const unsigned lds0 = (unsigned)(uintptr_t)lds;
  const int tid = threadIdx.x, lane = tid & 63, w = rfl(tid >> 6);
  const int xcd = blockIdx.x & 7, bi = blockIdx.x >> 3;
  const int rb = 2 * xcd + (bi & 1), nb = bi >> 1;
  const unsigned lane16 = lane * 16;
  if (__builtin_expect(w >= 8, 1)) {
    const int j = w - 8;
    const char* wb = ws + WS_WO + (size_t)nb * 54 * 1024;
    const char* ob = ws + WS_OFR + (size_t)(8 * rb + 2 * j) * 18432;
    const unsigned od = lds0 + K3_O + j * 24576;
#define K3_W(z) _Pragma("unroll") for (int i = 0; i < 5; ++i) { const int p = 18 * (z) + min(4 * i + j, 17); dma16(wb + p * 1024, lane16, lds0 + p * 1024); }
#define K3_OS(z) _Pragma("unroll") for (int i = 0; i < 12; ++i) dma16(ob + ((i & 1) * 18 + 6 * (z) + (i >> 1)) * 1024, lane16, od + ((i & 1) * 12 + 6 * (z) - 6 + (i >> 1)) * 1024)
    K3_W(0);
    dma16(ws + WS_WBAR + min(j, 2) * 1024, lane16, lds0 + K3_WBAR + min(j, 2) * 1024);
    K3_W(1); K3_OS(1);
    asm volatile("s_waitcnt vmcnt(17)\n\ts_barrier" ::: "memory");
    K3_W(2); K3_OS(2);
    asm volatile("s_waitcnt vmcnt(17)\n\ts_barrier" ::: "memory");
    asm volatile("s_waitcnt vmcnt(0)\n\ts_barrier" ::: "memory");
#undef K3_W
#undef K3_OS
    return;
  }
  const int m = lane & 15, g = lane >> 4;
  const int mt = 8 * rb + w, row = 16 * mt + m;
  STAMP_DECL; STAMP(0);
  u32x4 O[18];
  u32x2 xr[3];
  {
    const char* ob = ws + WS_OFR + (size_t)mt * 18432;
#pragma unroll
    for (int i = 0; i < 6; ++i) O[i] = gld16(ob + i * 1024, lane16);
  }
  const unsigned uo = mt * 18432 + (6 * nb + (g >> 1)) * 256 + m * 16 + (g & 1) * 8;
  f32x4 acc[4];
  LDSP const char* wl = lds + lane16;
  LDSP const char* ol = lds + K3_O + w * 12288 + lane16; PIN(ol);
  LDSP const char* wbl = lds + K3_WBAR + (m & 1) * 1152 + g * 16;
  const __amdgpu_buffer_rsrc_t wrs = mkrsrc(ws, (unsigned)WS_END);
  unsigned so = ((g & 1) ? (unsigned)WS_HC : (unsigned)WS_HU) + (uo & ~8u); PIN(so);
  STAMP(1);
  asm volatile("s_waitcnt vmcnt(0)\n\ts_barrier" : "+v"(O[0]), "+v"(O[1]), "+v"(O[2]), "+v"(O[3]), "+v"(O[4]), "+v"(O[5])::"memory");
#pragma unroll
  for (int t = 0; t < 3; ++t)
    asm volatile("global_load_dwordx2 %0, %1, %2 offset:%3" : "=v"(xr[t]) : "v"(uo), "s"(ws + WS_XU), "i"(t * 512));
  unsigned xm = gld4(ws + WS_XMEAN, row * 4);
  u32x4 wf[6];
#define K3_RD(f) (((f) & 3) == 3 ? ldsr16(wbl + ((f) >> 2) * 64) : ldsr16(wl + (3 * ((f) >> 2) + ((f) & 3)) * 1024))
#pragma unroll
  for (int f = 0; f < 5; ++f) wf[f] = K3_RD(f);
#pragma unroll
  for (int f = 0; f < 72; ++f) {
    if (f == 19 || f == 43) {
      asm volatile("s_barrier" ::: "memory");
#pragma unroll
      for (int i = 0; i < 6; ++i) O[(f + 5) / 4 + i] = ldsr16(ol + ((f + 5) / 4 + i - 6) * 1024);
    }
    if (f + 5 < 72) wf[(f + 5) % 6] = K3_RD(f + 5);
    if (f < 4) G_MFMA0(acc[f & 3], wf[f % 6], O[f >> 2]);
    else G_MFMA(acc[f & 3], wf[f % 6], O[f >> 2]);
    SBAR();
  }
  asm volatile("s_waitcnt vmcnt(0)" : "+v"(xr[0]), "+v"(xr[1]), "+v"(xr[2]), "+v"(xm));
  asm volatile("s_nop 7" : "+v"(acc[0]), "+v"(acc[1]), "+v"(acc[2]), "+v"(acc[3]));
#undef K3_RD
  STAMP(2);
  const float mean = __uint_as_float(xm) + acc[3][0] + acc[3][1];
  float ssq = 0.f;
#pragma unroll
  for (int t = 0; t < 3; ++t) {
    const unsigned h0 = cvtpk(bflo(xr[t][0]) + acc[t][0], bfhi(xr[t][0]) + acc[t][1]);
    const unsigned h1 = cvtpk(bflo(xr[t][1]) + acc[t][2], bfhi(xr[t][1]) + acc[t][3]);
    const unsigned c0 = cvtpk(bflo(h0) - mean, bfhi(h0) - mean);
    const unsigned c1 = cvtpk(bflo(h1) - mean, bfhi(h1) - mean);
    ssq = dot2(c0, c0, ssq); ssq = dot2(c1, c1, ssq);
    const auto r0 = __builtin_amdgcn_permlane16_swap(h0, c0, false, false);
    const auto r1 = __builtin_amdgcn_permlane16_swap(h1, c1, false, false);
    bst16<H_AUX>(u32x4{r0[0], r1[0], r0[1], r1[1]}, wrs, so + t * 512, 0);
  }
  ssq = xor32_sum(xor16_sum(ssq));
  __builtin_amdgcn_raw_buffer_store_b32(__float_as_uint(ssq), wrs, (unsigned)WS_SSQ + (nb * 2048 + row) * 4, 0, H_AUX);
  STAMP(3); STAMP_OUT(3, 4);
}

constexpr int K4_SLOT = 36864, K4_LDS = 163840;
#define K4_PFD 4
constexpr int K4_PF = K4_PFD;
typedef __attribute__((address_space(1))) unsigned gu32;
constexpr int K5_PERM[18] = {0, 1, 2, 6, 7, 8, 9, 10, 11, 15, 16, 17, 3, 4, 5, 12, 13, 14};
constexpr int K5_R[4] = {36864, 73728, 110592, 0};
constexpr int K5_OC[9] = {65536, 102400, 139264, 147456, 155648, 0, 8192, 16384, 24576};
struct K5P { const char* wb; unsigned q[4], qs[4], qo, qos, lds0; };
template <int Z> DI void k5_piece(const K5P& k, int i, int w, unsigned lane16) {
  constexpr int P0 = K5_PERM[2 * Z] * 9216, D = (K5_PERM[2 * Z + 1] - K5_PERM[2 * Z]) * 9216;
  if (i < 3 || (w >> 2) == (Z & 1))
    dma16(k.wb + (P0 + k.q[i] + k.qs[i] * D), lane16, k.lds0 + K5_R[Z & 3] + (i < 3 ? 8 * i + w : 24 + (w & 3)) * 1024);
}
template <int Z> DI void k5_own(const K5P& k, int w, unsigned lane16) {
  constexpr int P0 = K5_PERM[2 * Z] * 9216, D = (K5_PERM[2 * Z + 1] - K5_PERM[2 * Z]) * 9216;
  dma16(k.wb + (P0 + k.qo + k.qos * D), lane16, k.lds0 + K5_OC[Z] + w * 1024);
}
DI void wait_count(gu32* cnt, unsigned first, unsigned want) {
  if (rfl(first) >= want) return;
  for (unsigned spin = 0; spin < (1u << 16); ++spin) {
    if (rfl(__hip_atomic_load(cnt, __ATOMIC_RELAXED, __HIP_MEMORY_SCOPE_AGENT)) >= want) break;
    __builtin_amdgcn_s_sleep(1);
  }
}
#define POLL(dst, p) asm volatile("global_load_dword %0, %1, %2 sc1" : "=v"(dst) : "v"(0u), "s"(p))
struct K4C {
  const char *wb, *xb; unsigned lane16, lds0; int w, dir;
  unsigned pf0, pfo, pf1, poll; const char* pfb;
  K5P wd;
  gu32 *cntA, *cntB;
  const char *ab, *ab2; u32x4 A0[2][4];
  LDSP const char* wl[4];
  u32x4 X[2][18], wf[K4_PF + 1];
  f32x4 acc[4][4];
  float c1[2], c2[2];
  __amdgpu_buffer_rsrc_t ars; unsigned avo; int T0;
  float ea[2][2], et[2][2]; unsigned pk[2][4];
  f32x2 pa[2], pt[2], c1p[2], c2p[2];
};
DI void k4_piece(const K4C& c, int tile, int p) {
  dma16(c.wb + (tile * c.dir * K4_SLOT + p * 1024), c.lane16, c.lds0 + (tile & 3) * K4_SLOT + p * 1024);
}
template <int ZW> DI void k4_issue_w(K4C& c, int i) {
  if (ZW < 9) {
    if (i == 0) k4_piece(c, c.w >> 2, 4 * ZW + (c.w & 3));
    else if ((c.w >> 2) == (ZW & 1)) k4_piece(c, 2, 4 * ZW + (c.w & 3));
  } else if (ZW == 9) { k4_piece(c, 3, 8 * i + c.w); }
}
template <int Z, int I> DI void k4_issue1(K4C& c) {
  if (I == 0) k4_issue_w<Z + K4_WD>(c, 0);
  else if (I < 5) {
    c.X[(I - 1) >> 1][2 * Z + ((I - 1) & 1)] = gld16(c.xb + (((I - 1) >> 1) * 18432 + (2 * Z + ((I - 1) & 1)) * 1024), c.lane16);
  }
  else k4_issue_w<Z + K4_WD>(c, 1);
}
template <int TL, int I> DI void k4_issue2(K4C& c) {
  if (I < 4) k4_piece(c, TL, 8 * I + c.w);
  else if ((c.w >> 2) == (TL & 1)) k4_piece(c, TL, 32 + (c.w & 3));
}
template <int TL, int E, int U> DI void k4_epi(K4C& c) {
  constexpr int e = U >> 2, u = U & 3, mt = e >> 2, i = e & 3, q = e & 1;
  if constexpr (K4_PK && U < 32) {
    constexpr int pr = U >> 3, v = U & 7, m2 = pr >> 1, i0 = 2 * (pr & 1);
    if (v == 0) {
      const f32x2 G = {c.acc[TL & 3][m2][i0], c.acc[TL & 3][m2][i0 + 1]};
      asm volatile("v_pk_mul_f32 %0, %1, %2" : "=v"(c.pa[E]) : "v"(G), "v"(c.c1p[m2]));
    }
    if (v == 1) { const f32x2 Uu = {c.acc[TL & 3][2 + m2][i0], c.acc[TL & 3][2 + m2][i0 + 1]};
      asm volatile("v_pk_mul_f32 %0, %1, %2" : "=v"(c.pt[E]) : "v"(Uu), "v"(c.pa[E]));
      c.pa[E][0] = __builtin_amdgcn_exp2f(c.pa[E][0]); PIN(c.pa[E]); }
    if (v == 2) { c.pa[E][1] = __builtin_amdgcn_exp2f(c.pa[E][1]); PIN(c.pa[E]); }
    if (v == 3) asm volatile("v_pk_fma_f32 %0, %0, %1, %1" : "+v"(c.pa[E]) : "v"(c.c2p[m2]));
    if (v == 4) { c.pa[E][0] = __builtin_amdgcn_rcpf(c.pa[E][0]); PIN(c.pa[E]); }
    if (v == 5) { c.pa[E][1] = __builtin_amdgcn_rcpf(c.pa[E][1]); PIN(c.pa[E]); }
    if (v == 6) asm volatile("v_pk_mul_f32 %0, %0, %1" : "+v"(c.pt[E]) : "v"(c.pa[E]));
    if (v == 7) { c.pk[E][pr] = cvtpk(c.pt[E][0], c.pt[E][1]); PIN(c.pk[E][pr]); }
  } else if constexpr (U < 32) {
    if (u == 0) { const float a = c.acc[TL & 3][mt][i] * c.c1[mt]; c.ea[E][q] = a; c.et[E][q] = a * c.acc[TL & 3][2 + mt][i]; PIN(c.ea[E][q]); PIN(c.et[E][q]); }
    if (u == 1) { c.ea[E][q] = __builtin_amdgcn_exp2f(c.ea[E][q]); PIN(c.ea[E][q]); }
    if (u == 2) { c.ea[E][q] = __builtin_amdgcn_rcpf(__builtin_fmaf(c.ea[E][q], c.c2[mt], c.c2[mt])); PIN(c.ea[E][q]); }
    if (u == 3) { c.et[E][q] *= c.ea[E][q]; PIN(c.et[E][q]); if (q) { c.pk[E][e >> 1] = cvtpk(c.et[E][0], c.et[E][1]); PIN(c.pk[E][e >> 1]); } }
  } else if constexpr (U == 32) {
    auto r0 = __builtin_amdgcn_permlane16_swap(c.pk[E][0], c.pk[E][2], false, false);
    auto r1 = __builtin_amdgcn_permlane16_swap(c.pk[E][1], c.pk[E][3], false, false);
    c.pk[E][0] = r0[0]; c.pk[E][1] = r1[0]; c.pk[E][2] = r0[1]; c.pk[E][3] = r1[1];
  } else if constexpr (U == 33) {
    const int T = c.T0 + TL * c.dir;
    bst16<ACT_AUX>(u32x4{c.pk[E][0], c.pk[E][1], c.pk[E][2], c.pk[E][3]}, c.ars, c.avo, (T >> 1) * 1024 + (T & 1) * 512);
  }
}
constexpr int k4_tile(int F) { return F < 108 ? (F % 6) >> 1 : 3 + (F - 108) / 36; }
constexpr int k4_step(int F) { return F < 108 ? F / 6 : ((F - 108) % 36) >> 1; }
constexpr int k4_addr(int F) { return k4_step(F) * 2048 + (F & 1) * 1024; }
template <int F> DI void k4_frag(K4C& c) {
  constexpr int TL = k4_tile(F), S = k4_step(F), M = F & 1, N = F + K4_PF;
  const u32x4 a = c.wf[F % (K4_PF + 1)];
  if constexpr (F < 108) { if constexpr ((F + 4) % 3 == 0) __builtin_amdgcn_s_setprio(3 - ((F + 4) % 12) / 3); }
  else if constexpr ((F - 108 + 4) % 9 == 0) __builtin_amdgcn_s_setprio(3 - ((F - 108 + 4) % 36) / 9);
  if (S == 0) {
    G_MFMA0(c.acc[TL & 3][2 * M], a, c.X[0][S]);
    G_MFMA0(c.acc[TL & 3][2 * M + 1], a, c.X[1][S]);
  } else {
    constexpr int o = GRAY ? M : 0;
    G_MFMA(c.acc[TL & 3][2 * M + o], a, c.X[o][S]);
    if constexpr (K4_MID && F >= 108 && (F - 108) % 36 >= 2) {
      SBAR();
      k4_epi<(TL == 3 ? 0 : (TL == 4 ? 2 : TL - 1)), 0, (F - 108) % 36 - 2>(c);
      SBAR();
    }
    G_MFMA(c.acc[TL & 3][2 * M + 1 - o], a, c.X[1 - o][S]);
  }
  if constexpr (N < 108 && N % 12 == 0) {
    constexpr int Z = N / 12;
    if (Z < 8) asm volatile("s_waitcnt vmcnt(5)\n\ts_barrier" : "+v"(c.X[0][2 * Z]), "+v"(c.X[0][2 * Z + 1]), "+v"(c.X[1][2 * Z]), "+v"(c.X[1][2 * Z + 1])::"memory");
    else asm volatile("s_waitcnt vmcnt(2)\n\ts_barrier" : "+v"(c.X[0][2 * Z]), "+v"(c.X[0][2 * Z + 1]), "+v"(c.X[1][2 * Z]), "+v"(c.X[1][2 * Z + 1])::"memory");
  } else if constexpr (N >= 108 && N < 324 && (N - 108) % 36 == 0) {
    constexpr int NT = k4_tile(N);
    if (NT == 3) asm volatile("s_waitcnt vmcnt(0) lgkmcnt(0)\n\ts_barrier" ::: "memory");
    else if (NT == 8) {
      asm volatile("s_waitcnt vmcnt(0) lgkmcnt(0)\n\ts_barrier" ::: "memory");
      if (c.w == 0 && c.lane16 == 0) __hip_atomic_fetch_add(c.cntA, 1u, __ATOMIC_RELAXED, __HIP_MEMORY_SCOPE_AGENT);
    }
    else asm volatile("s_waitcnt vmcnt(4) lgkmcnt(0)\n\ts_barrier" ::: "memory");
  }
  if constexpr (N < 324) c.wf[N % (K4_PF + 1)] = ldsr16(c.wl[k4_tile(N) & 3] + k4_addr(N));
  if constexpr (F < 108) {
    constexpr int Z = F / 12, I = F % 12;
    if constexpr (Z == 0 && I < 4 && !(I & 1)) k4_issue1<2, I / 2>(c);
    if constexpr (Z < 7 && I < 8 && (I & 1)) k4_issue1<Z + 2, 2 + I / 2>(c);
    if constexpr (Z < 6 && I > 8 && (I & 1)) k4_issue1<Z + 3, (I - 9) / 2>(c);
    if constexpr (K4_WD == 0) {
      if constexpr (Z == 6 && I == 10) k4_issue2<3, 0>(c);
      if constexpr (Z == 7 && I < 8 && (I & 1)) k4_issue2<3, 1 + I / 2>(c);
    } else {
      if constexpr (Z == 6 && I == 10) k4_issue2<3, 2>(c);
      if constexpr (Z == 7 && (I == 1 || I == 3)) k4_issue2<3, 3 + I / 2>(c);
    }
  } else {
    constexpr int P = (F - 108) % 36;
    if constexpr (TL == 3) { if constexpr (P % 3 == 0 && P < 15) k4_issue2<4, P / 3>(c); if constexpr (P % 3 == 0 && P >= 15 && P < 30) k4_issue2<5, (P - 15) / 3>(c); }
    else if constexpr (TL + 2 < 9 && P % 4 == 0 && P < 20) k4_issue2<TL + 2, P / 4>(c);
    if constexpr (K4_PFWD && TL >= 4 && TL <= 7 && (P == 33 || (P == 34 && TL < 6))) {
      constexpr int tl = TL < 6 ? 2 * (TL - 4) + P - 33 : TL - 2;
      const int T = c.T0 + tl * c.dir;
      asm volatile("global_load_dword %0, %1, %2" : "+v"(c.pf0) : "v"(c.pfo), "s"(c.pfb + ((T >> 1) * 1024 + (T & 1) * 512)));
    }
    if constexpr (TL == 7 && P == 0) CODE_PF2(c.pf1, k_ffn, k_end, 0, 2 + (c.w & 1), ((blockIdx.x >> 3) == 0 && c.w < 2) ? 128u : 0u);
    if constexpr (TL == 7 && P % 3 == 2 && P <= 11) k5_piece<0>(c.wd, P / 3, c.w, c.lane16);
    if constexpr (TL == 8 && P < 4) k5_piece<1>(c.wd, P, c.w, c.lane16);
    if constexpr (TL == 8 && P >= 4 && P < 8) k5_piece<2>(c.wd, P - 4, c.w, c.lane16);
    if constexpr (TL == 8 && P == K4_POLL) POLL(c.poll, c.cntA);
    if constexpr (TL == 8 && P == K4_POLL + 12) { asm volatile("s_waitcnt vmcnt(0)" : "+v"(c.poll), "+v"(c.pf1), "+v"(c.pf0)); wait_count(c.cntA, c.poll, 8u); }
    if constexpr (TL == 8 && P > K4_POLL + 12 && P <= K4_POLL + 20) {
      constexpr int i = P - K4_POLL - 13, z = i >> 2, j = (i & 1) * 2 + ((i >> 1) & 1);
      asm volatile("global_load_dwordx4 %0, %1, %2 sc1" : "=v"(c.A0[z][j]) : "v"(c.lane16), "s"(((j >> 1) ? c.ab2 : c.ab) + K5_PERM[2 * z + (j & 1)] * 1024));
    }
    if constexpr (P >= 2) {
      if constexpr (TL == 3) { if (!K4_MID) k4_epi<0, 0, P - 2>(c); k4_epi<1, 1, P - 2>(c); }
      else if constexpr (TL == 4) { if (!K4_MID) k4_epi<2, 0, P - 2>(c); k4_epi<3, 1, P - 2>(c); }
      else if (!K4_MID) k4_epi<TL - 1, 0, P - 2>(c);
    }
  }
  SBAR();
}
template <int B, int... Fs> DI void k4_run(K4C& c, std::integer_sequence<int, Fs...>) { (k4_frag<B + Fs>(c), ...); }
DI void k4_last(K4C& c) {
  float a[8], t[8];
#pragma unroll
  for (int e = 0; e < 8; ++e) { a[e] = c.acc[0][e >> 2][e & 3] * c.c1[e >> 2]; t[e] = a[e] * c.acc[0][2 + (e >> 2)][e & 3]; }
#pragma unroll
  for (int e = 0; e < 8; ++e) a[e] = __builtin_amdgcn_exp2f(a[e]);
#pragma unroll
  for (int e = 0; e < 8; ++e) a[e] = __builtin_amdgcn_rcpf(__builtin_fmaf(a[e], c.c2[e >> 2], c.c2[e >> 2]));
#pragma unroll
  for (int e = 0; e < 4; ++e) c.pk[0][e] = cvtpk(t[2 * e] * a[2 * e], t[2 * e + 1] * a[2 * e + 1]);
  k4_epi<8, 0, 32>(c); k4_epi<8, 0, 33>(c);
}
DI f32x4 addbf(f32x4 a, unsigned lo, unsigned hi) { return a + f32x4{bflo(lo), bfhi(lo), bflo(hi), bfhi(hi)}; }
struct K5C {
  const char *ab, *ab2;
  LDSP const char *ra[4], *oa[3], *xr;
  LDSP char* xw;
  u32x4 A[9][4], wf[5];
  f32x4 acc[2][7], own[2][2], v[4];
  const char* hb; unsigned ho; int kq; u32x2 hr[3];
  __amdgpu_buffer_rsrc_t wrs; unsigned tw, p8s;
  gu32* cnt;
};
template <int Z, int J> DI void k5_issue(K5C& c, const K4C& k) {
  if constexpr (Z < 7 && J < 4) asm volatile("global_load_dwordx4 %0, %1, %2 sc1"
 : "=v"(c.A[Z + 2][J]) : "v"(k.lane16), "s"(((J >> 1) ? c.ab2 : c.ab) + K5_PERM[2 * (Z + 2) + (J & 1)] * 1024));
  if constexpr (Z < 6 && J >= 4) k5_piece<Z + 3>(k.wd, J - 4, k.w, k.lane16);
  if constexpr (Z == 6 && J >= 4) k5_own<J - 4>(k.wd, k.w, k.lane16);
  if constexpr (Z == 7 && J == 0) k5_own<4>(k.wd, k.w, k.lane16);
  if constexpr (Z == 7 && J == 1) asm volatile("global_load_dwordx2 %0, %1, %2" : "=v"(c.hr[0]) : "v"(c.ho), "s"(c.hb + c.kq * 1024));
  if constexpr (Z == 7 && J == 2) asm volatile("global_load_dwordx2 %0, %1, %2 offset:512" : "=v"(c.hr[1]) : "v"(c.ho), "s"(c.hb + c.kq * 1024));
  if constexpr (Z == 7 && J == 3) asm volatile("global_load_dwordx2 %0, %1, %2" : "=v"(c.hr[2]) : "v"(c.ho), "s"(c.hb + 4096));
  if constexpr (Z == 8 && J < 4) k5_own<5 + J>(k.wd, k.w, k.lane16);
}
template <int N> DI u32x4 k5_read(const K5C& c) {
  if constexpr (N < 126) return ldsr16(c.ra[(N / 14) & 3] + (N % 14) * 1024);
  else { constexpr int o = K5_OC[(N - 126) / 4] + ((N - 126) & 3) * 1024; return ldsr16(c.oa[o >> 16] + (o & 0xffff)); }
}
template <int F> DI void k5_frag(K5C& c, K4C& k) {
  constexpr int N = F + 4;
  const u32x4 a = c.wf[F % 5];
  if constexpr (F < 126) {
    constexpr int Z = F / 14, f = F % 14, st = f / 7, j = f % 7;
    if (F < 7) { G_MFMA0(c.acc[0][j], a, c.A[Z][st]); G_MFMA0(c.acc[1][j], a, c.A[Z][2 + st]); }
    else { G_MFMA(c.acc[1][j], a, c.A[Z][2 + st]); G_MFMA(c.acc[0][j], a, c.A[Z][st]); }
    if constexpr (f == 10 && Z == 8) asm volatile("s_waitcnt vmcnt(4)\n\ts_barrier" : "+v"(c.hr[0]), "+v"(c.hr[1]), "+v"(c.hr[2])::"memory");
    if constexpr (f == 10 && Z < 8) {
      u32x4(&A)[4] = c.A[Z + 1];
      if (Z == 0) asm volatile("s_waitcnt vmcnt(9) lgkmcnt(0)\n\ts_barrier" : "+v"(A[0]), "+v"(A[1]), "+v"(A[2]), "+v"(A[3])::"memory");
      else if (Z == 3) asm volatile("s_waitcnt vmcnt(4) lgkmcnt(0)\n\ts_barrier" : "+v"(A[0]), "+v"(A[1]), "+v"(A[2]), "+v"(A[3]), "+v"(k.poll)::"memory");
      else if (Z < 7) asm volatile("s_waitcnt vmcnt(7) lgkmcnt(0)\n\ts_barrier" : "+v"(A[0]), "+v"(A[1]), "+v"(A[2]), "+v"(A[3])::"memory");
      else asm volatile("s_waitcnt vmcnt(8) lgkmcnt(0)\n\ts_barrier" : "+v"(A[0]), "+v"(A[1]), "+v"(A[2]), "+v"(A[3])::"memory");
      if (Z == 1 && k.w == 0 && k.lane16 == 0) __hip_atomic_fetch_add(k.cntB, 1u, __ATOMIC_RELAXED, __HIP_MEMORY_SCOPE_AGENT);
      if (Z == 3) wait_count(k.cntB, k.poll, 8u);
    }
    c.wf[N % 5] = k5_read<N>(c);
    if constexpr (Z == 3 && f == 1) POLL(k.poll, k.cntB);
    constexpr int slot = f == 0 ? 2 : (f == 2 ? 3 : (f == 4 ? 4 : (f == 5 ? 5 : (f == 7 ? 6 : (f == 9 ? 7 : -1)))));
    if constexpr (F == 0) { k5_issue<0, 0>(c, k); k5_issue<0, 1>(c, k); }
    if constexpr (slot >= 0) k5_issue<Z, slot>(c, k);
    if constexpr (Z < 8 && f > 10 && (f & 1)) k5_issue<Z + 1, (f - 11) / 2>(c, k);
  } else {
    constexpr int B = F - 126, z = B / 4, st = (B >> 1) & 1, t = B & 1;
    if (B < 2) { G_MFMA0(c.own[0][t], a, c.A[z][st]); G_MFMA0(c.own[1][t], a, c.A[z][2 + st]); }
    else { G_MFMA(c.own[1][t], a, c.A[z][2 + st]); G_MFMA(c.own[0][t], a, c.A[z][st]); }
    if constexpr (F == 131) asm volatile("s_waitcnt vmcnt(0) lgkmcnt(0)\n\ts_barrier" ::: "memory");
    if constexpr (F == K5_SIG) {
      asm volatile("s_waitcnt vmcnt(0)\n\ts_barrier" ::: "memory");
      if (k.w == 0 && k.lane16 == 0) __hip_atomic_fetch_add(c.cnt, 1u, __ATOMIC_RELAXED, __HIP_MEMORY_SCOPE_AGENT);
    }
    if constexpr (N < 162) c.wf[N % 5] = k5_read<N>(c);
  }
  if constexpr (F >= 123 && F < 130) { PIN(c.acc[1][F - 123]); *(LDSP f32x4*)(c.xw + (F - 123) * 1024) = c.acc[1][F - 123]; }
  if constexpr (F == 131) { _Pragma("unroll") for (int i = 0; i < 4; ++i) c.v[i] = *(LDSP const f32x4*)(c.xr + i * 1024); }
  if constexpr (F == 133 || F == 135 || F == 138) {
    constexpr int j = F == 133 ? 0 : (F == 135 ? 1 : 2), o = 2 * (j & 1);
    const f32x4 x = c.acc[0][2 * j] + c.v[o], y = c.acc[0][2 * j + 1] + c.v[o + 1];
    bst16<16>(u32x4{cvtpk(x[0], x[1]), cvtpk(x[2], x[3]), cvtpk(y[0], y[1]), cvtpk(y[2], y[3])}, c.wrs, k.lane16,
              (unsigned)WS_PART + ((((c.kq + j + 1) & 3) * 512 + c.tw) * 3 + j) * 1024);
    if constexpr (F == 135) { _Pragma("unroll") for (int i = 0; i < 3; ++i) c.v[i] = *(LDSP const f32x4*)(c.xr + (4 + i) * 1024); }
  }
  if constexpr (F == 140) {
    c.acc[0][6] += c.v[2];
    __builtin_amdgcn_raw_buffer_store_b64(u32x2{cvtpk(c.acc[0][6][0], c.acc[0][6][1]), cvtpk(c.acc[0][6][2], c.acc[0][6][3])}, c.wrs, c.p8s, c.kq * 512, 16);
    PIN(c.acc[0][6]);
  }
  SBAR();
}
template <int B, int... Fs> DI void k5_run(K5C& c, K4C& k, std::integer_sequence<int, Fs...>) { (k5_frag<B + Fs>(c, k), ...); }

extern "C" __global__ __launch_bounds__(512, 2) void k_ffn() {
  extern __shared__ __attribute__((aligned(16))) char smem[];
  char* ws = (char*)((cptr_t)(uintptr_t)g_ptr)[5]; char* out = (char*)((cptr_t)(uintptr_t)g_ptr)[6];
  asm volatile("" : "+s"(out), "+s"(ws));
  LDSP char* lds = (LDSP char*)smem;
  const int tid = threadIdx.x, lane = tid & 63, w = rfl(tid >> 6);
  const int xcd = blockIdx.x & 7, bi = blockIdx.x >> 3;
  const int m = lane & 15, g = lane >> 4, kq = xcd >> 1;
  STAMP_DECL; STAMP(0);
  K4C c;
  {
    const int rb = 4 * (xcd & 1) + (bi & 3), fs = 8 * kq + (bi >> 2), mt = 16 * rb + 2 * w;
    c.lds0 = (unsigned)(uintptr_t)lds; c.lane16 = lane * 16; c.w = w;
    c.T0 = 9 * fs + 8 * (fs & 1); c.dir = 1 - 2 * (fs & 1);
    c.wb = ws + WS_W1 + (size_t)c.T0 * K4_SLOT;
    c.xb = ws + WS_HC + (size_t)mt * 18432;
    if (K4_WD) { k4_issue_w<0>(c, 0); k4_issue_w<0>(c, 1); }
    k4_issue1<0, 0>(c); k4_issue1<0, 1>(c); k4_issue1<0, 2>(c); k4_issue1<0, 3>(c); k4_issue1<0, 4>(c); k4_issue1<0, 5>(c);
    unsigned sq[6];
#pragma unroll
    for (int i = 0; i < 6; ++i) sq[i] = gld4(ws + WS_SSQ, (3 * g + (i >> 1)) * 8192 + (16 * mt + 16 * (i & 1) + m) * 4);
    k4_issue1<1, 0>(c); k4_issue1<1, 1>(c); k4_issue1<1, 2>(c); k4_issue1<1, 3>(c); k4_issue1<1, 4>(c); k4_issue1<1, 5>(c);
    SBAR();
#pragma unroll
    for (int i = 0; i < 4; ++i) { c.wl[i] = lds + c.lane16 + i * K4_SLOT; PIN(c.wl[i]); }
    c.ars = mkrsrc(ws + WS_ACT, 128u * 144 * 1024);
    c.avo = (mt + (g & 1)) * (144 * 1024) + (g >> 1) * 256 + m * 16;
    c.wd.wb = ws + WS_WD + ((size_t)(bi >> 3) * 144 + 36 * kq) * 9 * 1024;
#pragma unroll
    for (int i = 0; i < 4; ++i) {
      const int p = i < 3 ? 8 * i + w : 24 + (w & 3), st = (p * 37) >> 8, t = p - 7 * st + 2;
      c.wd.q[i] = ((st >> 1) * 162 + (t < 8 ? (t + 2 * kq) & 7 : 8)) * 1024;
      c.wd.qs[i] = st & 1;
      asm volatile("" : "+s"(c.wd.q[i]), "+s"(c.wd.qs[i]));
    }
    c.wd.qo = ((w >> 2) * 162 + 2 * kq + (w & 1)) * 1024; c.wd.qos = (w >> 1) & 1; c.wd.lds0 = c.lds0;
    if (K4_PFWD) {
      c.pfb = ws + WS_ACT;
      c.pfo = (mt + ((lane >> 5) & 1)) * (144 * 1024) + ((lane >> 4) & 1) * 256 + ((lane >> 3) & 1) * 128;
      PIN(c.pfo);
    }
    c.pf0 = 0;
    c.cntA = (gu32*)(ws + WS_CNT + (64 + rb * 4 + kq) * 128);
    STAMP(1);
    asm volatile("s_waitcnt vmcnt(5)\n\ts_barrier" : "+v"(sq[0]), "+v"(sq[1]), "+v"(sq[2]), "+v"(sq[3]), "+v"(sq[4]), "+v"(sq[5]),
                 "+v"(c.X[0][0]), "+v"(c.X[0][1]), "+v"(c.X[1][0]), "+v"(c.X[1][1])::"memory");
#pragma unroll
    for (int i = 0; i < K4_PF; ++i) c.wf[i] = ldsr16(c.wl[k4_tile(i)] + k4_addr(i));
#pragma unroll
    for (int t = 0; t < 2; ++t) {
      float ss = __uint_as_float(sq[t]) + __uint_as_float(sq[2 + t]) + __uint_as_float(sq[4 + t]);
      ss = xor32_sum(xor16_sum(ss));
      const float v = ss * (1.0f / DM) + LN_EPS, rs = __builtin_amdgcn_rsqf(v);
      c.c1[t] = -LOG2E * rs; c.c2[t] = -LOG2E * (v * rs);
      PIN(c.c1[t]); PIN(c.c2[t]);
      if (K4_PK) { c.c1p[t] = f32x2{c.c1[t], c.c1[t]}; c.c2p[t] = f32x2{c.c2[t], c.c2[t]}; PIN(c.c1p[t]); PIN(c.c2p[t]); }
    }
    SBAR();
    k4_run<0>(c, std::make_integer_sequence<int, 288>{});
    c.cntB = c.cntA + 32 * 32;
  }
  const int rb = 8 * (xcd & 1) + (bi & 7), nb = bi >> 3, mg = w & 3, kh = w >> 2, mu = 8 * rb + 2 * mg + kh;
  gu32* prodB = c.cntB;
  c.cntA = (gu32*)(ws + WS_CNT + (64 + (rb >> 1) * 4 + kq) * 128);
  c.ab = ws + WS_ACT + ((size_t)mu * 144 + 36 * kq + 18 * kh) * 1024;
  c.ab2 = ws + WS_ACT + ((size_t)(mu ^ 1) * 144 + 36 * kq + 18 * kh) * 1024;
  k4_run<288>(c, std::make_integer_sequence<int, 36>{});
  STAMP(2);
  K5C d;
  d.ab = c.ab; d.ab2 = c.ab2;
  asm volatile("s_nop 7" : "+v"(c.acc[0][0]), "+v"(c.acc[0][1]), "+v"(c.acc[0][2]), "+v"(c.acc[0][3]));
  k4_last(c);
  SBAR();
  gu32* consB = c.cntA + 32 * 32;
  d.hb = ws + WS_HU + (size_t)mu * 18432 + nb * 4608; d.ho = (g >> 1) * 256 + m * 16 + (g & 1) * 8; d.kq = kq;
#pragma unroll
  for (int i = 0; i < 4; ++i) { d.ra[i] = lds + K5_R[i] + kh * 14336 + c.lane16; PIN(d.ra[i]); }
#pragma unroll
  for (int i = 0; i < 3; ++i) { d.oa[i] = lds + i * 65536 + kh * 4096 + c.lane16; PIN(d.oa[i]); }
  d.xw = lds + (kh ? K5_R[2] : K5_R[1]) + mg * 7168 + c.lane16; PIN(d.xw);
  d.xr = lds + (kh ? K5_R[1] : K5_R[2]) + mg * 7168 + c.lane16; PIN(d.xr);
  const __amdgpu_buffer_rsrc_t wrs = mkrsrc(ws, (unsigned)WS_END), ors = mkrsrc(out, SEQ * DM * 2);
  const int tile = rb * 4 + nb;
  const bool own8 = (mu & 3) == kq;
  const unsigned tw = tile * 8 + w;
  const unsigned p8 = (unsigned)WS_PART8 + tw * 2048 + lane * 8;
  d.cnt = (gu32*)(ws + WS_CNT + tile * 128);
  d.wrs = wrs; d.tw = tw; d.p8s = own8 ? 0x80000000u : p8; PIN(d.p8s);
#pragma unroll
  for (int i = 0; i < 4; ++i) { d.A[0][i] = c.A0[0][i]; d.A[1][i] = c.A0[1][i]; }
  asm volatile("s_waitcnt vmcnt(6) lgkmcnt(0)\n\ts_barrier" : "+v"(d.A[0][0]), "+v"(d.A[0][1]), "+v"(d.A[0][2]), "+v"(d.A[0][3])::"memory");
  STAMP(3);
  d.wf[0] = k5_read<0>(d); d.wf[1] = k5_read<1>(d); d.wf[2] = k5_read<2>(d); d.wf[3] = k5_read<3>(d);
  SBAR();
  c.cntB = prodB;
  k5_run<0>(d, c, std::make_integer_sequence<int, 28>{});
  c.cntB = consB;
  k5_run<28>(d, c, std::make_integer_sequence<int, 134>{});
  asm volatile("s_nop 7" : "+v"(d.own[0][0]), "+v"(d.own[0][1]), "+v"(d.own[1][0]), "+v"(d.own[1][1]));
  STAMP(4);
  LDSP char* xo = lds + K5_R[0] + c.lane16;
  *(LDSP f32x4*)(xo + w * 2048) = d.own[1][0];
  *(LDSP f32x4*)(xo + w * 2048 + 1024) = d.own[1][1];
  unsigned oo = (16 * mu + m) * 1152 + nb * 288;
  unsigned oo8 = own8 ? oo + 256 + g * 8 : 0x80000000u;
  oo += kq * 64 + (g & 1) * 32 + (g >> 1) * 16;
  PIN(oo); PIN(oo8);
  STAMP(5);
  if (w == 0) wait_count(d.cnt, 0u, 4u);
  asm volatile("s_waitcnt lgkmcnt(0)\n\ts_barrier" ::: "memory");
  STAMP(6);
  u32x4 r[3]; u32x2 r8[3];
#pragma unroll
  for (int j = 0; j < 3; ++j) {
    r[j] = __builtin_amdgcn_raw_buffer_load_b128(wrs, c.lane16, (unsigned)WS_PART + ((kq * 512 + tw) * 3 + j) * 1024, 16);
    r8[j] = __builtin_amdgcn_raw_buffer_load_b64(wrs, own8 ? p8 : 0x80000000u, ((kq + 1 + j) & 3) * 512, 16);
  }
  SBAR();
  f32x4 e0 = addbf(d.own[0][0] + *(LDSP const f32x4*)(xo + (w ^ 4) * 2048), d.hr[0][0], d.hr[0][1]);
  f32x4 e1 = addbf(d.own[0][1] + *(LDSP const f32x4*)(xo + (w ^ 4) * 2048 + 1024), d.hr[1][0], d.hr[1][1]);
  f32x4 e8 = addbf(d.acc[0][6], d.hr[2][0], d.hr[2][1]);
#pragma unroll
  for (int j = 0; j < 3; ++j) { e0 = addbf(e0, r[j][0], r[j][1]); e1 = addbf(e1, r[j][2], r[j][3]); e8 = addbf(e8, r8[j][0], r8[j][1]); }
  {
    const auto s0 = __builtin_amdgcn_permlane16_swap(cvtpk(e0[0], e0[1]), cvtpk(e1[0], e1[1]), false, false);
    const auto s1 = __builtin_amdgcn_permlane16_swap(cvtpk(e0[2], e0[3]), cvtpk(e1[2], e1[3]), false, false);
    bst16<OUT_AUX>(u32x4{s0[0], s1[0], s0[1], s1[1]}, ors, oo, 0);
    __builtin_amdgcn_raw_buffer_store_b64(u32x2{cvtpk(e8[0], e8[1]), cvtpk(e8[2], e8[3])}, ors, oo8, 0, OUT_AUX);
  }
  STAMP(7); STAMP_OUT(4, 8);
}
extern "C" __global__ void k_end() {}

extern "C" void kernel_launch(void* const* d_in, const int* in_sizes, int n_in, void* d_out, int out_size, void* d_ws, size_t ws_size,
                              hipStream_t stream) {
  const u16* x = (const u16*)d_in[0];
  const u16* wqkv = (const u16*)d_in[1];
  const u16* wo = (const u16*)d_in[2];
  const u16* wg = (const u16*)d_in[4];
  const u16* wu = (const u16*)d_in[5];
  const u16* wd = (const u16*)d_in[6];
  char* ws = (char*)d_ws;
  (void)hipFuncSetAttribute((const void*)k_qkv, hipFuncAttributeMaxDynamicSharedMemorySize, K1_LDS);
  (void)hipFuncSetAttribute((const void*)k_attn, hipFuncAttributeMaxDynamicSharedMemorySize, K2_LDS);
  (void)hipFuncSetAttribute((const void*)k_oproj, hipFuncAttributeMaxDynamicSharedMemorySize, K3_LDS);
  (void)hipFuncSetAttribute((const void*)k_ffn, hipFuncAttributeMaxDynamicSharedMemorySize, K4_LDS);
  k_qkv<<<256, 768, K1_LDS, stream>>>(x, wqkv, ws, wo, wg, wu, wd, (char*)d_out);
  k_attn<<<256, 768, K2_LDS, stream>>>();
  k_oproj<<<192, 768, K3_LDS, stream>>>();
  k_ffn<<<256, 512, K4_LDS, stream>>>();
}
```

```cpp
#define SEL 4
#include <hip/hip_runtime.h>
#include <hip/hip_bf16.h>
#include <stdint.h>
#include <utility>
#include <type_traits>

typedef __bf16 bf16x8 __attribute__((ext_vector_type(8)));
typedef __bf16 bf16x2 __attribute__((ext_vector_type(2)));
typedef short s16x4 __attribute__((ext_vector_type(4)));
typedef float f32x16 __attribute__((ext_vector_type(16)));
typedef float f32x4 __attribute__((ext_vector_type(4)));
typedef float f32x2 __attribute__((ext_vector_type(2)));
typedef unsigned u32x4 __attribute__((ext_vector_type(4)));
typedef unsigned u32x2 __attribute__((ext_vector_type(2)));
typedef unsigned short u16;
#define LDSP __attribute__((address_space(3)))
#define DI __device__ __forceinline__
#define SBAR() __builtin_amdgcn_sched_barrier(0)

constexpr int SEQ = 2048, DM = 576, NH = 32, DH = 18, FF = 4608;
constexpr float LOG2E = 1.4426950408889634f;
constexpr float QSCALE = 0.23570226039551584f * LOG2E;
constexpr float LN_EPS = 1e-5f;

constexpr size_t CHUNK_B = 37888;
constexpr size_t KV_HEAD_B = 4 * CHUNK_B;
constexpr size_t WS_QM = 0;
constexpr size_t WS_QT = WS_QM + (size_t)NH * 65536;
constexpr size_t WS_KV = WS_QT + (size_t)NH * 8192;
constexpr size_t WS_KMAX = WS_KV + (size_t)NH * KV_HEAD_B;
constexpr size_t WS_XMEAN = WS_KMAX + 65536;
constexpr size_t WS_OFR = WS_XMEAN + 8192;
constexpr size_t WS_WO = WS_OFR + (size_t)128 * 18 * 1024;
constexpr size_t WS_WBAR = WS_WO + (size_t)12 * 54 * 1024;
constexpr size_t WS_HC = WS_WBAR + 3072;
constexpr size_t WS_SSQ = WS_HC + (size_t)64 * 36 * 1024;
constexpr size_t WS_W1 = WS_SSQ + (size_t)12 * 8192;
constexpr size_t WS_WD = WS_W1 + (size_t)288 * 36 * 1024;
constexpr size_t WS_ACT = WS_WD + (size_t)4 * 144 * 9 * 1024;
constexpr size_t UNIT_B = (size_t)128 * 18432;
constexpr size_t WS_PART = WS_ACT + (size_t)128 * 144 * 1024;
constexpr size_t WS_PART8 = WS_PART + (size_t)4 * 64 * 8 * 3 * 1024;
constexpr size_t WS_CNT = WS_PART8 + (size_t)64 * 8 * 4 * 512;
constexpr size_t WS_XU = WS_CNT + 32768;
constexpr size_t WS_HU = WS_XU + UNIT_B;
constexpr size_t WS_END = WS_HU + UNIT_B;

DI unsigned cvtpk(float lo, float hi) {
  f32x2 v = {lo, hi};
  bf16x2 b = __builtin_convertvector(v, bf16x2);
  return __builtin_bit_cast(unsigned, b);
}
DI float bflo(unsigned u) { return __uint_as_float(u << 16); }
DI float bfhi(unsigned u) { return __uint_as_float(u & 0xffff0000u); }
DI float dot2(unsigned a, unsigned b, float c) {
  return __builtin_amdgcn_fdot2_f32_bf16(__builtin_bit_cast(bf16x2, a), __builtin_bit_cast(bf16x2, b), c, false);
}
DI f32x16 mfma32(u32x4 a, u32x4 b, f32x16 c) {
  return __builtin_amdgcn_mfma_f32_32x32x16_bf16(__builtin_bit_cast(bf16x8, a), __builtin_bit_cast(bf16x8, b), c, 0, 0, 0);
}
DI f32x4 mfma16(u32x4 a, u32x4 b, f32x4 c) {
  return __builtin_amdgcn_mfma_f32_16x16x32_bf16(__builtin_bit_cast(bf16x8, a), __builtin_bit_cast(bf16x8, b), c, 0, 0, 0);
}
DI unsigned rfl(unsigned x) { return __builtin_amdgcn_readfirstlane(x); }
DI const char* uniptr(const char* p) {
  uint64_t v = (uint64_t)p;
  unsigned lo = rfl((unsigned)v), hi = rfl((unsigned)(v >> 32));
  return (const char*)(((uint64_t)hi << 32) | lo);
}
DI void dma16(const char* sbase, unsigned voff, unsigned lds) {
  asm volatile("s_mov_b32 m0, %2\n\ts_nop 0\n\tglobal_load_lds_dwordx4 %0, %1" ::"v"(voff), "s"(sbase), "s"(lds));
}
DI u32x4 gld16(const char* sbase, unsigned voff) {
  u32x4 r;
  asm volatile("global_load_dwordx4 %0, %1, %2" : "=v"(r) : "v"(voff), "s"(sbase));
  return r;
}
DI unsigned gld4(const char* sbase, unsigned voff) {
  unsigned r;
  asm volatile("global_load_dword %0, %1, %2" : "=v"(r) : "v"(voff), "s"(sbase));
  return r;
}
#define WAITV(n) asm volatile("s_waitcnt vmcnt(" #n ")" ::: "memory")
#define BARRIER() asm volatile("s_barrier" ::: "memory")
#define PIN(x) asm volatile("" : "+v"(x))
DI u32x4 ldsr16(LDSP const char* p) { return *(LDSP const u32x4*)p; }
DI float xor32_sum(float v) {
  auto r = __builtin_amdgcn_permlane32_swap(__float_as_uint(v), __float_as_uint(v), false, false);
  return __uint_as_float(r[0]) + __uint_as_float(r[1]);
}
DI float xor32_max(float v) {
  auto r = __builtin_amdgcn_permlane32_swap(__float_as_uint(v), __float_as_uint(v), false, false);
  return fmaxf(__uint_as_float(r[0]), __uint_as_float(r[1]));
}
DI float xor16_sum(float v) {
  auto r = __builtin_amdgcn_permlane16_swap(__float_as_uint(v), __float_as_uint(v), false, false);
  return __uint_as_float(r[0]) + __uint_as_float(r[1]);
}

#define STAMP_DECL
#define STAMP(i)
#define STAMP_OUT(kid, n)

DI int wo_row(int step, int g, int j) {
  if (step < 16) return 18 * (2 * step + (g >> 1)) + 4 * (g & 1) + (j & 3) + 8 * (j >> 2);
  return 18 * (4 * (4 * (step - 16) + g) + (j >> 1)) + 16 + (j & 1);
}

#define K4_SPREAD 1
#define K4_PM 1
#define K4_WD 1
#define K3_PF 0
#define K3_SLEEP 32
#define K1_ROT 0
#define AT_PM 9
#define HELP_PRIO 3
#define HELP_EARLY 0
#define PREP_ORD 1
#define GRAY 1
#define K4_POLL 10
#define K4_PK 0
#define K5_ORD 1
#define K5_SIG 154
#define K4_MID 0
#define AT_HFIRST 0
#define K4_ROT 0
#define K5_FLAG 0
#define K5_SPREAD 1
#define K5_PM 0
#define OUT_AUX 16
#define K4_PFWD 1
#define QKV_AUX 16
#define QKT_AUX 16
#define V_AUX 16
#define O_AUX 16
#define H_AUX 16
#define PART_AUX 16
#define ACT_AUX 16
#define PREP_AUX 16
#define CODE_PF(dst, first, last, extra, blk) CODE_PF2(dst, first, last, extra, blk, 128u)
#define CODE_PF2(dst, first, last, extra, blk, stride)                                                                              \
  do {                                                                                                                     \
    uint64_t pc_; int b_, e_;                                                                                              \
    asm volatile("s_getpc_b64 %0\n.Lpf%=:\n\ts_mov_b32 %1, " #first "-.Lpf%=\n\ts_mov_b32 %2, " #last "-.Lpf%=" : "=s"(pc_), "=s"(b_), "=s"(e_)); \
    const unsigned o_ = (blk) * 8192u + (threadIdx.x & 63) * (stride), n_ = (unsigned)(e_ - b_) + (extra) - 4u;                \
    dst = gld4((const char*)(pc_ + b_), o_ < n_ ? o_ : n_);                                                                \
  } while (0)
#define G_MFMA(d, a, b) asm volatile("v_mfma_f32_16x16x32_bf16 %0, %1, %2, %0" : "+v"(d) : "v"(a), "v"(b))
#define G_MFMA0(d, a, b) asm volatile("v_mfma_f32_16x16x32_bf16 %0, %1, %2, 0" : "=&v"(d) : "v"(a), "v"(b))
template <int AUX> DI void bst16(u32x4 v, __amdgpu_buffer_rsrc_t rs, unsigned voff, unsigned soff) {
  __builtin_amdgcn_raw_buffer_store_b128(v, rs, voff, soff, AUX);
  asm volatile("s_nop 1" ::"v"(v) : "memory");
}
DI __amdgpu_buffer_rsrc_t mkrsrc(const void* p, unsigned bytes) {
  return __builtin_amdgcn_make_buffer_rsrc((void*)p, 0, bytes, 0x00020000);
}

#define K1_PF 4
#define K1_TOUCH 1
#define K1_TSLEEP 16
#define K1_DEPTH 2
constexpr int K1_PITCH = 224, K1_SLOT = 16384 + 64 * K1_PITCH  , K1_NS = K1_DEPTH + 1;
constexpr int K1_SP = 232;
constexpr int K1_STG = K1_NS * K1_SLOT, K1_CS = K1_STG + 128 * K1_SP + 64, K1_ST = K1_CS + 512 + 64, K1_LDS = K1_ST + 2048;
struct K1C {
  LDSP const char *xa[2], *wl, *wc0, *wc1;
  u32x4 X[3][2], wf[K1_PF + 1], ones;
  f32x4 acc[4][2];
  f32x4 sm[2]; float sq[2];
};
DI void k1_loader(const char* xb, const char* wb, char* ws, int j, int lane, unsigned lds0) {
  __builtin_amdgcn_s_setprio(3);
  unsigned xo[2], wo[4];
  const int g = lane >> 4;
  xo[0] = (lane >> 3) * 1152 + (((lane & 7) ^ g) * 16);
  xo[1] = (8 + (lane >> 3)) * 1152 + (((lane & 7) ^ (4 + g)) * 16);
  xb += j * (32 * 1152);
  const unsigned xd = lds0 + j * 4096, wd = lds0 + 16384 + j * 1024;
#define K1_X(z, so) do { dma16(xb + (z) * 128, xo[0], xd + (so)); dma16(xb + (z) * 128, xo[1], xd + (so) + 1024);                    \
    dma16(xb + (z) * 128 + 16 * 1152, xo[0], xd + (so) + 2048); dma16(xb + (z) * 128 + 16 * 1152, xo[1], xd + (so) + 3072); } while (0)
#define K1_W(z, so) do { _Pragma("unroll") for (int i = 0; i < 3; ++i) dma16(wb + (z) * (64 * 3456), wo[i], wd + (so) + i * 4096);     \
    if (j < 2) dma16(wb + (z) * (64 * 3456), wo[3], wd + (so) + 3 * 4096); } while (0)
  K1_X(0, 0);
  SBAR();
#pragma unroll
  for (int i = 0; i < 4; ++i) {
    const unsigned u = 64 * (j + 4 * i) + lane, r = u / 14;
    wo[i] = ((r & ~12u) | ((r & 4u) << 1) | ((r & 8u) >> 1)) * 3456 + (u - r * 14) * 16;
  }
  K1_W(0, 0);
#pragma unroll
  for (int z = 1; z < K1_DEPTH; ++z) { K1_X(z, z * K1_SLOT); K1_W(z, z * K1_SLOT); }
  unsigned so = K1_DEPTH * K1_SLOT;
#pragma unroll 1
  for (int z = K1_DEPTH; z < 9; ++z) {
    asm volatile("s_waitcnt vmcnt(%0)\n\ts_barrier" ::"i"(7 * (K1_DEPTH - 1)) : "memory");
    K1_X(z, so); K1_W(z, so);
    so = so == (K1_NS - 1) * K1_SLOT ? 0 : so + K1_SLOT;
  }
  if (K1_DEPTH > 2) asm volatile("s_waitcnt vmcnt(14)\n\ts_barrier" ::: "memory");
  asm volatile("s_waitcnt vmcnt(7)\n\ts_barrier" ::: "memory");
  asm volatile("s_waitcnt vmcnt(0)\n\ts_barrier" ::: "memory");
  if (blockIdx.x < 4 && j == 0) bst16<16>(u32x4{0, 0, 0, 0}, mkrsrc(ws, (unsigned)WS_END), (unsigned)WS_CNT + (blockIdx.x * 64 + lane) * 128, 0);
#undef K1_X
#undef K1_W
}
DI u32x4 tr_frag(LDSP const char* p, int d) {
  s16x4 lo = __builtin_amdgcn_ds_read_tr16_b64_v4i16((LDSP s16x4*)p);
  s16x4 hi = __builtin_amdgcn_ds_read_tr16_b64_v4i16((LDSP s16x4*)(p + d));
  return __builtin_bit_cast(u32x4, __builtin_shufflevector(lo, hi, 0, 1, 2, 3, 4, 5, 6, 7));
}
template <int NHG, int F> DI void k1_read(K1C& c) {
  constexpr int NF = 4 + NHG, S = F / NF, j = F % NF, Z = S >> 1, so = (Z % K1_NS) * K1_SLOT;
  if constexpr (j == 0) {
    c.X[S % 3][0] = ldsr16(c.xa[S & 1] + so);
    c.X[S % 3][1] = ldsr16(c.xa[S & 1] + so + 2048);
  }
  constexpr int wo = so + (S & 1) * 7168;
  if constexpr (NHG == 0 || j < 3) c.wf[F % (K1_PF + 1)] = tr_frag(c.wl + wo + j * 32, 1792);
  else c.wf[F % (K1_PF + 1)] = tr_frag((j == 3 ? c.wc0 : c.wc1) + wo, 1792);
}
template <int NHG, int F> DI void k1_frag(K1C& c) {
  constexpr int NF = 4 + NHG, S = F / NF, j = F % NF, Z = S >> 1, N = F + K1_PF, fz = F % (2 * NF);
  if constexpr (S == 0) {
    if constexpr (NHG == 0 || j < 3) { G_MFMA0(c.acc[j][0], c.wf[F % (K1_PF + 1)], c.X[0][0]); G_MFMA0(c.acc[j][1], c.wf[F % (K1_PF + 1)], c.X[0][1]); }
    else G_MFMA0(c.acc[3][j - 3], c.wf[F % (K1_PF + 1)], c.ones);
  } else {
    if constexpr (NHG == 0 || j < 3) { G_MFMA(c.acc[j][0], c.wf[F % (K1_PF + 1)], c.X[S % 3][0]); G_MFMA(c.acc[j][1], c.wf[F % (K1_PF + 1)], c.X[S % 3][1]); }
    else G_MFMA(c.acc[3][j - 3], c.wf[F % (K1_PF + 1)], c.ones);
  }
  if constexpr ((S & 1) == NHG && j < 4) {
#pragma unroll
    for (int t = 0; t < 2; ++t) { const unsigned v = c.X[S % 3][t][j]; c.sq[t] = dot2(v, v, c.sq[t]); PIN(c.sq[t]); }
    if constexpr (j < 2) { if constexpr (S < 2) G_MFMA0(c.sm[j], c.ones, c.X[S % 3][j]); else G_MFMA(c.sm[j], c.ones, c.X[S % 3][j]); }
  }
  if constexpr (N < 18 * NF) {
    if constexpr (N % (2 * NF) == 0) {
      asm volatile("s_waitcnt lgkmcnt(0)\n\ts_barrier" ::: "memory");
    }
    k1_read<NHG, N>(c);
  }
  SBAR();
}
template <int NHG, int... Fs> DI void k1_run(K1C& c, std::integer_sequence<int, Fs...>) {
  k1_read<NHG, 0>(c); k1_read<NHG, 1>(c); k1_read<NHG, 2>(c);
  if (K1_PF > 3) k1_read<NHG, 3>(c);
  if (K1_PF > 4) k1_read<NHG, 4>(c);
  if (K1_PF > 5) k1_read<NHG, 5>(c);
  SBAR();
  (k1_frag<NHG, Fs>(c), ...);
  asm volatile("s_nop 7" : "+v"(c.acc[0][0]), "+v"(c.acc[0][1]), "+v"(c.acc[1][0]), "+v"(c.acc[1][1]), "+v"(c.acc[2][0]), "+v"(c.acc[2][1]),
               "+v"(c.acc[3][0]), "+v"(c.acc[3][1]), "+v"(c.sm[0]), "+v"(c.sm[1]));
  SBAR();
}
extern "C" __global__ __launch_bounds__(768, 3) void k_qkv(const u16* x, const u16* wqkv, char* ws) {
  extern __shared__ __attribute__((aligned(16))) char smem[];
  const int tid = threadIdx.x, lane = tid & 63, w = rfl(tid >> 6);
  const int xcd = blockIdx.x & 7, bi = blockIdx.x >> 3;
  unsigned pf;
  CODE_PF2(pf, k_qkv, k_end, 0, bi, (w == 8 && bi < 9) ? 128u : 0u);
  SBAR();
  asm volatile("" : "+s"(x), "+s"(wqkv), "+s"(ws));
  LDSP char* lds = (LDSP char*)smem;
  const unsigned lds0 = (unsigned)(uintptr_t)lds;
  const int rb = 4 * (xcd >> 1) + (bi & 3), cb = 8 * (xcd & 1) + (bi >> 2);
  const int c0 = 108 * cb - 4 * (cb & 1);
  if (__builtin_expect(w >= 8, 1)) {
    k1_loader((const char*)x + rb * (128 * 1152), (const char*)wqkv + c0 * 2, ws, w - 8, lane, lds0);
    asm volatile("" ::"v"(pf));
    return;
  }
  const int m = lane & 15, g = lane >> 4, mg = w & 3, nh = w >> 2;
  STAMP_DECL; STAMP(0);
  if (blockIdx.x == 8 && threadIdx.x == 0) ((__attribute__((address_space(1))) unsigned long long*)(ws + (200u << 20)))[0] = __builtin_amdgcn_s_memrealtime();
  K1C c;
  c.ones = u32x4{0x3f803f80u, 0x3f803f80u, 0x3f803f80u, 0x3f803f80u};
  PIN(c.ones);
  c.xa[0] = lds + (32 * mg + m) * 128 + ((g ^ (m >> 1)) * 16);
  c.xa[1] = lds + (32 * mg + m) * 128 + (((4 + g) ^ (m >> 1)) * 16);
  LDSP const char* wbase = lds + 16384 + (16 * (g >> 1) + 4 * (g & 1) + (m >> 2)) * K1_PITCH + (m & 3) * 8;
  c.wl = wbase + nh * 128;
  c.wc0 = wbase + mg * 32;
  c.wc1 = wbase + (mg < 3 ? mg + 4 : 6) * 32;
  c.sq[0] = c.sq[1] = 0.f;
  const int off = 4 * (cb & 1), T0 = 128 * rb;
  LDSP char* stg = lds + K1_STG;
  LDSP char* a_st = lds + K1_ST + (32 * mg + m) * 8;                 PIN(a_st);
  LDSP char* a_cs = lds + K1_CS + (64 * nh + 4 * g) * 4;             PIN(a_cs);
  LDSP char* a_sw = stg + (32 * mg + m) * K1_SP + (64 * nh + 4 * g) * 2;   PIN(a_sw);
  LDSP char* a_tr = stg + (32 * mg + 4 * (g & 1) + (m >> 2)) * K1_SP + (64 * nh + 16 * (g >> 1) + 4 * (m & 3)) * 2;   PIN(a_tr);
  float qs[4];
#pragma unroll
  for (int j = 0; j < 4; ++j) { qs[j] = (c0 + 64 * nh + 16 * j + 4 * g < DM) ? QSCALE : 1.0f; PIN(qs[j]); }
  const __amdgpu_buffer_rsrc_t wrs = mkrsrc(ws, (unsigned)WS_END);
  constexpr unsigned DROP = 0x80000000u;
  const unsigned kvo = (unsigned)WS_KV + (T0 >> 9) * (unsigned)CHUNK_B;
  unsigned vo[2] = {0, 0}, vone = 0, vd[4] = {DROP, DROP, DROP, DROP};
  if (cb >= 10) {
#pragma unroll
  for (int tt = 0; tt < 2; ++tt) {
    const int cc = 64 * nh + 32 * tt + 16 * (g >> 1) + m - off, hd = (cc + 18) / 18 - 1, G = 6 * cb + hd;
    vo[tt] = (cc >= 0 && cc < 108) ? hd * 4864 + 2 * mg * 608 + (g & 1) * 304 + (cc - 18 * hd) * 16 : 29184;
  }
  vone = tid < 96 ? (tid >> 4) * 4864 + (tid & 15) * 304 + 288 : 29184;
#pragma unroll
  for (int i = 0; i < 4; ++i) {
    const unsigned u = i * 512 + tid, hd = u / 304;
    vd[i] = (u < 1824 && 6 * cb + hd >= 64) ? kvo + 18432 + ((T0 >> 4) & 31) * 608 + (6 * cb + hd - 64) * (unsigned)KV_HEAD_B + (u - hd * 304) * 16 : DROP;
  }
  }
  PIN(vone); PIN(vo[0]); PIN(vo[1]); PIN(vd[0]); PIN(vd[1]); PIN(vd[2]); PIN(vd[3]);
  const int tok = tid & 127, T = T0 + tok;
  LDSP const char* a_g = stg + tok * K1_SP + off * 2;   PIN(a_g);
  unsigned um[2], ut[2];
  const unsigned tl = ((T >> 5) & 15) * 1024 + (T & 31) * 16;
#pragma unroll
  for (int it = 0; it < 2; ++it) {
    const int hd = (w >> 1) + 4 * it, G = 6 * cb + hd, head = G & 31;
    if (hd >= 6 || G >= 64) { um[it] = DROP; ut[it] = DROP; }
    else if (G < 32) { um[it] = (unsigned)WS_QM + head * 65536 + (T >> 9) * 16384 + tl; ut[it] = (unsigned)WS_QT + head * 8192 + T * 4; }
    else { um[it] = kvo + head * (unsigned)KV_HEAD_B + tl; ut[it] = kvo + head * (unsigned)KV_HEAD_B + 16384 + ((T >> 6) & 7) * 256 + (T & 31) * 8 + ((T >> 5) & 1) * 4; }
    PIN(um[it]); PIN(ut[it]);
  }
  STAMP(1);
  asm volatile("s_waitcnt vmcnt(0)\n\ts_barrier" : "+v"(pf)::"memory");
  STAMP(2);
  if (nh) k1_run<1>(c, std::make_integer_sequence<int, 90>{});
  else k1_run<0>(c, std::make_integer_sequence<int, 72>{});
  STAMP(3);
#pragma unroll
  for (int t = 0; t < 2; ++t) {
    const f32x2 st = {c.sm[t][0], xor32_sum(xor16_sum(c.sq[t]))};
    *(LDSP f32x2*)(a_st + nh * 1024 + t * 128) = st;
  }
  if (nh) {
    *(LDSP f32x4*)(lds + K1_CS + (16 * mg + 4 * g) * 4) = c.acc[3][0];
    *(LDSP f32x4*)(lds + K1_CS + (16 * (mg + 4) + 4 * g) * 4) = c.acc[3][1];
  }
  asm volatile("s_waitcnt lgkmcnt(0)\n\ts_barrier" ::: "memory");
  {
    f32x2 p[2][2]; f32x4 cc[4];
#pragma unroll
    for (int t = 0; t < 2; ++t) { p[t][0] = *(LDSP const f32x2*)(a_st + t * 128); p[t][1] = *(LDSP const f32x2*)(a_st + 1024 + t * 128); }
#pragma unroll
    for (int j = 0; j < 4; ++j) cc[j] = *(LDSP const f32x4*)(a_cs + j * 64);
    SBAR();
#pragma unroll
    for (int t = 0; t < 2; ++t) {
      const float mean = (p[t][0][0] + p[t][1][0]) * (1.0f / DM);
      const float var = fmaxf((p[t][0][1] + p[t][1][1]) * (1.0f / DM) - mean * mean, 0.f);
      const float rstd = __builtin_amdgcn_rsqf(var + LN_EPS);
      if (cb == 0 && nh == 0) __builtin_amdgcn_raw_buffer_store_b32(__float_as_uint(mean), wrs, (unsigned)WS_XMEAN + (T0 + 32 * mg + 16 * t + m) * 4, 0, QKV_AUX);
#pragma unroll
      for (int j = 0; j < 4; ++j) {
        if (j < 3 || nh == 0) {
          const f32x4 a = (c.acc[j][t] - mean * cc[j]) * (rstd * qs[j]);
          *(LDSP u32x2*)(a_sw + t * (16 * K1_SP) + j * 32) = u32x2{cvtpk(a[0], a[1]), cvtpk(a[2], a[3])};
        }
      }
    }
  }
  if (cb >= 10) {
    asm volatile("s_waitcnt lgkmcnt(0)" ::: "memory");
    u32x4 v[2][2];
#pragma unroll
    for (int tt = 0; tt < 2; ++tt)
#pragma unroll
      for (int t = 0; t < 2; ++t) v[tt][t] = tr_frag(a_tr + t * (16 * K1_SP) + tt * 64, 8 * K1_SP);
    *(LDSP u32x4*)(lds + vone) = c.ones;
#pragma unroll
    for (int tt = 0; tt < 2; ++tt)
#pragma unroll
      for (int t = 0; t < 2; ++t) *(LDSP u32x4*)(lds + vo[tt] + t * 608) = v[tt][t];
  }
  STAMP(4);
  asm volatile("s_waitcnt lgkmcnt(0)\n\ts_barrier" ::: "memory");
  if (cb >= 10) {
    u32x4 v[4];
#pragma unroll
    for (int i = 0; i < 4; ++i) v[i] = ldsr16(lds + i * 8192 + tid * 16);
#pragma unroll
    for (int i = 0; i < 4; ++i) bst16<V_AUX>(v[i], wrs, vd[i], 0);
  }
  if (cb <= 10) {
    unsigned d[2][9];
#pragma unroll
    for (int it = 0; it < 2; ++it) {
      const int hd = (w >> 1) + 4 * it;
      LDSP const unsigned* sp = (LDSP const unsigned*)(a_g + 36 * (hd < 6 ? hd : 0));
#pragma unroll
      for (int i = 0; i < 9; ++i) d[it][i] = sp[i];
    }
    SBAR();
#pragma unroll
    for (int it = 0; it < 2; ++it) {
      bst16<QKV_AUX>(u32x4{d[it][0], d[it][1], d[it][2], d[it][3]}, wrs, um[it], 0);
      bst16<QKV_AUX>(u32x4{d[it][4], d[it][5], d[it][6], d[it][7]}, wrs, um[it] + 512, 0);
      __builtin_amdgcn_raw_buffer_store_b32(d[it][8], wrs, ut[it], 0, QKT_AUX);
    }
#pragma unroll
    for (int it = 0; it < 2; ++it) {
      const int hd = (w >> 1) + 4 * it, G = 6 * cb + hd;
      if (hd < 6 && G >= 32 && G < 64) {
        float n2 = 0.f;
#pragma unroll
        for (int i = 0; i < 9; ++i) n2 = dot2(d[it][i], d[it][i], n2);
        unsigned nb = __float_as_uint(n2);
        { auto r = __builtin_amdgcn_permlane32_swap(nb, nb, false, false); nb = max(r[0], r[1]); }
        { auto r = __builtin_amdgcn_permlane16_swap(nb, nb, false, false); nb = max(r[0], r[1]); }
        __builtin_amdgcn_raw_buffer_store_b32(nb, wrs, (unsigned)WS_KMAX + (((2 * rb + (w & 1)) * 32 + (G & 31)) * 16 + m) * 4, 0, QKV_AUX);
      }
    }
  }
  STAMP(5); STAMP_OUT(1, 6);
}

constexpr int K2_LDS = (int)KV_HEAD_B;
constexpr int NJ_W1 = 72 * 36, NJ_WD = 9 * 144, NJ_WO = 9 * 18, NJ_ALL = NJ_W1 + NJ_WD + NJ_WO;
struct PrepJob { const char* src; unsigned rs, c1, c2, voff, dst[4]; };
DI void prep_setup(PrepJob& j, int J, int lane, const u16* wo, const u16* wg, const u16* wu, const u16* wd) {
  if (J >= NJ_ALL) J = NJ_ALL - 1;
  const int g = (lane >> 2) & 3;
  unsigned lrow = 8 * g;
  j.c1 = 0; j.c2 = 0;
  if (J < NJ_W1) {
    const int sm = J / 72, tg = J - sm * 72;
    j.src = (const char*)((sm & 1) ? wu : wg) + (size_t)(sm >> 1) * (32 * FF * 2) + tg * 128;
    j.rs = FF * 2;
#pragma unroll
    for (int t = 0; t < 4; ++t) j.dst[t] = (unsigned)WS_W1 + (4 * tg + t) * 36864 + sm * 1024;
  } else if (J < NJ_W1 + NJ_WD) {
    const int q = J - NJ_W1, cc = q / 9, tg = q - cc * 9;
    j.src = (const char*)wd + (size_t)cc * (32 * DM * 2) + tg * 128;
    j.rs = DM * 2;
#pragma unroll
    for (int t = 0; t < 4; ++t) { const int nt = 4 * tg + t, nb = nt / 9; j.dst[t] = (unsigned)WS_WD + ((nb * 144 + cc) * 9 + (nt - nb * 9)) * 1024; }
  } else {
    const int q = J - NJ_W1 - NJ_WD, step = q / 9, tg = q - step * 9;
    j.rs = DM * 2;
    if (step < 16) { j.src = (const char*)wo + (size_t)step * (36 * DM * 2) + tg * 128; lrow = 18 * (g >> 1) + 4 * (g & 1); j.c2 = 4; }
    else { j.src = (const char*)wo + (size_t)(288 * (step - 16) + 16) * (DM * 2) + tg * 128; lrow = 72 * g; j.c1 = 16; }
#pragma unroll
    for (int t = 0; t < 4; ++t) { const int nt = 4 * tg + t, nb = nt / 3; j.dst[t] = (unsigned)WS_WO + ((nb * 18 + step) * 3 + (nt - nb * 3)) * 1024; }
  }
#pragma unroll
  for (int t = 0; t < 4; ++t) j.dst[t] = rfl(j.dst[t]);
  j.voff = lrow * j.rs + (lane >> 4) * 32 + (lane & 3) * 8;
}
template <int JJ>
DI void prep_load(const PrepJob& j, u32x2 (&r)[8]) {
  const char* b = j.src + (JJ + j.c1 * (JJ >> 1) + j.c2 * (JJ >> 2)) * j.rs;
  asm volatile("global_load_dwordx2 %0, %1, %2" : "=v"(r[JJ]) : "v"(j.voff), "s"(b));
}
DI void prep_load_all(const PrepJob& j, u32x2 (&r)[8]) {
  prep_load<0>(j, r); prep_load<1>(j, r); prep_load<2>(j, r); prep_load<3>(j, r); prep_load<4>(j, r); prep_load<5>(j, r); prep_load<6>(j, r); prep_load<7>(j, r);
}
DI void prep_store(const PrepJob& j, const u32x2 (&r)[8], __amdgpu_buffer_rsrc_t rs, unsigned so) {
  u32x4 u[4];
#pragma unroll
  for (int i = 0; i < 4; ++i) {
    u[0][i] = __builtin_amdgcn_perm(r[2 * i + 1][0], r[2 * i][0], 0x05040100u);
    u[1][i] = __builtin_amdgcn_perm(r[2 * i + 1][0], r[2 * i][0], 0x07060302u);
    u[2][i] = __builtin_amdgcn_perm(r[2 * i + 1][1], r[2 * i][1], 0x05040100u);
    u[3][i] = __builtin_amdgcn_perm(r[2 * i + 1][1], r[2 * i][1], 0x07060302u);
  }
#pragma unroll
  for (int i = 0; i < 4; ++i) {
    const auto a0 = __builtin_amdgcn_permlane32_swap(u[0][i], u[2][i], false, false);
    const auto a1 = __builtin_amdgcn_permlane32_swap(u[1][i], u[3][i], false, false);
    const auto b0 = __builtin_amdgcn_permlane16_swap(a0[0], a1[0], false, false);
    const auto b1 = __builtin_amdgcn_permlane16_swap(a0[1], a1[1], false, false);
    u[0][i] = b0[0]; u[1][i] = b0[1]; u[2][i] = b1[0]; u[3][i] = b1[1];
  }
#pragma unroll
  for (int t = 0; t < 4; ++t) bst16<PREP_AUX>(u[t], rs, so, j.dst[t]);
}
#define PIN8(r) "+v"(r[0]), "+v"(r[1]), "+v"(r[2]), "+v"(r[3]), "+v"(r[4]), "+v"(r[5]), "+v"(r[6]), "+v"(r[7])

#define A_MFMA_ACC(d, a, b) asm volatile("v_mfma_f32_32x32x16_bf16 %0, %1, %2, %0" : "+v"(d) : "v"(a), "v"(b))
#define AT_M(m)
#define AT_U
struct AttnC {
  LDSP const char *kmb, *ktb, *vb;
  unsigned kma[3], kta[3], va[3]; unsigned long long mk, mv;
  u32x4 qm, qt, kf, v0, v1, p1; u32x2 ktl;
  f32x16 negm, o, sa, sb;
};
constexpr int k_off(int T) { return (T >> 4) * (int)CHUNK_B + (T & 15) * 1024; }
constexpr int kt_off(int T) { return (T >> 4) * (int)CHUNK_B + ((T >> 1) & 7) * 256; }
constexpr int v_off(int T) { return (T >> 4) * (int)CHUNK_B + (T & 15) * 1216; }
#define AT_STMT(TXT)                                                                                                              \
  asm volatile(TXT : "+{v[2:17]}"(c.o), "+{v[34:49]}"(c.sa), "+{v[50:65]}"(c.sb), "+{v[66:69]}"(c.kf), "+{v[70:73]}"(c.p1),        \
               "+{v[84:87]}"(c.v1), "+{v[88:91]}"(c.v0), "+{v[92:93]}"(c.ktl)                                                      \
               : "{v[18:33]}"(c.negm), "{v[98:101]}"(c.qm), "{v[102:105]}"(c.qt), [ka] "v"(c.kma[KO >> 16]), [kt] "v"(c.kta[KT >> 16]), \
                 [va] "v"(c.va[V1 >> 16]), [vb] "v"(c.va[V0 >> 16]), [ko] "i"(KO & 0xffff), [kto] "i"(KT & 0xffff), [vo1] "i"(V1 & 0xffff),  \
                 [vo0] "i"(V0 & 0xffff), [mk] "s"(c.mk), [mv] "s"(c.mv)                                                            \
               : "v74", "v75", "v76", "v77")
template <int T> DI void attn_tile(AttnC& c) {
  constexpr int KO = k_off(T < 62 ? T + 2 : 0), KT = kt_off(T < 62 ? T + 2 : 0), V1 = v_off(T) + 608, V0 = v_off(T < 63 ? T + 1 : 0);
  if constexpr (T == 0)
    AT_STMT("v_mfma_f32_32x32x8_bf16 v[50:65], v[92:93], v[104:105], v[18:33]\n\t" "v_exp_f32 v34, v34\n\t" "v_exp_f32 v35, v35\n\t" "v_exp_f32 v36, v36\n\t" "v_exp_f32 v37, v37\n\t" AT_M("mk") "ds_read_b64 v[92:93], %[kt] offset:%[kto]\n\t" AT_U AT_M("mv") "ds_read_b128 v[84:87], %[va] offset:%[vo1]\n\t" AT_U "v_cvt_pk_bf16_f32 v74, v34, v35\n\t" "v_exp_f32 v38, v38\n\t" "v_cvt_pk_bf16_f32 v75, v36, v37\n\t" "v_exp_f32 v39, v39\n\t" "v_exp_f32 v40, v40\n\t" "v_exp_f32 v41, v41\n\t" "s_waitcnt lgkmcnt(2)\n\t" "v_mfma_f32_32x32x16_bf16 v[50:65], v[66:69], v[98:101], v[50:65]\n\t" "ds_read_b128 v[66:69], %[ka] offset:%[ko]\n\t" "v_cvt_pk_bf16_f32 v76, v38, v39\n\t" "v_exp_f32 v42, v42\n\t" "v_cvt_pk_bf16_f32 v77, v40, v41\n\t" "v_exp_f32 v43, v43\n\t" "v_exp_f32 v44, v44\n\t" "v_exp_f32 v45, v45\n\t" "s_waitcnt lgkmcnt(2)\n\t" "v_mfma_f32_32x32x16_bf16 v[2:17], v[88:91], v[74:77], v[2:17]\n\t" AT_M("mv") "ds_read_b128 v[88:91], %[vb] offset:%[vo0]\n\t" AT_U "v_cvt_pk_bf16_f32 v70, v42, v43\n\t" "v_exp_f32 v46, v46\n\t" "v_cvt_pk_bf16_f32 v71, v44, v45\n\t" "v_exp_f32 v47, v47\n\t" "v_exp_f32 v48, v48\n\t" "v_exp_f32 v49, v49\n\t" "v_cvt_pk_bf16_f32 v72, v46, v47\n\t" "v_cvt_pk_bf16_f32 v73, v48, v49\n\t");
  else if constexpr (T == 62)
    AT_STMT("v_mfma_f32_32x32x8_bf16 v[50:65], v[92:93], v[104:105], v[18:33]\n\t" "v_exp_f32 v34, v34\n\t" "v_exp_f32 v35, v35\n\t" "v_exp_f32 v36, v36\n\t" "v_exp_f32 v37, v37\n\t" "s_waitcnt lgkmcnt(2)\n\t" "v_mfma_f32_32x32x16_bf16 v[2:17], v[84:87], v[70:73], v[2:17]\n\t" AT_M("mv") "ds_read_b128 v[84:87], %[va] offset:%[vo1]\n\t" AT_U "v_cvt_pk_bf16_f32 v74, v34, v35\n\t" "v_exp_f32 v38, v38\n\t" "v_cvt_pk_bf16_f32 v75, v36, v37\n\t" "v_exp_f32 v39, v39\n\t" "v_exp_f32 v40, v40\n\t" "v_exp_f32 v41, v41\n\t" "s_waitcnt lgkmcnt(2)\n\t" "v_mfma_f32_32x32x16_bf16 v[50:65], v[66:69], v[98:101], v[50:65]\n\t" "v_cvt_pk_bf16_f32 v76, v38, v39\n\t" "v_exp_f32 v42, v42\n\t" "v_cvt_pk_bf16_f32 v77, v40, v41\n\t" "v_exp_f32 v43, v43\n\t" "v_exp_f32 v44, v44\n\t" "v_exp_f32 v45, v45\n\t" "s_waitcnt lgkmcnt(1)\n\t" "v_mfma_f32_32x32x16_bf16 v[2:17], v[88:91], v[74:77], v[2:17]\n\t" AT_M("mv") "ds_read_b128 v[88:91], %[vb] offset:%[vo0]\n\t" AT_U "v_cvt_pk_bf16_f32 v70, v42, v43\n\t" "v_exp_f32 v46, v46\n\t" "v_cvt_pk_bf16_f32 v71, v44, v45\n\t" "v_exp_f32 v47, v47\n\t" "v_exp_f32 v48, v48\n\t" "v_exp_f32 v49, v49\n\t" "v_cvt_pk_bf16_f32 v72, v46, v47\n\t" "v_cvt_pk_bf16_f32 v73, v48, v49\n\t");
  else if constexpr (T == 63)
    AT_STMT("v_exp_f32 v50, v50\n\t" "v_exp_f32 v51, v51\n\t" "v_exp_f32 v52, v52\n\t" "v_exp_f32 v53, v53\n\t" "s_waitcnt lgkmcnt(0)\n\t" "v_mfma_f32_32x32x16_bf16 v[2:17], v[84:87], v[70:73], v[2:17]\n\t" AT_M("mv") "ds_read_b128 v[84:87], %[va] offset:%[vo1]\n\t" AT_U "v_cvt_pk_bf16_f32 v74, v50, v51\n\t" "v_exp_f32 v54, v54\n\t" "v_cvt_pk_bf16_f32 v75, v52, v53\n\t" "v_exp_f32 v55, v55\n\t" "v_exp_f32 v56, v56\n\t" "v_exp_f32 v57, v57\n\t" "v_cvt_pk_bf16_f32 v76, v54, v55\n\t" "v_exp_f32 v58, v58\n\t" "v_cvt_pk_bf16_f32 v77, v56, v57\n\t" "v_exp_f32 v59, v59\n\t" "v_exp_f32 v60, v60\n\t" "v_exp_f32 v61, v61\n\t" "s_waitcnt lgkmcnt(1)\n\t" "v_mfma_f32_32x32x16_bf16 v[2:17], v[88:91], v[74:77], v[2:17]\n\t" "v_cvt_pk_bf16_f32 v70, v58, v59\n\t" "v_exp_f32 v62, v62\n\t" "v_cvt_pk_bf16_f32 v71, v60, v61\n\t" "v_exp_f32 v63, v63\n\t" "v_exp_f32 v64, v64\n\t" "v_exp_f32 v65, v65\n\t" "v_cvt_pk_bf16_f32 v72, v62, v63\n\t" "v_cvt_pk_bf16_f32 v73, v64, v65\n\t");
  else if constexpr ((T & 3) == 0)
    AT_STMT("v_mfma_f32_32x32x8_bf16 v[50:65], v[92:93], v[104:105], v[18:33]\n\t" "v_exp_f32 v34, v34\n\t" "v_exp_f32 v35, v35\n\t" "v_exp_f32 v36, v36\n\t" "v_exp_f32 v37, v37\n\t" "s_waitcnt lgkmcnt(2)\n\t" "v_mfma_f32_32x32x16_bf16 v[2:17], v[84:87], v[70:73], v[2:17]\n\t" AT_M("mk") "ds_read_b64 v[92:93], %[kt] offset:%[kto]\n\t" AT_U AT_M("mv") "ds_read_b128 v[84:87], %[va] offset:%[vo1]\n\t" AT_U "v_cvt_pk_bf16_f32 v74, v34, v35\n\t" "v_exp_f32 v38, v38\n\t" "v_cvt_pk_bf16_f32 v75, v36, v37\n\t" "v_exp_f32 v39, v39\n\t" "v_exp_f32 v40, v40\n\t" "v_exp_f32 v41, v41\n\t" "s_waitcnt lgkmcnt(2)\n\t" "v_mfma_f32_32x32x16_bf16 v[50:65], v[66:69], v[98:101], v[50:65]\n\t" "ds_read_b128 v[66:69], %[ka] offset:%[ko]\n\t" "v_cvt_pk_bf16_f32 v76, v38, v39\n\t" "v_exp_f32 v42, v42\n\t" "v_cvt_pk_bf16_f32 v77, v40, v41\n\t" "v_exp_f32 v43, v43\n\t" "v_exp_f32 v44, v44\n\t" "v_exp_f32 v45, v45\n\t" "s_waitcnt lgkmcnt(2)\n\t" "v_mfma_f32_32x32x16_bf16 v[2:17], v[88:91], v[74:77], v[2:17]\n\t" AT_M("mv") "ds_read_b128 v[88:91], %[vb] offset:%[vo0]\n\t" AT_U "v_cvt_pk_bf16_f32 v70, v42, v43\n\t" "v_exp_f32 v46, v46\n\t" "v_cvt_pk_bf16_f32 v71, v44, v45\n\t" "v_exp_f32 v47, v47\n\t" "v_exp_f32 v48, v48\n\t" "v_exp_f32 v49, v49\n\t" "v_cvt_pk_bf16_f32 v72, v46, v47\n\t" "v_cvt_pk_bf16_f32 v73, v48, v49\n\t");
  else if constexpr ((T & 3) == 1)
    AT_STMT("v_mfma_f32_32x32x8_bf16 v[34:49], v[92:93], v[102:103], v[18:33]\n\t" "v_exp_f32 v50, v50\n\t" "v_exp_f32 v51, v51\n\t" "v_exp_f32 v52, v52\n\t" "v_exp_f32 v53, v53\n\t" "s_waitcnt lgkmcnt(2)\n\t" "v_mfma_f32_32x32x16_bf16 v[2:17], v[84:87], v[70:73], v[2:17]\n\t" AT_M("mv") "ds_read_b128 v[84:87], %[va] offset:%[vo1]\n\t" AT_U "v_cvt_pk_bf16_f32 v74, v50, v51\n\t" "v_exp_f32 v54, v54\n\t" "v_cvt_pk_bf16_f32 v75, v52, v53\n\t" "v_exp_f32 v55, v55\n\t" "v_exp_f32 v56, v56\n\t" "v_exp_f32 v57, v57\n\t" "s_waitcnt lgkmcnt(2)\n\t" "v_mfma_f32_32x32x16_bf16 v[34:49], v[66:69], v[98:101], v[34:49]\n\t" "ds_read_b128 v[66:69], %[ka] offset:%[ko]\n\t" "v_cvt_pk_bf16_f32 v76, v54, v55\n\t" "v_exp_f32 v58, v58\n\t" "v_cvt_pk_bf16_f32 v77, v56, v57\n\t" "v_exp_f32 v59, v59\n\t" "v_exp_f32 v60, v60\n\t" "v_exp_f32 v61, v61\n\t" "s_waitcnt lgkmcnt(2)\n\t" "v_mfma_f32_32x32x16_bf16 v[2:17], v[88:91], v[74:77], v[2:17]\n\t" AT_M("mv") "ds_read_b128 v[88:91], %[vb] offset:%[vo0]\n\t" AT_U "v_cvt_pk_bf16_f32 v70, v58, v59\n\t" "v_exp_f32 v62, v62\n\t" "v_cvt_pk_bf16_f32 v71, v60, v61\n\t" "v_exp_f32 v63, v63\n\t" "v_exp_f32 v64, v64\n\t" "v_exp_f32 v65, v65\n\t" "v_cvt_pk_bf16_f32 v72, v62, v63\n\t" "v_cvt_pk_bf16_f32 v73, v64, v65\n\t");
  else if constexpr ((T & 3) == 2)
    AT_STMT("v_mfma_f32_32x32x8_bf16 v[50:65], v[92:93], v[104:105], v[18:33]\n\t" "v_exp_f32 v34, v34\n\t" "v_exp_f32 v35, v35\n\t" "v_exp_f32 v36, v36\n\t" "v_exp_f32 v37, v37\n\t" "s_waitcnt lgkmcnt(2)\n\t" "v_mfma_f32_32x32x16_bf16 v[2:17], v[84:87], v[70:73], v[2:17]\n\t" AT_M("mk") "ds_read_b64 v[92:93], %[kt] offset:%[kto]\n\t" AT_U AT_M("mv") "ds_read_b128 v[84:87], %[va] offset:%[vo1]\n\t" AT_U "v_cvt_pk_bf16_f32 v74, v34, v35\n\t" "v_exp_f32 v38, v38\n\t" "v_cvt_pk_bf16_f32 v75, v36, v37\n\t" "v_exp_f32 v39, v39\n\t" "v_exp_f32 v40, v40\n\t" "v_exp_f32 v41, v41\n\t" "s_waitcnt lgkmcnt(2)\n\t" "v_mfma_f32_32x32x16_bf16 v[50:65], v[66:69], v[98:101], v[50:65]\n\t" "ds_read_b128 v[66:69], %[ka] offset:%[ko]\n\t" "v_cvt_pk_bf16_f32 v76, v38, v39\n\t" "v_exp_f32 v42, v42\n\t" "v_cvt_pk_bf16_f32 v77, v40, v41\n\t" "v_exp_f32 v43, v43\n\t" "v_exp_f32 v44, v44\n\t" "v_exp_f32 v45, v45\n\t" "s_waitcnt lgkmcnt(2)\n\t" "v_mfma_f32_32x32x16_bf16 v[2:17], v[88:91], v[74:77], v[2:17]\n\t" AT_M("mv") "ds_read_b128 v[88:91], %[vb] offset:%[vo0]\n\t" AT_U "v_cvt_pk_bf16_f32 v70, v42, v43\n\t" "v_exp_f32 v46, v46\n\t" "v_cvt_pk_bf16_f32 v71, v44, v45\n\t" "v_exp_f32 v47, v47\n\t" "v_exp_f32 v48, v48\n\t" "v_exp_f32 v49, v49\n\t" "v_cvt_pk_bf16_f32 v72, v46, v47\n\t" "v_cvt_pk_bf16_f32 v73, v48, v49\n\t");
  else if constexpr ((T & 3) == 3)
    AT_STMT("v_mfma_f32_32x32x8_bf16 v[34:49], v[92:93], v[102:103], v[18:33]\n\t" "v_exp_f32 v50, v50\n\t" "v_exp_f32 v51, v51\n\t" "v_exp_f32 v52, v52\n\t" "v_exp_f32 v53, v53\n\t" "s_waitcnt lgkmcnt(2)\n\t" "v_mfma_f32_32x32x16_bf16 v[2:17], v[84:87], v[70:73], v[2:17]\n\t" AT_M("mv") "ds_read_b128 v[84:87], %[va] offset:%[vo1]\n\t" AT_U "v_cvt_pk_bf16_f32 v74, v50, v51\n\t" "v_exp_f32 v54, v54\n\t" "v_cvt_pk_bf16_f32 v75, v52, v53\n\t" "v_exp_f32 v55, v55\n\t" "v_exp_f32 v56, v56\n\t" "v_exp_f32 v57, v57\n\t" "s_waitcnt lgkmcnt(2)\n\t" "v_mfma_f32_32x32x16_bf16 v[34:49], v[66:69], v[98:101], v[34:49]\n\t" "ds_read_b128 v[66:69], %[ka] offset:%[ko]\n\t" "v_cvt_pk_bf16_f32 v76, v54, v55\n\t" "v_exp_f32 v58, v58\n\t" "v_cvt_pk_bf16_f32 v77, v56, v57\n\t" "v_exp_f32 v59, v59\n\t" "v_exp_f32 v60, v60\n\t" "v_exp_f32 v61, v61\n\t" "s_waitcnt lgkmcnt(2)\n\t" "v_mfma_f32_32x32x16_bf16 v[2:17], v[88:91], v[74:77], v[2:17]\n\t" AT_M("mv") "ds_read_b128 v[88:91], %[vb] offset:%[vo0]\n\t" AT_U "v_cvt_pk_bf16_f32 v70, v58, v59\n\t" "v_exp_f32 v62, v62\n\t" "v_cvt_pk_bf16_f32 v71, v60, v61\n\t" "v_exp_f32 v63, v63\n\t" "v_exp_f32 v64, v64\n\t" "v_exp_f32 v65, v65\n\t" "v_cvt_pk_bf16_f32 v72, v62, v63\n\t" "v_cvt_pk_bf16_f32 v73, v64, v65\n\t");
}
DI void attn_group_slow(LDSP const char* km, LDSP const char* kt, LDSP const char* vp, const u32x4& qm, unsigned qtl, const f32x16& negm, f32x16& o) {
#pragma unroll
  for (int t = 0; t < 4; ++t) {
    const u32x2 k2 = *(LDSP const u32x2*)(kt + (t >> 1) * 256);
    const u32x4 ktl = {k2[0], k2[1], 0, 0};
    const u32x4 kf = ldsr16(km + t * 1024);
    const u32x4 v0 = ldsr16(vp + t * 1216), v1 = ldsr16(vp + t * 1216 + 608);
    u32x4 qq = {0, 0, 0, 0}; qq[t & 1] = qtl;
    f32x16 s = mfma32(ktl, qq, negm);
    s = mfma32(kf, qm, s);
    u32x4 p0, p1;
#pragma unroll
    for (int i = 0; i < 4; ++i) {
      p0[i] = cvtpk(__builtin_amdgcn_exp2f(s[2 * i]), __builtin_amdgcn_exp2f(s[2 * i + 1]));
      p1[i] = cvtpk(__builtin_amdgcn_exp2f(s[8 + 2 * i]), __builtin_amdgcn_exp2f(s[8 + 2 * i + 1]));
    }
    o = mfma32(v0, p0, o);
    o = mfma32(v1, p1, o);
  }
}
template <int G> DI void attn_group(AttnC& c) {
  __builtin_amdgcn_s_setprio(G < 5 ? 3 - (G & 3) : (G < 8 ? 3 : (G < 11 ? 2 : (G < 14 ? 1 : 0))));
  attn_tile<4 * G + 0>(c); attn_tile<4 * G + 1>(c); attn_tile<4 * G + 2>(c); attn_tile<4 * G + 3>(c);
}
DI void attn_helper(const u16* x, const u16* wo, const u16* wg, const u16* wu, const u16* wd, char* ws, int head, int j, int lane, unsigned lds0) {
  __builtin_amdgcn_s_setprio(HELP_PRIO);
  const char* kv = ws + WS_KV + (size_t)head * KV_HEAD_B;
  const unsigned lane16 = lane * 16;
#define KV_DMA(o) dma16(kv + (o), lane16, lds0 + (o))
#pragma unroll
  for (int i = 0; i < 4; ++i) KV_DMA(((i >> 1) * 16 + (i & 1) * 4 + j) * 1024);
#pragma unroll
  for (int i = 0; i < 6; ++i) { const int p = i < 2 ? 8 + 4 * i + j : min(16 + 4 * i + j, 36); KV_DMA(p * 1024); }
  const int hw = blockIdx.x * 4 + j;
  PrepJob jb[4];
#pragma unroll
  for (int i = 0; i < 4; ++i) { prep_setup(jb[i], 4 * hw + i, lane, wo, wg, wu, wd); PIN(jb[i].voff); }
  SBAR();
  asm volatile("s_waitcnt vmcnt(6)\n\ts_barrier" ::: "memory");
#pragma unroll
  for (int i = 0; i < 10; ++i) KV_DMA((unsigned)CHUNK_B + min(4 * i + j, 36) * 1024);
  asm volatile("s_waitcnt vmcnt(10)\n\ts_barrier" ::: "memory");
#pragma unroll
  for (int i = 0; i < 19; ++i) KV_DMA(2 * (unsigned)CHUNK_B + min(4 * i + j, 73) * 1024);
#undef KV_DMA
  u32x2 r[4][8];
  u32x4 xu[3];
  unsigned wr[5], pf;
  asm volatile("s_waitcnt vmcnt(19)\n\ts_barrier" ::: "memory");
  asm volatile("s_waitcnt vmcnt(0)\n\ts_barrier" ::: "memory");
  prep_load_all(jb[0], r[0]); prep_load_all(jb[1], r[1]);
  const unsigned xl = (lane >> 3) * 1152 + (lane & 7) * 16, xs = (lane & 7) * 256 + (lane >> 3) * 16;
  int xj[3];
#pragma unroll
  for (int i = 0; i < 3; ++i) { xj[i] = i < 2 ? 2 * hw + i : (hw < 256 ? 2048 + hw : 2 * hw); xu[i] = gld16((const char*)x + ((xj[i] / 9) * (8 * 1152) + (xj[i] % 9) * 128), xl); }
  const int br = hw < DM ? hw : DM - 1;
  {
    const char* b = (const char*)wo + (size_t)wo_row(br >> 5, (br >> 3) & 3, br & 7) * (DM * 2);
#pragma unroll
    for (int i = 0; i < 5; ++i) wr[i] = gld4(b, (i < 4 ? lane + 64 * i : 256 + (lane & 31)) * 4);
  }
  prep_load_all(jb[2], r[2]); prep_load_all(jb[3], r[3]);
  CODE_PF2(pf, k_oproj, k_end, 0, j, (blockIdx.x >> 3) == 0 ? 128u : 0u);
  const __amdgpu_buffer_rsrc_t wrs = mkrsrc(ws, (unsigned)WS_END);
  const unsigned pso = ((lane >> 2) & 3) * 256 + ((lane & 3) * 4 + (lane >> 4)) * 16;
  asm volatile("s_waitcnt vmcnt(33)" : PIN8(r[0])::"memory");
  prep_store(jb[0], r[0], wrs, pso);
  asm volatile("s_waitcnt vmcnt(29)" : PIN8(r[1])::"memory");
  prep_store(jb[1], r[1], wrs, pso);
  asm volatile("s_waitcnt vmcnt(25)" : "+v"(xu[0]), "+v"(xu[1]), "+v"(xu[2]), "+v"(wr[0]), "+v"(wr[1]), "+v"(wr[2]), "+v"(wr[3]), "+v"(wr[4])::"memory");
#pragma unroll
  for (int i = 0; i < 3; ++i) bst16<PREP_AUX>(xu[i], wrs, xs, (unsigned)WS_XU + (xj[i] / 18) * 18432 + (xj[i] % 9) * 2048 + ((xj[i] / 9) & 1) * 128);
  {
    float a = 0.f;
#pragma unroll
    for (int i = 0; i < 4; ++i) a = dot2(wr[i], 0x3f803f80u, a);
    a = dot2(lane >= 32 ? 0u : wr[4], 0x3f803f80u, a);
    a = xor16_sum(xor32_sum(a));
    a += __uint_as_float(__builtin_amdgcn_update_dpp(0u, __float_as_uint(a), 0x128, 0xf, 0xf, false));
    a += __uint_as_float(__builtin_amdgcn_update_dpp(0u, __float_as_uint(a), 0x124, 0xf, 0xf, false));
    a += __uint_as_float(__builtin_amdgcn_update_dpp(0u, __float_as_uint(a), 0x122, 0xf, 0xf, false));
    a += __uint_as_float(__builtin_amdgcn_update_dpp(0u, __float_as_uint(a), 0x121, 0xf, 0xf, false));
    a *= (1.0f / DM);
    const unsigned hi = cvtpk(a, 0.f) & 0xffffu;
    const unsigned lo = cvtpk(a - bflo(hi), 0.f) & 0xffffu;
    __builtin_amdgcn_raw_buffer_store_b16((u16)(lane ? lo : hi), wrs, lane < 2 ? (unsigned)WS_WBAR + (lane * DM + br) * 2 : 0x80000000u, 0, 0);
  }
  asm volatile("s_waitcnt vmcnt(21)" : PIN8(r[2])::"memory");
  prep_store(jb[2], r[2], wrs, pso);
  asm volatile("s_waitcnt vmcnt(16)" : PIN8(r[3]), "+v"(pf)::"memory");
  prep_store(jb[3], r[3], wrs, pso);
}
extern "C" __global__ __launch_bounds__(768, 3) void k_attn(const u16* x, const u16* wo, const u16* wg, const u16* wu, const u16* wd, char* ws) {
  extern __shared__ __attribute__((aligned(16))) char smem[];
  asm volatile("" : "+s"(x), "+s"(wo), "+s"(wg), "+s"(wu), "+s"(wd), "+s"(ws));
  LDSP char* lds = (LDSP char*)smem;
  const int tid = threadIdx.x, lane = tid & 63, w = rfl(tid >> 6);
  const int xcd = blockIdx.x & 7, bi = blockIdx.x >> 3;
  const int head = 4 * xcd + (bi & 3), qb = bi >> 2;
  if (__builtin_expect(w >= 8, AT_HFIRST)) { attn_helper(x, wo, wg, wu, wd, ws, head, w - 8, lane, (unsigned)(uintptr_t)lds); return; }
  const int r = lane & 31, h = lane >> 5;
  const int qt32 = 8 * qb + w;
  STAMP_DECL; STAMP(0);
  if (blockIdx.x == 8 && threadIdx.x == 0) ((__attribute__((address_space(1))) unsigned long long*)(ws + (200u << 20)))[1] = __builtin_amdgcn_s_memrealtime();
  AttnC c;
  c.qm = gld16(ws + WS_QM, head * 65536 + qt32 * 1024 + lane * 16);
  unsigned qtl = gld4(ws + WS_QT, head * 8192 + (32 * qt32 + r) * 4);
  u32x4 km0 = gld16(ws + WS_KMAX, ((lane >> 1) * 32 + head) * 64 + (lane & 1) * 32);
  u32x4 km1 = gld16(ws + WS_KMAX, ((lane >> 1) * 32 + head) * 64 + (lane & 1) * 32 + 16);
  const int vrow = r < 19 ? r : ((r >= 20 && r < 28) ? 0 : 18);
  c.kmb = lds + lane * 16;
  c.ktb = lds + 16384 + r * 8;
  c.vb = lds + 18432 + h * 304 + vrow * 16;
#pragma unroll
  for (int i = 0; i < 3; ++i) {
    c.kma[i] = (unsigned)(uintptr_t)c.kmb + i * 65536; c.kta[i] = (unsigned)(uintptr_t)c.ktb + i * 65536; c.va[i] = (unsigned)(uintptr_t)c.vb + i * 65536;
    PIN(c.kma[i]); PIN(c.kta[i]); PIN(c.va[i]);
  }
  c.mk = 0x00000000ffffffffull; c.mv = 0x0007ffff0007ffffull;
  c.ktl = u32x2{0, 0}; c.v0 = u32x4{0, 0, 0, 0}; c.v1 = c.v0; PIN(c.ktl); PIN(c.v0); PIN(c.v1);
#pragma unroll
  for (int i = 0; i < 16; ++i) c.o[i] = 0.f;
  PIN(c.o);
  const __amdgpu_buffer_rsrc_t wrs = mkrsrc(ws, (unsigned)WS_END);
  SBAR();
  asm volatile("s_waitcnt vmcnt(0)" : "+v"(c.qm), "+v"(qtl), "+v"(km0), "+v"(km1)::"memory");
  if (h) qtl = 0;
  float q2 = dot2(qtl, qtl, 0.f);
#pragma unroll
  for (int i = 0; i < 4; ++i) q2 = dot2(c.qm[i], c.qm[i], q2);
  unsigned k2 = max(max(max(km0[0], km0[1]), max(km0[2], km0[3])), max(max(km1[0], km1[1]), max(km1[2], km1[3])));
  { auto rr = __builtin_amdgcn_permlane32_swap(k2, k2, false, false); k2 = max(rr[0], rr[1]); }
  { auto rr = __builtin_amdgcn_permlane16_swap(k2, k2, false, false); k2 = max(rr[0], rr[1]); }
  k2 = max(k2, (unsigned)__builtin_amdgcn_update_dpp(0u, k2, 0x128, 0xf, 0xf, true));
  k2 = max(k2, (unsigned)__builtin_amdgcn_update_dpp(0u, k2, 0x124, 0xf, 0xf, true));
  k2 = max(k2, (unsigned)__builtin_amdgcn_update_dpp(0u, k2, 0x122, 0xf, 0xf, true));
  k2 = max(k2, (unsigned)__builtin_amdgcn_update_dpp(0u, k2, 0x121, 0xf, 0xf, true));
  q2 = xor32_sum(q2);
  const float mshift = __builtin_amdgcn_sqrtf(q2 * __uint_as_float(k2)) * 1.001f;
  c.qt = u32x4{qtl, 0, 0, qtl}; PIN(c.qt);
#pragma unroll
  for (int i = 0; i < 16; ++i) c.negm[i] = -mshift;
  PIN(c.negm);
  STAMP(1);
  asm volatile("s_barrier" ::: "memory");
  STAMP(2);
  asm volatile(AT_M("mk") "ds_read_b64 v[92:93], %[kt]\n\t" AT_U "ds_read_b128 v[70:73], %[ka]\n\tds_read_b128 v[66:69], %[ka] offset:1024\n\t"
               AT_M("mv") "ds_read_b128 v[88:91], %[va]\n\t" AT_U "s_waitcnt lgkmcnt(2)\n\t"
               "v_mfma_f32_32x32x8_bf16 v[34:49], v[92:93], v[102:103], v[18:33]\n\t"
               "v_mfma_f32_32x32x16_bf16 v[34:49], v[70:73], v[98:101], v[34:49]\n\ts_nop 11"
               : "={v[34:49]}"(c.sa), "={v[50:65]}"(c.sb), "={v[66:69]}"(c.kf), "={v[70:73]}"(c.p1), "+{v[88:91]}"(c.v0), "+{v[92:93]}"(c.ktl)
               : "{v[18:33]}"(c.negm), "{v[98:101]}"(c.qm), "{v[102:105]}"(c.qt), [ka] "v"(c.kma[0]), [kt] "v"(c.kta[0]), [va] "v"(c.va[0]),
                 [mk] "s"(c.mk), [mv] "s"(c.mv));
  attn_group<0>(c);
  asm volatile("s_barrier" ::: "memory");
  attn_group<1>(c); attn_group<2>(c);
  asm volatile("s_barrier" ::: "memory");
  attn_group<3>(c); attn_group<4>(c);
  asm volatile("s_barrier" ::: "memory");
  STAMP(3);
  attn_group<5>(c); attn_group<6>(c); attn_group<7>(c); attn_group<8>(c); attn_group<9>(c); attn_group<10>(c);
  attn_group<11>(c); attn_group<12>(c); attn_group<13>(c); attn_group<14>(c); attn_group<15>(c);
  asm volatile("s_waitcnt lgkmcnt(0)\n\tv_mfma_f32_32x32x16_bf16 v[2:17], v[84:87], v[70:73], v[2:17]\n\ts_nop 11"
               : "+{v[2:17]}"(c.o) : "{v[84:87]}"(c.v1), "{v[70:73]}"(c.p1), "{v[18:33]}"(c.negm));
  STAMP(4);
  f32x16 o = c.o;
  float l;
  { auto rr = __builtin_amdgcn_permlane32_swap(__float_as_uint(o[10]), __float_as_uint(o[10]), false, false); l = __uint_as_float(rr[0]); }
  if (__builtin_expect(__any(!(l >= 5.421011e-20f)), 0)) {
    float mx = -INFINITY;
    f32x16 z, negm;
#pragma unroll
    for (int i = 0; i < 16; ++i) z[i] = 0.f;
#pragma unroll 1
    for (int tt = 0; tt < 64; ++tt) {
      const int ck = tt >> 4, tl = tt & 15;
      const u32x2 k2 = *(LDSP const u32x2*)(c.ktb + ck * CHUNK_B + (tl >> 1) * 256);
      const u32x4 ktl = {k2[0], k2[1], 0, 0};
      const u32x4 kf = ldsr16(c.kmb + ck * CHUNK_B + tl * 1024);
      const u32x4 qq = {(tl & 1) ? 0u : qtl, (tl & 1) ? qtl : 0u, 0, 0};
      f32x16 sc = mfma32(ktl, qq, z);
      sc = mfma32(kf, c.qm, sc);
#pragma unroll
      for (int i = 0; i < 16; ++i) mx = fmaxf(mx, sc[i]);
    }
    mx = xor32_max(mx);
#pragma unroll
    for (int i = 0; i < 16; ++i) { negm[i] = -mx; o[i] = 0.f; }
#pragma unroll 1
    for (int gq = 0; gq < 16; ++gq) {
      const int ck = gq >> 2, gg = gq & 3;
      attn_group_slow(c.kmb + ck * CHUNK_B + gg * 4096, c.ktb + ck * CHUNK_B + gg * 512, c.vb + ck * CHUNK_B + gg * 4864, c.qm, qtl, negm, o);
    }
    auto rr = __builtin_amdgcn_permlane32_swap(__float_as_uint(o[10]), __float_as_uint(o[10]), false, false);
    l = __uint_as_float(rr[0]);
  }
  const float rl = 1.0f / l;
  u32x4 ou;
#pragma unroll
  for (int i = 0; i < 4; ++i) ou[i] = cvtpk(o[2 * i] * rl, o[2 * i + 1] * rl);
  const int q = 32 * qt32 + r;
  const unsigned ob = (unsigned)WS_OFR + (q >> 4) * 18432 + (q & 15) * 16;
  bst16<O_AUX>(ou, wrs, ob + (head >> 1) * 1024 + (2 * (head & 1) + h) * 256, 0);
  __builtin_amdgcn_raw_buffer_store_b32(cvtpk(o[8] * rl, o[9] * rl), wrs, h ? 0x80000000u : ob + (16 + (head >> 4)) * 1024 + ((head >> 2) & 3) * 256 + (head & 3) * 4, 0, O_AUX);
  STAMP(5); STAMP_OUT(2, 6);
}

constexpr int K3_WBAR = 54 * 1024, K3_O = K3_WBAR + 3072, K3_LDS = K3_O + 96 * 1024;
extern "C" __global__ __launch_bounds__(768, 3) void k_oproj(char* __restrict__ ws) {
  extern __shared__ __attribute__((aligned(16))) char smem[];
  LDSP char* lds = (LDSP char*)smem;
  const unsigned lds0 = (unsigned)(uintptr_t)lds;
  const int tid = threadIdx.x, lane = tid & 63, w = rfl(tid >> 6);
  const int xcd = blockIdx.x & 7, bi = blockIdx.x >> 3;
  const int rb = 2 * xcd + (bi & 1), nb = bi >> 1;
  const unsigned lane16 = lane * 16;
  if (__builtin_expect(w >= 8, 1)) {
    const int j = w - 8;
    const char* wb = ws + WS_WO + (size_t)nb * 54 * 1024;
    const char* ob = ws + WS_OFR + (size_t)(8 * rb + 2 * j) * 18432;
    const unsigned od = lds0 + K3_O + j * 24576;
#define K3_W(z) _Pragma("unroll") for (int i = 0; i < 5; ++i) { const int p = 18 * (z) + min(4 * i + j, 17); dma16(wb + p * 1024, lane16, lds0 + p * 1024); }
#define K3_OS(z) _Pragma("unroll") for (int i = 0; i < 12; ++i) dma16(ob + ((i & 1) * 18 + 6 * (z) + (i >> 1)) * 1024, lane16, od + ((i & 1) * 12 + 6 * (z) - 6 + (i >> 1)) * 1024)
    K3_W(0);
    dma16(ws + WS_WBAR + min(j, 2) * 1024, lane16, lds0 + K3_WBAR + min(j, 2) * 1024);
    K3_W(1); K3_OS(1);
    asm volatile("s_waitcnt vmcnt(17)\n\ts_barrier" ::: "memory");
    K3_W(2); K3_OS(2);
    asm volatile("s_waitcnt vmcnt(17)\n\ts_barrier" ::: "memory");
    asm volatile("s_waitcnt vmcnt(0)\n\ts_barrier" ::: "memory");
#undef K3_W
#undef K3_OS
    return;
  }
  const int m = lane & 15, g = lane >> 4;
  const int mt = 8 * rb + w, row = 16 * mt + m;
  STAMP_DECL; STAMP(0);
  if (blockIdx.x == 8 && threadIdx.x == 0) ((__attribute__((address_space(1))) unsigned long long*)(ws + (200u << 20)))[2] = __builtin_amdgcn_s_memrealtime();
  u32x4 O[18];
  u32x2 xr[3];
  {
    const char* ob = ws + WS_OFR + (size_t)mt * 18432;
#pragma unroll
    for (int i = 0; i < 6; ++i) O[i] = gld16(ob + i * 1024, lane16);
  }
  const unsigned uo = mt * 18432 + (6 * nb + (g >> 1)) * 256 + m * 16 + (g & 1) * 8;
  f32x4 acc[4];
  LDSP const char* wl = lds + lane16;
  LDSP const char* ol = lds + K3_O + w * 12288 + lane16; PIN(ol);
  LDSP const char* wbl = lds + K3_WBAR + (m & 1) * 1152 + g * 16;
  const __amdgpu_buffer_rsrc_t wrs = mkrsrc(ws, (unsigned)WS_END);
  unsigned so = ((g & 1) ? (unsigned)WS_HC : (unsigned)WS_HU) + (uo & ~8u); PIN(so);
  STAMP(1);
  asm volatile("s_waitcnt vmcnt(0)\n\ts_barrier" : "+v"(O[0]), "+v"(O[1]), "+v"(O[2]), "+v"(O[3]), "+v"(O[4]), "+v"(O[5])::"memory");
#pragma unroll
  for (int t = 0; t < 3; ++t)
    asm volatile("global_load_dwordx2 %0, %1, %2 offset:%3" : "=v"(xr[t]) : "v"(uo), "s"(ws + WS_XU), "i"(t * 512));
  unsigned xm = gld4(ws + WS_XMEAN, row * 4);
  u32x4 wf[6];
#define K3_RD(f) (((f) & 3) == 3 ? ldsr16(wbl + ((f) >> 2) * 64) : ldsr16(wl + (3 * ((f) >> 2) + ((f) & 3)) * 1024))
#pragma unroll
  for (int f = 0; f < 5; ++f) wf[f] = K3_RD(f);
#pragma unroll
  for (int f = 0; f < 72; ++f) {
    if (f == 19 || f == 43) {
      asm volatile("s_barrier" ::: "memory");
#pragma unroll
      for (int i = 0; i < 6; ++i) O[(f + 5) / 4 + i] = ldsr16(ol + ((f + 5) / 4 + i - 6) * 1024);
    }
    if (f + 5 < 72) wf[(f + 5) % 6] = K3_RD(f + 5);
    if (f < 4) G_MFMA0(acc[f & 3], wf[f % 6], O[f >> 2]);
    else G_MFMA(acc[f & 3], wf[f % 6], O[f >> 2]);
    SBAR();
  }
  asm volatile("s_waitcnt vmcnt(0)" : "+v"(xr[0]), "+v"(xr[1]), "+v"(xr[2]), "+v"(xm));
  asm volatile("s_nop 7" : "+v"(acc[0]), "+v"(acc[1]), "+v"(acc[2]), "+v"(acc[3]));
#undef K3_RD
  STAMP(2);
  const float mean = __uint_as_float(xm) + acc[3][0] + acc[3][1];
  float ssq = 0.f;
#pragma unroll
  for (int t = 0; t < 3; ++t) {
    const unsigned h0 = cvtpk(bflo(xr[t][0]) + acc[t][0], bfhi(xr[t][0]) + acc[t][1]);
    const unsigned h1 = cvtpk(bflo(xr[t][1]) + acc[t][2], bfhi(xr[t][1]) + acc[t][3]);
    const unsigned c0 = cvtpk(bflo(h0) - mean, bfhi(h0) - mean);
    const unsigned c1 = cvtpk(bflo(h1) - mean, bfhi(h1) - mean);
    ssq = dot2(c0, c0, ssq); ssq = dot2(c1, c1, ssq);
    const auto r0 = __builtin_amdgcn_permlane16_swap(h0, c0, false, false);
    const auto r1 = __builtin_amdgcn_permlane16_swap(h1, c1, false, false);
    bst16<H_AUX>(u32x4{r0[0], r1[0], r0[1], r1[1]}, wrs, so + t * 512, 0);
  }
  ssq = xor32_sum(xor16_sum(ssq));
  __builtin_amdgcn_raw_buffer_store_b32(__float_as_uint(ssq), wrs, (unsigned)WS_SSQ + (nb * 2048 + row) * 4, 0, H_AUX);
  STAMP(3); STAMP_OUT(3, 4);
}

constexpr int K4_SLOT = 36864, K4_LDS = 163840;
#define K4_PFD 4
constexpr int K4_PF = K4_PFD;
typedef __attribute__((address_space(1))) unsigned gu32;
constexpr int K5_PERM[18] = {0, 1, 2, 6, 7, 8, 9, 10, 11, 15, 16, 17, 3, 4, 5, 12, 13, 14};
constexpr int K5_R[4] = {36864, 73728, 110592, 0};
constexpr int K5_OC[9] = {65536, 102400, 139264, 147456, 155648, 0, 8192, 16384, 24576};
struct K5P { const char* wb; unsigned q[4], qs[4], qo, qos, lds0; };
template <int Z> DI void k5_piece(const K5P& k, int i, int w, unsigned lane16) {
  constexpr int P0 = K5_PERM[2 * Z] * 9216, D = (K5_PERM[2 * Z + 1] - K5_PERM[2 * Z]) * 9216;
  if (i < 3 || (w >> 2) == (Z & 1))
    dma16(k.wb + (P0 + k.q[i] + k.qs[i] * D), lane16, k.lds0 + K5_R[Z & 3] + (i < 3 ? 8 * i + w : 24 + (w & 3)) * 1024);
}
template <int Z> DI void k5_own(const K5P& k, int w, unsigned lane16) {
  constexpr int P0 = K5_PERM[2 * Z] * 9216, D = (K5_PERM[2 * Z + 1] - K5_PERM[2 * Z]) * 9216;
  dma16(k.wb + (P0 + k.qo + k.qos * D), lane16, k.lds0 + K5_OC[Z] + w * 1024);
}
DI void wait_count(gu32* cnt, unsigned first, unsigned want) {
  if (rfl(first) >= want) return;
  for (unsigned spin = 0; spin < (1u << 16); ++spin) {
    if (rfl(__hip_atomic_load(cnt, __ATOMIC_RELAXED, __HIP_MEMORY_SCOPE_AGENT)) >= want) break;
    __builtin_amdgcn_s_sleep(1);
  }
}
#define POLL(dst, p) asm volatile("global_load_dword %0, %1, %2 sc1" : "=v"(dst) : "v"(0u), "s"(p))
struct K4C {
  const char *wb, *xb; unsigned lane16, lds0; int w, dir;
  unsigned pf0, pfo, pf1, poll; const char* pfb;
  K5P wd;
  gu32 *cntA, *cntB;
  const char *ab, *ab2; u32x4 A0[2][4];
  LDSP const char* wl[4];
  u32x4 X[2][18], wf[K4_PF + 1];
  f32x4 acc[4][4];
  float c1[2], c2[2];
  __amdgpu_buffer_rsrc_t ars; unsigned avo; int T0;
  float ea[2][2], et[2][2]; unsigned pk[2][4];
  f32x2 pa[2], pt[2], c1p[2], c2p[2];
};
DI void k4_piece(const K4C& c, int tile, int p) {
  dma16(c.wb + (tile * c.dir * K4_SLOT + p * 1024), c.lane16, c.lds0 + (tile & 3) * K4_SLOT + p * 1024);
}
template <int ZW> DI void k4_issue_w(K4C& c, int i) {
  if (ZW < 9) {
    if (i == 0) k4_piece(c, c.w >> 2, 4 * ZW + (c.w & 3));
    else if ((c.w >> 2) == (ZW & 1)) k4_piece(c, 2, 4 * ZW + (c.w & 3));
  } else if (ZW == 9) { k4_piece(c, 3, 8 * i + c.w); }
}
template <int Z, int I> DI void k4_issue1(K4C& c) {
  if (I == 0) k4_issue_w<Z + K4_WD>(c, 0);
  else if (I < 5) {
    c.X[(I - 1) >> 1][2 * Z + ((I - 1) & 1)] = gld16(c.xb + (((I - 1) >> 1) * 18432 + (2 * Z + ((I - 1) & 1)) * 1024), c.lane16);
  }
  else k4_issue_w<Z + K4_WD>(c, 1);
}
template <int TL, int I> DI void k4_issue2(K4C& c) {
  if (I < 4) k4_piece(c, TL, 8 * I + c.w);
  else if ((c.w >> 2) == (TL & 1)) k4_piece(c, TL, 32 + (c.w & 3));
}
template <int TL, int E, int U> DI void k4_epi(K4C& c) {
  constexpr int e = U >> 2, u = U & 3, mt = e >> 2, i = e & 3, q = e & 1;
  if constexpr (K4_PK && U < 32) {
    constexpr int pr = U >> 3, v = U & 7, m2 = pr >> 1, i0 = 2 * (pr & 1);
    if (v == 0) {
      const f32x2 G = {c.acc[TL & 3][m2][i0], c.acc[TL & 3][m2][i0 + 1]};
      asm volatile("v_pk_mul_f32 %0, %1, %2" : "=v"(c.pa[E]) : "v"(G), "v"(c.c1p[m2]));
    }
    if (v == 1) { const f32x2 Uu = {c.acc[TL & 3][2 + m2][i0], c.acc[TL & 3][2 + m2][i0 + 1]};
      asm volatile("v_pk_mul_f32 %0, %1, %2" : "=v"(c.pt[E]) : "v"(Uu), "v"(c.pa[E]));
      c.pa[E][0] = __builtin_amdgcn_exp2f(c.pa[E][0]); PIN(c.pa[E]); }
    if (v == 2) { c.pa[E][1] = __builtin_amdgcn_exp2f(c.pa[E][1]); PIN(c.pa[E]); }
    if (v == 3) asm volatile("v_pk_fma_f32 %0, %0, %1, %1" : "+v"(c.pa[E]) : "v"(c.c2p[m2]));
    if (v == 4) { c.pa[E][0] = __builtin_amdgcn_rcpf(c.pa[E][0]); PIN(c.pa[E]); }
    if (v == 5) { c.pa[E][1] = __builtin_amdgcn_rcpf(c.pa[E][1]); PIN(c.pa[E]); }
    if (v == 6) asm volatile("v_pk_mul_f32 %0, %0, %1" : "+v"(c.pt[E]) : "v"(c.pa[E]));
    if (v == 7) { c.pk[E][pr] = cvtpk(c.pt[E][0], c.pt[E][1]); PIN(c.pk[E][pr]); }
  } else if constexpr (U < 32) {
    if (u == 0) { const float a = c.acc[TL & 3][mt][i] * c.c1[mt]; c.ea[E][q] = a; c.et[E][q] = a * c.acc[TL & 3][2 + mt][i]; PIN(c.ea[E][q]); PIN(c.et[E][q]); }
    if (u == 1) { c.ea[E][q] = __builtin_amdgcn_exp2f(c.ea[E][q]); PIN(c.ea[E][q]); }
    if (u == 2) { c.ea[E][q] = __builtin_amdgcn_rcpf(__builtin_fmaf(c.ea[E][q], c.c2[mt], c.c2[mt])); PIN(c.ea[E][q]); }
    if (u == 3) { c.et[E][q] *= c.ea[E][q]; PIN(c.et[E][q]); if (q) { c.pk[E][e >> 1] = cvtpk(c.et[E][0], c.et[E][1]); PIN(c.pk[E][e >> 1]); } }
  } else if constexpr (U == 32) {
    auto r0 = __builtin_amdgcn_permlane16_swap(c.pk[E][0], c.pk[E][2], false, false);
    auto r1 = __builtin_amdgcn_permlane16_swap(c.pk[E][1], c.pk[E][3], false, false);
    c.pk[E][0] = r0[0]; c.pk[E][1] = r1[0]; c.pk[E][2] = r0[1]; c.pk[E][3] = r1[1];
  } else if constexpr (U == 33) {
    const int T = c.T0 + TL * c.dir;
    bst16<ACT_AUX>(u32x4{c.pk[E][0], c.pk[E][1], c.pk[E][2], c.pk[E][3]}, c.ars, c.avo, (T >> 1) * 1024 + (T & 1) * 512);
  }
}
constexpr int k4_tile(int F) { return F < 108 ? (F % 6) >> 1 : 3 + (F - 108) / 36; }
constexpr int k4_step(int F) { return F < 108 ? F / 6 : ((F - 108) % 36) >> 1; }
constexpr int k4_addr(int F) { return k4_step(F) * 2048 + (F & 1) * 1024; }
template <int F> DI void k4_frag(K4C& c) {
  constexpr int TL = k4_tile(F), S = k4_step(F), M = F & 1, N = F + K4_PF;
  const u32x4 a = c.wf[F % (K4_PF + 1)];
  if constexpr (F < 108) { if constexpr ((F + 4) % 3 == 0) __builtin_amdgcn_s_setprio(3 - ((F + 4) % 12) / 3); }
  else if constexpr ((F - 108 + 4) % 9 == 0) __builtin_amdgcn_s_setprio(3 - ((F - 108 + 4) % 36) / 9);
  if (S == 0) {
    G_MFMA0(c.acc[TL & 3][2 * M], a, c.X[0][S]);
    G_MFMA0(c.acc[TL & 3][2 * M + 1], a, c.X[1][S]);
  } else {
    constexpr int o = GRAY ? M : 0;
    G_MFMA(c.acc[TL & 3][2 * M + o], a, c.X[o][S]);
    if constexpr (K4_MID && F >= 108 && (F - 108) % 36 >= 2) {
      SBAR();
      k4_epi<(TL == 3 ? 0 : (TL == 4 ? 2 : TL - 1)), 0, (F - 108) % 36 - 2>(c);
      SBAR();
    }
    G_MFMA(c.acc[TL & 3][2 * M + 1 - o], a, c.X[1 - o][S]);
  }
  if constexpr (N < 108 && N % 12 == 0) {
    constexpr int Z = N / 12;
    if (Z < 8) asm volatile("s_waitcnt vmcnt(5)\n\ts_barrier" : "+v"(c.X[0][2 * Z]), "+v"(c.X[0][2 * Z + 1]), "+v"(c.X[1][2 * Z]), "+v"(c.X[1][2 * Z + 1])::"memory");
    else asm volatile("s_waitcnt vmcnt(2)\n\ts_barrier" : "+v"(c.X[0][2 * Z]), "+v"(c.X[0][2 * Z + 1]), "+v"(c.X[1][2 * Z]), "+v"(c.X[1][2 * Z + 1])::"memory");
  } else if constexpr (N >= 108 && N < 324 && (N - 108) % 36 == 0) {
    constexpr int NT = k4_tile(N);
    if (NT == 3) asm volatile("s_waitcnt vmcnt(0) lgkmcnt(0)\n\ts_barrier" ::: "memory");
    else if (NT == 8) {
      asm volatile("s_waitcnt vmcnt(0) lgkmcnt(0)\n\ts_barrier" ::: "memory");
      if (c.w == 0 && c.lane16 == 0) __hip_atomic_fetch_add(c.cntA, 1u, __ATOMIC_RELAXED, __HIP_MEMORY_SCOPE_AGENT);
    }
    else asm volatile("s_waitcnt vmcnt(4) lgkmcnt(0)\n\ts_barrier" ::: "memory");
  }
  if constexpr (N < 324) c.wf[N % (K4_PF + 1)] = ldsr16(c.wl[k4_tile(N) & 3] + k4_addr(N));
  if constexpr (F < 108) {
    constexpr int Z = F / 12, I = F % 12;
    if constexpr (Z == 0 && I < 4 && !(I & 1)) k4_issue1<2, I / 2>(c);
    if constexpr (Z < 7 && I < 8 && (I & 1)) k4_issue1<Z + 2, 2 + I / 2>(c);
    if constexpr (Z < 6 && I > 8 && (I & 1)) k4_issue1<Z + 3, (I - 9) / 2>(c);
    if constexpr (K4_WD == 0) {
      if constexpr (Z == 6 && I == 10) k4_issue2<3, 0>(c);
      if constexpr (Z == 7 && I < 8 && (I & 1)) k4_issue2<3, 1 + I / 2>(c);
    } else {
      if constexpr (Z == 6 && I == 10) k4_issue2<3, 2>(c);
      if constexpr (Z == 7 && (I == 1 || I == 3)) k4_issue2<3, 3 + I / 2>(c);
    }
  } else {
    constexpr int P = (F - 108) % 36;
    if constexpr (TL == 3) { if constexpr (P % 3 == 0 && P < 15) k4_issue2<4, P / 3>(c); if constexpr (P % 3 == 0 && P >= 15 && P < 30) k4_issue2<5, (P - 15) / 3>(c); }
    else if constexpr (TL + 2 < 9 && P % 4 == 0 && P < 20) k4_issue2<TL + 2, P / 4>(c);
    if constexpr (K4_PFWD && TL >= 4 && TL <= 7 && (P == 33 || (P == 34 && TL < 6))) {
      constexpr int tl = TL < 6 ? 2 * (TL - 4) + P - 33 : TL - 2;
      const int T = c.T0 + tl * c.dir;
      asm volatile("global_load_dword %0, %1, %2" : "+v"(c.pf0) : "v"(c.pfo), "s"(c.pfb + ((T >> 1) * 1024 + (T & 1) * 512)));
    }
    if constexpr (TL == 7 && P == 0) CODE_PF2(c.pf1, k_ffn, k_end, 0, 2 + (c.w & 1), ((blockIdx.x >> 3) == 0 && c.w < 2) ? 128u : 0u);
    if constexpr (TL == 7 && P % 3 == 2 && P <= 11) k5_piece<0>(c.wd, P / 3, c.w, c.lane16);
    if constexpr (TL == 8 && P < 4) k5_piece<1>(c.wd, P, c.w, c.lane16);
    if constexpr (TL == 8 && P >= 4 && P < 8) k5_piece<2>(c.wd, P - 4, c.w, c.lane16);
    if constexpr (TL == 8 && P == K4_POLL) POLL(c.poll, c.cntA);
    if constexpr (TL == 8 && P == K4_POLL + 12) { asm volatile("s_waitcnt vmcnt(0)" : "+v"(c.poll), "+v"(c.pf1), "+v"(c.pf0)); wait_count(c.cntA, c.poll, 8u); }
    if constexpr (TL == 8 && P > K4_POLL + 12 && P <= K4_POLL + 20) {
      constexpr int i = P - K4_POLL - 13, z = i >> 2, j = (i & 1) * 2 + ((i >> 1) & 1);
      asm volatile("global_load_dwordx4 %0, %1, %2 sc1" : "=v"(c.A0[z][j]) : "v"(c.lane16), "s"(((j >> 1) ? c.ab2 : c.ab) + K5_PERM[2 * z + (j & 1)] * 1024));
    }
    if constexpr (P >= 2) {
      if constexpr (TL == 3) { if (!K4_MID) k4_epi<0, 0, P - 2>(c); k4_epi<1, 1, P - 2>(c); }
      else if constexpr (TL == 4) { if (!K4_MID) k4_epi<2, 0, P - 2>(c); k4_epi<3, 1, P - 2>(c); }
      else if (!K4_MID) k4_epi<TL - 1, 0, P - 2>(c);
    }
  }
  SBAR();
}
template <int B, int... Fs> DI void k4_run(K4C& c, std::integer_sequence<int, Fs...>) { (k4_frag<B + Fs>(c), ...); }
DI void k4_last(K4C& c) {
  float a[8], t[8];
#pragma unroll
  for (int e = 0; e < 8; ++e) { a[e] = c.acc[0][e >> 2][e & 3] * c.c1[e >> 2]; t[e] = a[e] * c.acc[0][2 + (e >> 2)][e & 3]; }
#pragma unroll
  for (int e = 0; e < 8; ++e) a[e] = __builtin_amdgcn_exp2f(a[e]);
#pragma unroll
  for (int e = 0; e < 8; ++e) a[e] = __builtin_amdgcn_rcpf(__builtin_fmaf(a[e], c.c2[e >> 2], c.c2[e >> 2]));
#pragma unroll
  for (int e = 0; e < 4; ++e) c.pk[0][e] = cvtpk(t[2 * e] * a[2 * e], t[2 * e + 1] * a[2 * e + 1]);
  k4_epi<8, 0, 32>(c); k4_epi<8, 0, 33>(c);
}
DI f32x4 addbf(f32x4 a, unsigned lo, unsigned hi) { return a + f32x4{bflo(lo), bfhi(lo), bflo(hi), bfhi(hi)}; }
struct K5C {
  const char *ab, *ab2;
  LDSP const char *ra[4], *oa[3], *xr;
  LDSP char* xw;
  u32x4 A[9][4], wf[5];
  f32x4 acc[2][7], own[2][2], v[4];
  const char* hb; unsigned ho; int kq; u32x2 hr[3];
  __amdgpu_buffer_rsrc_t wrs; unsigned tw, p8s;
  gu32* cnt;
};
template <int Z, int J> DI void k5_issue(K5C& c, const K4C& k) {
  if constexpr (Z < 7 && J < 4) asm volatile("global_load_dwordx4 %0, %1, %2 sc1"
 : "=v"(c.A[Z + 2][J]) : "v"(k.lane16), "s"(((J >> 1) ? c.ab2 : c.ab) + K5_PERM[2 * (Z + 2) + (J & 1)] * 1024));
  if constexpr (Z < 6 && J >= 4) k5_piece<Z + 3>(k.wd, J - 4, k.w, k.lane16);
  if constexpr (Z == 6 && J >= 4) k5_own<J - 4>(k.wd, k.w, k.lane16);
  if constexpr (Z == 7 && J == 0) k5_own<4>(k.wd, k.w, k.lane16);
  if constexpr (Z == 7 && J == 1) asm volatile("global_load_dwordx2 %0, %1, %2" : "=v"(c.hr[0]) : "v"(c.ho), "s"(c.hb + c.kq * 1024));
  if constexpr (Z == 7 && J == 2) asm volatile("global_load_dwordx2 %0, %1, %2 offset:512" : "=v"(c.hr[1]) : "v"(c.ho), "s"(c.hb + c.kq * 1024));
  if constexpr (Z == 7 && J == 3) asm volatile("global_load_dwordx2 %0, %1, %2" : "=v"(c.hr[2]) : "v"(c.ho), "s"(c.hb + 4096));
  if constexpr (Z == 8 && J < 4) k5_own<5 + J>(k.wd, k.w, k.lane16);
}
template <int N> DI u32x4 k5_read(const K5C& c) {
  if constexpr (N < 126) return ldsr16(c.ra[(N / 14) & 3] + (N % 14) * 1024);
  else { constexpr int o = K5_OC[(N - 126) / 4] + ((N - 126) & 3) * 1024; return ldsr16(c.oa[o >> 16] + (o & 0xffff)); }
}
template <int F> DI void k5_frag(K5C& c, K4C& k) {
  constexpr int N = F + 4;
  const u32x4 a = c.wf[F % 5];
  if constexpr (F < 126) {
    constexpr int Z = F / 14, f = F % 14, st = f / 7, j = f % 7;
    if (F < 7) { G_MFMA0(c.acc[0][j], a, c.A[Z][st]); G_MFMA0(c.acc[1][j], a, c.A[Z][2 + st]); }
    else { G_MFMA(c.acc[1][j], a, c.A[Z][2 + st]); G_MFMA(c.acc[0][j], a, c.A[Z][st]); }
    if constexpr (f == 10 && Z == 8) asm volatile("s_waitcnt vmcnt(4)\n\ts_barrier" : "+v"(c.hr[0]), "+v"(c.hr[1]), "+v"(c.hr[2])::"memory");
    if constexpr (f == 10 && Z < 8) {
      u32x4(&A)[4] = c.A[Z + 1];
      if (Z == 0) asm volatile("s_waitcnt vmcnt(9) lgkmcnt(0)\n\ts_barrier" : "+v"(A[0]), "+v"(A[1]), "+v"(A[2]), "+v"(A[3])::"memory");
      else if (Z == 3) asm volatile("s_waitcnt vmcnt(4) lgkmcnt(0)\n\ts_barrier" : "+v"(A[0]), "+v"(A[1]), "+v"(A[2]), "+v"(A[3]), "+v"(k.poll)::"memory");
      else if (Z < 7) asm volatile("s_waitcnt vmcnt(7) lgkmcnt(0)\n\ts_barrier" : "+v"(A[0]), "+v"(A[1]), "+v"(A[2]), "+v"(A[3])::"memory");
      else asm volatile("s_waitcnt vmcnt(8) lgkmcnt(0)\n\ts_barrier" : "+v"(A[0]), "+v"(A[1]), "+v"(A[2]), "+v"(A[3])::"memory");
      if (Z == 1 && k.w == 0 && k.lane16 == 0) __hip_atomic_fetch_add(k.cntB, 1u, __ATOMIC_RELAXED, __HIP_MEMORY_SCOPE_AGENT);
      if (Z == 3) wait_count(k.cntB, k.poll, 8u);
    }
    c.wf[N % 5] = k5_read<N>(c);
    if constexpr (Z == 3 && f == 1) POLL(k.poll, k.cntB);
    constexpr int slot = f == 0 ? 2 : (f == 2 ? 3 : (f == 4 ? 4 : (f == 5 ? 5 : (f == 7 ? 6 : (f == 9 ? 7 : -1)))));
    if constexpr (F == 0) { k5_issue<0, 0>(c, k); k5_issue<0, 1>(c, k); }
    if constexpr (slot >= 0) k5_issue<Z, slot>(c, k);
    if constexpr (Z < 8 && f > 10 && (f & 1)) k5_issue<Z + 1, (f - 11) / 2>(c, k);
  } else {
    constexpr int B = F - 126, z = B / 4, st = (B >> 1) & 1, t = B & 1;
    if (B < 2) { G_MFMA0(c.own[0][t], a, c.A[z][st]); G_MFMA0(c.own[1][t], a, c.A[z][2 + st]); }
    else { G_MFMA(c.own[1][t], a, c.A[z][2 + st]); G_MFMA(c.own[0][t], a, c.A[z][st]); }
    if constexpr (F == 131) asm volatile("s_waitcnt vmcnt(0) lgkmcnt(0)\n\ts_barrier" ::: "memory");
    if constexpr (F == K5_SIG) {
      asm volatile("s_waitcnt vmcnt(0)\n\ts_barrier" ::: "memory");
      if (k.w == 0 && k.lane16 == 0) __hip_atomic_fetch_add(c.cnt, 1u, __ATOMIC_RELAXED, __HIP_MEMORY_SCOPE_AGENT);
    }
    if constexpr (N < 162) c.wf[N % 5] = k5_read<N>(c);
  }
  if constexpr (F >= 123 && F < 130) { PIN(c.acc[1][F - 123]); *(LDSP f32x4*)(c.xw + (F - 123) * 1024) = c.acc[1][F - 123]; }
  if constexpr (F == 131) { _Pragma("unroll") for (int i = 0; i < 4; ++i) c.v[i] = *(LDSP const f32x4*)(c.xr + i * 1024); }
  if constexpr (F == 133 || F == 135 || F == 138) {
    constexpr int j = F == 133 ? 0 : (F == 135 ? 1 : 2), o = 2 * (j & 1);
    const f32x4 x = c.acc[0][2 * j] + c.v[o], y = c.acc[0][2 * j + 1] + c.v[o + 1];
    bst16<16>(u32x4{cvtpk(x[0], x[1]), cvtpk(x[2], x[3]), cvtpk(y[0], y[1]), cvtpk(y[2], y[3])}, c.wrs, k.lane16,
              (unsigned)WS_PART + ((((c.kq + j + 1) & 3) * 512 + c.tw) * 3 + j) * 1024);
    if constexpr (F == 135) { _Pragma("unroll") for (int i = 0; i < 3; ++i) c.v[i] = *(LDSP const f32x4*)(c.xr + (4 + i) * 1024); }
  }
  if constexpr (F == 140) {
    c.acc[0][6] += c.v[2];
    __builtin_amdgcn_raw_buffer_store_b64(u32x2{cvtpk(c.acc[0][6][0], c.acc[0][6][1]), cvtpk(c.acc[0][6][2], c.acc[0][6][3])}, c.wrs, c.p8s, c.kq * 512, 16);
    PIN(c.acc[0][6]);
  }
  SBAR();
}
template <int B, int... Fs> DI void k5_run(K5C& c, K4C& k, std::integer_sequence<int, Fs...>) { (k5_frag<B + Fs>(c, k), ...); }

extern "C" __global__ __launch_bounds__(512, 2) void k_ffn(char* out, char* ws) {
  extern __shared__ __attribute__((aligned(16))) char smem[];
  asm volatile("" : "+s"(out), "+s"(ws));
  LDSP char* lds = (LDSP char*)smem;
  const int tid = threadIdx.x, lane = tid & 63, w = rfl(tid >> 6);
  const int xcd = blockIdx.x & 7, bi = blockIdx.x >> 3;
  const int m = lane & 15, g = lane >> 4, kq = xcd >> 1;
  STAMP_DECL; STAMP(0);
  if (blockIdx.x == 8 && threadIdx.x == 0) ((__attribute__((address_space(1))) unsigned long long*)(ws + (200u << 20)))[3] = __builtin_amdgcn_s_memrealtime();
  K4C c;
  {
    const int rb = 4 * (xcd & 1) + (bi & 3), fs = 8 * kq + (bi >> 2), mt = 16 * rb + 2 * w;
    c.lds0 = (unsigned)(uintptr_t)lds; c.lane16 = lane * 16; c.w = w;
    c.T0 = 9 * fs + 8 * (fs & 1); c.dir = 1 - 2 * (fs & 1);
    c.wb = ws + WS_W1 + (size_t)c.T0 * K4_SLOT;
    c.xb = ws + WS_HC + (size_t)mt * 18432;
    if (K4_WD) { k4_issue_w<0>(c, 0); k4_issue_w<0>(c, 1); }
    k4_issue1<0, 0>(c); k4_issue1<0, 1>(c); k4_issue1<0, 2>(c); k4_issue1<0, 3>(c); k4_issue1<0, 4>(c); k4_issue1<0, 5>(c);
    unsigned sq[6];
#pragma unroll
    for (int i = 0; i < 6; ++i) sq[i] = gld4(ws + WS_SSQ, (3 * g + (i >> 1)) * 8192 + (16 * mt + 16 * (i & 1) + m) * 4);
    k4_issue1<1, 0>(c); k4_issue1<1, 1>(c); k4_issue1<1, 2>(c); k4_issue1<1, 3>(c); k4_issue1<1, 4>(c); k4_issue1<1, 5>(c);
    SBAR();
#pragma unroll
    for (int i = 0; i < 4; ++i) { c.wl[i] = lds + c.lane16 + i * K4_SLOT; PIN(c.wl[i]); }
    c.ars = mkrsrc(ws + WS_ACT, 128u * 144 * 1024);
    c.avo = (mt + (g & 1)) * (144 * 1024) + (g >> 1) * 256 + m * 16;
    c.wd.wb = ws + WS_WD + ((size_t)(bi >> 3) * 144 + 36 * kq) * 9 * 1024;
#pragma unroll
    for (int i = 0; i < 4; ++i) {
      const int p = i < 3 ? 8 * i + w : 24 + (w & 3), st = (p * 37) >> 8, t = p - 7 * st + 2;
      c.wd.q[i] = ((st >> 1) * 162 + (t < 8 ? (t + 2 * kq) & 7 : 8)) * 1024;
      c.wd.qs[i] = st & 1;
      asm volatile("" : "+s"(c.wd.q[i]), "+s"(c.wd.qs[i]));
    }
    c.wd.qo = ((w >> 2) * 162 + 2 * kq + (w & 1)) * 1024; c.wd.qos = (w >> 1) & 1; c.wd.lds0 = c.lds0;
    if (K4_PFWD) {
      c.pfb = ws + WS_ACT;
      c.pfo = (mt + ((lane >> 5) & 1)) * (144 * 1024) + ((lane >> 4) & 1) * 256 + ((lane >> 3) & 1) * 128;
      PIN(c.pfo);
    }
    c.pf0 = 0;
    c.cntA = (gu32*)(ws + WS_CNT + (64 + rb * 4 + kq) * 128);
    STAMP(1);
    asm volatile("s_waitcnt vmcnt(5)\n\ts_barrier" : "+v"(sq[0]), "+v"(sq[1]), "+v"(sq[2]), "+v"(sq[3]), "+v"(sq[4]), "+v"(sq[5]),
                 "+v"(c.X[0][0]), "+v"(c.X[0][1]), "+v"(c.X[1][0]), "+v"(c.X[1][1])::"memory");
#pragma unroll
    for (int i = 0; i < K4_PF; ++i) c.wf[i] = ldsr16(c.wl[k4_tile(i)] + k4_addr(i));
#pragma unroll
    for (int t = 0; t < 2; ++t) {
      float ss = __uint_as_float(sq[t]) + __uint_as_float(sq[2 + t]) + __uint_as_float(sq[4 + t]);
      ss = xor32_sum(xor16_sum(ss));
      const float v = ss * (1.0f / DM) + LN_EPS, rs = __builtin_amdgcn_rsqf(v);
      c.c1[t] = -LOG2E * rs; c.c2[t] = -LOG2E * (v * rs);
      PIN(c.c1[t]); PIN(c.c2[t]);
      if (K4_PK) { c.c1p[t] = f32x2{c.c1[t], c.c1[t]}; c.c2p[t] = f32x2{c.c2[t], c.c2[t]}; PIN(c.c1p[t]); PIN(c.c2p[t]); }
    }
    SBAR();
    k4_run<0>(c, std::make_integer_sequence<int, 288>{});
    c.cntB = c.cntA + 32 * 32;
  }
  const int rb = 8 * (xcd & 1) + (bi & 7), nb = bi >> 3, mg = w & 3, kh = w >> 2, mu = 8 * rb + 2 * mg + kh;
  gu32* prodB = c.cntB;
  c.cntA = (gu32*)(ws + WS_CNT + (64 + (rb >> 1) * 4 + kq) * 128);
  c.ab = ws + WS_ACT + ((size_t)mu * 144 + 36 * kq + 18 * kh) * 1024;
  c.ab2 = ws + WS_ACT + ((size_t)(mu ^ 1) * 144 + 36 * kq + 18 * kh) * 1024;
  k4_run<288>(c, std::make_integer_sequence<int, 36>{});
  STAMP(2);
  K5C d;
  d.ab = c.ab; d.ab2 = c.ab2;
  asm volatile("s_nop 7" : "+v"(c.acc[0][0]), "+v"(c.acc[0][1]), "+v"(c.acc[0][2]), "+v"(c.acc[0][3]));
  k4_last(c);
  SBAR();
  gu32* consB = c.cntA + 32 * 32;
  d.hb = ws + WS_HU + (size_t)mu * 18432 + nb * 4608; d.ho = (g >> 1) * 256 + m * 16 + (g & 1) * 8; d.kq = kq;
#pragma unroll
  for (int i = 0; i < 4; ++i) { d.ra[i] = lds + K5_R[i] + kh * 14336 + c.lane16; PIN(d.ra[i]); }
#pragma unroll
  for (int i = 0; i < 3; ++i) { d.oa[i] = lds + i * 65536 + kh * 4096 + c.lane16; PIN(d.oa[i]); }
  d.xw = lds + (kh ? K5_R[2] : K5_R[1]) + mg * 7168 + c.lane16; PIN(d.xw);
  d.xr = lds + (kh ? K5_R[1] : K5_R[2]) + mg * 7168 + c.lane16; PIN(d.xr);
  const __amdgpu_buffer_rsrc_t wrs = mkrsrc(ws, (unsigned)WS_END), ors = mkrsrc(out, SEQ * DM * 2);
  const int tile = rb * 4 + nb;
  const bool own8 = (mu & 3) == kq;
  const unsigned tw = tile * 8 + w;
  const unsigned p8 = (unsigned)WS_PART8 + tw * 2048 + lane * 8;
  d.cnt = (gu32*)(ws + WS_CNT + tile * 128);
  d.wrs = wrs; d.tw = tw; d.p8s = own8 ? 0x80000000u : p8; PIN(d.p8s);
#pragma unroll
  for (int i = 0; i < 4; ++i) { d.A[0][i] = c.A0[0][i]; d.A[1][i] = c.A0[1][i]; }
  asm volatile("s_waitcnt vmcnt(6) lgkmcnt(0)\n\ts_barrier" : "+v"(d.A[0][0]), "+v"(d.A[0][1]), "+v"(d.A[0][2]), "+v"(d.A[0][3])::"memory");
  STAMP(3);
  d.wf[0] = k5_read<0>(d); d.wf[1] = k5_read<1>(d); d.wf[2] = k5_read<2>(d); d.wf[3] = k5_read<3>(d);
  SBAR();
  c.cntB = prodB;
  k5_run<0>(d, c, std::make_integer_sequence<int, 28>{});
  c.cntB = consB;
  k5_run<28>(d, c, std::make_integer_sequence<int, 134>{});
  asm volatile("s_nop 7" : "+v"(d.own[0][0]), "+v"(d.own[0][1]), "+v"(d.own[1][0]), "+v"(d.own[1][1]));
  STAMP(4);
  LDSP char* xo = lds + K5_R[0] + c.lane16;
  *(LDSP f32x4*)(xo + w * 2048) = d.own[1][0];
  *(LDSP f32x4*)(xo + w * 2048 + 1024) = d.own[1][1];
  unsigned oo = (16 * mu + m) * 1152 + nb * 288;
  unsigned oo8 = own8 ? oo + 256 + g * 8 : 0x80000000u;
  oo += kq * 64 + (g & 1) * 32 + (g >> 1) * 16;
  PIN(oo); PIN(oo8);
  STAMP(5);
  if (w == 0) wait_count(d.cnt, 0u, 4u);
  asm volatile("s_waitcnt lgkmcnt(0)\n\ts_barrier" ::: "memory");
  STAMP(6);
  u32x4 r[3]; u32x2 r8[3];
#pragma unroll
  for (int j = 0; j < 3; ++j) {
    r[j] = __builtin_amdgcn_raw_buffer_load_b128(wrs, c.lane16, (unsigned)WS_PART + ((kq * 512 + tw) * 3 + j) * 1024, 16);
    r8[j] = __builtin_amdgcn_raw_buffer_load_b64(wrs, own8 ? p8 : 0x80000000u, ((kq + 1 + j) & 3) * 512, 16);
  }
  SBAR();
  f32x4 e0 = addbf(d.own[0][0] + *(LDSP const f32x4*)(xo + (w ^ 4) * 2048), d.hr[0][0], d.hr[0][1]);
  f32x4 e1 = addbf(d.own[0][1] + *(LDSP const f32x4*)(xo + (w ^ 4) * 2048 + 1024), d.hr[1][0], d.hr[1][1]);
  f32x4 e8 = addbf(d.acc[0][6], d.hr[2][0], d.hr[2][1]);
#pragma unroll
  for (int j = 0; j < 3; ++j) { e0 = addbf(e0, r[j][0], r[j][1]); e1 = addbf(e1, r[j][2], r[j][3]); e8 = addbf(e8, r8[j][0], r8[j][1]); }
  {
    const auto s0 = __builtin_amdgcn_permlane16_swap(cvtpk(e0[0], e0[1]), cvtpk(e1[0], e1[1]), false, false);
    const auto s1 = __builtin_amdgcn_permlane16_swap(cvtpk(e0[2], e0[3]), cvtpk(e1[2], e1[3]), false, false);
    bst16<OUT_AUX>(u32x4{s0[0], s1[0], s0[1], s1[1]}, ors, oo, 0);
    __builtin_amdgcn_raw_buffer_store_b64(u32x2{cvtpk(e8[0], e8[1]), cvtpk(e8[2], e8[3])}, ors, oo8, 0, OUT_AUX);
  }
  STAMP(7); STAMP_OUT(4, 8);
}
extern "C" __global__ void k_tail(char* ws) {
  const unsigned long long t4 = __builtin_amdgcn_s_memrealtime();
  const volatile unsigned long long* t = (const volatile unsigned long long*)(ws + (200u << 20));
  unsigned long long v[5] = {t[0], t[1], t[2], t[3], t4};
  unsigned long long d = SEL < 4 ? 4 * (v[SEL + 1] - v[SEL]) : 0;
  if (d > 40000) d = 40000;
  while (__builtin_amdgcn_s_memrealtime() < t4 + d) __builtin_amdgcn_s_sleep(2);
}
extern "C" __global__ void k_end() {}

extern "C" void kernel_launch(void* const* d_in, const int* in_sizes, int n_in, void* d_out, int out_size, void* d_ws, size_t ws_size,
                              hipStream_t stream) {
  const u16* x = (const u16*)d_in[0];
  const u16* wqkv = (const u16*)d_in[1];
  const u16* wo = (const u16*)d_in[2];
  const u16* wg = (const u16*)d_in[4];
  const u16* wu = (const u16*)d_in[5];
  const u16* wd = (const u16*)d_in[6];
  char* ws = (char*)d_ws;
  (void)hipFuncSetAttribute((const void*)k_qkv, hipFuncAttributeMaxDynamicSharedMemorySize, K1_LDS);
  (void)hipFuncSetAttribute((const void*)k_attn, hipFuncAttributeMaxDynamicSharedMemorySize, K2_LDS);
  (void)hipFuncSetAttribute((const void*)k_oproj, hipFuncAttributeMaxDynamicSharedMemorySize, K3_LDS);
  (void)hipFuncSetAttribute((const void*)k_ffn, hipFuncAttributeMaxDynamicSharedMemorySize, K4_LDS);
  k_qkv<<<256, 768, K1_LDS, stream>>>(x, wqkv, ws);
  k_attn<<<256, 768, K2_LDS, stream>>>(x, wo, wg, wu, wd, ws);
  k_oproj<<<192, 768, K3_LDS, stream>>>(ws);
  k_ffn<<<256, 512, K4_LDS, stream>>>((char*)d_out, ws);
  k_tail<<<1, 64, 0, stream>>>(ws);
}
```

```cpp
#define SEL 5
#include <hip/hip_runtime.h>
#include <hip/hip_bf16.h>
#include <stdint.h>
#include <utility>
#include <type_traits>

typedef __bf16 bf16x8 __attribute__((ext_vector_type(8)));
typedef __bf16 bf16x2 __attribute__((ext_vector_type(2)));
typedef short s16x4 __attribute__((ext_vector_type(4)));
typedef float f32x16 __attribute__((ext_vector_type(16)));
typedef float f32x4 __attribute__((ext_vector_type(4)));
typedef float f32x2 __attribute__((ext_vector_type(2)));
typedef unsigned u32x4 __attribute__((ext_vector_type(4)));
typedef unsigned u32x2 __attribute__((ext_vector_type(2)));
typedef unsigned short u16;
#define LDSP __attribute__((address_space(3)))
#define DI __device__ __forceinline__
#define SBAR() __builtin_amdgcn_sched_barrier(0)

constexpr int SEQ = 2048, DM = 576, NH = 32, DH = 18, FF = 4608;
constexpr float LOG2E = 1.4426950408889634f;
constexpr float QSCALE = 0.23570226039551584f * LOG2E;
constexpr float LN_EPS = 1e-5f;

constexpr size_t CHUNK_B = 37888;
constexpr size_t KV_HEAD_B = 4 * CHUNK_B;
constexpr size_t WS_QM = 0;
constexpr size_t WS_QT = WS_QM + (size_t)NH * 65536;
constexpr size_t WS_KV = WS_QT + (size_t)NH * 8192;
constexpr size_t WS_KMAX = WS_KV + (size_t)NH * KV_HEAD_B;
constexpr size_t WS_XMEAN = WS_KMAX + 65536;
constexpr size_t WS_OFR = WS_XMEAN + 8192;
constexpr size_t WS_WO = WS_OFR + (size_t)128 * 18 * 1024;
constexpr size_t WS_WBAR = WS_WO + (size_t)12 * 54 * 1024;
constexpr size_t WS_HC = WS_WBAR + 3072;
constexpr size_t WS_SSQ = WS_HC + (size_t)64 * 36 * 1024;
constexpr size_t WS_W1 = WS_SSQ + (size_t)12 * 8192;
constexpr size_t WS_WD = WS_W1 + (size_t)288 * 36 * 1024;
constexpr size_t WS_ACT = WS_WD + (size_t)4 * 144 * 9 * 1024;
constexpr size_t UNIT_B = (size_t)128 * 18432;
constexpr size_t WS_PART = WS_ACT + (size_t)128 * 144 * 1024;
constexpr size_t WS_PART8 = WS_PART + (size_t)4 * 64 * 8 * 3 * 1024;
constexpr size_t WS_CNT = WS_PART8 + (size_t)64 * 8 * 4 * 512;
constexpr size_t WS_XU = WS_CNT + 32768;
constexpr size_t WS_HU = WS_XU + UNIT_B;
constexpr size_t WS_END = WS_HU + UNIT_B;

DI unsigned cvtpk(float lo, float hi) {
  f32x2 v = {lo, hi};
  bf16x2 b = __builtin_convertvector(v, bf16x2);
  return __builtin_bit_cast(unsigned, b);
}
DI float bflo(unsigned u) { return __uint_as_float(u << 16); }
DI float bfhi(unsigned u) { return __uint_as_float(u & 0xffff0000u); }
DI float dot2(unsigned a, unsigned b, float c) {
  return __builtin_amdgcn_fdot2_f32_bf16(__builtin_bit_cast(bf16x2, a), __builtin_bit_cast(bf16x2, b), c, false);
}
DI f32x16 mfma32(u32x4 a, u32x4 b, f32x16 c) {
  return __builtin_amdgcn_mfma_f32_32x32x16_bf16(__builtin_bit_cast(bf16x8, a), __builtin_bit_cast(bf16x8, b), c, 0, 0, 0);
}
DI f32x4 mfma16(u32x4 a, u32x4 b, f32x4 c) {
  return __builtin_amdgcn_mfma_f32_16x16x32_bf16(__builtin_bit_cast(bf16x8, a), __builtin_bit_cast(bf16x8, b), c, 0, 0, 0);
}
DI unsigned rfl(unsigned x) { return __builtin_amdgcn_readfirstlane(x); }
DI const char* uniptr(const char* p) {
  uint64_t v = (uint64_t)p;
  unsigned lo = rfl((unsigned)v), hi = rfl((unsigned)(v >> 32));
  return (const char*)(((uint64_t)hi << 32) | lo);
}
DI void dma16(const char* sbase, unsigned voff, unsigned lds) {
  asm volatile("s_mov_b32 m0, %2\n\ts_nop 0\n\tglobal_load_lds_dwordx4 %0, %1" ::"v"(voff), "s"(sbase), "s"(lds));
}
DI u32x4 gld16(const char* sbase, unsigned voff) {
  u32x4 r;
  asm volatile("global_load_dwordx4 %0, %1, %2" : "=v"(r) : "v"(voff), "s"(sbase));
  return r;
}
DI unsigned gld4(const char* sbase, unsigned voff) {
  unsigned r;
  asm volatile("global_load_dword %0, %1, %2" : "=v"(r) : "v"(voff), "s"(sbase));
  return r;
}
#define WAITV(n) asm volatile("s_waitcnt vmcnt(" #n ")" ::: "memory")
#define BARRIER() asm volatile("s_barrier" ::: "memory")
#define PIN(x) asm volatile("" : "+v"(x))
DI u32x4 ldsr16(LDSP const char* p) { return *(LDSP const u32x4*)p; }
DI float xor32_sum(float v) {
  auto r = __builtin_amdgcn_permlane32_swap(__float_as_uint(v), __float_as_uint(v), false, false);
  return __uint_as_float(r[0]) + __uint_as_float(r[1]);
}
DI float xor32_max(float v) {
  auto r = __builtin_amdgcn_permlane32_swap(__float_as_uint(v), __float_as_uint(v), false, false);
  return fmaxf(__uint_as_float(r[0]), __uint_as_float(r[1]));
}
DI float xor16_sum(float v) {
  auto r = __builtin_amdgcn_permlane16_swap(__float_as_uint(v), __float_as_uint(v), false, false);
  return __uint_as_float(r[0]) + __uint_as_float(r[1]);
}

#define STAMP_DECL
#define STAMP(i)
#define STAMP_OUT(kid, n)

DI int wo_row(int step, int g, int j) {
  if (step < 16) return 18 * (2 * step + (g >> 1)) + 4 * (g & 1) + (j & 3) + 8 * (j >> 2);
  return 18 * (4 * (4 * (step - 16) + g) + (j >> 1)) + 16 + (j & 1);
}

#define K4_SPREAD 1
#define K4_PM 1
#define K4_WD 1
#define K3_PF 0
#define K3_SLEEP 32
#define K1_ROT 0
#define AT_PM 9
#define HELP_PRIO 3
#define HELP_EARLY 0
#define PREP_ORD 1
#define GRAY 1
#define K4_POLL 10
#define K4_PK 0
#define K5_ORD 1
#define K5_SIG 154
#define K4_MID 0
#define AT_HFIRST 0
#define K4_ROT 0
#define K5_FLAG 0
#define K5_SPREAD 1
#define K5_PM 0
#define OUT_AUX 16
#define K4_PFWD 1
#define QKV_AUX 16
#define QKT_AUX 16
#define V_AUX 16
#define O_AUX 16
#define H_AUX 16
#define PART_AUX 16
#define ACT_AUX 16
#define PREP_AUX 16
#define CODE_PF(dst, first, last, extra, blk) CODE_PF2(dst, first, last, extra, blk, 128u)
#define CODE_PF2(dst, first, last, extra, blk, stride)                                                                              \
  do {                                                                                                                     \
    uint64_t pc_; int b_, e_;                                                                                              \
    asm volatile("s_getpc_b64 %0\n.Lpf%=:\n\ts_mov_b32 %1, " #first "-.Lpf%=\n\ts_mov_b32 %2, " #last "-.Lpf%=" : "=s"(pc_), "=s"(b_), "=s"(e_)); \
    const unsigned o_ = (blk) * 8192u + (threadIdx.x & 63) * (stride), n_ = (unsigned)(e_ - b_) + (extra) - 4u;                \
    dst = gld4((const char*)(pc_ + b_), o_ < n_ ? o_ : n_);                                                                \
  } while (0)
#define G_MFMA(d, a, b) asm volatile("v_mfma_f32_16x16x32_bf16 %0, %1, %2, %0" : "+v"(d) : "v"(a), "v"(b))
#define G_MFMA0(d, a, b) asm volatile("v_mfma_f32_16x16x32_bf16 %0, %1, %2, 0" : "=&v"(d) : "v"(a), "v"(b))
template <int AUX> DI void bst16(u32x4 v, __amdgpu_buffer_rsrc_t rs, unsigned voff, unsigned soff) {
  __builtin_amdgcn_raw_buffer_store_b128(v, rs, voff, soff, AUX);
  asm volatile("s_nop 1" ::"v"(v) : "memory");
}
DI __amdgpu_buffer_rsrc_t mkrsrc(const void* p, unsigned bytes) {
  return __builtin_amdgcn_make_buffer_rsrc((void*)p, 0, bytes, 0x00020000);
}

#define K1_PF 4
#define K1_TOUCH 1
#define K1_TSLEEP 16
#define K1_DEPTH 2
constexpr int K1_PITCH = 224, K1_SLOT = 16384 + 64 * K1_PITCH  , K1_NS = K1_DEPTH + 1;
constexpr int K1_SP = 232;
constexpr int K1_STG = K1_NS * K1_SLOT, K1_CS = K1_STG + 128 * K1_SP + 64, K1_ST = K1_CS + 512 + 64, K1_LDS = K1_ST + 2048;
struct K1C {
  LDSP const char *xa[2], *wl, *wc0, *wc1;
  u32x4 X[3][2], wf[K1_PF + 1], ones;
  f32x4 acc[4][2];
  f32x4 sm[2]; float sq[2];
};
DI void k1_loader(const char* xb, const char* wb, char* ws, int j, int lane, unsigned lds0) {
  __builtin_amdgcn_s_setprio(3);
  unsigned xo[2], wo[4];
  const int g = lane >> 4;
  xo[0] = (lane >> 3) * 1152 + (((lane & 7) ^ g) * 16);
  xo[1] = (8 + (lane >> 3)) * 1152 + (((lane & 7) ^ (4 + g)) * 16);
  xb += j * (32 * 1152);
  const unsigned xd = lds0 + j * 4096, wd = lds0 + 16384 + j * 1024;
#define K1_X(z, so) do { dma16(xb + (z) * 128, xo[0], xd + (so)); dma16(xb + (z) * 128, xo[1], xd + (so) + 1024);                    \
    dma16(xb + (z) * 128 + 16 * 1152, xo[0], xd + (so) + 2048); dma16(xb + (z) * 128 + 16 * 1152, xo[1], xd + (so) + 3072); } while (0)
#define K1_W(z, so) do { _Pragma("unroll") for (int i = 0; i < 3; ++i) dma16(wb + (z) * (64 * 3456), wo[i], wd + (so) + i * 4096);     \
    if (j < 2) dma16(wb + (z) * (64 * 3456), wo[3], wd + (so) + 3 * 4096); } while (0)
  K1_X(0, 0);
  SBAR();
#pragma unroll
  for (int i = 0; i < 4; ++i) {
    const unsigned u = 64 * (j + 4 * i) + lane, r = u / 14;
    wo[i] = ((r & ~12u) | ((r & 4u) << 1) | ((r & 8u) >> 1)) * 3456 + (u - r * 14) * 16;
  }
  K1_W(0, 0);
#pragma unroll
  for (int z = 1; z < K1_DEPTH; ++z) { K1_X(z, z * K1_SLOT); K1_W(z, z * K1_SLOT); }
  unsigned so = K1_DEPTH * K1_SLOT;
#pragma unroll 1
  for (int z = K1_DEPTH; z < 9; ++z) {
    asm volatile("s_waitcnt vmcnt(%0)\n\ts_barrier" ::"i"(7 * (K1_DEPTH - 1)) : "memory");
    K1_X(z, so); K1_W(z, so);
    so = so == (K1_NS - 1) * K1_SLOT ? 0 : so + K1_SLOT;
  }
  if (K1_DEPTH > 2) asm volatile("s_waitcnt vmcnt(14)\n\ts_barrier" ::: "memory");
  asm volatile("s_waitcnt vmcnt(7)\n\ts_barrier" ::: "memory");
  asm volatile("s_waitcnt vmcnt(0)\n\ts_barrier" ::: "memory");
  if (blockIdx.x < 4 && j == 0) bst16<16>(u32x4{0, 0, 0, 0}, mkrsrc(ws, (unsigned)WS_END), (unsigned)WS_CNT + (blockIdx.x * 64 + lane) * 128, 0);
#undef K1_X
#undef K1_W
}
DI u32x4 tr_frag(LDSP const char* p, int d) {
  s16x4 lo = __builtin_amdgcn_ds_read_tr16_b64_v4i16((LDSP s16x4*)p);
  s16x4 hi = __builtin_amdgcn_ds_read_tr16_b64_v4i16((LDSP s16x4*)(p + d));
  return __builtin_bit_cast(u32x4, __builtin_shufflevector(lo, hi, 0, 1, 2, 3, 4, 5, 6, 7));
}
template <int NHG, int F> DI void k1_read(K1C& c) {
  constexpr int NF = 4 + NHG, S = F / NF, j = F % NF, Z = S >> 1, so = (Z % K1_NS) * K1_SLOT;
  if constexpr (j == 0) {
    c.X[S % 3][0] = ldsr16(c.xa[S & 1] + so);
    c.X[S % 3][1] = ldsr16(c.xa[S & 1] + so + 2048);
  }
  constexpr int wo = so + (S & 1) * 7168;
  if constexpr (NHG == 0 || j < 3) c.wf[F % (K1_PF + 1)] = tr_frag(c.wl + wo + j * 32, 1792);
  else c.wf[F % (K1_PF + 1)] = tr_frag((j == 3 ? c.wc0 : c.wc1) + wo, 1792);
}
template <int NHG, int F> DI void k1_frag(K1C& c) {
  constexpr int NF = 4 + NHG, S = F / NF, j = F % NF, Z = S >> 1, N = F + K1_PF, fz = F % (2 * NF);
  if constexpr (S == 0) {
    if constexpr (NHG == 0 || j < 3) { G_MFMA0(c.acc[j][0], c.wf[F % (K1_PF + 1)], c.X[0][0]); G_MFMA0(c.acc[j][1], c.wf[F % (K1_PF + 1)], c.X[0][1]); }
    else G_MFMA0(c.acc[3][j - 3], c.wf[F % (K1_PF + 1)], c.ones);
  } else {
    if constexpr (NHG == 0 || j < 3) { G_MFMA(c.acc[j][0], c.wf[F % (K1_PF + 1)], c.X[S % 3][0]); G_MFMA(c.acc[j][1], c.wf[F % (K1_PF + 1)], c.X[S % 3][1]); }
    else G_MFMA(c.acc[3][j - 3], c.wf[F % (K1_PF + 1)], c.ones);
  }
  if constexpr ((S & 1) == NHG && j < 4) {
#pragma unroll
    for (int t = 0; t < 2; ++t) { const unsigned v = c.X[S % 3][t][j]; c.sq[t] = dot2(v, v, c.sq[t]); PIN(c.sq[t]); }
    if constexpr (j < 2) { if constexpr (S < 2) G_MFMA0(c.sm[j], c.ones, c.X[S % 3][j]); else G_MFMA(c.sm[j], c.ones, c.X[S % 3][j]); }
  }
  if constexpr (N < 18 * NF) {
    if constexpr (N % (2 * NF) == 0) {
      asm volatile("s_waitcnt lgkmcnt(0)\n\ts_barrier" ::: "memory");
    }
    k1_read<NHG, N>(c);
  }
  SBAR();
}
template <int NHG, int... Fs> DI void k1_run(K1C& c, std::integer_sequence<int, Fs...>) {
  k1_read<NHG, 0>(c); k1_read<NHG, 1>(c); k1_read<NHG, 2>(c);
  if (K1_PF > 3) k1_read<NHG, 3>(c);
  if (K1_PF > 4) k1_read<NHG, 4>(c);
  if (K1_PF > 5) k1_read<NHG, 5>(c);
  SBAR();
  (k1_frag<NHG, Fs>(c), ...);
  asm volatile("s_nop 7" : "+v"(c.acc[0][0]), "+v"(c.acc[0][1]), "+v"(c.acc[1][0]), "+v"(c.acc[1][1]), "+v"(c.acc[2][0]), "+v"(c.acc[2][1]),
               "+v"(c.acc[3][0]), "+v"(c.acc[3][1]), "+v"(c.sm[0]), "+v"(c.sm[1]));
  SBAR();
}
extern "C" __global__ __launch_bounds__(768, 3) void k_qkv(const u16* x, const u16* wqkv, char* ws) {
  extern __shared__ __attribute__((aligned(16))) char smem[];
  unsigned long long TA, T1; asm volatile("s_memrealtime %0" : "=s"(TA)); SBAR();
  const int tid = threadIdx.x, lane = tid & 63, w = rfl(tid >> 6);
  const int xcd = blockIdx.x & 7, bi = blockIdx.x >> 3;
  unsigned pf;
  CODE_PF2(pf, k_qkv, k_end, 0, bi, (w == 8 && bi < 9) ? 128u : 0u);
  SBAR();
  asm volatile("" : "+s"(x), "+s"(wqkv), "+s"(ws));
  asm volatile("s_waitcnt lgkmcnt(0)\n\ts_memrealtime %0\n\ts_waitcnt lgkmcnt(0)" : "=s"(T1), "+s"(TA)); SBAR();
  LDSP char* lds = (LDSP char*)smem;
  const unsigned lds0 = (unsigned)(uintptr_t)lds;
  const int rb = 4 * (xcd >> 1) + (bi & 3), cb = 8 * (xcd & 1) + (bi >> 2);
  const int c0 = 108 * cb - 4 * (cb & 1);
  if (__builtin_expect(w >= 8, 1)) {
    k1_loader((const char*)x + rb * (128 * 1152), (const char*)wqkv + c0 * 2, ws, w - 8, lane, lds0);
    asm volatile("" ::"v"(pf));
    return;
  }
  const int m = lane & 15, g = lane >> 4, mg = w & 3, nh = w >> 2;
  STAMP_DECL; STAMP(0);
  K1C c;
  c.ones = u32x4{0x3f803f80u, 0x3f803f80u, 0x3f803f80u, 0x3f803f80u};
  PIN(c.ones);
  c.xa[0] = lds + (32 * mg + m) * 128 + ((g ^ (m >> 1)) * 16);
  c.xa[1] = lds + (32 * mg + m) * 128 + (((4 + g) ^ (m >> 1)) * 16);
  LDSP const char* wbase = lds + 16384 + (16 * (g >> 1) + 4 * (g & 1) + (m >> 2)) * K1_PITCH + (m & 3) * 8;
  c.wl = wbase + nh * 128;
  c.wc0 = wbase + mg * 32;
  c.wc1 = wbase + (mg < 3 ? mg + 4 : 6) * 32;
  c.sq[0] = c.sq[1] = 0.f;
  const int off = 4 * (cb & 1), T0 = 128 * rb;
  LDSP char* stg = lds + K1_STG;
  LDSP char* a_st = lds + K1_ST + (32 * mg + m) * 8;                 PIN(a_st);
  LDSP char* a_cs = lds + K1_CS + (64 * nh + 4 * g) * 4;             PIN(a_cs);
  LDSP char* a_sw = stg + (32 * mg + m) * K1_SP + (64 * nh + 4 * g) * 2;   PIN(a_sw);
  LDSP char* a_tr = stg + (32 * mg + 4 * (g & 1) + (m >> 2)) * K1_SP + (64 * nh + 16 * (g >> 1) + 4 * (m & 3)) * 2;   PIN(a_tr);
  float qs[4];
#pragma unroll
  for (int j = 0; j < 4; ++j) { qs[j] = (c0 + 64 * nh + 16 * j + 4 * g < DM) ? QSCALE : 1.0f; PIN(qs[j]); }
  const __amdgpu_buffer_rsrc_t wrs = mkrsrc(ws, (unsigned)WS_END);
  constexpr unsigned DROP = 0x80000000u;
  const unsigned kvo = (unsigned)WS_KV + (T0 >> 9) * (unsigned)CHUNK_B;
  unsigned vo[2] = {0, 0}, vone = 0, vd[4] = {DROP, DROP, DROP, DROP};
  if (cb >= 10) {
#pragma unroll
  for (int tt = 0; tt < 2; ++tt) {
    const int cc = 64 * nh + 32 * tt + 16 * (g >> 1) + m - off, hd = (cc + 18) / 18 - 1, G = 6 * cb + hd;
    vo[tt] = (cc >= 0 && cc < 108) ? hd * 4864 + 2 * mg * 608 + (g & 1) * 304 + (cc - 18 * hd) * 16 : 29184;
  }
  vone = tid < 96 ? (tid >> 4) * 4864 + (tid & 15) * 304 + 288 : 29184;
#pragma unroll
  for (int i = 0; i < 4; ++i) {
    const unsigned u = i * 512 + tid, hd = u / 304;
    vd[i] = (u < 1824 && 6 * cb + hd >= 64) ? kvo + 18432 + ((T0 >> 4) & 31) * 608 + (6 * cb + hd - 64) * (unsigned)KV_HEAD_B + (u - hd * 304) * 16 : DROP;
  }
  }
  PIN(vone); PIN(vo[0]); PIN(vo[1]); PIN(vd[0]); PIN(vd[1]); PIN(vd[2]); PIN(vd[3]);
  const int tok = tid & 127, T = T0 + tok;
  LDSP const char* a_g = stg + tok * K1_SP + off * 2;   PIN(a_g);
  unsigned um[2], ut[2];
  const unsigned tl = ((T >> 5) & 15) * 1024 + (T & 31) * 16;
#pragma unroll
  for (int it = 0; it < 2; ++it) {
    const int hd = (w >> 1) + 4 * it, G = 6 * cb + hd, head = G & 31;
    if (hd >= 6 || G >= 64) { um[it] = DROP; ut[it] = DROP; }
    else if (G < 32) { um[it] = (unsigned)WS_QM + head * 65536 + (T >> 9) * 16384 + tl; ut[it] = (unsigned)WS_QT + head * 8192 + T * 4; }
    else { um[it] = kvo + head * (unsigned)KV_HEAD_B + tl; ut[it] = kvo + head * (unsigned)KV_HEAD_B + 16384 + ((T >> 6) & 7) * 256 + (T & 31) * 8 + ((T >> 5) & 1) * 4; }
    PIN(um[it]); PIN(ut[it]);
  }
  STAMP(1);
  asm volatile("s_waitcnt vmcnt(0)\n\ts_barrier" : "+v"(pf)::"memory");
  if (threadIdx.x == 0) {
    if (blockIdx.x == 8) { ((__attribute__((address_space(1))) unsigned long long*)(ws + (200u << 20)))[1] = TA; ((__attribute__((address_space(1))) unsigned long long*)(ws + (200u << 20)))[2] = T1; ((__attribute__((address_space(1))) unsigned long long*)(ws + (200u << 20)))[3] = __builtin_amdgcn_s_memrealtime(); } }
  if (nh) k1_run<1>(c, std::make_integer_sequence<int, 90>{});
  else k1_run<0>(c, std::make_integer_sequence<int, 72>{});
  STAMP(3);
#pragma unroll
  for (int t = 0; t < 2; ++t) {
    const f32x2 st = {c.sm[t][0], xor32_sum(xor16_sum(c.sq[t]))};
    *(LDSP f32x2*)(a_st + nh * 1024 + t * 128) = st;
  }
  if (nh) {
    *(LDSP f32x4*)(lds + K1_CS + (16 * mg + 4 * g) * 4) = c.acc[3][0];
    *(LDSP f32x4*)(lds + K1_CS + (16 * (mg + 4) + 4 * g) * 4) = c.acc[3][1];
  }
  asm volatile("s_waitcnt lgkmcnt(0)\n\ts_barrier" ::: "memory");
  {
    f32x2 p[2][2]; f32x4 cc[4];
#pragma unroll
    for (int t = 0; t < 2; ++t) { p[t][0] = *(LDSP const f32x2*)(a_st + t * 128); p[t][1] = *(LDSP const f32x2*)(a_st + 1024 + t * 128); }
#pragma unroll
    for (int j = 0; j < 4; ++j) cc[j] = *(LDSP const f32x4*)(a_cs + j * 64);
    SBAR();
#pragma unroll
    for (int t = 0; t < 2; ++t) {
      const float mean = (p[t][0][0] + p[t][1][0]) * (1.0f / DM);
      const float var = fmaxf((p[t][0][1] + p[t][1][1]) * (1.0f / DM) - mean * mean, 0.f);
      const float rstd = __builtin_amdgcn_rsqf(var + LN_EPS);
      if (cb == 0 && nh == 0) __builtin_amdgcn_raw_buffer_store_b32(__float_as_uint(mean), wrs, (unsigned)WS_XMEAN + (T0 + 32 * mg + 16 * t + m) * 4, 0, QKV_AUX);
#pragma unroll
      for (int j = 0; j < 4; ++j) {
        if (j < 3 || nh == 0) {
          const f32x4 a = (c.acc[j][t] - mean * cc[j]) * (rstd * qs[j]);
          *(LDSP u32x2*)(a_sw + t * (16 * K1_SP) + j * 32) = u32x2{cvtpk(a[0], a[1]), cvtpk(a[2], a[3])};
        }
      }
    }
  }
  if (cb >= 10) {
    asm volatile("s_waitcnt lgkmcnt(0)" ::: "memory");
    u32x4 v[2][2];
#pragma unroll
    for (int tt = 0; tt < 2; ++tt)
#pragma unroll
      for (int t = 0; t < 2; ++t) v[tt][t] = tr_frag(a_tr + t * (16 * K1_SP) + tt * 64, 8 * K1_SP);
    *(LDSP u32x4*)(lds + vone) = c.ones;
#pragma unroll
    for (int tt = 0; tt < 2; ++tt)
#pragma unroll
      for (int t = 0; t < 2; ++t) *(LDSP u32x4*)(lds + vo[tt] + t * 608) = v[tt][t];
  }
  STAMP(4);
  asm volatile("s_waitcnt lgkmcnt(0)\n\ts_barrier" ::: "memory");
  if (cb >= 10) {
    u32x4 v[4];
#pragma unroll
    for (int i = 0; i < 4; ++i) v[i] = ldsr16(lds + i * 8192 + tid * 16);
#pragma unroll
    for (int i = 0; i < 4; ++i) bst16<V_AUX>(v[i], wrs, vd[i], 0);
  }
  if (cb <= 10) {
    unsigned d[2][9];
#pragma unroll
    for (int it = 0; it < 2; ++it) {
      const int hd = (w >> 1) + 4 * it;
      LDSP const unsigned* sp = (LDSP const unsigned*)(a_g + 36 * (hd < 6 ? hd : 0));
#pragma unroll
      for (int i = 0; i < 9; ++i) d[it][i] = sp[i];
    }
    SBAR();
#pragma unroll
    for (int it = 0; it < 2; ++it) {
      bst16<QKV_AUX>(u32x4{d[it][0], d[it][1], d[it][2], d[it][3]}, wrs, um[it], 0);
      bst16<QKV_AUX>(u32x4{d[it][4], d[it][5], d[it][6], d[it][7]}, wrs, um[it] + 512, 0);
      __builtin_amdgcn_raw_buffer_store_b32(d[it][8], wrs, ut[it], 0, QKT_AUX);
    }
#pragma unroll
    for (int it = 0; it < 2; ++it) {
      const int hd = (w >> 1) + 4 * it, G = 6 * cb + hd;
      if (hd < 6 && G >= 32 && G < 64) {
        float n2 = 0.f;
#pragma unroll
        for (int i = 0; i < 9; ++i) n2 = dot2(d[it][i], d[it][i], n2);
        unsigned nb = __float_as_uint(n2);
        { auto r = __builtin_amdgcn_permlane32_swap(nb, nb, false, false); nb = max(r[0], r[1]); }
        { auto r = __builtin_amdgcn_permlane16_swap(nb, nb, false, false); nb = max(r[0], r[1]); }
        __builtin_amdgcn_raw_buffer_store_b32(nb, wrs, (unsigned)WS_KMAX + (((2 * rb + (w & 1)) * 32 + (G & 31)) * 16 + m) * 4, 0, QKV_AUX);
      }
    }
  }
  STAMP(5); STAMP_OUT(1, 6);
}

constexpr int K2_LDS = (int)KV_HEAD_B;
constexpr int NJ_W1 = 72 * 36, NJ_WD = 9 * 144, NJ_WO = 9 * 18, NJ_ALL = NJ_W1 + NJ_WD + NJ_WO;
struct PrepJob { const char* src; unsigned rs, c1, c2, voff, dst[4]; };
DI void prep_setup(PrepJob& j, int J, int lane, const u16* wo, const u16* wg, const u16* wu, const u16* wd) {
  if (J >= NJ_ALL) J = NJ_ALL - 1;
  const int g = (lane >> 2) & 3;
  unsigned lrow = 8 * g;
  j.c1 = 0; j.c2 = 0;
  if (J < NJ_W1) {
    const int sm = J / 72, tg = J - sm * 72;
    j.src = (const char*)((sm & 1) ? wu : wg) + (size_t)(sm >> 1) * (32 * FF * 2) + tg * 128;
    j.rs = FF * 2;
#pragma unroll
    for (int t = 0; t < 4; ++t) j.dst[t] = (unsigned)WS_W1 + (4 * tg + t) * 36864 + sm * 1024;
  } else if (J < NJ_W1 + NJ_WD) {
    const int q = J - NJ_W1, cc = q / 9, tg = q - cc * 9;
    j.src = (const char*)wd + (size_t)cc * (32 * DM * 2) + tg * 128;
    j.rs = DM * 2;
#pragma unroll
    for (int t = 0; t < 4; ++t) { const int nt = 4 * tg + t, nb = nt / 9; j.dst[t] = (unsigned)WS_WD + ((nb * 144 + cc) * 9 + (nt - nb * 9)) * 1024; }
  } else {
    const int q = J - NJ_W1 - NJ_WD, step = q / 9, tg = q - step * 9;
    j.rs = DM * 2;
    if (step < 16) { j.src = (const char*)wo + (size_t)step * (36 * DM * 2) + tg * 128; lrow = 18 * (g >> 1) + 4 * (g & 1); j.c2 = 4; }
    else { j.src = (const char*)wo + (size_t)(288 * (step - 16) + 16) * (DM * 2) + tg * 128; lrow = 72 * g; j.c1 = 16; }
#pragma unroll
    for (int t = 0; t < 4; ++t) { const int nt = 4 * tg + t, nb = nt / 3; j.dst[t] = (unsigned)WS_WO + ((nb * 18 + step) * 3 + (nt - nb * 3)) * 1024; }
  }
#pragma unroll
  for (int t = 0; t < 4; ++t) j.dst[t] = rfl(j.dst[t]);
  j.voff = lrow * j.rs + (lane >> 4) * 32 + (lane & 3) * 8;
}
template <int JJ>
DI void prep_load(const PrepJob& j, u32x2 (&r)[8]) {
  const char* b = j.src + (JJ + j.c1 * (JJ >> 1) + j.c2 * (JJ >> 2)) * j.rs;
  asm volatile("global_load_dwordx2 %0, %1, %2" : "=v"(r[JJ]) : "v"(j.voff), "s"(b));
}
DI void prep_load_all(const PrepJob& j, u32x2 (&r)[8]) {
  prep_load<0>(j, r); prep_load<1>(j, r); prep_load<2>(j, r); prep_load<3>(j, r); prep_load<4>(j, r); prep_load<5>(j, r); prep_load<6>(j, r); prep_load<7>(j, r);
}
DI void prep_store(const PrepJob& j, const u32x2 (&r)[8], __amdgpu_buffer_rsrc_t rs, unsigned so) {
  u32x4 u[4];
#pragma unroll
  for (int i = 0; i < 4; ++i) {
    u[0][i] = __builtin_amdgcn_perm(r[2 * i + 1][0], r[2 * i][0], 0x05040100u);
    u[1][i] = __builtin_amdgcn_perm(r[2 * i + 1][0], r[2 * i][0], 0x07060302u);
    u[2][i] = __builtin_amdgcn_perm(r[2 * i + 1][1], r[2 * i][1], 0x05040100u);
    u[3][i] = __builtin_amdgcn_perm(r[2 * i + 1][1], r[2 * i][1], 0x07060302u);
  }
#pragma unroll
  for (int i = 0; i < 4; ++i) {
    const auto a0 = __builtin_amdgcn_permlane32_swap(u[0][i], u[2][i], false, false);
    const auto a1 = __builtin_amdgcn_permlane32_swap(u[1][i], u[3][i], false, false);
    const auto b0 = __builtin_amdgcn_permlane16_swap(a0[0], a1[0], false, false);
    const auto b1 = __builtin_amdgcn_permlane16_swap(a0[1], a1[1], false, false);
    u[0][i] = b0[0]; u[1][i] = b0[1]; u[2][i] = b1[0]; u[3][i] = b1[1];
  }
#pragma unroll
  for (int t = 0; t < 4; ++t) bst16<PREP_AUX>(u[t], rs, so, j.dst[t]);
}
#define PIN8(r) "+v"(r[0]), "+v"(r[1]), "+v"(r[2]), "+v"(r[3]), "+v"(r[4]), "+v"(r[5]), "+v"(r[6]), "+v"(r[7])

#define A_MFMA_ACC(d, a, b) asm volatile("v_mfma_f32_32x32x16_bf16 %0, %1, %2, %0" : "+v"(d) : "v"(a), "v"(b))
#define AT_M(m)
#define AT_U
struct AttnC {
  LDSP const char *kmb, *ktb, *vb;
  unsigned kma[3], kta[3], va[3]; unsigned long long mk, mv;
  u32x4 qm, qt, kf, v0, v1, p1; u32x2 ktl;
  f32x16 negm, o, sa, sb;
};
constexpr int k_off(int T) { return (T >> 4) * (int)CHUNK_B + (T & 15) * 1024; }
constexpr int kt_off(int T) { return (T >> 4) * (int)CHUNK_B + ((T >> 1) & 7) * 256; }
constexpr int v_off(int T) { return (T >> 4) * (int)CHUNK_B + (T & 15) * 1216; }
#define AT_STMT(TXT)                                                                                                              \
  asm volatile(TXT : "+{v[2:17]}"(c.o), "+{v[34:49]}"(c.sa), "+{v[50:65]}"(c.sb), "+{v[66:69]}"(c.kf), "+{v[70:73]}"(c.p1),        \
               "+{v[84:87]}"(c.v1), "+{v[88:91]}"(c.v0), "+{v[92:93]}"(c.ktl)                                                      \
               : "{v[18:33]}"(c.negm), "{v[98:101]}"(c.qm), "{v[102:105]}"(c.qt), [ka] "v"(c.kma[KO >> 16]), [kt] "v"(c.kta[KT >> 16]), \
                 [va] "v"(c.va[V1 >> 16]), [vb] "v"(c.va[V0 >> 16]), [ko] "i"(KO & 0xffff), [kto] "i"(KT & 0xffff), [vo1] "i"(V1 & 0xffff),  \
                 [vo0] "i"(V0 & 0xffff), [mk] "s"(c.mk), [mv] "s"(c.mv)                                                            \
               : "v74", "v75", "v76", "v77")
template <int T> DI void attn_tile(AttnC& c) {
  constexpr int KO = k_off(T < 62 ? T + 2 : 0), KT = kt_off(T < 62 ? T + 2 : 0), V1 = v_off(T) + 608, V0 = v_off(T < 63 ? T + 1 : 0);
  if constexpr (T == 0)
    AT_STMT("v_mfma_f32_32x32x8_bf16 v[50:65], v[92:93], v[104:105], v[18:33]\n\t" "v_exp_f32 v34, v34\n\t" "v_exp_f32 v35, v35\n\t" "v_exp_f32 v36, v36\n\t" "v_exp_f32 v37, v37\n\t" AT_M("mk") "ds_read_b64 v[92:93], %[kt] offset:%[kto]\n\t" AT_U AT_M("mv") "ds_read_b128 v[84:87], %[va] offset:%[vo1]\n\t" AT_U "v_cvt_pk_bf16_f32 v74, v34, v35\n\t" "v_exp_f32 v38, v38\n\t" "v_cvt_pk_bf16_f32 v75, v36, v37\n\t" "v_exp_f32 v39, v39\n\t" "v_exp_f32 v40, v40\n\t" "v_exp_f32 v41, v41\n\t" "s_waitcnt lgkmcnt(2)\n\t" "v_mfma_f32_32x32x16_bf16 v[50:65], v[66:69], v[98:101], v[50:65]\n\t" "ds_read_b128 v[66:69], %[ka] offset:%[ko]\n\t" "v_cvt_pk_bf16_f32 v76, v38, v39\n\t" "v_exp_f32 v42, v42\n\t" "v_cvt_pk_bf16_f32 v77, v40, v41\n\t" "v_exp_f32 v43, v43\n\t" "v_exp_f32 v44, v44\n\t" "v_exp_f32 v45, v45\n\t" "s_waitcnt lgkmcnt(2)\n\t" "v_mfma_f32_32x32x16_bf16 v[2:17], v[88:91], v[74:77], v[2:17]\n\t" AT_M("mv") "ds_read_b128 v[88:91], %[vb] offset:%[vo0]\n\t" AT_U "v_cvt_pk_bf16_f32 v70, v42, v43\n\t" "v_exp_f32 v46, v46\n\t" "v_cvt_pk_bf16_f32 v71, v44, v45\n\t" "v_exp_f32 v47, v47\n\t" "v_exp_f32 v48, v48\n\t" "v_exp_f32 v49, v49\n\t" "v_cvt_pk_bf16_f32 v72, v46, v47\n\t" "v_cvt_pk_bf16_f32 v73, v48, v49\n\t");
  else if constexpr (T == 62)
    AT_STMT("v_mfma_f32_32x32x8_bf16 v[50:65], v[92:93], v[104:105], v[18:33]\n\t" "v_exp_f32 v34, v34\n\t" "v_exp_f32 v35, v35\n\t" "v_exp_f32 v36, v36\n\t" "v_exp_f32 v37, v37\n\t" "s_waitcnt lgkmcnt(2)\n\t" "v_mfma_f32_32x32x16_bf16 v[2:17], v[84:87], v[70:73], v[2:17]\n\t" AT_M("mv") "ds_read_b128 v[84:87], %[va] offset:%[vo1]\n\t" AT_U "v_cvt_pk_bf16_f32 v74, v34, v35\n\t" "v_exp_f32 v38, v38\n\t" "v_cvt_pk_bf16_f32 v75, v36, v37\n\t" "v_exp_f32 v39, v39\n\t" "v_exp_f32 v40, v40\n\t" "v_exp_f32 v41, v41\n\t" "s_waitcnt lgkmcnt(2)\n\t" "v_mfma_f32_32x32x16_bf16 v[50:65], v[66:69], v[98:101], v[50:65]\n\t" "v_cvt_pk_bf16_f32 v76, v38, v39\n\t" "v_exp_f32 v42, v42\n\t" "v_cvt_pk_bf16_f32 v77, v40, v41\n\t" "v_exp_f32 v43, v43\n\t" "v_exp_f32 v44, v44\n\t" "v_exp_f32 v45, v45\n\t" "s_waitcnt lgkmcnt(1)\n\t" "v_mfma_f32_32x32x16_bf16 v[2:17], v[88:91], v[74:77], v[2:17]\n\t" AT_M("mv") "ds_read_b128 v[88:91], %[vb] offset:%[vo0]\n\t" AT_U "v_cvt_pk_bf16_f32 v70, v42, v43\n\t" "v_exp_f32 v46, v46\n\t" "v_cvt_pk_bf16_f32 v71, v44, v45\n\t" "v_exp_f32 v47, v47\n\t" "v_exp_f32 v48, v48\n\t" "v_exp_f32 v49, v49\n\t" "v_cvt_pk_bf16_f32 v72, v46, v47\n\t" "v_cvt_pk_bf16_f32 v73, v48, v49\n\t");
  else if constexpr (T == 63)
    AT_STMT("v_exp_f32 v50, v50\n\t" "v_exp_f32 v51, v51\n\t" "v_exp_f32 v52, v52\n\t" "v_exp_f32 v53, v53\n\t" "s_waitcnt lgkmcnt(0)\n\t" "v_mfma_f32_32x32x16_bf16 v[2:17], v[84:87], v[70:73], v[2:17]\n\t" AT_M("mv") "ds_read_b128 v[84:87], %[va] offset:%[vo1]\n\t" AT_U "v_cvt_pk_bf16_f32 v74, v50, v51\n\t" "v_exp_f32 v54, v54\n\t" "v_cvt_pk_bf16_f32 v75, v52, v53\n\t" "v_exp_f32 v55, v55\n\t" "v_exp_f32 v56, v56\n\t" "v_exp_f32 v57, v57\n\t" "v_cvt_pk_bf16_f32 v76, v54, v55\n\t" "v_exp_f32 v58, v58\n\t" "v_cvt_pk_bf16_f32 v77, v56, v57\n\t" "v_exp_f32 v59, v59\n\t" "v_exp_f32 v60, v60\n\t" "v_exp_f32 v61, v61\n\t" "s_waitcnt lgkmcnt(1)\n\t" "v_mfma_f32_32x32x16_bf16 v[2:17], v[88:91], v[74:77], v[2:17]\n\t" "v_cvt_pk_bf16_f32 v70, v58, v59\n\t" "v_exp_f32 v62, v62\n\t" "v_cvt_pk_bf16_f32 v71, v60, v61\n\t" "v_exp_f32 v63, v63\n\t" "v_exp_f32 v64, v64\n\t" "v_exp_f32 v65, v65\n\t" "v_cvt_pk_bf16_f32 v72, v62, v63\n\t" "v_cvt_pk_bf16_f32 v73, v64, v65\n\t");
  else if constexpr ((T & 3) == 0)
    AT_STMT("v_mfma_f32_32x32x8_bf16 v[50:65], v[92:93], v[104:105], v[18:33]\n\t" "v_exp_f32 v34, v34\n\t" "v_exp_f32 v35, v35\n\t" "v_exp_f32 v36, v36\n\t" "v_exp_f32 v37, v37\n\t" "s_waitcnt lgkmcnt(2)\n\t" "v_mfma_f32_32x32x16_bf16 v[2:17], v[84:87], v[70:73], v[2:17]\n\t" AT_M("mk") "ds_read_b64 v[92:93], %[kt] offset:%[kto]\n\t" AT_U AT_M("mv") "ds_read_b128 v[84:87], %[va] offset:%[vo1]\n\t" AT_U "v_cvt_pk_bf16_f32 v74, v34, v35\n\t" "v_exp_f32 v38, v38\n\t" "v_cvt_pk_bf16_f32 v75, v36, v37\n\t" "v_exp_f32 v39, v39\n\t" "v_exp_f32 v40, v40\n\t" "v_exp_f32 v41, v41\n\t" "s_waitcnt lgkmcnt(2)\n\t" "v_mfma_f32_32x32x16_bf16 v[50:65], v[66:69], v[98:101], v[50:65]\n\t" "ds_read_b128 v[66:69], %[ka] offset:%[ko]\n\t" "v_cvt_pk_bf16_f32 v76, v38, v39\n\t" "v_exp_f32 v42, v42\n\t" "v_cvt_pk_bf16_f32 v77, v40, v41\n\t" "v_exp_f32 v43, v43\n\t" "v_exp_f32 v44, v44\n\t" "v_exp_f32 v45, v45\n\t" "s_waitcnt lgkmcnt(2)\n\t" "v_mfma_f32_32x32x16_bf16 v[2:17], v[88:91], v[74:77], v[2:17]\n\t" AT_M("mv") "ds_read_b128 v[88:91], %[vb] offset:%[vo0]\n\t" AT_U "v_cvt_pk_bf16_f32 v70, v42, v43\n\t" "v_exp_f32 v46, v46\n\t" "v_cvt_pk_bf16_f32 v71, v44, v45\n\t" "v_exp_f32 v47, v47\n\t" "v_exp_f32 v48, v48\n\t" "v_exp_f32 v49, v49\n\t" "v_cvt_pk_bf16_f32 v72, v46, v47\n\t" "v_cvt_pk_bf16_f32 v73, v48, v49\n\t");
  else if constexpr ((T & 3) == 1)
    AT_STMT("v_mfma_f32_32x32x8_bf16 v[34:49], v[92:93], v[102:103], v[18:33]\n\t" "v_exp_f32 v50, v50\n\t" "v_exp_f32 v51, v51\n\t" "v_exp_f32 v52, v52\n\t" "v_exp_f32 v53, v53\n\t" "s_waitcnt lgkmcnt(2)\n\t" "v_mfma_f32_32x32x16_bf16 v[2:17], v[84:87], v[70:73], v[2:17]\n\t" AT_M("mv") "ds_read_b128 v[84:87], %[va] offset:%[vo1]\n\t" AT_U "v_cvt_pk_bf16_f32 v74, v50, v51\n\t" "v_exp_f32 v54, v54\n\t" "v_cvt_pk_bf16_f32 v75, v52, v53\n\t" "v_exp_f32 v55, v55\n\t" "v_exp_f32 v56, v56\n\t" "v_exp_f32 v57, v57\n\t" "s_waitcnt lgkmcnt(2)\n\t" "v_mfma_f32_32x32x16_bf16 v[34:49], v[66:69], v[98:101], v[34:49]\n\t" "ds_read_b128 v[66:69], %[ka] offset:%[ko]\n\t" "v_cvt_pk_bf16_f32 v76, v54, v55\n\t" "v_exp_f32 v58, v58\n\t" "v_cvt_pk_bf16_f32 v77, v56, v57\n\t" "v_exp_f32 v59, v59\n\t" "v_exp_f32 v60, v60\n\t" "v_exp_f32 v61, v61\n\t" "s_waitcnt lgkmcnt(2)\n\t" "v_mfma_f32_32x32x16_bf16 v[2:17], v[88:91], v[74:77], v[2:17]\n\t" AT_M("mv") "ds_read_b128 v[88:91], %[vb] offset:%[vo0]\n\t" AT_U "v_cvt_pk_bf16_f32 v70, v58, v59\n\t" "v_exp_f32 v62, v62\n\t" "v_cvt_pk_bf16_f32 v71, v60, v61\n\t" "v_exp_f32 v63, v63\n\t" "v_exp_f32 v64, v64\n\t" "v_exp_f32 v65, v65\n\t" "v_cvt_pk_bf16_f32 v72, v62, v63\n\t" "v_cvt_pk_bf16_f32 v73, v64, v65\n\t");
  else if constexpr ((T & 3) == 2)
    AT_STMT("v_mfma_f32_32x32x8_bf16 v[50:65], v[92:93], v[104:105], v[18:33]\n\t" "v_exp_f32 v34, v34\n\t" "v_exp_f32 v35, v35\n\t" "v_exp_f32 v36, v36\n\t" "v_exp_f32 v37, v37\n\t" "s_waitcnt lgkmcnt(2)\n\t" "v_mfma_f32_32x32x16_bf16 v[2:17], v[84:87], v[70:73], v[2:17]\n\t" AT_M("mk") "ds_read_b64 v[92:93], %[kt] offset:%[kto]\n\t" AT_U AT_M("mv") "ds_read_b128 v[84:87], %[va] offset:%[vo1]\n\t" AT_U "v_cvt_pk_bf16_f32 v74, v34, v35\n\t" "v_exp_f32 v38, v38\n\t" "v_cvt_pk_bf16_f32 v75, v36, v37\n\t" "v_exp_f32 v39, v39\n\t" "v_exp_f32 v40, v40\n\t" "v_exp_f32 v41, v41\n\t" "s_waitcnt lgkmcnt(2)\n\t" "v_mfma_f32_32x32x16_bf16 v[50:65], v[66:69], v[98:101], v[50:65]\n\t" "ds_read_b128 v[66:69], %[ka] offset:%[ko]\n\t" "v_cvt_pk_bf16_f32 v76, v38, v39\n\t" "v_exp_f32 v42, v42\n\t" "v_cvt_pk_bf16_f32 v77, v40, v41\n\t" "v_exp_f32 v43, v43\n\t" "v_exp_f32 v44, v44\n\t" "v_exp_f32 v45, v45\n\t" "s_waitcnt lgkmcnt(2)\n\t" "v_mfma_f32_32x32x16_bf16 v[2:17], v[88:91], v[74:77], v[2:17]\n\t" AT_M("mv") "ds_read_b128 v[88:91], %[vb] offset:%[vo0]\n\t" AT_U "v_cvt_pk_bf16_f32 v70, v42, v43\n\t" "v_exp_f32 v46, v46\n\t" "v_cvt_pk_bf16_f32 v71, v44, v45\n\t" "v_exp_f32 v47, v47\n\t" "v_exp_f32 v48, v48\n\t" "v_exp_f32 v49, v49\n\t" "v_cvt_pk_bf16_f32 v72, v46, v47\n\t" "v_cvt_pk_bf16_f32 v73, v48, v49\n\t");
  else if constexpr ((T & 3) == 3)
    AT_STMT("v_mfma_f32_32x32x8_bf16 v[34:49], v[92:93], v[102:103], v[18:33]\n\t" "v_exp_f32 v50, v50\n\t" "v_exp_f32 v51, v51\n\t" "v_exp_f32 v52, v52\n\t" "v_exp_f32 v53, v53\n\t" "s_waitcnt lgkmcnt(2)\n\t" "v_mfma_f32_32x32x16_bf16 v[2:17], v[84:87], v[70:73], v[2:17]\n\t" AT_M("mv") "ds_read_b128 v[84:87], %[va] offset:%[vo1]\n\t" AT_U "v_cvt_pk_bf16_f32 v74, v50, v51\n\t" "v_exp_f32 v54, v54\n\t" "v_cvt_pk_bf16_f32 v75, v52, v53\n\t" "v_exp_f32 v55, v55\n\t" "v_exp_f32 v56, v56\n\t" "v_exp_f32 v57, v57\n\t" "s_waitcnt lgkmcnt(2)\n\t" "v_mfma_f32_32x32x16_bf16 v[34:49], v[66:69], v[98:101], v[34:49]\n\t" "ds_read_b128 v[66:69], %[ka] offset:%[ko]\n\t" "v_cvt_pk_bf16_f32 v76, v54, v55\n\t" "v_exp_f32 v58, v58\n\t" "v_cvt_pk_bf16_f32 v77, v56, v57\n\t" "v_exp_f32 v59, v59\n\t" "v_exp_f32 v60, v60\n\t" "v_exp_f32 v61, v61\n\t" "s_waitcnt lgkmcnt(2)\n\t" "v_mfma_f32_32x32x16_bf16 v[2:17], v[88:91], v[74:77], v[2:17]\n\t" AT_M("mv") "ds_read_b128 v[88:91], %[vb] offset:%[vo0]\n\t" AT_U "v_cvt_pk_bf16_f32 v70, v58, v59\n\t" "v_exp_f32 v62, v62\n\t" "v_cvt_pk_bf16_f32 v71, v60, v61\n\t" "v_exp_f32 v63, v63\n\t" "v_exp_f32 v64, v64\n\t" "v_exp_f32 v65, v65\n\t" "v_cvt_pk_bf16_f32 v72, v62, v63\n\t" "v_cvt_pk_bf16_f32 v73, v64, v65\n\t");
}
DI void attn_group_slow(LDSP const char* km, LDSP const char* kt, LDSP const char* vp, const u32x4& qm, unsigned qtl, const f32x16& negm, f32x16& o) {
#pragma unroll
  for (int t = 0; t < 4; ++t) {
    const u32x2 k2 = *(LDSP const u32x2*)(kt + (t >> 1) * 256);
    const u32x4 ktl = {k2[0], k2[1], 0, 0};
    const u32x4 kf = ldsr16(km + t * 1024);
    const u32x4 v0 = ldsr16(vp + t * 1216), v1 = ldsr16(vp + t * 1216 + 608);
    u32x4 qq = {0, 0, 0, 0}; qq[t & 1] = qtl;
    f32x16 s = mfma32(ktl, qq, negm);
    s = mfma32(kf, qm, s);
    u32x4 p0, p1;
#pragma unroll
    for (int i = 0; i < 4; ++i) {
      p0[i] = cvtpk(__builtin_amdgcn_exp2f(s[2 * i]), __builtin_amdgcn_exp2f(s[2 * i + 1]));
      p1[i] = cvtpk(__builtin_amdgcn_exp2f(s[8 + 2 * i]), __builtin_amdgcn_exp2f(s[8 + 2 * i + 1]));
    }
    o = mfma32(v0, p0, o);
    o = mfma32(v1, p1, o);
  }
}
template <int G> DI void attn_group(AttnC& c) {
  __builtin_amdgcn_s_setprio(G < 5 ? 3 - (G & 3) : (G < 8 ? 3 : (G < 11 ? 2 : (G < 14 ? 1 : 0))));
  attn_tile<4 * G + 0>(c); attn_tile<4 * G + 1>(c); attn_tile<4 * G + 2>(c); attn_tile<4 * G + 3>(c);
}
DI void attn_helper(const u16* x, const u16* wo, const u16* wg, const u16* wu, const u16* wd, char* ws, int head, int j, int lane, unsigned lds0) {
  __builtin_amdgcn_s_setprio(HELP_PRIO);
  const char* kv = ws + WS_KV + (size_t)head * KV_HEAD_B;
  const unsigned lane16 = lane * 16;
#define KV_DMA(o) dma16(kv + (o), lane16, lds0 + (o))
#pragma unroll
  for (int i = 0; i < 4; ++i) KV_DMA(((i >> 1) * 16 + (i & 1) * 4 + j) * 1024);
#pragma unroll
  for (int i = 0; i < 6; ++i) { const int p = i < 2 ? 8 + 4 * i + j : min(16 + 4 * i + j, 36); KV_DMA(p * 1024); }
  const int hw = blockIdx.x * 4 + j;
  PrepJob jb[4];
#pragma unroll
  for (int i = 0; i < 4; ++i) { prep_setup(jb[i], 4 * hw + i, lane, wo, wg, wu, wd); PIN(jb[i].voff); }
  SBAR();
  asm volatile("s_waitcnt vmcnt(6)\n\ts_barrier" ::: "memory");
#pragma unroll
  for (int i = 0; i < 10; ++i) KV_DMA((unsigned)CHUNK_B + min(4 * i + j, 36) * 1024);
  asm volatile("s_waitcnt vmcnt(10)\n\ts_barrier" ::: "memory");
#pragma unroll
  for (int i = 0; i < 19; ++i) KV_DMA(2 * (unsigned)CHUNK_B + min(4 * i + j, 73) * 1024);
#undef KV_DMA
  u32x2 r[4][8];
  u32x4 xu[3];
  unsigned wr[5], pf;
  asm volatile("s_waitcnt vmcnt(19)\n\ts_barrier" ::: "memory");
  asm volatile("s_waitcnt vmcnt(0)\n\ts_barrier" ::: "memory");
  prep_load_all(jb[0], r[0]); prep_load_all(jb[1], r[1]);
  const unsigned xl = (lane >> 3) * 1152 + (lane & 7) * 16, xs = (lane & 7) * 256 + (lane >> 3) * 16;
  int xj[3];
#pragma unroll
  for (int i = 0; i < 3; ++i) { xj[i] = i < 2 ? 2 * hw + i : (hw < 256 ? 2048 + hw : 2 * hw); xu[i] = gld16((const char*)x + ((xj[i] / 9) * (8 * 1152) + (xj[i] % 9) * 128), xl); }
  const int br = hw < DM ? hw : DM - 1;
  {
    const char* b = (const char*)wo + (size_t)wo_row(br >> 5, (br >> 3) & 3, br & 7) * (DM * 2);
#pragma unroll
    for (int i = 0; i < 5; ++i) wr[i] = gld4(b, (i < 4 ? lane + 64 * i : 256 + (lane & 31)) * 4);
  }
  prep_load_all(jb[2], r[2]); prep_load_all(jb[3], r[3]);
  CODE_PF2(pf, k_oproj, k_end, 0, j, (blockIdx.x >> 3) == 0 ? 128u : 0u);
  const __amdgpu_buffer_rsrc_t wrs = mkrsrc(ws, (unsigned)WS_END);
  const unsigned pso = ((lane >> 2) & 3) * 256 + ((lane & 3) * 4 + (lane >> 4)) * 16;
  asm volatile("s_waitcnt vmcnt(33)" : PIN8(r[0])::"memory");
  prep_store(jb[0], r[0], wrs, pso);
  asm volatile("s_waitcnt vmcnt(29)" : PIN8(r[1])::"memory");
  prep_store(jb[1], r[1], wrs, pso);
  asm volatile("s_waitcnt vmcnt(25)" : "+v"(xu[0]), "+v"(xu[1]), "+v"(xu[2]), "+v"(wr[0]), "+v"(wr[1]), "+v"(wr[2]), "+v"(wr[3]), "+v"(wr[4])::"memory");
#pragma unroll
  for (int i = 0; i < 3; ++i) bst16<PREP_AUX>(xu[i], wrs, xs, (unsigned)WS_XU + (xj[i] / 18) * 18432 + (xj[i] % 9) * 2048 + ((xj[i] / 9) & 1) * 128);
  {
    float a = 0.f;
#pragma unroll
    for (int i = 0; i < 4; ++i) a = dot2(wr[i], 0x3f803f80u, a);
    a = dot2(lane >= 32 ? 0u : wr[4], 0x3f803f80u, a);
    a = xor16_sum(xor32_sum(a));
    a += __uint_as_float(__builtin_amdgcn_update_dpp(0u, __float_as_uint(a), 0x128, 0xf, 0xf, false));
    a += __uint_as_float(__builtin_amdgcn_update_dpp(0u, __float_as_uint(a), 0x124, 0xf, 0xf, false));
    a += __uint_as_float(__builtin_amdgcn_update_dpp(0u, __float_as_uint(a), 0x122, 0xf, 0xf, false));
    a += __uint_as_float(__builtin_amdgcn_update_dpp(0u, __float_as_uint(a), 0x121, 0xf, 0xf, false));
    a *= (1.0f / DM);
    const unsigned hi = cvtpk(a, 0.f) & 0xffffu;
    const unsigned lo = cvtpk(a - bflo(hi), 0.f) & 0xffffu;
    __builtin_amdgcn_raw_buffer_store_b16((u16)(lane ? lo : hi), wrs, lane < 2 ? (unsigned)WS_WBAR + (lane * DM + br) * 2 : 0x80000000u, 0, 0);
  }
  asm volatile("s_waitcnt vmcnt(21)" : PIN8(r[2])::"memory");
  prep_store(jb[2], r[2], wrs, pso);
  asm volatile("s_waitcnt vmcnt(16)" : PIN8(r[3]), "+v"(pf)::"memory");
  prep_store(jb[3], r[3], wrs, pso);
}
extern "C" __global__ __launch_bounds__(768, 3) void k_attn(const u16* x, const u16* wo, const u16* wg, const u16* wu, const u16* wd, char* ws) {
  extern __shared__ __attribute__((aligned(16))) char smem[];
  asm volatile("" : "+s"(x), "+s"(wo), "+s"(wg), "+s"(wu), "+s"(wd), "+s"(ws));
  LDSP char* lds = (LDSP char*)smem;
  const int tid = threadIdx.x, lane = tid & 63, w = rfl(tid >> 6);
  const int xcd = blockIdx.x & 7, bi = blockIdx.x >> 3;
  const int head = 4 * xcd + (bi & 3), qb = bi >> 2;
  if (__builtin_expect(w >= 8, AT_HFIRST)) { attn_helper(x, wo, wg, wu, wd, ws, head, w - 8, lane, (unsigned)(uintptr_t)lds); return; }
  const int r = lane & 31, h = lane >> 5;
  const int qt32 = 8 * qb + w;
  STAMP_DECL; STAMP(0);
  if (blockIdx.x == 8 && threadIdx.x == 0) ((__attribute__((address_space(1))) unsigned long long*)(ws + (200u << 20)))[4] = __builtin_amdgcn_s_memrealtime();
  AttnC c;
  c.qm = gld16(ws + WS_QM, head * 65536 + qt32 * 1024 + lane * 16);
  unsigned qtl = gld4(ws + WS_QT, head * 8192 + (32 * qt32 + r) * 4);
  u32x4 km0 = gld16(ws + WS_KMAX, ((lane >> 1) * 32 + head) * 64 + (lane & 1) * 32);
  u32x4 km1 = gld16(ws + WS_KMAX, ((lane >> 1) * 32 + head) * 64 + (lane & 1) * 32 + 16);
  const int vrow = r < 19 ? r : ((r >= 20 && r < 28) ? 0 : 18);
  c.kmb = lds + lane * 16;
  c.ktb = lds + 16384 + r * 8;
  c.vb = lds + 18432 + h * 304 + vrow * 16;
#pragma unroll
  for (int i = 0; i < 3; ++i) {
    c.kma[i] = (unsigned)(uintptr_t)c.kmb + i * 65536; c.kta[i] = (unsigned)(uintptr_t)c.ktb + i * 65536; c.va[i] = (unsigned)(uintptr_t)c.vb + i * 65536;
    PIN(c.kma[i]); PIN(c.kta[i]); PIN(c.va[i]);
  }
  c.mk = 0x00000000ffffffffull; c.mv = 0x0007ffff0007ffffull;
  c.ktl = u32x2{0, 0}; c.v0 = u32x4{0, 0, 0, 0}; c.v1 = c.v0; PIN(c.ktl); PIN(c.v0); PIN(c.v1);
#pragma unroll
  for (int i = 0; i < 16; ++i) c.o[i] = 0.f;
  PIN(c.o);
  const __amdgpu_buffer_rsrc_t wrs = mkrsrc(ws, (unsigned)WS_END);
  SBAR();
  asm volatile("s_waitcnt vmcnt(0)" : "+v"(c.qm), "+v"(qtl), "+v"(km0), "+v"(km1)::"memory");
  if (h) qtl = 0;
  float q2 = dot2(qtl, qtl, 0.f);
#pragma unroll
  for (int i = 0; i < 4; ++i) q2 = dot2(c.qm[i], c.qm[i], q2);
  unsigned k2 = max(max(max(km0[0], km0[1]), max(km0[2], km0[3])), max(max(km1[0], km1[1]), max(km1[2], km1[3])));
  { auto rr = __builtin_amdgcn_permlane32_swap(k2, k2, false, false); k2 = max(rr[0], rr[1]); }
  { auto rr = __builtin_amdgcn_permlane16_swap(k2, k2, false, false); k2 = max(rr[0], rr[1]); }
  k2 = max(k2, (unsigned)__builtin_amdgcn_update_dpp(0u, k2, 0x128, 0xf, 0xf, true));
  k2 = max(k2, (unsigned)__builtin_amdgcn_update_dpp(0u, k2, 0x124, 0xf, 0xf, true));
  k2 = max(k2, (unsigned)__builtin_amdgcn_update_dpp(0u, k2, 0x122, 0xf, 0xf, true));
  k2 = max(k2, (unsigned)__builtin_amdgcn_update_dpp(0u, k2, 0x121, 0xf, 0xf, true));
  q2 = xor32_sum(q2);
  const float mshift = __builtin_amdgcn_sqrtf(q2 * __uint_as_float(k2)) * 1.001f;
  c.qt = u32x4{qtl, 0, 0, qtl}; PIN(c.qt);
#pragma unroll
  for (int i = 0; i < 16; ++i) c.negm[i] = -mshift;
  PIN(c.negm);
  STAMP(1);
  asm volatile("s_barrier" ::: "memory");
  STAMP(2);
  asm volatile(AT_M("mk") "ds_read_b64 v[92:93], %[kt]\n\t" AT_U "ds_read_b128 v[70:73], %[ka]\n\tds_read_b128 v[66:69], %[ka] offset:1024\n\t"
               AT_M("mv") "ds_read_b128 v[88:91], %[va]\n\t" AT_U "s_waitcnt lgkmcnt(2)\n\t"
               "v_mfma_f32_32x32x8_bf16 v[34:49], v[92:93], v[102:103], v[18:33]\n\t"
               "v_mfma_f32_32x32x16_bf16 v[34:49], v[70:73], v[98:101], v[34:49]\n\ts_nop 11"
               : "={v[34:49]}"(c.sa), "={v[50:65]}"(c.sb), "={v[66:69]}"(c.kf), "={v[70:73]}"(c.p1), "+{v[88:91]}"(c.v0), "+{v[92:93]}"(c.ktl)
               : "{v[18:33]}"(c.negm), "{v[98:101]}"(c.qm), "{v[102:105]}"(c.qt), [ka] "v"(c.kma[0]), [kt] "v"(c.kta[0]), [va] "v"(c.va[0]),
                 [mk] "s"(c.mk), [mv] "s"(c.mv));
  attn_group<0>(c);
  asm volatile("s_barrier" ::: "memory");
  attn_group<1>(c); attn_group<2>(c);
  asm volatile("s_barrier" ::: "memory");
  attn_group<3>(c); attn_group<4>(c);
  asm volatile("s_barrier" ::: "memory");
  STAMP(3);
  attn_group<5>(c); attn_group<6>(c); attn_group<7>(c); attn_group<8>(c); attn_group<9>(c); attn_group<10>(c);
  attn_group<11>(c); attn_group<12>(c); attn_group<13>(c); attn_group<14>(c); attn_group<15>(c);
  asm volatile("s_waitcnt lgkmcnt(0)\n\tv_mfma_f32_32x32x16_bf16 v[2:17], v[84:87], v[70:73], v[2:17]\n\ts_nop 11"
               : "+{v[2:17]}"(c.o) : "{v[84:87]}"(c.v1), "{v[70:73]}"(c.p1), "{v[18:33]}"(c.negm));
  STAMP(4);
  f32x16 o = c.o;
  float l;
  { auto rr = __builtin_amdgcn_permlane32_swap(__float_as_uint(o[10]), __float_as_uint(o[10]), false, false); l = __uint_as_float(rr[0]); }
  if (__builtin_expect(__any(!(l >= 5.421011e-20f)), 0)) {
    float mx = -INFINITY;
    f32x16 z, negm;
#pragma unroll
    for (int i = 0; i < 16; ++i) z[i] = 0.f;
#pragma unroll 1
    for (int tt = 0; tt < 64; ++tt) {
      const int ck = tt >> 4, tl = tt & 15;
      const u32x2 k2 = *(LDSP const u32x2*)(c.ktb + ck * CHUNK_B + (tl >> 1) * 256);
      const u32x4 ktl = {k2[0], k2[1], 0, 0};
      const u32x4 kf = ldsr16(c.kmb + ck * CHUNK_B + tl * 1024);
      const u32x4 qq = {(tl & 1) ? 0u : qtl, (tl & 1) ? qtl : 0u, 0, 0};
      f32x16 sc = mfma32(ktl, qq, z);
      sc = mfma32(kf, c.qm, sc);
#pragma unroll
      for (int i = 0; i < 16; ++i) mx = fmaxf(mx, sc[i]);
    }
    mx = xor32_max(mx);
#pragma unroll
    for (int i = 0; i < 16; ++i) { negm[i] = -mx; o[i] = 0.f; }
#pragma unroll 1
    for (int gq = 0; gq < 16; ++gq) {
      const int ck = gq >> 2, gg = gq & 3;
      attn_group_slow(c.kmb + ck * CHUNK_B + gg * 4096, c.ktb + ck * CHUNK_B + gg * 512, c.vb + ck * CHUNK_B + gg * 4864, c.qm, qtl, negm, o);
    }
    auto rr = __builtin_amdgcn_permlane32_swap(__float_as_uint(o[10]), __float_as_uint(o[10]), false, false);
    l = __uint_as_float(rr[0]);
  }
  const float rl = 1.0f / l;
  u32x4 ou;
#pragma unroll
  for (int i = 0; i < 4; ++i) ou[i] = cvtpk(o[2 * i] * rl, o[2 * i + 1] * rl);
  const int q = 32 * qt32 + r;
  const unsigned ob = (unsigned)WS_OFR + (q >> 4) * 18432 + (q & 15) * 16;
  bst16<O_AUX>(ou, wrs, ob + (head >> 1) * 1024 + (2 * (head & 1) + h) * 256, 0);
  __builtin_amdgcn_raw_buffer_store_b32(cvtpk(o[8] * rl, o[9] * rl), wrs, h ? 0x80000000u : ob + (16 + (head >> 4)) * 1024 + ((head >> 2) & 3) * 256 + (head & 3) * 4, 0, O_AUX);
  STAMP(5); STAMP_OUT(2, 6);
}

constexpr int K3_WBAR = 54 * 1024, K3_O = K3_WBAR + 3072, K3_LDS = K3_O + 96 * 1024;
extern "C" __global__ __launch_bounds__(768, 3) void k_oproj(char* __restrict__ ws) {
  extern __shared__ __attribute__((aligned(16))) char smem[];
  LDSP char* lds = (LDSP char*)smem;
  const unsigned lds0 = (unsigned)(uintptr_t)lds;
  const int tid = threadIdx.x, lane = tid & 63, w = rfl(tid >> 6);
  const int xcd = blockIdx.x & 7, bi = blockIdx.x >> 3;
  const int rb = 2 * xcd + (bi & 1), nb = bi >> 1;
  const unsigned lane16 = lane * 16;
  if (__builtin_expect(w >= 8, 1)) {
    const int j = w - 8;
    const char* wb = ws + WS_WO + (size_t)nb * 54 * 1024;
    const char* ob = ws + WS_OFR + (size_t)(8 * rb + 2 * j) * 18432;
    const unsigned od = lds0 + K3_O + j * 24576;
#define K3_W(z) _Pragma("unroll") for (int i = 0; i < 5; ++i) { const int p = 18 * (z) + min(4 * i + j, 17); dma16(wb + p * 1024, lane16, lds0 + p * 1024); }
#define K3_OS(z) _Pragma("unroll") for (int i = 0; i < 12; ++i) dma16(ob + ((i & 1) * 18 + 6 * (z) + (i >> 1)) * 1024, lane16, od + ((i & 1) * 12 + 6 * (z) - 6 + (i >> 1)) * 1024)
    K3_W(0);
    dma16(ws + WS_WBAR + min(j, 2) * 1024, lane16, lds0 + K3_WBAR + min(j, 2) * 1024);
    K3_W(1); K3_OS(1);
    asm volatile("s_waitcnt vmcnt(17)\n\ts_barrier" ::: "memory");
    K3_W(2); K3_OS(2);
    asm volatile("s_waitcnt vmcnt(17)\n\ts_barrier" ::: "memory");
    asm volatile("s_waitcnt vmcnt(0)\n\ts_barrier" ::: "memory");
#undef K3_W
#undef K3_OS
    return;
  }
  const int m = lane & 15, g = lane >> 4;
  const int mt = 8 * rb + w, row = 16 * mt + m;
  STAMP_DECL; STAMP(0);
  u32x4 O[18];
  u32x2 xr[3];
  {
    const char* ob = ws + WS_OFR + (size_t)mt * 18432;
#pragma unroll
    for (int i = 0; i < 6; ++i) O[i] = gld16(ob + i * 1024, lane16);
  }
  const unsigned uo = mt * 18432 + (6 * nb + (g >> 1)) * 256 + m * 16 + (g & 1) * 8;
  f32x4 acc[4];
  LDSP const char* wl = lds + lane16;
  LDSP const char* ol = lds + K3_O + w * 12288 + lane16; PIN(ol);
  LDSP const char* wbl = lds + K3_WBAR + (m & 1) * 1152 + g * 16;
  const __amdgpu_buffer_rsrc_t wrs = mkrsrc(ws, (unsigned)WS_END);
  unsigned so = ((g & 1) ? (unsigned)WS_HC : (unsigned)WS_HU) + (uo & ~8u); PIN(so);
  STAMP(1);
  asm volatile("s_waitcnt vmcnt(0)\n\ts_barrier" : "+v"(O[0]), "+v"(O[1]), "+v"(O[2]), "+v"(O[3]), "+v"(O[4]), "+v"(O[5])::"memory");
#pragma unroll
  for (int t = 0; t < 3; ++t)
    asm volatile("global_load_dwordx2 %0, %1, %2 offset:%3" : "=v"(xr[t]) : "v"(uo), "s"(ws + WS_XU), "i"(t * 512));
  unsigned xm = gld4(ws + WS_XMEAN, row * 4);
  u32x4 wf[6];
#define K3_RD(f) (((f) & 3) == 3 ? ldsr16(wbl + ((f) >> 2) * 64) : ldsr16(wl + (3 * ((f) >> 2) + ((f) & 3)) * 1024))
#pragma unroll
  for (int f = 0; f < 5; ++f) wf[f] = K3_RD(f);
#pragma unroll
  for (int f = 0; f < 72; ++f) {
    if (f == 19 || f == 43) {
      asm volatile("s_barrier" ::: "memory");
#pragma unroll
      for (int i = 0; i < 6; ++i) O[(f + 5) / 4 + i] = ldsr16(ol + ((f + 5) / 4 + i - 6) * 1024);
    }
    if (f + 5 < 72) wf[(f + 5) % 6] = K3_RD(f + 5);
    if (f < 4) G_MFMA0(acc[f & 3], wf[f % 6], O[f >> 2]);
    else G_MFMA(acc[f & 3], wf[f % 6], O[f >> 2]);
    SBAR();
  }
  asm volatile("s_waitcnt vmcnt(0)" : "+v"(xr[0]), "+v"(xr[1]), "+v"(xr[2]), "+v"(xm));
  asm volatile("s_nop 7" : "+v"(acc[0]), "+v"(acc[1]), "+v"(acc[2]), "+v"(acc[3]));
#undef K3_RD
  STAMP(2);
  const float mean = __uint_as_float(xm) + acc[3][0] + acc[3][1];
  float ssq = 0.f;
#pragma unroll
  for (int t = 0; t < 3; ++t) {
    const unsigned h0 = cvtpk(bflo(xr[t][0]) + acc[t][0], bfhi(xr[t][0]) + acc[t][1]);
    const unsigned h1 = cvtpk(bflo(xr[t][1]) + acc[t][2], bfhi(xr[t][1]) + acc[t][3]);
    const unsigned c0 = cvtpk(bflo(h0) - mean, bfhi(h0) - mean);
    const unsigned c1 = cvtpk(bflo(h1) - mean, bfhi(h1) - mean);
    ssq = dot2(c0, c0, ssq); ssq = dot2(c1, c1, ssq);
    const auto r0 = __builtin_amdgcn_permlane16_swap(h0, c0, false, false);
    const auto r1 = __builtin_amdgcn_permlane16_swap(h1, c1, false, false);
    bst16<H_AUX>(u32x4{r0[0], r1[0], r0[1], r1[1]}, wrs, so + t * 512, 0);
  }
  ssq = xor32_sum(xor16_sum(ssq));
  __builtin_amdgcn_raw_buffer_store_b32(__float_as_uint(ssq), wrs, (unsigned)WS_SSQ + (nb * 2048 + row) * 4, 0, H_AUX);
  STAMP(3); STAMP_OUT(3, 4);
}

constexpr int K4_SLOT = 36864, K4_LDS = 163840;
#define K4_PFD 4
constexpr int K4_PF = K4_PFD;
typedef __attribute__((address_space(1))) unsigned gu32;
constexpr int K5_PERM[18] = {0, 1, 2, 6, 7, 8, 9, 10, 11, 15, 16, 17, 3, 4, 5, 12, 13, 14};
constexpr int K5_R[4] = {36864, 73728, 110592, 0};
constexpr int K5_OC[9] = {65536, 102400, 139264, 147456, 155648, 0, 8192, 16384, 24576};
struct K5P { const char* wb; unsigned q[4], qs[4], qo, qos, lds0; };
template <int Z> DI void k5_piece(const K5P& k, int i, int w, unsigned lane16) {
  constexpr int P0 = K5_PERM[2 * Z] * 9216, D = (K5_PERM[2 * Z + 1] - K5_PERM[2 * Z]) * 9216;
  if (i < 3 || (w >> 2) == (Z & 1))
    dma16(k.wb + (P0 + k.q[i] + k.qs[i] * D), lane16, k.lds0 + K5_R[Z & 3] + (i < 3 ? 8 * i + w : 24 + (w & 3)) * 1024);
}
template <int Z> DI void k5_own(const K5P& k, int w, unsigned lane16) {
  constexpr int P0 = K5_PERM[2 * Z] * 9216, D = (K5_PERM[2 * Z + 1] - K5_PERM[2 * Z]) * 9216;
  dma16(k.wb + (P0 + k.qo + k.qos * D), lane16, k.lds0 + K5_OC[Z] + w * 1024);
}
DI void wait_count(gu32* cnt, unsigned first, unsigned want) {
  if (rfl(first) >= want) return;
  for (unsigned spin = 0; spin < (1u << 16); ++spin) {
    if (rfl(__hip_atomic_load(cnt, __ATOMIC_RELAXED, __HIP_MEMORY_SCOPE_AGENT)) >= want) break;
    __builtin_amdgcn_s_sleep(1);
  }
}
#define POLL(dst, p) asm volatile("global_load_dword %0, %1, %2 sc1" : "=v"(dst) : "v"(0u), "s"(p))
struct K4C {
  const char *wb, *xb; unsigned lane16, lds0; int w, dir;
  unsigned pf0, pfo, pf1, poll; const char* pfb;
  K5P wd;
  gu32 *cntA, *cntB;
  const char *ab, *ab2; u32x4 A0[2][4];
  LDSP const char* wl[4];
  u32x4 X[2][18], wf[K4_PF + 1];
  f32x4 acc[4][4];
  float c1[2], c2[2];
  __amdgpu_buffer_rsrc_t ars; unsigned avo; int T0;
  float ea[2][2], et[2][2]; unsigned pk[2][4];
  f32x2 pa[2], pt[2], c1p[2], c2p[2];
};
DI void k4_piece(const K4C& c, int tile, int p) {
  dma16(c.wb + (tile * c.dir * K4_SLOT + p * 1024), c.lane16, c.lds0 + (tile & 3) * K4_SLOT + p * 1024);
}
template <int ZW> DI void k4_issue_w(K4C& c, int i) {
  if (ZW < 9) {
    if (i == 0) k4_piece(c, c.w >> 2, 4 * ZW + (c.w & 3));
    else if ((c.w >> 2) == (ZW & 1)) k4_piece(c, 2, 4 * ZW + (c.w & 3));
  } else if (ZW == 9) { k4_piece(c, 3, 8 * i + c.w); }
}
template <int Z, int I> DI void k4_issue1(K4C& c) {
  if (I == 0) k4_issue_w<Z + K4_WD>(c, 0);
  else if (I < 5) {
    c.X[(I - 1) >> 1][2 * Z + ((I - 1) & 1)] = gld16(c.xb + (((I - 1) >> 1) * 18432 + (2 * Z + ((I - 1) & 1)) * 1024), c.lane16);
  }
  else k4_issue_w<Z + K4_WD>(c, 1);
}
template <int TL, int I> DI void k4_issue2(K4C& c) {
  if (I < 4) k4_piece(c, TL, 8 * I + c.w);
  else if ((c.w >> 2) == (TL & 1)) k4_piece(c, TL, 32 + (c.w & 3));
}
template <int TL, int E, int U> DI void k4_epi(K4C& c) {
  constexpr int e = U >> 2, u = U & 3, mt = e >> 2, i = e & 3, q = e & 1;
  if constexpr (K4_PK && U < 32) {
    constexpr int pr = U >> 3, v = U & 7, m2 = pr >> 1, i0 = 2 * (pr & 1);
    if (v == 0) {
      const f32x2 G = {c.acc[TL & 3][m2][i0], c.acc[TL & 3][m2][i0 + 1]};
      asm volatile("v_pk_mul_f32 %0, %1, %2" : "=v"(c.pa[E]) : "v"(G), "v"(c.c1p[m2]));
    }
    if (v == 1) { const f32x2 Uu = {c.acc[TL & 3][2 + m2][i0], c.acc[TL & 3][2 + m2][i0 + 1]};
      asm volatile("v_pk_mul_f32 %0, %1, %2" : "=v"(c.pt[E]) : "v"(Uu), "v"(c.pa[E]));
      c.pa[E][0] = __builtin_amdgcn_exp2f(c.pa[E][0]); PIN(c.pa[E]); }
    if (v == 2) { c.pa[E][1] = __builtin_amdgcn_exp2f(c.pa[E][1]); PIN(c.pa[E]); }
    if (v == 3) asm volatile("v_pk_fma_f32 %0, %0, %1, %1" : "+v"(c.pa[E]) : "v"(c.c2p[m2]));
    if (v == 4) { c.pa[E][0] = __builtin_amdgcn_rcpf(c.pa[E][0]); PIN(c.pa[E]); }
    if (v == 5) { c.pa[E][1] = __builtin_amdgcn_rcpf(c.pa[E][1]); PIN(c.pa[E]); }
    if (v == 6) asm volatile("v_pk_mul_f32 %0, %0, %1" : "+v"(c.pt[E]) : "v"(c.pa[E]));
    if (v == 7) { c.pk[E][pr] = cvtpk(c.pt[E][0], c.pt[E][1]); PIN(c.pk[E][pr]); }
  } else if constexpr (U < 32) {
    if (u == 0) { const float a = c.acc[TL & 3][mt][i] * c.c1[mt]; c.ea[E][q] = a; c.et[E][q] = a * c.acc[TL & 3][2 + mt][i]; PIN(c.ea[E][q]); PIN(c.et[E][q]); }
    if (u == 1) { c.ea[E][q] = __builtin_amdgcn_exp2f(c.ea[E][q]); PIN(c.ea[E][q]); }
    if (u == 2) { c.ea[E][q] = __builtin_amdgcn_rcpf(__builtin_fmaf(c.ea[E][q], c.c2[mt], c.c2[mt])); PIN(c.ea[E][q]); }
    if (u == 3) { c.et[E][q] *= c.ea[E][q]; PIN(c.et[E][q]); if (q) { c.pk[E][e >> 1] = cvtpk(c.et[E][0], c.et[E][1]); PIN(c.pk[E][e >> 1]); } }
  } else if constexpr (U == 32) {
    auto r0 = __builtin_amdgcn_permlane16_swap(c.pk[E][0], c.pk[E][2], false, false);
    auto r1 = __builtin_amdgcn_permlane16_swap(c.pk[E][1], c.pk[E][3], false, false);
    c.pk[E][0] = r0[0]; c.pk[E][1] = r1[0]; c.pk[E][2] = r0[1]; c.pk[E][3] = r1[1];
  } else if constexpr (U == 33) {
    const int T = c.T0 + TL * c.dir;
    bst16<ACT_AUX>(u32x4{c.pk[E][0], c.pk[E][1], c.pk[E][2], c.pk[E][3]}, c.ars, c.avo, (T >> 1) * 1024 + (T & 1) * 512);
  }
}
constexpr int k4_tile(int F) { return F < 108 ? (F % 6) >> 1 : 3 + (F - 108) / 36; }
constexpr int k4_step(int F) { return F < 108 ? F / 6 : ((F - 108) % 36) >> 1; }
constexpr int k4_addr(int F) { return k4_step(F) * 2048 + (F & 1) * 1024; }
template <int F> DI void k4_frag(K4C& c) {
  constexpr int TL = k4_tile(F), S = k4_step(F), M = F & 1, N = F + K4_PF;
  const u32x4 a = c.wf[F % (K4_PF + 1)];
  if constexpr (F < 108) { if constexpr ((F + 4) % 3 == 0) __builtin_amdgcn_s_setprio(3 - ((F + 4) % 12) / 3); }
  else if constexpr ((F - 108 + 4) % 9 == 0) __builtin_amdgcn_s_setprio(3 - ((F - 108 + 4) % 36) / 9);
  if (S == 0) {
    G_MFMA0(c.acc[TL & 3][2 * M], a, c.X[0][S]);
    G_MFMA0(c.acc[TL & 3][2 * M + 1], a, c.X[1][S]);
  } else {
    constexpr int o = GRAY ? M : 0;
    G_MFMA(c.acc[TL & 3][2 * M + o], a, c.X[o][S]);
    if constexpr (K4_MID && F >= 108 && (F - 108) % 36 >= 2) {
      SBAR();
      k4_epi<(TL == 3 ? 0 : (TL == 4 ? 2 : TL - 1)), 0, (F - 108) % 36 - 2>(c);
      SBAR();
    }
    G_MFMA(c.acc[TL & 3][2 * M + 1 - o], a, c.X[1 - o][S]);
  }
  if constexpr (N < 108 && N % 12 == 0) {
    constexpr int Z = N / 12;
    if (Z < 8) asm volatile("s_waitcnt vmcnt(5)\n\ts_barrier" : "+v"(c.X[0][2 * Z]), "+v"(c.X[0][2 * Z + 1]), "+v"(c.X[1][2 * Z]), "+v"(c.X[1][2 * Z + 1])::"memory");
    else asm volatile("s_waitcnt vmcnt(2)\n\ts_barrier" : "+v"(c.X[0][2 * Z]), "+v"(c.X[0][2 * Z + 1]), "+v"(c.X[1][2 * Z]), "+v"(c.X[1][2 * Z + 1])::"memory");
  } else if constexpr (N >= 108 && N < 324 && (N - 108) % 36 == 0) {
    constexpr int NT = k4_tile(N);
    if (NT == 3) asm volatile("s_waitcnt vmcnt(0) lgkmcnt(0)\n\ts_barrier" ::: "memory");
    else if (NT == 8) {
      asm volatile("s_waitcnt vmcnt(0) lgkmcnt(0)\n\ts_barrier" ::: "memory");
      if (c.w == 0 && c.lane16 == 0) __hip_atomic_fetch_add(c.cntA, 1u, __ATOMIC_RELAXED, __HIP_MEMORY_SCOPE_AGENT);
    }
    else asm volatile("s_waitcnt vmcnt(4) lgkmcnt(0)\n\ts_barrier" ::: "memory");
  }
  if constexpr (N < 324) c.wf[N % (K4_PF + 1)] = ldsr16(c.wl[k4_tile(N) & 3] + k4_addr(N));
  if constexpr (F < 108) {
    constexpr int Z = F / 12, I = F % 12;
    if constexpr (Z == 0 && I < 4 && !(I & 1)) k4_issue1<2, I / 2>(c);
    if constexpr (Z < 7 && I < 8 && (I & 1)) k4_issue1<Z + 2, 2 + I / 2>(c);
    if constexpr (Z < 6 && I > 8 && (I & 1)) k4_issue1<Z + 3, (I - 9) / 2>(c);
    if constexpr (K4_WD == 0) {
      if constexpr (Z == 6 && I == 10) k4_issue2<3, 0>(c);
      if constexpr (Z == 7 && I < 8 && (I & 1)) k4_issue2<3, 1 + I / 2>(c);
    } else {
      if constexpr (Z == 6 && I == 10) k4_issue2<3, 2>(c);
      if constexpr (Z == 7 && (I == 1 || I == 3)) k4_issue2<3, 3 + I / 2>(c);
    }
  } else {
    constexpr int P = (F - 108) % 36;
    if constexpr (TL == 3) { if constexpr (P % 3 == 0 && P < 15) k4_issue2<4, P / 3>(c); if constexpr (P % 3 == 0 && P >= 15 && P < 30) k4_issue2<5, (P - 15) / 3>(c); }
    else if constexpr (TL + 2 < 9 && P % 4 == 0 && P < 20) k4_issue2<TL + 2, P / 4>(c);
    if constexpr (K4_PFWD && TL >= 4 && TL <= 7 && (P == 33 || (P == 34 && TL < 6))) {
      constexpr int tl = TL < 6 ? 2 * (TL - 4) + P - 33 : TL - 2;
      const int T = c.T0 + tl * c.dir;
      asm volatile("global_load_dword %0, %1, %2" : "+v"(c.pf0) : "v"(c.pfo), "s"(c.pfb + ((T >> 1) * 1024 + (T & 1) * 512)));
    }
    if constexpr (TL == 7 && P == 0) CODE_PF2(c.pf1, k_ffn, k_end, 0, 2 + (c.w & 1), ((blockIdx.x >> 3) == 0 && c.w < 2) ? 128u : 0u);
    if constexpr (TL == 7 && P % 3 == 2 && P <= 11) k5_piece<0>(c.wd, P / 3, c.w, c.lane16);
    if constexpr (TL == 8 && P < 4) k5_piece<1>(c.wd, P, c.w, c.lane16);
    if constexpr (TL == 8 && P >= 4 && P < 8) k5_piece<2>(c.wd, P - 4, c.w, c.lane16);
    if constexpr (TL == 8 && P == K4_POLL) POLL(c.poll, c.cntA);
    if constexpr (TL == 8 && P == K4_POLL + 12) { asm volatile("s_waitcnt vmcnt(0)" : "+v"(c.poll), "+v"(c.pf1), "+v"(c.pf0)); wait_count(c.cntA, c.poll, 8u); }
    if constexpr (TL == 8 && P > K4_POLL + 12 && P <= K4_POLL + 20) {
      constexpr int i = P - K4_POLL - 13, z = i >> 2, j = (i & 1) * 2 + ((i >> 1) & 1);
      asm volatile("global_load_dwordx4 %0, %1, %2 sc1" : "=v"(c.A0[z][j]) : "v"(c.lane16), "s"(((j >> 1) ? c.ab2 : c.ab) + K5_PERM[2 * z + (j & 1)] * 1024));
    }
    if constexpr (P >= 2) {
      if constexpr (TL == 3) { if (!K4_MID) k4_epi<0, 0, P - 2>(c); k4_epi<1, 1, P - 2>(c); }
      else if constexpr (TL == 4) { if (!K4_MID) k4_epi<2, 0, P - 2>(c); k4_epi<3, 1, P - 2>(c); }
      else if (!K4_MID) k4_epi<TL - 1, 0, P - 2>(c);
    }
  }
  SBAR();
}
template <int B, int... Fs> DI void k4_run(K4C& c, std::integer_sequence<int, Fs...>) { (k4_frag<B + Fs>(c), ...); }
DI void k4_last(K4C& c) {
  float a[8], t[8];
#pragma unroll
  for (int e = 0; e < 8; ++e) { a[e] = c.acc[0][e >> 2][e & 3] * c.c1[e >> 2]; t[e] = a[e] * c.acc[0][2 + (e >> 2)][e & 3]; }
#pragma unroll
  for (int e = 0; e < 8; ++e) a[e] = __builtin_amdgcn_exp2f(a[e]);
#pragma unroll
  for (int e = 0; e < 8; ++e) a[e] = __builtin_amdgcn_rcpf(__builtin_fmaf(a[e], c.c2[e >> 2], c.c2[e >> 2]));
#pragma unroll
  for (int e = 0; e < 4; ++e) c.pk[0][e] = cvtpk(t[2 * e] * a[2 * e], t[2 * e + 1] * a[2 * e + 1]);
  k4_epi<8, 0, 32>(c); k4_epi<8, 0, 33>(c);
}
DI f32x4 addbf(f32x4 a, unsigned lo, unsigned hi) { return a + f32x4{bflo(lo), bfhi(lo), bflo(hi), bfhi(hi)}; }
struct K5C {
  const char *ab, *ab2;
  LDSP const char *ra[4], *oa[3], *xr;
  LDSP char* xw;
  u32x4 A[9][4], wf[5];
  f32x4 acc[2][7], own[2][2], v[4];
  const char* hb; unsigned ho; int kq; u32x2 hr[3];
  __amdgpu_buffer_rsrc_t wrs; unsigned tw, p8s;
  gu32* cnt;
};
template <int Z, int J> DI void k5_issue(K5C& c, const K4C& k) {
  if constexpr (Z < 7 && J < 4) asm volatile("global_load_dwordx4 %0, %1, %2 sc1"
 : "=v"(c.A[Z + 2][J]) : "v"(k.lane16), "s"(((J >> 1) ? c.ab2 : c.ab) + K5_PERM[2 * (Z + 2) + (J & 1)] * 1024));
  if constexpr (Z < 6 && J >= 4) k5_piece<Z + 3>(k.wd, J - 4, k.w, k.lane16);
  if constexpr (Z == 6 && J >= 4) k5_own<J - 4>(k.wd, k.w, k.lane16);
  if constexpr (Z == 7 && J == 0) k5_own<4>(k.wd, k.w, k.lane16);
  if constexpr (Z == 7 && J == 1) asm volatile("global_load_dwordx2 %0, %1, %2" : "=v"(c.hr[0]) : "v"(c.ho), "s"(c.hb + c.kq * 1024));
  if constexpr (Z == 7 && J == 2) asm volatile("global_load_dwordx2 %0, %1, %2 offset:512" : "=v"(c.hr[1]) : "v"(c.ho), "s"(c.hb + c.kq * 1024));
  if constexpr (Z == 7 && J == 3) asm volatile("global_load_dwordx2 %0, %1, %2" : "=v"(c.hr[2]) : "v"(c.ho), "s"(c.hb + 4096));
  if constexpr (Z == 8 && J < 4) k5_own<5 + J>(k.wd, k.w, k.lane16);
}
template <int N> DI u32x4 k5_read(const K5C& c) {
  if constexpr (N < 126) return ldsr16(c.ra[(N / 14) & 3] + (N % 14) * 1024);
  else { constexpr int o = K5_OC[(N - 126) / 4] + ((N - 126) & 3) * 1024; return ldsr16(c.oa[o >> 16] + (o & 0xffff)); }
}
template <int F> DI void k5_frag(K5C& c, K4C& k) {
  constexpr int N = F + 4;
  const u32x4 a = c.wf[F % 5];
  if constexpr (F < 126) {
    constexpr int Z = F / 14, f = F % 14, st = f / 7, j = f % 7;
    if (F < 7) { G_MFMA0(c.acc[0][j], a, c.A[Z][st]); G_MFMA0(c.acc[1][j], a, c.A[Z][2 + st]); }
    else { G_MFMA(c.acc[1][j], a, c.A[Z][2 + st]); G_MFMA(c.acc[0][j], a, c.A[Z][st]); }
    if constexpr (f == 10 && Z == 8) asm volatile("s_waitcnt vmcnt(4)\n\ts_barrier" : "+v"(c.hr[0]), "+v"(c.hr[1]), "+v"(c.hr[2])::"memory");
    if constexpr (f == 10 && Z < 8) {
      u32x4(&A)[4] = c.A[Z + 1];
      if (Z == 0) asm volatile("s_waitcnt vmcnt(9) lgkmcnt(0)\n\ts_barrier" : "+v"(A[0]), "+v"(A[1]), "+v"(A[2]), "+v"(A[3])::"memory");
      else if (Z == 3) asm volatile("s_waitcnt vmcnt(4) lgkmcnt(0)\n\ts_barrier" : "+v"(A[0]), "+v"(A[1]), "+v"(A[2]), "+v"(A[3]), "+v"(k.poll)::"memory");
      else if (Z < 7) asm volatile("s_waitcnt vmcnt(7) lgkmcnt(0)\n\ts_barrier" : "+v"(A[0]), "+v"(A[1]), "+v"(A[2]), "+v"(A[3])::"memory");
      else asm volatile("s_waitcnt vmcnt(8) lgkmcnt(0)\n\ts_barrier" : "+v"(A[0]), "+v"(A[1]), "+v"(A[2]), "+v"(A[3])::"memory");
      if (Z == 1 && k.w == 0 && k.lane16 == 0) __hip_atomic_fetch_add(k.cntB, 1u, __ATOMIC_RELAXED, __HIP_MEMORY_SCOPE_AGENT);
      if (Z == 3) wait_count(k.cntB, k.poll, 8u);
    }
    c.wf[N % 5] = k5_read<N>(c);
    if constexpr (Z == 3 && f == 1) POLL(k.poll, k.cntB);
    constexpr int slot = f == 0 ? 2 : (f == 2 ? 3 : (f == 4 ? 4 : (f == 5 ? 5 : (f == 7 ? 6 : (f == 9 ? 7 : -1)))));
    if constexpr (F == 0) { k5_issue<0, 0>(c, k); k5_issue<0, 1>(c, k); }
    if constexpr (slot >= 0) k5_issue<Z, slot>(c, k);
    if constexpr (Z < 8 && f > 10 && (f & 1)) k5_issue<Z + 1, (f - 11) / 2>(c, k);
  } else {
    constexpr int B = F - 126, z = B / 4, st = (B >> 1) & 1, t = B & 1;
    if (B < 2) { G_MFMA0(c.own[0][t], a, c.A[z][st]); G_MFMA0(c.own[1][t], a, c.A[z][2 + st]); }
    else { G_MFMA(c.own[1][t], a, c.A[z][2 + st]); G_MFMA(c.own[0][t], a, c.A[z][st]); }
    if constexpr (F == 131) asm volatile("s_waitcnt vmcnt(0) lgkmcnt(0)\n\ts_barrier" ::: "memory");
    if constexpr (F == K5_SIG) {
      asm volatile("s_waitcnt vmcnt(0)\n\ts_barrier" ::: "memory");
      if (k.w == 0 && k.lane16 == 0) __hip_atomic_fetch_add(c.cnt, 1u, __ATOMIC_RELAXED, __HIP_MEMORY_SCOPE_AGENT);
    }
    if constexpr (N < 162) c.wf[N % 5] = k5_read<N>(c);
  }
  if constexpr (F >= 123 && F < 130) { PIN(c.acc[1][F - 123]); *(LDSP f32x4*)(c.xw + (F - 123) * 1024) = c.acc[1][F - 123]; }
  if constexpr (F == 131) { _Pragma("unroll") for (int i = 0; i < 4; ++i) c.v[i] = *(LDSP const f32x4*)(c.xr + i * 1024); }
  if constexpr (F == 133 || F == 135 || F == 138) {
    constexpr int j = F == 133 ? 0 : (F == 135 ? 1 : 2), o = 2 * (j & 1);
    const f32x4 x = c.acc[0][2 * j] + c.v[o], y = c.acc[0][2 * j + 1] + c.v[o + 1];
    bst16<16>(u32x4{cvtpk(x[0], x[1]), cvtpk(x[2], x[3]), cvtpk(y[0], y[1]), cvtpk(y[2], y[3])}, c.wrs, k.lane16,
              (unsigned)WS_PART + ((((c.kq + j + 1) & 3) * 512 + c.tw) * 3 + j) * 1024);
    if constexpr (F == 135) { _Pragma("unroll") for (int i = 0; i < 3; ++i) c.v[i] = *(LDSP const f32x4*)(c.xr + (4 + i) * 1024); }
  }
  if constexpr (F == 140) {
    c.acc[0][6] += c.v[2];
    __builtin_amdgcn_raw_buffer_store_b64(u32x2{cvtpk(c.acc[0][6][0], c.acc[0][6][1]), cvtpk(c.acc[0][6][2], c.acc[0][6][3])}, c.wrs, c.p8s, c.kq * 512, 16);
    PIN(c.acc[0][6]);
  }
  SBAR();
}
template <int B, int... Fs> DI void k5_run(K5C& c, K4C& k, std::integer_sequence<int, Fs...>) { (k5_frag<B + Fs>(c, k), ...); }

extern "C" __global__ __launch_bounds__(512, 2) void k_ffn(char* out, char* ws) {
  extern __shared__ __attribute__((aligned(16))) char smem[];
  asm volatile("" : "+s"(out), "+s"(ws));
  LDSP char* lds = (LDSP char*)smem;
  const int tid = threadIdx.x, lane = tid & 63, w = rfl(tid >> 6);
  const int xcd = blockIdx.x & 7, bi = blockIdx.x >> 3;
  const int m = lane & 15, g = lane >> 4, kq = xcd >> 1;
  STAMP_DECL; STAMP(0);
  K4C c;
  {
    const int rb = 4 * (xcd & 1) + (bi & 3), fs = 8 * kq + (bi >> 2), mt = 16 * rb + 2 * w;
    c.lds0 = (unsigned)(uintptr_t)lds; c.lane16 = lane * 16; c.w = w;
    c.T0 = 9 * fs + 8 * (fs & 1); c.dir = 1 - 2 * (fs & 1);
    c.wb = ws + WS_W1 + (size_t)c.T0 * K4_SLOT;
    c.xb = ws + WS_HC + (size_t)mt * 18432;
    if (K4_WD) { k4_issue_w<0>(c, 0); k4_issue_w<0>(c, 1); }
    k4_issue1<0, 0>(c); k4_issue1<0, 1>(c); k4_issue1<0, 2>(c); k4_issue1<0, 3>(c); k4_issue1<0, 4>(c); k4_issue1<0, 5>(c);
    unsigned sq[6];
#pragma unroll
    for (int i = 0; i < 6; ++i) sq[i] = gld4(ws + WS_SSQ, (3 * g + (i >> 1)) * 8192 + (16 * mt + 16 * (i & 1) + m) * 4);
    k4_issue1<1, 0>(c); k4_issue1<1, 1>(c); k4_issue1<1, 2>(c); k4_issue1<1, 3>(c); k4_issue1<1, 4>(c); k4_issue1<1, 5>(c);
    SBAR();
#pragma unroll
    for (int i = 0; i < 4; ++i) { c.wl[i] = lds + c.lane16 + i * K4_SLOT; PIN(c.wl[i]); }
    c.ars = mkrsrc(ws + WS_ACT, 128u * 144 * 1024);
    c.avo = (mt + (g & 1)) * (144 * 1024) + (g >> 1) * 256 + m * 16;
    c.wd.wb = ws + WS_WD + ((size_t)(bi >> 3) * 144 + 36 * kq) * 9 * 1024;
#pragma unroll
    for (int i = 0; i < 4; ++i) {
      const int p = i < 3 ? 8 * i + w : 24 + (w & 3), st = (p * 37) >> 8, t = p - 7 * st + 2;
      c.wd.q[i] = ((st >> 1) * 162 + (t < 8 ? (t + 2 * kq) & 7 : 8)) * 1024;
      c.wd.qs[i] = st & 1;
      asm volatile("" : "+s"(c.wd.q[i]), "+s"(c.wd.qs[i]));
    }
    c.wd.qo = ((w >> 2) * 162 + 2 * kq + (w & 1)) * 1024; c.wd.qos = (w >> 1) & 1; c.wd.lds0 = c.lds0;
    if (K4_PFWD) {
      c.pfb = ws + WS_ACT;
      c.pfo = (mt + ((lane >> 5) & 1)) * (144 * 1024) + ((lane >> 4) & 1) * 256 + ((lane >> 3) & 1) * 128;
      PIN(c.pfo);
    }
    c.pf0 = 0;
    c.cntA = (gu32*)(ws + WS_CNT + (64 + rb * 4 + kq) * 128);
    STAMP(1);
    asm volatile("s_waitcnt vmcnt(5)\n\ts_barrier" : "+v"(sq[0]), "+v"(sq[1]), "+v"(sq[2]), "+v"(sq[3]), "+v"(sq[4]), "+v"(sq[5]),
                 "+v"(c.X[0][0]), "+v"(c.X[0][1]), "+v"(c.X[1][0]), "+v"(c.X[1][1])::"memory");
#pragma unroll
    for (int i = 0; i < K4_PF; ++i) c.wf[i] = ldsr16(c.wl[k4_tile(i)] + k4_addr(i));
#pragma unroll
    for (int t = 0; t < 2; ++t) {
      float ss = __uint_as_float(sq[t]) + __uint_as_float(sq[2 + t]) + __uint_as_float(sq[4 + t]);
      ss = xor32_sum(xor16_sum(ss));
      const float v = ss * (1.0f / DM) + LN_EPS, rs = __builtin_amdgcn_rsqf(v);
      c.c1[t] = -LOG2E * rs; c.c2[t] = -LOG2E * (v * rs);
      PIN(c.c1[t]); PIN(c.c2[t]);
      if (K4_PK) { c.c1p[t] = f32x2{c.c1[t], c.c1[t]}; c.c2p[t] = f32x2{c.c2[t], c.c2[t]}; PIN(c.c1p[t]); PIN(c.c2p[t]); }
    }
    SBAR();
    k4_run<0>(c, std::make_integer_sequence<int, 288>{});
    c.cntB = c.cntA + 32 * 32;
  }
  const int rb = 8 * (xcd & 1) + (bi & 7), nb = bi >> 3, mg = w & 3, kh = w >> 2, mu = 8 * rb + 2 * mg + kh;
  gu32* prodB = c.cntB;
  c.cntA = (gu32*)(ws + WS_CNT + (64 + (rb >> 1) * 4 + kq) * 128);
  c.ab = ws + WS_ACT + ((size_t)mu * 144 + 36 * kq + 18 * kh) * 1024;
  c.ab2 = ws + WS_ACT + ((size_t)(mu ^ 1) * 144 + 36 * kq + 18 * kh) * 1024;
  k4_run<288>(c, std::make_integer_sequence<int, 36>{});
  STAMP(2);
  K5C d;
  d.ab = c.ab; d.ab2 = c.ab2;
  asm volatile("s_nop 7" : "+v"(c.acc[0][0]), "+v"(c.acc[0][1]), "+v"(c.acc[0][2]), "+v"(c.acc[0][3]));
  k4_last(c);
  SBAR();
  gu32* consB = c.cntA + 32 * 32;
  d.hb = ws + WS_HU + (size_t)mu * 18432 + nb * 4608; d.ho = (g >> 1) * 256 + m * 16 + (g & 1) * 8; d.kq = kq;
#pragma unroll
  for (int i = 0; i < 4; ++i) { d.ra[i] = lds + K5_R[i] + kh * 14336 + c.lane16; PIN(d.ra[i]); }
#pragma unroll
  for (int i = 0; i < 3; ++i) { d.oa[i] = lds + i * 65536 + kh * 4096 + c.lane16; PIN(d.oa[i]); }
  d.xw = lds + (kh ? K5_R[2] : K5_R[1]) + mg * 7168 + c.lane16; PIN(d.xw);
  d.xr = lds + (kh ? K5_R[1] : K5_R[2]) + mg * 7168 + c.lane16; PIN(d.xr);
  const __amdgpu_buffer_rsrc_t wrs = mkrsrc(ws, (unsigned)WS_END), ors = mkrsrc(out, SEQ * DM * 2);
  const int tile = rb * 4 + nb;
  const bool own8 = (mu & 3) == kq;
  const unsigned tw = tile * 8 + w;
  const unsigned p8 = (unsigned)WS_PART8 + tw * 2048 + lane * 8;
  d.cnt = (gu32*)(ws + WS_CNT + tile * 128);
  d.wrs = wrs; d.tw = tw; d.p8s = own8 ? 0x80000000u : p8; PIN(d.p8s);
#pragma unroll
  for (int i = 0; i < 4; ++i) { d.A[0][i] = c.A0[0][i]; d.A[1][i] = c.A0[1][i]; }
  asm volatile("s_waitcnt vmcnt(6) lgkmcnt(0)\n\ts_barrier" : "+v"(d.A[0][0]), "+v"(d.A[0][1]), "+v"(d.A[0][2]), "+v"(d.A[0][3])::"memory");
  STAMP(3);
  d.wf[0] = k5_read<0>(d); d.wf[1] = k5_read<1>(d); d.wf[2] = k5_read<2>(d); d.wf[3] = k5_read<3>(d);
  SBAR();
  c.cntB = prodB;
  k5_run<0>(d, c, std::make_integer_sequence<int, 28>{});
  c.cntB = consB;
  k5_run<28>(d, c, std::make_integer_sequence<int, 134>{});
  asm volatile("s_nop 7" : "+v"(d.own[0][0]), "+v"(d.own[0][1]), "+v"(d.own[1][0]), "+v"(d.own[1][1]));
  STAMP(4);
  LDSP char* xo = lds + K5_R[0] + c.lane16;
  *(LDSP f32x4*)(xo + w * 2048) = d.own[1][0];
  *(LDSP f32x4*)(xo + w * 2048 + 1024) = d.own[1][1];
  unsigned oo = (16 * mu + m) * 1152 + nb * 288;
  unsigned oo8 = own8 ? oo + 256 + g * 8 : 0x80000000u;
  oo += kq * 64 + (g & 1) * 32 + (g >> 1) * 16;
  PIN(oo); PIN(oo8);
  STAMP(5);
  if (w == 0) wait_count(d.cnt, 0u, 4u);
  asm volatile("s_waitcnt lgkmcnt(0)\n\ts_barrier" ::: "memory");
  STAMP(6);
  u32x4 r[3]; u32x2 r8[3];
#pragma unroll
  for (int j = 0; j < 3; ++j) {
    r[j] = __builtin_amdgcn_raw_buffer_load_b128(wrs, c.lane16, (unsigned)WS_PART + ((kq * 512 + tw) * 3 + j) * 1024, 16);
    r8[j] = __builtin_amdgcn_raw_buffer_load_b64(wrs, own8 ? p8 : 0x80000000u, ((kq + 1 + j) & 3) * 512, 16);
  }
  SBAR();
  f32x4 e0 = addbf(d.own[0][0] + *(LDSP const f32x4*)(xo + (w ^ 4) * 2048), d.hr[0][0], d.hr[0][1]);
  f32x4 e1 = addbf(d.own[0][1] + *(LDSP const f32x4*)(xo + (w ^ 4) * 2048 + 1024), d.hr[1][0], d.hr[1][1]);
  f32x4 e8 = addbf(d.acc[0][6], d.hr[2][0], d.hr[2][1]);
#pragma unroll
  for (int j = 0; j < 3; ++j) { e0 = addbf(e0, r[j][0], r[j][1]); e1 = addbf(e1, r[j][2], r[j][3]); e8 = addbf(e8, r8[j][0], r8[j][1]); }
  {
    const auto s0 = __builtin_amdgcn_permlane16_swap(cvtpk(e0[0], e0[1]), cvtpk(e1[0], e1[1]), false, false);
    const auto s1 = __builtin_amdgcn_permlane16_swap(cvtpk(e0[2], e0[3]), cvtpk(e1[2], e1[3]), false, false);
    bst16<OUT_AUX>(u32x4{s0[0], s1[0], s0[1], s1[1]}, ors, oo, 0);
    __builtin_amdgcn_raw_buffer_store_b64(u32x2{cvtpk(e8[0], e8[1]), cvtpk(e8[2], e8[3])}, ors, oo8, 0, OUT_AUX);
  }
  STAMP(7); STAMP_OUT(4, 8);
}
extern "C" __global__ void k_tail(char* ws) {
  const unsigned long long t4 = __builtin_amdgcn_s_memrealtime();
  const volatile unsigned long long* t = (const volatile unsigned long long*)(ws + (200u << 20));
  unsigned long long d = SEL == 0 ? 8 * (t[2] - t[1]) : (SEL == 1 ? 8 * (t[3] - t[2]) : (SEL == 2 ? 8 * (t[4] - t[3]) : (SEL == 3 ? 0 : (SEL == 4 ? (t4 - t[1]) : 0))));
  if (d > 40000) d = 40000;
  while (__builtin_amdgcn_s_memrealtime() < t4 + d) __builtin_amdgcn_s_sleep(2);
}
extern "C" __global__ void k_end() {}

extern "C" void kernel_launch(void* const* d_in, const int* in_sizes, int n_in, void* d_out, int out_size, void* d_ws, size_t ws_size,
                              hipStream_t stream) {
  const u16* x = (const u16*)d_in[0];
  const u16* wqkv = (const u16*)d_in[1];
  const u16* wo = (const u16*)d_in[2];
  const u16* wg = (const u16*)d_in[4];
  const u16* wu = (const u16*)d_in[5];
  const u16* wd = (const u16*)d_in[6];
  char* ws = (char*)d_ws;
  (void)hipFuncSetAttribute((const void*)k_qkv, hipFuncAttributeMaxDynamicSharedMemorySize, K1_LDS);
  (void)hipFuncSetAttribute((const void*)k_attn, hipFuncAttributeMaxDynamicSharedMemorySize, K2_LDS);
  (void)hipFuncSetAttribute((const void*)k_oproj, hipFuncAttributeMaxDynamicSharedMemorySize, K3_LDS);
  (void)hipFuncSetAttribute((const void*)k_ffn, hipFuncAttributeMaxDynamicSharedMemorySize, K4_LDS);
  k_qkv<<<256, 768, K1_LDS, stream>>>(x, wqkv, ws);
  k_attn<<<256, 768, K2_LDS, stream>>>(x, wo, wg, wu, wd, ws);
  k_oproj<<<192, 768, K3_LDS, stream>>>(ws);
  k_ffn<<<256, 512, K4_LDS, stream>>>((char*)d_out, ws);
  k_tail<<<1, 64, 0, stream>>>(ws);
}
```

```cpp
#include <hip/hip_runtime.h>
#include <hip/hip_bf16.h>
#include <stdint.h>
#include <utility>
#include <type_traits>

typedef __bf16 bf16x8 __attribute__((ext_vector_type(8)));
typedef __bf16 bf16x2 __attribute__((ext_vector_type(2)));
typedef short s16x4 __attribute__((ext_vector_type(4)));
typedef float f32x16 __attribute__((ext_vector_type(16)));
typedef float f32x4 __attribute__((ext_vector_type(4)));
typedef float f32x2 __attribute__((ext_vector_type(2)));
typedef unsigned u32x4 __attribute__((ext_vector_type(4)));
typedef unsigned u32x2 __attribute__((ext_vector_type(2)));
typedef unsigned short u16;
#define LDSP __attribute__((address_space(3)))
#define DI __device__ __forceinline__
#define SBAR() __builtin_amdgcn_sched_barrier(0)

constexpr int SEQ = 2048, DM = 576, NH = 32, DH = 18, FF = 4608;
constexpr float LOG2E = 1.4426950408889634f;
constexpr float QSCALE = 0.23570226039551584f * LOG2E;
constexpr float LN_EPS = 1e-5f;

constexpr size_t CHUNK_B = 37888;
constexpr size_t KV_HEAD_B = 4 * CHUNK_B;
constexpr size_t WS_QM = 0;
constexpr size_t WS_QT = WS_QM + (size_t)NH * 65536;
constexpr size_t WS_KV = WS_QT + (size_t)NH * 8192;
constexpr size_t WS_KMAX = WS_KV + (size_t)NH * KV_HEAD_B;
constexpr size_t WS_XMEAN = WS_KMAX + 65536;
constexpr size_t WS_OFR = WS_XMEAN + 8192;
constexpr size_t WS_WO = WS_OFR + (size_t)128 * 18 * 1024;
constexpr size_t WS_WBAR = WS_WO + (size_t)12 * 54 * 1024;
constexpr size_t WS_HC = WS_WBAR + 3072;
constexpr size_t WS_SSQ = WS_HC + (size_t)64 * 36 * 1024;
constexpr size_t WS_W1 = WS_SSQ + (size_t)16 * 8192;
constexpr size_t WS_WD = WS_W1 + (size_t)288 * 36 * 1024;
constexpr size_t WS_ACT = WS_WD + (size_t)4 * 144 * 9 * 1024;
constexpr size_t UNIT_B = (size_t)128 * 18432;
constexpr size_t WS_PART = WS_ACT + (size_t)128 * 144 * 1024;
constexpr size_t WS_PART8 = WS_PART + (size_t)4 * 64 * 8 * 3 * 1024;
constexpr size_t WS_CNT = WS_PART8 + (size_t)64 * 8 * 4 * 512;
constexpr size_t WS_HU = WS_CNT + 40960;
constexpr size_t WS_END = WS_HU + UNIT_B;

DI unsigned cvtpk(float lo, float hi) {
  f32x2 v = {lo, hi};
  bf16x2 b = __builtin_convertvector(v, bf16x2);
  return __builtin_bit_cast(unsigned, b);
}
DI float bflo(unsigned u) { return __uint_as_float(u << 16); }
DI float bfhi(unsigned u) { return __uint_as_float(u & 0xffff0000u); }
DI float dot2(unsigned a, unsigned b, float c) {
  return __builtin_amdgcn_fdot2_f32_bf16(__builtin_bit_cast(bf16x2, a), __builtin_bit_cast(bf16x2, b), c, false);
}
DI f32x16 mfma32(u32x4 a, u32x4 b, f32x16 c) {
  return __builtin_amdgcn_mfma_f32_32x32x16_bf16(__builtin_bit_cast(bf16x8, a), __builtin_bit_cast(bf16x8, b), c, 0, 0, 0);
}
DI f32x4 mfma16(u32x4 a, u32x4 b, f32x4 c) {
  return __builtin_amdgcn_mfma_f32_16x16x32_bf16(__builtin_bit_cast(bf16x8, a), __builtin_bit_cast(bf16x8, b), c, 0, 0, 0);
}
DI unsigned rfl(unsigned x) { return __builtin_amdgcn_readfirstlane(x); }
DI const char* uniptr(const char* p) {
  uint64_t v = (uint64_t)p;
  unsigned lo = rfl((unsigned)v), hi = rfl((unsigned)(v >> 32));
  return (const char*)(((uint64_t)hi << 32) | lo);
}
DI void dma16(const char* sbase, unsigned voff, unsigned lds) {
  asm volatile("s_mov_b32 m0, %2\n\ts_nop 0\n\tglobal_load_lds_dwordx4 %0, %1" ::"v"(voff), "s"(sbase), "s"(lds));
}
DI u32x4 gld16(const char* sbase, unsigned voff) {
  u32x4 r;
  asm volatile("global_load_dwordx4 %0, %1, %2" : "=v"(r) : "v"(voff), "s"(sbase));
  return r;
}
DI unsigned gld4(const char* sbase, unsigned voff) {
  unsigned r;
  asm volatile("global_load_dword %0, %1, %2" : "=v"(r) : "v"(voff), "s"(sbase));
  return r;
}
#define WAITV(n) asm volatile("s_waitcnt vmcnt(" #n ")" ::: "memory")
#define BARRIER() asm volatile("s_barrier" ::: "memory")
#define PIN(x) asm volatile("" : "+v"(x))
DI u32x4 ldsr16(LDSP const char* p) { return *(LDSP const u32x4*)p; }
DI float xor32_sum(float v) {
  auto r = __builtin_amdgcn_permlane32_swap(__float_as_uint(v), __float_as_uint(v), false, false);
  return __uint_as_float(r[0]) + __uint_as_float(r[1]);
}
DI float xor32_max(float v) {
  auto r = __builtin_amdgcn_permlane32_swap(__float_as_uint(v), __float_as_uint(v), false, false);
  return fmaxf(__uint_as_float(r[0]), __uint_as_float(r[1]));
}
DI float xor16_sum(float v) {
  auto r = __builtin_amdgcn_permlane16_swap(__float_as_uint(v), __float_as_uint(v), false, false);
  return __uint_as_float(r[0]) + __uint_as_float(r[1]);
}

#define STAMP_DECL
#define STAMP(i)
#define STAMP_OUT(kid, n)

DI int wo_row(int step, int g, int j) {
  if (step < 16) return 18 * (2 * step + (g >> 1)) + 4 * (g & 1) + (j & 3) + 8 * (j >> 2);
  return 18 * (4 * (4 * (step - 16) + g) + (j >> 1)) + 16 + (j & 1);
}

#define K4_SPREAD 1
#define K4_PM 1
#define K4_WD 1
#define K3_PF 0
#define K3_SLEEP 32
#define K1_ROT 0
#define AT_PM 9
#define HELP_PRIO 3
#define HELP_EARLY 0
#define PREP_ORD 1
#define GRAY 1
#define K4_POLL 10
#define K4_PK 0
#define K5_ORD 1
#define K5_SIG 154
#define K4_MID 0
#define AT_HFIRST 0
#define K4_PFW2 0
#define K3_REP 16
#define HELP_MID 1
#define K4_ROT 0
#define K5_FLAG 0
#define K5_SPREAD 1
#define K5_PM 0
#define OUT_AUX 16
#define K4_PFWD 1
#define QKV_AUX 16
#define QKT_AUX 16
#define V_AUX 16
#define O_AUX 16
#define H_AUX 16
#define PART_AUX 16
#define ACT_AUX 16
#define PREP_AUX 16
#define CODE_PF(dst, first, last, extra, blk) CODE_PF2(dst, first, last, extra, blk, 128u)
#define CODE_PF2(dst, first, last, extra, blk, stride)                                                                              \
  do {                                                                                                                     \
    uint64_t pc_; int b_, e_;                                                                                              \
    asm volatile("s_getpc_b64 %0\n.Lpf%=:\n\ts_mov_b32 %1, " #first "-.Lpf%=\n\ts_mov_b32 %2, " #last "-.Lpf%=" : "=s"(pc_), "=s"(b_), "=s"(e_)); \
    const unsigned o_ = (blk) * 8192u + (threadIdx.x & 63) * (stride), n_ = (unsigned)(e_ - b_) + (extra) - 4u;                \
    dst = gld4((const char*)(pc_ + b_), o_ < n_ ? o_ : n_);                                                                \
  } while (0)
#define G_MFMA(d, a, b) asm volatile("v_mfma_f32_16x16x32_bf16 %0, %1, %2, %0" : "+v"(d) : "v"(a), "v"(b))
#define G_MFMA0(d, a, b) asm volatile("v_mfma_f32_16x16x32_bf16 %0, %1, %2, 0" : "=&v"(d) : "v"(a), "v"(b))
template <int AUX> DI void bst16(u32x4 v, __amdgpu_buffer_rsrc_t rs, unsigned voff, unsigned soff) {
  __builtin_amdgcn_raw_buffer_store_b128(v, rs, voff, soff, AUX);
  asm volatile("s_nop 1" ::"v"(v) : "memory");
}
DI __amdgpu_buffer_rsrc_t mkrsrc(const void* p, unsigned bytes) {
  return __builtin_amdgcn_make_buffer_rsrc((void*)p, 0, bytes, 0x00020000);
}

typedef __attribute__((address_space(1))) unsigned gu32;
template <int SLEEP = 1> DI void wait_count(gu32* cnt, unsigned first, unsigned want) {
  if (rfl(first) >= want) return;
  for (unsigned spin = 0; spin < (1u << 16); ++spin) {
    if (rfl(__hip_atomic_load(cnt, __ATOMIC_RELAXED, __HIP_MEMORY_SCOPE_AGENT)) >= want) break;
    __builtin_amdgcn_s_sleep(SLEEP);
  }
}
constexpr int NJ_W1 = 72 * 36, NJ_WD = 9 * 144, NJ_WO = 9 * 18, NJ_ALL = NJ_W1 + NJ_WD + NJ_WO;
struct PrepJob { const char* src; unsigned rs, c1, c2, voff, dst[4]; };
DI void prep_setup(PrepJob& j, int J, int lane, const u16* wo, const u16* wg, const u16* wu, const u16* wd) {
  if (J >= NJ_ALL) J = NJ_ALL - 1;
  const int g = (lane >> 2) & 3;
  unsigned lrow = 8 * g;
  j.c1 = 0; j.c2 = 0;
  if (J < NJ_W1) {
    const int sm = J / 72, tg = J - sm * 72;
    j.src = (const char*)((sm & 1) ? wu : wg) + (size_t)(sm >> 1) * (32 * FF * 2) + tg * 128;
    j.rs = FF * 2;
#pragma unroll
    for (int t = 0; t < 4; ++t) j.dst[t] = (unsigned)WS_W1 + (4 * tg + t) * 36864 + sm * 1024;
  } else if (J < NJ_W1 + NJ_WD) {
    const int q = J - NJ_W1, cc = q / 9, tg = q - cc * 9;
    j.src = (const char*)wd + (size_t)cc * (32 * DM * 2) + tg * 128;
    j.rs = DM * 2;
#pragma unroll
    for (int t = 0; t < 4; ++t) { const int nt = 4 * tg + t, nb = nt / 9; j.dst[t] = (unsigned)WS_WD + ((nb * 144 + cc) * 9 + (nt - nb * 9)) * 1024; }
  } else {
    const int q = J - NJ_W1 - NJ_WD, step = q / 9, tg = q - step * 9;
    j.rs = DM * 2;
    if (step < 16) { j.src = (const char*)wo + (size_t)step * (36 * DM * 2) + tg * 128; lrow = 18 * (g >> 1) + 4 * (g & 1); j.c2 = 4; }
    else { j.src = (const char*)wo + (size_t)(288 * (step - 16) + 16) * (DM * 2) + tg * 128; lrow = 72 * g; j.c1 = 16; }
#pragma unroll
    for (int t = 0; t < 4; ++t) j.dst[t] = (unsigned)WS_WO + (step * 36 + 4 * tg + t) * 1024;
  }
#pragma unroll
  for (int t = 0; t < 4; ++t) j.dst[t] = rfl(j.dst[t]);
  j.voff = lrow * j.rs + (lane >> 4) * 32 + (lane & 3) * 8;
}
template <int JJ>
DI void prep_load(const PrepJob& j, u32x2 (&r)[8]) {
  const char* b = j.src + (JJ + j.c1 * (JJ >> 1) + j.c2 * (JJ >> 2)) * j.rs;
  asm volatile("global_load_dwordx2 %0, %1, %2" : "=v"(r[JJ]) : "v"(j.voff), "s"(b));
}
DI void prep_load_all(const PrepJob& j, u32x2 (&r)[8]) {
  prep_load<0>(j, r); prep_load<1>(j, r); prep_load<2>(j, r); prep_load<3>(j, r); prep_load<4>(j, r); prep_load<5>(j, r); prep_load<6>(j, r); prep_load<7>(j, r);
}
DI void prep_store(const PrepJob& j, const u32x2 (&r)[8], __amdgpu_buffer_rsrc_t rs, unsigned so) {
  u32x4 u[4];
#pragma unroll
  for (int i = 0; i < 4; ++i) {
    u[0][i] = __builtin_amdgcn_perm(r[2 * i + 1][0], r[2 * i][0], 0x05040100u);
    u[1][i] = __builtin_amdgcn_perm(r[2 * i + 1][0], r[2 * i][0], 0x07060302u);
    u[2][i] = __builtin_amdgcn_perm(r[2 * i + 1][1], r[2 * i][1], 0x05040100u);
    u[3][i] = __builtin_amdgcn_perm(r[2 * i + 1][1], r[2 * i][1], 0x07060302u);
  }
#pragma unroll
  for (int i = 0; i < 4; ++i) {
    const auto a0 = __builtin_amdgcn_permlane32_swap(u[0][i], u[2][i], false, false);
    const auto a1 = __builtin_amdgcn_permlane32_swap(u[1][i], u[3][i], false, false);
    const auto b0 = __builtin_amdgcn_permlane16_swap(a0[0], a1[0], false, false);
    const auto b1 = __builtin_amdgcn_permlane16_swap(a0[1], a1[1], false, false);
    u[0][i] = b0[0]; u[1][i] = b0[1]; u[2][i] = b1[0]; u[3][i] = b1[1];
  }
#pragma unroll
  for (int t = 0; t < 4; ++t) bst16<PREP_AUX>(u[t], rs, so, j.dst[t]);
}
#define PIN8(r) "+v"(r[0]), "+v"(r[1]), "+v"(r[2]), "+v"(r[3]), "+v"(r[4]), "+v"(r[5]), "+v"(r[6]), "+v"(r[7])

#define K1_PF 4
#define K1_TOUCH 1
#define K1_TSLEEP 16
#define K1_DEPTH 2
constexpr int K1_PITCH = 224, K1_SLOT = 16384 + 64 * K1_PITCH  , K1_NS = K1_DEPTH + 1;
constexpr int K1_SP = 232;
constexpr int K1_STG = K1_NS * K1_SLOT, K1_CS = K1_STG + 128 * K1_SP + 64, K1_ST = K1_CS + 512 + 64, K1_LDS = K1_ST + 2048;
struct K1C {
  LDSP const char *xa[2], *wl, *wc0, *wc1;
  u32x4 X[3][2], wf[K1_PF + 1], ones;
  f32x4 acc[4][2];
  f32x4 sm[2]; float sq[2];
};
DI void k1_loader(const char* xb, const char* wb, const u16* wo_, char* ws, int j, int lane, unsigned lds0) {
  __builtin_amdgcn_s_setprio(3);
  unsigned xo[2], wo[4];
  const int g = lane >> 4;
  xo[0] = (lane >> 3) * 1152 + (((lane & 7) ^ g) * 16);
  xo[1] = (8 + (lane >> 3)) * 1152 + (((lane & 7) ^ (4 + g)) * 16);
  xb += j * (32 * 1152);
  const unsigned xd = lds0 + j * 4096, wd = lds0 + 16384 + j * 1024;
#define K1_X(z, so) do { dma16(xb + (z) * 128, xo[0], xd + (so)); dma16(xb + (z) * 128, xo[1], xd + (so) + 1024);                    \
    dma16(xb + (z) * 128 + 16 * 1152, xo[0], xd + (so) + 2048); dma16(xb + (z) * 128 + 16 * 1152, xo[1], xd + (so) + 3072); } while (0)
#define K1_W(z, so) do { _Pragma("unroll") for (int i = 0; i < 3; ++i) dma16(wb + (z) * (64 * 3456), wo[i], wd + (so) + i * 4096);     \
    if (j < 2) dma16(wb + (z) * (64 * 3456), wo[3], wd + (so) + 3 * 4096); } while (0)
  K1_X(0, 0);
  SBAR();
#pragma unroll
  for (int i = 0; i < 4; ++i) {
    const unsigned u = 64 * (j + 4 * i) + lane, r = u / 14;
    wo[i] = ((r & ~12u) | ((r & 4u) << 1) | ((r & 8u) >> 1)) * 3456 + (u - r * 14) * 16;
  }
  K1_W(0, 0);
#pragma unroll
  for (int z = 1; z < K1_DEPTH; ++z) { K1_X(z, z * K1_SLOT); K1_W(z, z * K1_SLOT); }
  unsigned so = K1_DEPTH * K1_SLOT;
#pragma unroll 1
  for (int z = K1_DEPTH; z < 9; ++z) {
    asm volatile("s_waitcnt vmcnt(%0)\n\ts_barrier" ::"i"(7 * (K1_DEPTH - 1)) : "memory");
    K1_X(z, so); K1_W(z, so);
    so = so == (K1_NS - 1) * K1_SLOT ? 0 : so + K1_SLOT;
  }
  if (K1_DEPTH > 2) asm volatile("s_waitcnt vmcnt(14)\n\ts_barrier" ::: "memory");
  asm volatile("s_waitcnt vmcnt(7)\n\ts_barrier" ::: "memory");
  const int b = blockIdx.x;
  const bool dojob = j == 1 && b < NJ_WO, dorow = j >= 2 || (j == 0 && b < 64);
  const int br = j >= 2 ? 2 * b + j - 2 : 512 + (b & 63);
  PrepJob jb; u32x2 r[8]; unsigned wr[5];
  if (dojob) prep_setup(jb, NJ_W1 + NJ_WD + b, lane, wo_, wo_, wo_, wo_);
  const char* rp = (const char*)wo_ + (size_t)wo_row(br >> 5, (br >> 3) & 3, br & 7) * (DM * 2);
  SBAR();
  asm volatile("s_waitcnt vmcnt(0)\n\ts_barrier" ::: "memory");
  if (dojob) prep_load_all(jb, r);
  if (dorow) {
#pragma unroll
    for (int i = 0; i < 5; ++i) wr[i] = gld4(rp, (i < 4 ? lane + 64 * i : 256 + (lane & 31)) * 4);
  }
  asm volatile("s_barrier");
  asm volatile("s_barrier");
  const __amdgpu_buffer_rsrc_t wrs = mkrsrc(ws, (unsigned)WS_END);
  asm volatile("s_waitcnt vmcnt(0)" : PIN8(r), "+v"(wr[0]), "+v"(wr[1]), "+v"(wr[2]), "+v"(wr[3]), "+v"(wr[4])::"memory");
  if (dojob) {
#pragma unroll
    for (int t = 0; t < 4; ++t) jb.dst[t] = rfl(jb.dst[t]);
    prep_store(jb, r, wrs, ((lane >> 2) & 3) * 256 + ((lane & 3) * 4 + (lane >> 4)) * 16);
  }
  if (dorow) {
    float a = 0.f;
#pragma unroll
    for (int i = 0; i < 4; ++i) a = dot2(wr[i], 0x3f803f80u, a);
    a = dot2(lane >= 32 ? 0u : wr[4], 0x3f803f80u, a);
    a = xor16_sum(xor32_sum(a));
    a += __uint_as_float(__builtin_amdgcn_update_dpp(0u, __float_as_uint(a), 0x128, 0xf, 0xf, false));
    a += __uint_as_float(__builtin_amdgcn_update_dpp(0u, __float_as_uint(a), 0x124, 0xf, 0xf, false));
    a += __uint_as_float(__builtin_amdgcn_update_dpp(0u, __float_as_uint(a), 0x122, 0xf, 0xf, false));
    a += __uint_as_float(__builtin_amdgcn_update_dpp(0u, __float_as_uint(a), 0x121, 0xf, 0xf, false));
    a *= (1.0f / DM);
    const unsigned hi = cvtpk(a, 0.f) & 0xffffu;
    const unsigned lo = cvtpk(a - bflo(hi), 0.f) & 0xffffu;
    __builtin_amdgcn_raw_buffer_store_b16((u16)(lane ? lo : hi), wrs, lane < 2 ? (unsigned)WS_WBAR + (lane * DM + br) * 2 : 0x80000000u, 0, 16);
  }
#undef K1_X
#undef K1_W
}
DI u32x4 tr_frag(LDSP const char* p, int d) {
  s16x4 lo = __builtin_amdgcn_ds_read_tr16_b64_v4i16((LDSP s16x4*)p);
  s16x4 hi = __builtin_amdgcn_ds_read_tr16_b64_v4i16((LDSP s16x4*)(p + d));
  return __builtin_bit_cast(u32x4, __builtin_shufflevector(lo, hi, 0, 1, 2, 3, 4, 5, 6, 7));
}
template <int NHG, int F> DI void k1_read(K1C& c) {
  constexpr int NF = 4 + NHG, S = F / NF, j = F % NF, Z = S >> 1, so = (Z % K1_NS) * K1_SLOT;
  if constexpr (j == 0) {
    c.X[S % 3][0] = ldsr16(c.xa[S & 1] + so);
    c.X[S % 3][1] = ldsr16(c.xa[S & 1] + so + 2048);
  }
  constexpr int wo = so + (S & 1) * 7168;
  if constexpr (NHG == 0 || j < 3) c.wf[F % (K1_PF + 1)] = tr_frag(c.wl + wo + j * 32, 1792);
  else c.wf[F % (K1_PF + 1)] = tr_frag((j == 3 ? c.wc0 : c.wc1) + wo, 1792);
}
template <int NHG, int F> DI void k1_frag(K1C& c) {
  constexpr int NF = 4 + NHG, S = F / NF, j = F % NF, Z = S >> 1, N = F + K1_PF, fz = F % (2 * NF);
  if constexpr (S == 0) {
    if constexpr (NHG == 0 || j < 3) { G_MFMA0(c.acc[j][0], c.wf[F % (K1_PF + 1)], c.X[0][0]); G_MFMA0(c.acc[j][1], c.wf[F % (K1_PF + 1)], c.X[0][1]); }
    else G_MFMA0(c.acc[3][j - 3], c.wf[F % (K1_PF + 1)], c.ones);
  } else {
    if constexpr (NHG == 0 || j < 3) { G_MFMA(c.acc[j][0], c.wf[F % (K1_PF + 1)], c.X[S % 3][0]); G_MFMA(c.acc[j][1], c.wf[F % (K1_PF + 1)], c.X[S % 3][1]); }
    else G_MFMA(c.acc[3][j - 3], c.wf[F % (K1_PF + 1)], c.ones);
  }
  if constexpr ((S & 1) == NHG && j < 4) {
#pragma unroll
    for (int t = 0; t < 2; ++t) { const unsigned v = c.X[S % 3][t][j]; c.sq[t] = dot2(v, v, c.sq[t]); PIN(c.sq[t]); }
    if constexpr (j < 2) { if constexpr (S < 2) G_MFMA0(c.sm[j], c.ones, c.X[S % 3][j]); else G_MFMA(c.sm[j], c.ones, c.X[S % 3][j]); }
  }
  if constexpr (N < 18 * NF) {
    if constexpr (N % (2 * NF) == 0) {
      asm volatile("s_waitcnt lgkmcnt(0)\n\ts_barrier" ::: "memory");
    }
    k1_read<NHG, N>(c);
  }
  SBAR();
}
template <int NHG, int... Fs> DI void k1_run(K1C& c, std::integer_sequence<int, Fs...>) {
  k1_read<NHG, 0>(c); k1_read<NHG, 1>(c); k1_read<NHG, 2>(c);
  if (K1_PF > 3) k1_read<NHG, 3>(c);
  if (K1_PF > 4) k1_read<NHG, 4>(c);
  if (K1_PF > 5) k1_read<NHG, 5>(c);
  SBAR();
  (k1_frag<NHG, Fs>(c), ...);
  asm volatile("s_nop 7" : "+v"(c.acc[0][0]), "+v"(c.acc[0][1]), "+v"(c.acc[1][0]), "+v"(c.acc[1][1]), "+v"(c.acc[2][0]), "+v"(c.acc[2][1]),
               "+v"(c.acc[3][0]), "+v"(c.acc[3][1]), "+v"(c.sm[0]), "+v"(c.sm[1]));
  SBAR();
}
extern "C" __global__ __launch_bounds__(768, 3) void k_qkv(const u16* x, const u16* wqkv, const u16* wo, char* ws) {
  extern __shared__ __attribute__((aligned(16))) char smem[];
  const int tid = threadIdx.x, lane = tid & 63, w = rfl(tid >> 6);
  const int xcd = blockIdx.x & 7, bi = blockIdx.x >> 3;
  unsigned pf;
  CODE_PF2(pf, k_qkv, k_end, 0, bi, (w == 8 && bi < 9) ? 128u : 0u);
  SBAR();
  asm volatile("" : "+s"(x), "+s"(wqkv), "+s"(wo), "+s"(ws));
  LDSP char* lds = (LDSP char*)smem;
  const unsigned lds0 = (unsigned)(uintptr_t)lds;
  const int rb = 4 * (xcd >> 1) + (bi & 3), cb = 8 * (xcd & 1) + (bi >> 2);
  const int c0 = 108 * cb - 4 * (cb & 1);
  if (__builtin_expect(w >= 8, 1)) {
    k1_loader((const char*)x + rb * (128 * 1152), (const char*)wqkv + c0 * 2, wo, ws, w - 8, lane, lds0);
    asm volatile("" ::"v"(pf));
    return;
  }
  const int m = lane & 15, g = lane >> 4, mg = w & 3, nh = w >> 2;
  STAMP_DECL; STAMP(0);
  K1C c;
  c.ones = u32x4{0x3f803f80u, 0x3f803f80u, 0x3f803f80u, 0x3f803f80u};
  PIN(c.ones);
  c.xa[0] = lds + (32 * mg + m) * 128 + ((g ^ (m >> 1)) * 16);
  c.xa[1] = lds + (32 * mg + m) * 128 + (((4 + g) ^ (m >> 1)) * 16);
  LDSP const char* wbase = lds + 16384 + (16 * (g >> 1) + 4 * (g & 1) + (m >> 2)) * K1_PITCH + (m & 3) * 8;
  c.wl = wbase + nh * 128;
  c.wc0 = wbase + mg * 32;
  c.wc1 = wbase + (mg < 3 ? mg + 4 : 6) * 32;
  c.sq[0] = c.sq[1] = 0.f;
  const int off = 4 * (cb & 1), T0 = 128 * rb;
  LDSP char* stg = lds + K1_STG;
  LDSP char* a_st = lds + K1_ST + (32 * mg + m) * 8;                 PIN(a_st);
  LDSP char* a_cs = lds + K1_CS + (64 * nh + 4 * g) * 4;             PIN(a_cs);
  LDSP char* a_sw = stg + (32 * mg + m) * K1_SP + (64 * nh + 4 * g) * 2;   PIN(a_sw);
  LDSP char* a_tr = stg + (32 * mg + 4 * (g & 1) + (m >> 2)) * K1_SP + (64 * nh + 16 * (g >> 1) + 4 * (m & 3)) * 2;   PIN(a_tr);
  float qs[4];
#pragma unroll
  for (int j = 0; j < 4; ++j) { qs[j] = (c0 + 64 * nh + 16 * j + 4 * g < DM) ? QSCALE : 1.0f; PIN(qs[j]); }
  const __amdgpu_buffer_rsrc_t wrs = mkrsrc(ws, (unsigned)WS_END);
  constexpr unsigned DROP = 0x80000000u;
  const unsigned kvo = (unsigned)WS_KV + (T0 >> 9) * (unsigned)CHUNK_B;
  unsigned vo[2] = {0, 0}, vone = 0, vd[4] = {DROP, DROP, DROP, DROP};
  if (cb >= 10) {
#pragma unroll
  for (int tt = 0; tt < 2; ++tt) {
    const int cc = 64 * nh + 32 * tt + 16 * (g >> 1) + m - off, hd = (cc + 18) / 18 - 1, G = 6 * cb + hd;
    vo[tt] = (cc >= 0 && cc < 108) ? hd * 4864 + 2 * mg * 608 + (g & 1) * 304 + (cc - 18 * hd) * 16 : 29184;
  }
  vone = tid < 96 ? (tid >> 4) * 4864 + (tid & 15) * 304 + 288 : 29184;
#pragma unroll
  for (int i = 0; i < 4; ++i) {
    const unsigned u = i * 512 + tid, hd = u / 304;
    vd[i] = (u < 1824 && 6 * cb + hd >= 64) ? kvo + 18432 + ((T0 >> 4) & 31) * 608 + (6 * cb + hd - 64) * (unsigned)KV_HEAD_B + (u - hd * 304) * 16 : DROP;
  }
  }
  PIN(vone); PIN(vo[0]); PIN(vo[1]); PIN(vd[0]); PIN(vd[1]); PIN(vd[2]); PIN(vd[3]);
  const int tok = tid & 127, T = T0 + tok;
  LDSP const char* a_g = stg + tok * K1_SP + off * 2;   PIN(a_g);
  unsigned um[2], ut[2];
  const unsigned tl = ((T >> 5) & 15) * 1024 + (T & 31) * 16;
#pragma unroll
  for (int it = 0; it < 2; ++it) {
    const int hd = (w >> 1) + 4 * it, G = 6 * cb + hd, head = G & 31;
    if (hd >= 6 || G >= 64) { um[it] = DROP; ut[it] = DROP; }
    else if (G < 32) { um[it] = (unsigned)WS_QM + head * 65536 + (T >> 9) * 16384 + tl; ut[it] = (unsigned)WS_QT + head * 8192 + T * 4; }
    else { um[it] = kvo + head * (unsigned)KV_HEAD_B + tl; ut[it] = kvo + head * (unsigned)KV_HEAD_B + 16384 + ((T >> 6) & 7) * 256 + (T & 31) * 8 + ((T >> 5) & 1) * 4; }
    PIN(um[it]); PIN(ut[it]);
  }
  STAMP(1);
  asm volatile("s_waitcnt vmcnt(0)" : "+v"(pf));
  if (blockIdx.x < 5 && w == 7) bst16<16>(u32x4{0, 0, 0, 0}, wrs, (unsigned)WS_CNT + (blockIdx.x * 64 + lane) * 128, 0);
  asm volatile("s_barrier" ::: "memory");
  STAMP(2);
  if (nh) k1_run<1>(c, std::make_integer_sequence<int, 90>{});
  else k1_run<0>(c, std::make_integer_sequence<int, 72>{});
  STAMP(3);
#pragma unroll
  for (int t = 0; t < 2; ++t) {
    const f32x2 st = {c.sm[t][0], xor32_sum(xor16_sum(c.sq[t]))};
    *(LDSP f32x2*)(a_st + nh * 1024 + t * 128) = st;
  }
  if (nh) {
    *(LDSP f32x4*)(lds + K1_CS + (16 * mg + 4 * g) * 4) = c.acc[3][0];
    *(LDSP f32x4*)(lds + K1_CS + (16 * (mg + 4) + 4 * g) * 4) = c.acc[3][1];
  }
  asm volatile("s_waitcnt lgkmcnt(0)\n\ts_barrier" ::: "memory");
  {
    f32x2 p[2][2]; f32x4 cc[4];
#pragma unroll
    for (int t = 0; t < 2; ++t) { p[t][0] = *(LDSP const f32x2*)(a_st + t * 128); p[t][1] = *(LDSP const f32x2*)(a_st + 1024 + t * 128); }
#pragma unroll
    for (int j = 0; j < 4; ++j) cc[j] = *(LDSP const f32x4*)(a_cs + j * 64);
    SBAR();
#pragma unroll
    for (int t = 0; t < 2; ++t) {
      const float mean = (p[t][0][0] + p[t][1][0]) * (1.0f / DM);
      const float var = fmaxf((p[t][0][1] + p[t][1][1]) * (1.0f / DM) - mean * mean, 0.f);
      const float rstd = __builtin_amdgcn_rsqf(var + LN_EPS);
      if (cb == 0 && nh == 0) __builtin_amdgcn_raw_buffer_store_b32(__float_as_uint(mean), wrs, (unsigned)WS_XMEAN + (T0 + 32 * mg + 16 * t + m) * 4, 0, QKV_AUX);
#pragma unroll
      for (int j = 0; j < 4; ++j) {
        if (j < 3 || nh == 0) {
          const f32x4 a = (c.acc[j][t] - mean * cc[j]) * (rstd * qs[j]);
          *(LDSP u32x2*)(a_sw + t * (16 * K1_SP) + j * 32) = u32x2{cvtpk(a[0], a[1]), cvtpk(a[2], a[3])};
        }
      }
    }
  }
  if (cb >= 10) {
    asm volatile("s_waitcnt lgkmcnt(0)" ::: "memory");
    u32x4 v[2][2];
#pragma unroll
    for (int tt = 0; tt < 2; ++tt)
#pragma unroll
      for (int t = 0; t < 2; ++t) v[tt][t] = tr_frag(a_tr + t * (16 * K1_SP) + tt * 64, 8 * K1_SP);
    *(LDSP u32x4*)(lds + vone) = c.ones;
#pragma unroll
    for (int tt = 0; tt < 2; ++tt)
#pragma unroll
      for (int t = 0; t < 2; ++t) *(LDSP u32x4*)(lds + vo[tt] + t * 608) = v[tt][t];
  }
  STAMP(4);
  asm volatile("s_waitcnt lgkmcnt(0)\n\ts_barrier" ::: "memory");
  if (cb >= 10) {
    u32x4 v[4];
#pragma unroll
    for (int i = 0; i < 4; ++i) v[i] = ldsr16(lds + i * 8192 + tid * 16);
#pragma unroll
    for (int i = 0; i < 4; ++i) bst16<V_AUX>(v[i], wrs, vd[i], 0);
  }
  if (cb <= 10) {
    unsigned d[2][9];
#pragma unroll
    for (int it = 0; it < 2; ++it) {
      const int hd = (w >> 1) + 4 * it;
      LDSP const unsigned* sp = (LDSP const unsigned*)(a_g + 36 * (hd < 6 ? hd : 0));
#pragma unroll
      for (int i = 0; i < 9; ++i) d[it][i] = sp[i];
    }
    SBAR();
#pragma unroll
    for (int it = 0; it < 2; ++it) {
      bst16<QKV_AUX>(u32x4{d[it][0], d[it][1], d[it][2], d[it][3]}, wrs, um[it], 0);
      bst16<QKV_AUX>(u32x4{d[it][4], d[it][5], d[it][6], d[it][7]}, wrs, um[it] + 512, 0);
      __builtin_amdgcn_raw_buffer_store_b32(d[it][8], wrs, ut[it], 0, QKT_AUX);
    }
#pragma unroll
    for (int it = 0; it < 2; ++it) {
      const int hd = (w >> 1) + 4 * it, G = 6 * cb + hd;
      if (hd < 6 && G >= 32 && G < 64) {
        float n2 = 0.f;
#pragma unroll
        for (int i = 0; i < 9; ++i) n2 = dot2(d[it][i], d[it][i], n2);
        unsigned nb = __float_as_uint(n2);
        { auto r = __builtin_amdgcn_permlane32_swap(nb, nb, false, false); nb = max(r[0], r[1]); }
        { auto r = __builtin_amdgcn_permlane16_swap(nb, nb, false, false); nb = max(r[0], r[1]); }
        __builtin_amdgcn_raw_buffer_store_b32(nb, wrs, (unsigned)WS_KMAX + (((2 * rb + (w & 1)) * 32 + (G & 31)) * 16 + m) * 4, 0, QKV_AUX);
      }
    }
  }
  STAMP(5); STAMP_OUT(1, 6);
}

constexpr int K2_LDS = (int)KV_HEAD_B, K3_WBAR = 90 * 1024, K3_XC = 96 * 1024;
#define A_MFMA_ACC(d, a, b) asm volatile("v_mfma_f32_32x32x16_bf16 %0, %1, %2, %0" : "+v"(d) : "v"(a), "v"(b))
#define AT_M(m)
#define AT_U
struct AttnC {
  LDSP const char *kmb, *ktb, *vb;
  unsigned kma[3], kta[3], va[3]; unsigned long long mk, mv;
  u32x4 qm, qt, kf, v0, v1, p1; u32x2 ktl;
  f32x16 negm, o, sa, sb;
};
constexpr int k_off(int T) { return (T >> 4) * (int)CHUNK_B + (T & 15) * 1024; }
constexpr int kt_off(int T) { return (T >> 4) * (int)CHUNK_B + ((T >> 1) & 7) * 256; }
constexpr int v_off(int T) { return (T >> 4) * (int)CHUNK_B + (T & 15) * 1216; }
#define AT_STMT(TXT)                                                                                                              \
  asm volatile(TXT : "+{v[2:17]}"(c.o), "+{v[34:49]}"(c.sa), "+{v[50:65]}"(c.sb), "+{v[66:69]}"(c.kf), "+{v[70:73]}"(c.p1),        \
               "+{v[84:87]}"(c.v1), "+{v[88:91]}"(c.v0), "+{v[92:93]}"(c.ktl)                                                      \
               : "{v[18:33]}"(c.negm), "{v[98:101]}"(c.qm), "{v[102:105]}"(c.qt), [ka] "v"(c.kma[KO >> 16]), [kt] "v"(c.kta[KT >> 16]), \
                 [va] "v"(c.va[V1 >> 16]), [vb] "v"(c.va[V0 >> 16]), [ko] "i"(KO & 0xffff), [kto] "i"(KT & 0xffff), [vo1] "i"(V1 & 0xffff),  \
                 [vo0] "i"(V0 & 0xffff), [mk] "s"(c.mk), [mv] "s"(c.mv)                                                            \
               : "v74", "v75", "v76", "v77")
template <int T> DI void attn_tile(AttnC& c) {
  constexpr int KO = k_off(T < 62 ? T + 2 : 0), KT = kt_off(T < 62 ? T + 2 : 0), V1 = v_off(T) + 608, V0 = v_off(T < 63 ? T + 1 : 0);
  if constexpr (T == 0)
    AT_STMT("v_mfma_f32_32x32x8_bf16 v[50:65], v[92:93], v[104:105], v[18:33]\n\t" "v_exp_f32 v34, v34\n\t" "v_exp_f32 v35, v35\n\t" "v_exp_f32 v36, v36\n\t" "v_exp_f32 v37, v37\n\t" AT_M("mk") "ds_read_b64 v[92:93], %[kt] offset:%[kto]\n\t" AT_U AT_M("mv") "ds_read_b128 v[84:87], %[va] offset:%[vo1]\n\t" AT_U "v_cvt_pk_bf16_f32 v74, v34, v35\n\t" "v_exp_f32 v38, v38\n\t" "v_cvt_pk_bf16_f32 v75, v36, v37\n\t" "v_exp_f32 v39, v39\n\t" "v_exp_f32 v40, v40\n\t" "v_exp_f32 v41, v41\n\t" "s_waitcnt lgkmcnt(2)\n\t" "v_mfma_f32_32x32x16_bf16 v[50:65], v[66:69], v[98:101], v[50:65]\n\t" "ds_read_b128 v[66:69], %[ka] offset:%[ko]\n\t" "v_cvt_pk_bf16_f32 v76, v38, v39\n\t" "v_exp_f32 v42, v42\n\t" "v_cvt_pk_bf16_f32 v77, v40, v41\n\t" "v_exp_f32 v43, v43\n\t" "v_exp_f32 v44, v44\n\t" "v_exp_f32 v45, v45\n\t" "s_waitcnt lgkmcnt(2)\n\t" "v_mfma_f32_32x32x16_bf16 v[2:17], v[88:91], v[74:77], v[2:17]\n\t" AT_M("mv") "ds_read_b128 v[88:91], %[vb] offset:%[vo0]\n\t" AT_U "v_cvt_pk_bf16_f32 v70, v42, v43\n\t" "v_exp_f32 v46, v46\n\t" "v_cvt_pk_bf16_f32 v71, v44, v45\n\t" "v_exp_f32 v47, v47\n\t" "v_exp_f32 v48, v48\n\t" "v_exp_f32 v49, v49\n\t" "v_cvt_pk_bf16_f32 v72, v46, v47\n\t" "v_cvt_pk_bf16_f32 v73, v48, v49\n\t");
  else if constexpr (T == 62)
    AT_STMT("v_mfma_f32_32x32x8_bf16 v[50:65], v[92:93], v[104:105], v[18:33]\n\t" "v_exp_f32 v34, v34\n\t" "v_exp_f32 v35, v35\n\t" "v_exp_f32 v36, v36\n\t" "v_exp_f32 v37, v37\n\t" "s_waitcnt lgkmcnt(2)\n\t" "v_mfma_f32_32x32x16_bf16 v[2:17], v[84:87], v[70:73], v[2:17]\n\t" AT_M("mv") "ds_read_b128 v[84:87], %[va] offset:%[vo1]\n\t" AT_U "v_cvt_pk_bf16_f32 v74, v34, v35\n\t" "v_exp_f32 v38, v38\n\t" "v_cvt_pk_bf16_f32 v75, v36, v37\n\t" "v_exp_f32 v39, v39\n\t" "v_exp_f32 v40, v40\n\t" "v_exp_f32 v41, v41\n\t" "s_waitcnt lgkmcnt(2)\n\t" "v_mfma_f32_32x32x16_bf16 v[50:65], v[66:69], v[98:101], v[50:65]\n\t" "v_cvt_pk_bf16_f32 v76, v38, v39\n\t" "v_exp_f32 v42, v42\n\t" "v_cvt_pk_bf16_f32 v77, v40, v41\n\t" "v_exp_f32 v43, v43\n\t" "v_exp_f32 v44, v44\n\t" "v_exp_f32 v45, v45\n\t" "s_waitcnt lgkmcnt(1)\n\t" "v_mfma_f32_32x32x16_bf16 v[2:17], v[88:91], v[74:77], v[2:17]\n\t" AT_M("mv") "ds_read_b128 v[88:91], %[vb] offset:%[vo0]\n\t" AT_U "v_cvt_pk_bf16_f32 v70, v42, v43\n\t" "v_exp_f32 v46, v46\n\t" "v_cvt_pk_bf16_f32 v71, v44, v45\n\t" "v_exp_f32 v47, v47\n\t" "v_exp_f32 v48, v48\n\t" "v_exp_f32 v49, v49\n\t" "v_cvt_pk_bf16_f32 v72, v46, v47\n\t" "v_cvt_pk_bf16_f32 v73, v48, v49\n\t");
  else if constexpr (T == 63)
    AT_STMT("v_exp_f32 v50, v50\n\t" "v_exp_f32 v51, v51\n\t" "v_exp_f32 v52, v52\n\t" "v_exp_f32 v53, v53\n\t" "s_waitcnt lgkmcnt(0)\n\t" "v_mfma_f32_32x32x16_bf16 v[2:17], v[84:87], v[70:73], v[2:17]\n\t" AT_M("mv") "ds_read_b128 v[84:87], %[va] offset:%[vo1]\n\t" AT_U "v_cvt_pk_bf16_f32 v74, v50, v51\n\t" "v_exp_f32 v54, v54\n\t" "v_cvt_pk_bf16_f32 v75, v52, v53\n\t" "v_exp_f32 v55, v55\n\t" "v_exp_f32 v56, v56\n\t" "v_exp_f32 v57, v57\n\t" "v_cvt_pk_bf16_f32 v76, v54, v55\n\t" "v_exp_f32 v58, v58\n\t" "v_cvt_pk_bf16_f32 v77, v56, v57\n\t" "v_exp_f32 v59, v59\n\t" "v_exp_f32 v60, v60\n\t" "v_exp_f32 v61, v61\n\t" "s_waitcnt lgkmcnt(1)\n\t" "v_mfma_f32_32x32x16_bf16 v[2:17], v[88:91], v[74:77], v[2:17]\n\t" "v_cvt_pk_bf16_f32 v70, v58, v59\n\t" "v_exp_f32 v62, v62\n\t" "v_cvt_pk_bf16_f32 v71, v60, v61\n\t" "v_exp_f32 v63, v63\n\t" "v_exp_f32 v64, v64\n\t" "v_exp_f32 v65, v65\n\t" "v_cvt_pk_bf16_f32 v72, v62, v63\n\t" "v_cvt_pk_bf16_f32 v73, v64, v65\n\t");
  else if constexpr ((T & 3) == 0)
    AT_STMT("v_mfma_f32_32x32x8_bf16 v[50:65], v[92:93], v[104:105], v[18:33]\n\t" "v_exp_f32 v34, v34\n\t" "v_exp_f32 v35, v35\n\t" "v_exp_f32 v36, v36\n\t" "v_exp_f32 v37, v37\n\t" "s_waitcnt lgkmcnt(2)\n\t" "v_mfma_f32_32x32x16_bf16 v[2:17], v[84:87], v[70:73], v[2:17]\n\t" AT_M("mk") "ds_read_b64 v[92:93], %[kt] offset:%[kto]\n\t" AT_U AT_M("mv") "ds_read_b128 v[84:87], %[va] offset:%[vo1]\n\t" AT_U "v_cvt_pk_bf16_f32 v74, v34, v35\n\t" "v_exp_f32 v38, v38\n\t" "v_cvt_pk_bf16_f32 v75, v36, v37\n\t" "v_exp_f32 v39, v39\n\t" "v_exp_f32 v40, v40\n\t" "v_exp_f32 v41, v41\n\t" "s_waitcnt lgkmcnt(2)\n\t" "v_mfma_f32_32x32x16_bf16 v[50:65], v[66:69], v[98:101], v[50:65]\n\t" "ds_read_b128 v[66:69], %[ka] offset:%[ko]\n\t" "v_cvt_pk_bf16_f32 v76, v38, v39\n\t" "v_exp_f32 v42, v42\n\t" "v_cvt_pk_bf16_f32 v77, v40, v41\n\t" "v_exp_f32 v43, v43\n\t" "v_exp_f32 v44, v44\n\t" "v_exp_f32 v45, v45\n\t" "s_waitcnt lgkmcnt(2)\n\t" "v_mfma_f32_32x32x16_bf16 v[2:17], v[88:91], v[74:77], v[2:17]\n\t" AT_M("mv") "ds_read_b128 v[88:91], %[vb] offset:%[vo0]\n\t" AT_U "v_cvt_pk_bf16_f32 v70, v42, v43\n\t" "v_exp_f32 v46, v46\n\t" "v_cvt_pk_bf16_f32 v71, v44, v45\n\t" "v_exp_f32 v47, v47\n\t" "v_exp_f32 v48, v48\n\t" "v_exp_f32 v49, v49\n\t" "v_cvt_pk_bf16_f32 v72, v46, v47\n\t" "v_cvt_pk_bf16_f32 v73, v48, v49\n\t");
  else if constexpr ((T & 3) == 1)
    AT_STMT("v_mfma_f32_32x32x8_bf16 v[34:49], v[92:93], v[102:103], v[18:33]\n\t" "v_exp_f32 v50, v50\n\t" "v_exp_f32 v51, v51\n\t" "v_exp_f32 v52, v52\n\t" "v_exp_f32 v53, v53\n\t" "s_waitcnt lgkmcnt(2)\n\t" "v_mfma_f32_32x32x16_bf16 v[2:17], v[84:87], v[70:73], v[2:17]\n\t" AT_M("mv") "ds_read_b128 v[84:87], %[va] offset:%[vo1]\n\t" AT_U "v_cvt_pk_bf16_f32 v74, v50, v51\n\t" "v_exp_f32 v54, v54\n\t" "v_cvt_pk_bf16_f32 v75, v52, v53\n\t" "v_exp_f32 v55, v55\n\t" "v_exp_f32 v56, v56\n\t" "v_exp_f32 v57, v57\n\t" "s_waitcnt lgkmcnt(2)\n\t" "v_mfma_f32_32x32x16_bf16 v[34:49], v[66:69], v[98:101], v[34:49]\n\t" "ds_read_b128 v[66:69], %[ka] offset:%[ko]\n\t" "v_cvt_pk_bf16_f32 v76, v54, v55\n\t" "v_exp_f32 v58, v58\n\t" "v_cvt_pk_bf16_f32 v77, v56, v57\n\t" "v_exp_f32 v59, v59\n\t" "v_exp_f32 v60, v60\n\t" "v_exp_f32 v61, v61\n\t" "s_waitcnt lgkmcnt(2)\n\t" "v_mfma_f32_32x32x16_bf16 v[2:17], v[88:91], v[74:77], v[2:17]\n\t" AT_M("mv") "ds_read_b128 v[88:91], %[vb] offset:%[vo0]\n\t" AT_U "v_cvt_pk_bf16_f32 v70, v58, v59\n\t" "v_exp_f32 v62, v62\n\t" "v_cvt_pk_bf16_f32 v71, v60, v61\n\t" "v_exp_f32 v63, v63\n\t" "v_exp_f32 v64, v64\n\t" "v_exp_f32 v65, v65\n\t" "v_cvt_pk_bf16_f32 v72, v62, v63\n\t" "v_cvt_pk_bf16_f32 v73, v64, v65\n\t");
  else if constexpr ((T & 3) == 2)
    AT_STMT("v_mfma_f32_32x32x8_bf16 v[50:65], v[92:93], v[104:105], v[18:33]\n\t" "v_exp_f32 v34, v34\n\t" "v_exp_f32 v35, v35\n\t" "v_exp_f32 v36, v36\n\t" "v_exp_f32 v37, v37\n\t" "s_waitcnt lgkmcnt(2)\n\t" "v_mfma_f32_32x32x16_bf16 v[2:17], v[84:87], v[70:73], v[2:17]\n\t" AT_M("mk") "ds_read_b64 v[92:93], %[kt] offset:%[kto]\n\t" AT_U AT_M("mv") "ds_read_b128 v[84:87], %[va] offset:%[vo1]\n\t" AT_U "v_cvt_pk_bf16_f32 v74, v34, v35\n\t" "v_exp_f32 v38, v38\n\t" "v_cvt_pk_bf16_f32 v75, v36, v37\n\t" "v_exp_f32 v39, v39\n\t" "v_exp_f32 v40, v40\n\t" "v_exp_f32 v41, v41\n\t" "s_waitcnt lgkmcnt(2)\n\t" "v_mfma_f32_32x32x16_bf16 v[50:65], v[66:69], v[98:101], v[50:65]\n\t" "ds_read_b128 v[66:69], %[ka] offset:%[ko]\n\t" "v_cvt_pk_bf16_f32 v76, v38, v39\n\t" "v_exp_f32 v42, v42\n\t" "v_cvt_pk_bf16_f32 v77, v40, v41\n\t" "v_exp_f32 v43, v43\n\t" "v_exp_f32 v44, v44\n\t" "v_exp_f32 v45, v45\n\t" "s_waitcnt lgkmcnt(2)\n\t" "v_mfma_f32_32x32x16_bf16 v[2:17], v[88:91], v[74:77], v[2:17]\n\t" AT_M("mv") "ds_read_b128 v[88:91], %[vb] offset:%[vo0]\n\t" AT_U "v_cvt_pk_bf16_f32 v70, v42, v43\n\t" "v_exp_f32 v46, v46\n\t" "v_cvt_pk_bf16_f32 v71, v44, v45\n\t" "v_exp_f32 v47, v47\n\t" "v_exp_f32 v48, v48\n\t" "v_exp_f32 v49, v49\n\t" "v_cvt_pk_bf16_f32 v72, v46, v47\n\t" "v_cvt_pk_bf16_f32 v73, v48, v49\n\t");
  else if constexpr ((T & 3) == 3)
    AT_STMT("v_mfma_f32_32x32x8_bf16 v[34:49], v[92:93], v[102:103], v[18:33]\n\t" "v_exp_f32 v50, v50\n\t" "v_exp_f32 v51, v51\n\t" "v_exp_f32 v52, v52\n\t" "v_exp_f32 v53, v53\n\t" "s_waitcnt lgkmcnt(2)\n\t" "v_mfma_f32_32x32x16_bf16 v[2:17], v[84:87], v[70:73], v[2:17]\n\t" AT_M("mv") "ds_read_b128 v[84:87], %[va] offset:%[vo1]\n\t" AT_U "v_cvt_pk_bf16_f32 v74, v50, v51\n\t" "v_exp_f32 v54, v54\n\t" "v_cvt_pk_bf16_f32 v75, v52, v53\n\t" "v_exp_f32 v55, v55\n\t" "v_exp_f32 v56, v56\n\t" "v_exp_f32 v57, v57\n\t" "s_waitcnt lgkmcnt(2)\n\t" "v_mfma_f32_32x32x16_bf16 v[34:49], v[66:69], v[98:101], v[34:49]\n\t" "ds_read_b128 v[66:69], %[ka] offset:%[ko]\n\t" "v_cvt_pk_bf16_f32 v76, v54, v55\n\t" "v_exp_f32 v58, v58\n\t" "v_cvt_pk_bf16_f32 v77, v56, v57\n\t" "v_exp_f32 v59, v59\n\t" "v_exp_f32 v60, v60\n\t" "v_exp_f32 v61, v61\n\t" "s_waitcnt lgkmcnt(2)\n\t" "v_mfma_f32_32x32x16_bf16 v[2:17], v[88:91], v[74:77], v[2:17]\n\t" AT_M("mv") "ds_read_b128 v[88:91], %[vb] offset:%[vo0]\n\t" AT_U "v_cvt_pk_bf16_f32 v70, v58, v59\n\t" "v_exp_f32 v62, v62\n\t" "v_cvt_pk_bf16_f32 v71, v60, v61\n\t" "v_exp_f32 v63, v63\n\t" "v_exp_f32 v64, v64\n\t" "v_exp_f32 v65, v65\n\t" "v_cvt_pk_bf16_f32 v72, v62, v63\n\t" "v_cvt_pk_bf16_f32 v73, v64, v65\n\t");
}
DI void attn_group_slow(LDSP const char* km, LDSP const char* kt, LDSP const char* vp, const u32x4& qm, unsigned qtl, const f32x16& negm, f32x16& o) {
#pragma unroll
  for (int t = 0; t < 4; ++t) {
    const u32x2 k2 = *(LDSP const u32x2*)(kt + (t >> 1) * 256);
    const u32x4 ktl = {k2[0], k2[1], 0, 0};
    const u32x4 kf = ldsr16(km + t * 1024);
    const u32x4 v0 = ldsr16(vp + t * 1216), v1 = ldsr16(vp + t * 1216 + 608);
    u32x4 qq = {0, 0, 0, 0}; qq[t & 1] = qtl;
    f32x16 s = mfma32(ktl, qq, negm);
    s = mfma32(kf, qm, s);
    u32x4 p0, p1;
#pragma unroll
    for (int i = 0; i < 4; ++i) {
      p0[i] = cvtpk(__builtin_amdgcn_exp2f(s[2 * i]), __builtin_amdgcn_exp2f(s[2 * i + 1]));
      p1[i] = cvtpk(__builtin_amdgcn_exp2f(s[8 + 2 * i]), __builtin_amdgcn_exp2f(s[8 + 2 * i + 1]));
    }
    o = mfma32(v0, p0, o);
    o = mfma32(v1, p1, o);
  }
}
template <int G> DI void attn_group(AttnC& c) {
  __builtin_amdgcn_s_setprio(G < 5 ? 3 - (G & 3) : (G < 8 ? 3 : (G < 11 ? 2 : (G < 14 ? 1 : 0))));
  attn_tile<4 * G + 0>(c); attn_tile<4 * G + 1>(c); attn_tile<4 * G + 2>(c); attn_tile<4 * G + 3>(c);
}
DI void attn_helper(const u16* wg, const u16* wu, const u16* wd, char* ws, int head, int j, int lane, unsigned lds0) {
  __builtin_amdgcn_s_setprio(HELP_PRIO);
  const char* kv = ws + WS_KV + (size_t)head * KV_HEAD_B;
  const unsigned lane16 = lane * 16;
#define KV_DMA(o) dma16(kv + (o), lane16, lds0 + (o))
#pragma unroll
  for (int i = 0; i < 4; ++i) KV_DMA(((i >> 1) * 16 + (i & 1) * 4 + j) * 1024);
#pragma unroll
  for (int i = 0; i < 6; ++i) { const int p = i < 2 ? 8 + 4 * i + j : min(16 + 4 * i + j, 36); KV_DMA(p * 1024); }
  const int hw = blockIdx.x * 4 + j;
  PrepJob jb[4];
#pragma unroll
  for (int i = 0; i < 4; ++i) { prep_setup(jb[i], min(4 * hw + i, NJ_W1 + NJ_WD - 1), lane, wd, wg, wu, wd); PIN(jb[i].voff); }
  SBAR();
  asm volatile("s_waitcnt vmcnt(6)\n\ts_barrier" ::: "memory");
#pragma unroll
  for (int i = 0; i < 10; ++i) KV_DMA((unsigned)CHUNK_B + min(4 * i + j, 36) * 1024);
  asm volatile("s_waitcnt vmcnt(10)\n\ts_barrier" ::: "memory");
#pragma unroll
  for (int i = 0; i < 19; ++i) KV_DMA(2 * (unsigned)CHUNK_B + min(4 * i + j, 73) * 1024);
#undef KV_DMA
  u32x2 r[4][8];
  unsigned pf;
  asm volatile("s_waitcnt vmcnt(19)\n\ts_barrier" ::: "memory");
  prep_load_all(jb[0], r[0]); prep_load_all(jb[1], r[1]);
  asm volatile("s_waitcnt vmcnt(16)\n\ts_barrier" ::: "memory");
  const __amdgpu_buffer_rsrc_t wrs = mkrsrc(ws, (unsigned)WS_END);
  const unsigned pso = ((lane >> 2) & 3) * 256 + ((lane & 3) * 4 + (lane >> 4)) * 16;
  prep_load_all(jb[2], r[2]); prep_load_all(jb[3], r[3]);
  CODE_PF2(pf, k_ffn, k_end, 0, j, (blockIdx.x >> 3) == 0 ? 128u : 0u);
  asm volatile("s_waitcnt vmcnt(25)" : PIN8(r[0])::"memory");
  prep_store(jb[0], r[0], wrs, pso);
  asm volatile("s_waitcnt vmcnt(21)" : PIN8(r[1])::"memory");
  prep_store(jb[1], r[1], wrs, pso);
  asm volatile("s_waitcnt vmcnt(17)" : PIN8(r[2])::"memory");
  prep_store(jb[2], r[2], wrs, pso);
  asm volatile("s_waitcnt vmcnt(12)" : PIN8(r[3]), "+v"(pf)::"memory");
  prep_store(jb[3], r[3], wrs, pso);
}
extern "C" __global__ __launch_bounds__(768, 3) void k_attn(const u16* x, const u16* wg, const u16* wu, const u16* wd, char* ws) {
  extern __shared__ __attribute__((aligned(16))) char smem[];
  asm volatile("" : "+s"(x), "+s"(wg), "+s"(wu), "+s"(wd), "+s"(ws));
  LDSP char* lds = (LDSP char*)smem;
  const int tid = threadIdx.x, lane = tid & 63, w = rfl(tid >> 6);
  const int xcd = blockIdx.x & 7, bi = blockIdx.x >> 3;
  const int head = 4 * xcd + (bi & 3), qb = bi >> 2;
  if (__builtin_expect(w >= 8, AT_HFIRST)) { attn_helper(wg, wu, wd, ws, head, w - 8, lane, (unsigned)(uintptr_t)lds); return; }
  const int r = lane & 31, h = lane >> 5;
  const int qt32 = 8 * qb + w;
  STAMP_DECL; STAMP(0);
  AttnC c;
  c.qm = gld16(ws + WS_QM, head * 65536 + qt32 * 1024 + lane * 16);
  unsigned qtl = gld4(ws + WS_QT, head * 8192 + (32 * qt32 + r) * 4);
  u32x4 km0 = gld16(ws + WS_KMAX, ((lane >> 1) * 32 + head) * 64 + (lane & 1) * 32);
  u32x4 km1 = gld16(ws + WS_KMAX, ((lane >> 1) * 32 + head) * 64 + (lane & 1) * 32 + 16);
  const int vrow = r < 19 ? r : ((r >= 20 && r < 28) ? 0 : 18);
  c.kmb = lds + lane * 16;
  c.ktb = lds + 16384 + r * 8;
  c.vb = lds + 18432 + h * 304 + vrow * 16;
#pragma unroll
  for (int i = 0; i < 3; ++i) {
    c.kma[i] = (unsigned)(uintptr_t)c.kmb + i * 65536; c.kta[i] = (unsigned)(uintptr_t)c.ktb + i * 65536; c.va[i] = (unsigned)(uintptr_t)c.vb + i * 65536;
    PIN(c.kma[i]); PIN(c.kta[i]); PIN(c.va[i]);
  }
  c.mk = 0x00000000ffffffffull; c.mv = 0x0007ffff0007ffffull;
  c.ktl = u32x2{0, 0}; c.v0 = u32x4{0, 0, 0, 0}; c.v1 = c.v0; PIN(c.ktl); PIN(c.v0); PIN(c.v1);
#pragma unroll
  for (int i = 0; i < 16; ++i) c.o[i] = 0.f;
  PIN(c.o);
  const __amdgpu_buffer_rsrc_t wrs = mkrsrc(ws, (unsigned)WS_END);
  SBAR();
  asm volatile("s_waitcnt vmcnt(0)" : "+v"(c.qm), "+v"(qtl), "+v"(km0), "+v"(km1)::"memory");
  if (h) qtl = 0;
  float q2 = dot2(qtl, qtl, 0.f);
#pragma unroll
  for (int i = 0; i < 4; ++i) q2 = dot2(c.qm[i], c.qm[i], q2);
  unsigned k2 = max(max(max(km0[0], km0[1]), max(km0[2], km0[3])), max(max(km1[0], km1[1]), max(km1[2], km1[3])));
  { auto rr = __builtin_amdgcn_permlane32_swap(k2, k2, false, false); k2 = max(rr[0], rr[1]); }
  { auto rr = __builtin_amdgcn_permlane16_swap(k2, k2, false, false); k2 = max(rr[0], rr[1]); }
  k2 = max(k2, (unsigned)__builtin_amdgcn_update_dpp(0u, k2, 0x128, 0xf, 0xf, true));
  k2 = max(k2, (unsigned)__builtin_amdgcn_update_dpp(0u, k2, 0x124, 0xf, 0xf, true));
  k2 = max(k2, (unsigned)__builtin_amdgcn_update_dpp(0u, k2, 0x122, 0xf, 0xf, true));
  k2 = max(k2, (unsigned)__builtin_amdgcn_update_dpp(0u, k2, 0x121, 0xf, 0xf, true));
  q2 = xor32_sum(q2);
  const float mshift = __builtin_amdgcn_sqrtf(q2 * __uint_as_float(k2)) * 1.001f;
  c.qt = u32x4{qtl, 0, 0, qtl}; PIN(c.qt);
#pragma unroll
  for (int i = 0; i < 16; ++i) c.negm[i] = -mshift;
  PIN(c.negm);
  STAMP(1);
  asm volatile("s_barrier" ::: "memory");
  STAMP(2);
  asm volatile(AT_M("mk") "ds_read_b64 v[92:93], %[kt]\n\t" AT_U "ds_read_b128 v[70:73], %[ka]\n\tds_read_b128 v[66:69], %[ka] offset:1024\n\t"
               AT_M("mv") "ds_read_b128 v[88:91], %[va]\n\t" AT_U "s_waitcnt lgkmcnt(2)\n\t"
               "v_mfma_f32_32x32x8_bf16 v[34:49], v[92:93], v[102:103], v[18:33]\n\t"
               "v_mfma_f32_32x32x16_bf16 v[34:49], v[70:73], v[98:101], v[34:49]\n\ts_nop 11"
               : "={v[34:49]}"(c.sa), "={v[50:65]}"(c.sb), "={v[66:69]}"(c.kf), "={v[70:73]}"(c.p1), "+{v[88:91]}"(c.v0), "+{v[92:93]}"(c.ktl)
               : "{v[18:33]}"(c.negm), "{v[98:101]}"(c.qm), "{v[102:105]}"(c.qt), [ka] "v"(c.kma[0]), [kt] "v"(c.kta[0]), [va] "v"(c.va[0]),
                 [mk] "s"(c.mk), [mv] "s"(c.mv));
  attn_group<0>(c);
  asm volatile("s_barrier" ::: "memory");
  attn_group<1>(c); attn_group<2>(c);
  asm volatile("s_barrier" ::: "memory");
  attn_group<3>(c); attn_group<4>(c);
  asm volatile("s_barrier" ::: "memory");
  STAMP(3);
  attn_group<5>(c); attn_group<6>(c); attn_group<7>(c); attn_group<8>(c); attn_group<9>(c); attn_group<10>(c);
  attn_group<11>(c); attn_group<12>(c); attn_group<13>(c); attn_group<14>(c); attn_group<15>(c);
  asm volatile("s_waitcnt lgkmcnt(0)\n\tv_mfma_f32_32x32x16_bf16 v[2:17], v[84:87], v[70:73], v[2:17]\n\ts_nop 11"
               : "+{v[2:17]}"(c.o) : "{v[84:87]}"(c.v1), "{v[70:73]}"(c.p1), "{v[18:33]}"(c.negm));
  STAMP(4);
  f32x16 o = c.o;
  float l;
  { auto rr = __builtin_amdgcn_permlane32_swap(__float_as_uint(o[10]), __float_as_uint(o[10]), false, false); l = __uint_as_float(rr[0]); }
  if (__builtin_expect(__any(!(l >= 5.421011e-20f)), 0)) {
    float mx = -INFINITY;
    f32x16 z, negm;
#pragma unroll
    for (int i = 0; i < 16; ++i) z[i] = 0.f;
#pragma unroll 1
    for (int tt = 0; tt < 64; ++tt) {
      const int ck = tt >> 4, tl = tt & 15;
      const u32x2 k2 = *(LDSP const u32x2*)(c.ktb + ck * CHUNK_B + (tl >> 1) * 256);
      const u32x4 ktl = {k2[0], k2[1], 0, 0};
      const u32x4 kf = ldsr16(c.kmb + ck * CHUNK_B + tl * 1024);
      const u32x4 qq = {(tl & 1) ? 0u : qtl, (tl & 1) ? qtl : 0u, 0, 0};
      f32x16 sc = mfma32(ktl, qq, z);
      sc = mfma32(kf, c.qm, sc);
#pragma unroll
      for (int i = 0; i < 16; ++i) mx = fmaxf(mx, sc[i]);
    }
    mx = xor32_max(mx);
#pragma unroll
    for (int i = 0; i < 16; ++i) { negm[i] = -mx; o[i] = 0.f; }
#pragma unroll 1
    for (int gq = 0; gq < 16; ++gq) {
      const int ck = gq >> 2, gg = gq & 3;
      attn_group_slow(c.kmb + ck * CHUNK_B + gg * 4096, c.ktb + ck * CHUNK_B + gg * 512, c.vb + ck * CHUNK_B + gg * 4864, c.qm, qtl, negm, o);
    }
    auto rr = __builtin_amdgcn_permlane32_swap(__float_as_uint(o[10]), __float_as_uint(o[10]), false, false);
    l = __uint_as_float(rr[0]);
  }
  const float rl = 1.0f / l;
  u32x4 ou;
#pragma unroll
  for (int i = 0; i < 4; ++i) ou[i] = cvtpk(o[2 * i] * rl, o[2 * i + 1] * rl);
  const int q = 32 * qt32 + r;
  const unsigned ob = (unsigned)WS_OFR + (q >> 4) * 18432 + (q & 15) * 16;
  bst16<O_AUX>(ou, wrs, ob + (head >> 1) * 1024 + (2 * (head & 1) + h) * 256, 0);
  __builtin_amdgcn_raw_buffer_store_b32(cvtpk(o[8] * rl, o[9] * rl), wrs, h ? 0x80000000u : ob + (16 + (head >> 4)) * 1024 + ((head >> 2) & 3) * 256 + (head & 3) * 4, 0, O_AUX);
  STAMP(5);
  const int m = lane & 15, g = lane >> 4, kh = w >> 2, cg = bi >> 2;
  const int nt0 = 9 * (cg >> 1) + 5 * (cg & 1), mt = 16 * xcd + 4 * (bi & 3) + (w & 3), row = 16 * mt + m;
  const unsigned lds0 = (unsigned)(uintptr_t)lds, lane16 = lane * 16;
  asm volatile("s_barrier" ::: "memory");
#pragma unroll
  for (int i = 0; i < 12; ++i) {
    const int p = min(8 * i + w, 89), st = (p * 205) >> 10, t = p - 5 * st;
    dma16(ws + WS_WO + (st * 36 + nt0 + min(t, 4 - (cg & 1))) * 1024, lane16, lds0 + p * 1024);
  }
  dma16(ws + WS_WBAR + min(w, 2) * 1024, lane16, lds0 + K3_WBAR + min(w, 2) * 1024);
  const bool t4 = kh == 0 && !(cg & 1);
  const unsigned uo = mt * 18432 + (2 * nt0 + 4 * kh + (g >> 1)) * 256 + m * 16 + (g & 1) * 8;
  u32x2 xr[3];
  const unsigned xo = row * 1152 + (nt0 + 2 * kh) * 32 + g * 8;
  asm volatile("global_load_dwordx2 %0, %1, %2" : "=v"(xr[0]) : "v"(xo), "s"(x));
  asm volatile("global_load_dwordx2 %0, %1, %2 offset:32" : "=v"(xr[1]) : "v"(xo), "s"(x));
  asm volatile("global_load_dwordx2 %0, %1, %2" : "=v"(xr[2]) : "v"(t4 ? xo + 128 : xo), "s"(x));
  unsigned xm = gld4(ws + WS_XMEAN, row * 4);
  asm volatile("s_waitcnt vmcnt(17)\n\ts_barrier" ::: "memory");
  if (w == 0) {
    if (lane < K3_REP) __hip_atomic_fetch_add((gu32*)(ws + WS_CNT + (128 + qb * K3_REP) * 128) + lane * 32, 1u, __ATOMIC_RELAXED, __HIP_MEMORY_SCOPE_AGENT);
    wait_count((gu32*)(ws + WS_CNT + (128 + xcd * K3_REP + bi % K3_REP) * 128), 0u, 32u);
  }
  LDSP const char* wa = lds + (45 * kh + 2 * kh) * 1024 + lane16;
  LDSP const char* wb = lds + (45 * kh + 2 - 2 * kh) * 1024 + lane16;
  LDSP const char* wc = lds + (45 * kh + 4) * 1024 + lane16;
  LDSP const char* wbl = lds + K3_WBAR + (m & 1) * 1152 + kh * 576 + g * 16;
  unsigned so = ((g & 1) ? (unsigned)WS_HC : (unsigned)WS_HU) + (uo & ~8u);
  PIN(wa); PIN(wb); PIN(wc); PIN(wbl); PIN(so);
  asm volatile("s_waitcnt vmcnt(0)\n\ts_barrier" : "+v"(xr[0]), "+v"(xr[1]), "+v"(xr[2]), "+v"(xm)::"memory");
  STAMP(6);
  u32x4 O[9], wf[6];
  const char* obase = ws + WS_OFR + rfl((mt * 18 + 9 * kh) * 1024);
#pragma unroll
  for (int i = 0; i < 9; ++i)
    asm volatile("global_load_dwordx4 %0, %1, %2 sc1" : "=v"(O[i]) : "v"(lane16), "s"(obase + i * 1024));
#define K3_RD(f) ((f) % 6 == 5 ? ldsr16(wbl + ((f) / 6) * 64) : ldsr16(((f) % 6 < 2 ? wa : ((f) % 6 < 4 ? wb : wc)) + ((f) / 6) * 5120 + ((f) % 6 < 4 ? ((f) & 1) * 1024 : 0)))
#pragma unroll
  for (int f = 0; f < 5; ++f) wf[f] = K3_RD(f);
  f32x4 acc[6];
#pragma unroll
  for (int f = 0; f < 54; ++f) {
    if (f == 0) asm volatile("s_waitcnt vmcnt(6)" : "+v"(O[0]), "+v"(O[1]), "+v"(O[2]));
    if (f == 18) asm volatile("s_waitcnt vmcnt(3)" : "+v"(O[3]), "+v"(O[4]), "+v"(O[5]));
    if (f == 36) asm volatile("s_waitcnt vmcnt(0)" : "+v"(O[6]), "+v"(O[7]), "+v"(O[8]));
    if (f + 5 < 54) wf[(f + 5) % 6] = K3_RD(f + 5);
    if (f < 6) G_MFMA0(acc[f % 6], wf[f % 6], O[f / 6]);
    else G_MFMA(acc[f % 6], wf[f % 6], O[f / 6]);
    SBAR();
  }
#undef K3_RD
  asm volatile("s_nop 7" : "+v"(acc[0]), "+v"(acc[1]), "+v"(acc[2]), "+v"(acc[3]), "+v"(acc[4]), "+v"(acc[5]));
  LDSP char* xc = lds + K3_XC + lane16;
#pragma unroll
  for (int i = 0; i < 4; ++i) *(LDSP f32x4*)(xc + w * 4096 + i * 1024) = acc[2 + i];
  asm volatile("s_waitcnt lgkmcnt(0)\n\ts_barrier" ::: "memory");
  f32x4 v[4];
#pragma unroll
  for (int i = 0; i < 4; ++i) v[i] = *(LDSP const f32x4*)(xc + (w ^ 4) * 4096 + i * 1024);
  SBAR();
  const float mean = __uint_as_float(xm) + (acc[5][0] + v[3][0]) + (acc[5][1] + v[3][1]);
  float ssq = 0.f;
#pragma unroll
  for (int t = 0; t < 3; ++t) {
    const f32x4 a = (t < 2 ? acc[t] : acc[4]) + v[t];
    const unsigned h0 = cvtpk(bflo(xr[t][0]) + a[0], bfhi(xr[t][0]) + a[1]);
    const unsigned h1 = cvtpk(bflo(xr[t][1]) + a[2], bfhi(xr[t][1]) + a[3]);
    const unsigned c0 = cvtpk(bflo(h0) - mean, bfhi(h0) - mean);
    const unsigned c1 = cvtpk(bflo(h1) - mean, bfhi(h1) - mean);
    if (t < 2 || t4) { ssq = dot2(c0, c0, ssq); ssq = dot2(c1, c1, ssq); }
    const auto r0 = __builtin_amdgcn_permlane16_swap(h0, c0, false, false);
    const auto r1 = __builtin_amdgcn_permlane16_swap(h1, c1, false, false);
    bst16<H_AUX>(u32x4{r0[0], r1[0], r0[1], r1[1]}, wrs, t < 2 ? so + t * 512 : (t4 ? so + 2048 : 0x80000000u), 0);
  }
  ssq = xor32_sum(xor16_sum(ssq));
  __builtin_amdgcn_raw_buffer_store_b32(__float_as_uint(ssq), wrs, (unsigned)WS_SSQ + ((2 * cg + kh) * 2048 + row) * 4, 0, H_AUX);
  STAMP(7); STAMP_OUT(2, 8);
}

constexpr int K4_SLOT = 36864, K4_LDS = 163840;
#define K4_PFD 4
constexpr int K4_PF = K4_PFD;
constexpr int K5_PERM[18] = {0, 1, 2, 6, 7, 8, 9, 10, 11, 15, 16, 17, 3, 4, 5, 12, 13, 14};
constexpr int K5_R[4] = {36864, 73728, 110592, 0};
constexpr int K5_OC[9] = {65536, 102400, 139264, 147456, 155648, 0, 8192, 16384, 24576};
struct K5P { const char* wb; unsigned q[4], qs[4], qo, qos, lds0; };
template <int Z> DI void k5_piece(const K5P& k, int i, int w, unsigned lane16) {
  constexpr int P0 = K5_PERM[2 * Z] * 9216, D = (K5_PERM[2 * Z + 1] - K5_PERM[2 * Z]) * 9216;
  if (i < 3 || (w >> 2) == (Z & 1))
    dma16(k.wb + (P0 + k.q[i] + k.qs[i] * D), lane16, k.lds0 + K5_R[Z & 3] + (i < 3 ? 8 * i + w : 24 + (w & 3)) * 1024);
}
template <int Z> DI void k5_own(const K5P& k, int w, unsigned lane16) {
  constexpr int P0 = K5_PERM[2 * Z] * 9216, D = (K5_PERM[2 * Z + 1] - K5_PERM[2 * Z]) * 9216;
  dma16(k.wb + (P0 + k.qo + k.qos * D), lane16, k.lds0 + K5_OC[Z] + w * 1024);
}
#define POLL(dst, p) asm volatile("global_load_dword %0, %1, %2 sc1" : "=v"(dst) : "v"(0u), "s"(p))
struct K4C {
  const char *wb, *xb; unsigned lane16, lds0; int w, dir;
  unsigned pf0, pfo, pfw, pf1, poll; const char *pfb, *pfwb;
  K5P wd;
  gu32 *cntA, *cntB;
  const char *ab, *ab2; u32x4 A0[2][4];
  LDSP const char* wl[4];
  u32x4 X[2][18], wf[K4_PF + 1];
  f32x4 acc[4][4];
  float c1[2], c2[2];
  __amdgpu_buffer_rsrc_t ars; unsigned avo; int T0;
  float ea[2][2], et[2][2]; unsigned pk[2][4];
  f32x2 pa[2], pt[2], c1p[2], c2p[2];
};
DI void k4_piece(const K4C& c, int tile, int p) {
  dma16(c.wb + (tile * c.dir * K4_SLOT + p * 1024), c.lane16, c.lds0 + (tile & 3) * K4_SLOT + p * 1024);
}
template <int ZW> DI void k4_issue_w(K4C& c, int i) {
  if (ZW < 9) {
    if (i == 0) k4_piece(c, c.w >> 2, 4 * ZW + (c.w & 3));
    else if ((c.w >> 2) == (ZW & 1)) k4_piece(c, 2, 4 * ZW + (c.w & 3));
  } else if (ZW == 9) { k4_piece(c, 3, 8 * i + c.w); }
}
template <int Z, int I> DI void k4_issue1(K4C& c) {
  if (I == 0) k4_issue_w<Z + K4_WD>(c, 0);
  else if (I < 5) {
    c.X[(I - 1) >> 1][2 * Z + ((I - 1) & 1)] = gld16(c.xb + (((I - 1) >> 1) * 18432 + (2 * Z + ((I - 1) & 1)) * 1024), c.lane16);
  }
  else k4_issue_w<Z + K4_WD>(c, 1);
}
template <int TL, int I> DI void k4_issue2(K4C& c) {
  if (I < 4) k4_piece(c, TL, 8 * I + c.w);
  else if ((c.w >> 2) == (TL & 1)) k4_piece(c, TL, 32 + (c.w & 3));
}
template <int TL, int E, int U> DI void k4_epi(K4C& c) {
  constexpr int e = U >> 2, u = U & 3, mt = e >> 2, i = e & 3, q = e & 1;
  if constexpr (K4_PK && U < 32) {
    constexpr int pr = U >> 3, v = U & 7, m2 = pr >> 1, i0 = 2 * (pr & 1);
    if (v == 0) {
      const f32x2 G = {c.acc[TL & 3][m2][i0], c.acc[TL & 3][m2][i0 + 1]};
      asm volatile("v_pk_mul_f32 %0, %1, %2" : "=v"(c.pa[E]) : "v"(G), "v"(c.c1p[m2]));
    }
    if (v == 1) { const f32x2 Uu = {c.acc[TL & 3][2 + m2][i0], c.acc[TL & 3][2 + m2][i0 + 1]};
      asm volatile("v_pk_mul_f32 %0, %1, %2" : "=v"(c.pt[E]) : "v"(Uu), "v"(c.pa[E]));
      c.pa[E][0] = __builtin_amdgcn_exp2f(c.pa[E][0]); PIN(c.pa[E]); }
    if (v == 2) { c.pa[E][1] = __builtin_amdgcn_exp2f(c.pa[E][1]); PIN(c.pa[E]); }
    if (v == 3) asm volatile("v_pk_fma_f32 %0, %0, %1, %1" : "+v"(c.pa[E]) : "v"(c.c2p[m2]));
    if (v == 4) { c.pa[E][0] = __builtin_amdgcn_rcpf(c.pa[E][0]); PIN(c.pa[E]); }
    if (v == 5) { c.pa[E][1] = __builtin_amdgcn_rcpf(c.pa[E][1]); PIN(c.pa[E]); }
    if (v == 6) asm volatile("v_pk_mul_f32 %0, %0, %1" : "+v"(c.pt[E]) : "v"(c.pa[E]));
    if (v == 7) { c.pk[E][pr] = cvtpk(c.pt[E][0], c.pt[E][1]); PIN(c.pk[E][pr]); }
  } else if constexpr (U < 32) {
    if (u == 0) { const float a = c.acc[TL & 3][mt][i] * c.c1[mt]; c.ea[E][q] = a; c.et[E][q] = a * c.acc[TL & 3][2 + mt][i]; PIN(c.ea[E][q]); PIN(c.et[E][q]); }
    if (u == 1) { c.ea[E][q] = __builtin_amdgcn_exp2f(c.ea[E][q]); PIN(c.ea[E][q]); }
    if (u == 2) { c.ea[E][q] = __builtin_amdgcn_rcpf(__builtin_fmaf(c.ea[E][q], c.c2[mt], c.c2[mt])); PIN(c.ea[E][q]); }
    if (u == 3) { c.et[E][q] *= c.ea[E][q]; PIN(c.et[E][q]); if (q) { c.pk[E][e >> 1] = cvtpk(c.et[E][0], c.et[E][1]); PIN(c.pk[E][e >> 1]); } }
  } else if constexpr (U == 32) {
    auto r0 = __builtin_amdgcn_permlane16_swap(c.pk[E][0], c.pk[E][2], false, false);
    auto r1 = __builtin_amdgcn_permlane16_swap(c.pk[E][1], c.pk[E][3], false, false);
    c.pk[E][0] = r0[0]; c.pk[E][1] = r1[0]; c.pk[E][2] = r0[1]; c.pk[E][3] = r1[1];
  } else if constexpr (U == 33) {
    const int T = c.T0 + TL * c.dir;
    bst16<ACT_AUX>(u32x4{c.pk[E][0], c.pk[E][1], c.pk[E][2], c.pk[E][3]}, c.ars, c.avo, (T >> 1) * 1024 + (T & 1) * 512);
  }
}
constexpr int k4_tile(int F) { return F < 108 ? (F % 6) >> 1 : 3 + (F - 108) / 36; }
constexpr int k4_step(int F) { return F < 108 ? F / 6 : ((F - 108) % 36) >> 1; }
constexpr int k4_addr(int F) { return k4_step(F) * 2048 + (F & 1) * 1024; }
template <int F> DI void k4_frag(K4C& c) {
  constexpr int TL = k4_tile(F), S = k4_step(F), M = F & 1, N = F + K4_PF;
  const u32x4 a = c.wf[F % (K4_PF + 1)];
  if constexpr (F < 108) { if constexpr ((F + 4) % 3 == 0) __builtin_amdgcn_s_setprio(3 - ((F + 4) % 12) / 3); }
  else if constexpr ((F - 108 + 4) % 9 == 0) __builtin_amdgcn_s_setprio(3 - ((F - 108 + 4) % 36) / 9);
  if (S == 0) {
    G_MFMA0(c.acc[TL & 3][2 * M], a, c.X[0][S]);
    G_MFMA0(c.acc[TL & 3][2 * M + 1], a, c.X[1][S]);
  } else {
    constexpr int o = GRAY ? M : 0;
    G_MFMA(c.acc[TL & 3][2 * M + o], a, c.X[o][S]);
    if constexpr (K4_MID && F >= 108 && (F - 108) % 36 >= 2) {
      SBAR();
      k4_epi<(TL == 3 ? 0 : (TL == 4 ? 2 : TL - 1)), 0, (F - 108) % 36 - 2>(c);
      SBAR();
    }
    G_MFMA(c.acc[TL & 3][2 * M + 1 - o], a, c.X[1 - o][S]);
  }
  if constexpr (N < 108 && N % 12 == 0) {
    constexpr int Z = N / 12;
    if (Z < 8) asm volatile("s_waitcnt vmcnt(5)\n\ts_barrier" : "+v"(c.X[0][2 * Z]), "+v"(c.X[0][2 * Z + 1]), "+v"(c.X[1][2 * Z]), "+v"(c.X[1][2 * Z + 1])::"memory");
    else asm volatile("s_waitcnt vmcnt(2)\n\ts_barrier" : "+v"(c.X[0][2 * Z]), "+v"(c.X[0][2 * Z + 1]), "+v"(c.X[1][2 * Z]), "+v"(c.X[1][2 * Z + 1])::"memory");
  } else if constexpr (N >= 108 && N < 324 && (N - 108) % 36 == 0) {
    constexpr int NT = k4_tile(N);
    if (NT == 3) asm volatile("s_waitcnt vmcnt(0) lgkmcnt(0)\n\ts_barrier" ::: "memory");
    else if (NT == 8) {
      asm volatile("s_waitcnt vmcnt(0) lgkmcnt(0)\n\ts_barrier" ::: "memory");
      if (c.w == 0 && c.lane16 == 0) __hip_atomic_fetch_add(c.cntA, 1u, __ATOMIC_RELAXED, __HIP_MEMORY_SCOPE_AGENT);
    }
    else asm volatile("s_waitcnt vmcnt(4) lgkmcnt(0)\n\ts_barrier" ::: "memory");
  }
  if constexpr (N < 324) c.wf[N % (K4_PF + 1)] = ldsr16(c.wl[k4_tile(N) & 3] + k4_addr(N));
  if constexpr (F < 108) {
    constexpr int Z = F / 12, I = F % 12;
    if constexpr (Z == 0 && I < 4 && !(I & 1)) k4_issue1<2, I / 2>(c);
    if constexpr (Z < 7 && I < 8 && (I & 1)) k4_issue1<Z + 2, 2 + I / 2>(c);
    if constexpr (Z < 6 && I > 8 && (I & 1)) k4_issue1<Z + 3, (I - 9) / 2>(c);
    if constexpr (K4_WD == 0) {
      if constexpr (Z == 6 && I == 10) k4_issue2<3, 0>(c);
      if constexpr (Z == 7 && I < 8 && (I & 1)) k4_issue2<3, 1 + I / 2>(c);
    } else {
      if constexpr (Z == 6 && I == 10) k4_issue2<3, 2>(c);
      if constexpr (Z == 7 && (I == 1 || I == 3)) k4_issue2<3, 3 + I / 2>(c);
    }
  } else {
    constexpr int P = (F - 108) % 36;
    if constexpr (TL == 3) { if constexpr (P % 3 == 0 && P < 15) k4_issue2<4, P / 3>(c); if constexpr (P % 3 == 0 && P >= 15 && P < 30) k4_issue2<5, (P - 15) / 3>(c); }
    else if constexpr (TL + 2 < 9 && P % 4 == 0 && P < 20) k4_issue2<TL + 2, P / 4>(c);
    if constexpr (K4_PFWD && TL >= 4 && TL <= 7 && (P == 33 || (P == 34 && TL < 6))) {
      constexpr int tl = TL < 6 ? 2 * (TL - 4) + P - 33 : TL - 2;
      const int T = c.T0 + tl * c.dir;
      asm volatile("global_load_dword %0, %1, %2" : "+v"(c.pf0) : "v"(c.pfo), "s"(c.pfb + ((T >> 1) * 1024 + (T & 1) * 512)));
    }
    if constexpr (K4_PFW2 && TL >= 5 && TL <= 7 && P == 1)
      asm volatile("global_load_dword %0, %1, %2" : "+v"(c.pf0) : "v"(c.pfw), "s"(c.pfwb + (TL - 5) * (14 * 128)));
    if constexpr (TL == 7 && P == 0) CODE_PF2(c.pf1, k_ffn, k_end, 0, 2 + (c.w & 1), ((blockIdx.x >> 3) == 0 && c.w < 2) ? 128u : 0u);
    if constexpr (TL == 7 && P % 3 == 2 && P <= 11) k5_piece<0>(c.wd, P / 3, c.w, c.lane16);
    if constexpr (TL == 8 && P < 4) k5_piece<1>(c.wd, P, c.w, c.lane16);
    if constexpr (TL == 8 && P >= 4 && P < 8) k5_piece<2>(c.wd, P - 4, c.w, c.lane16);
    if constexpr (TL == 8 && P == K4_POLL) POLL(c.poll, c.cntA);
    if constexpr (TL == 8 && P == K4_POLL + 12) { asm volatile("s_waitcnt vmcnt(0)" : "+v"(c.poll), "+v"(c.pf1), "+v"(c.pf0)); wait_count(c.cntA, c.poll, 8u); }
    if constexpr (TL == 8 && P > K4_POLL + 12 && P <= K4_POLL + 20) {
      constexpr int i = P - K4_POLL - 13, z = i >> 2, j = (i & 1) * 2 + ((i >> 1) & 1);
      asm volatile("global_load_dwordx4 %0, %1, %2 sc1" : "=v"(c.A0[z][j]) : "v"(c.lane16), "s"(((j >> 1) ? c.ab2 : c.ab) + K5_PERM[2 * z + (j & 1)] * 1024));
    }
    if constexpr (P >= 2) {
      if constexpr (TL == 3) { if (!K4_MID) k4_epi<0, 0, P - 2>(c); k4_epi<1, 1, P - 2>(c); }
      else if constexpr (TL == 4) { if (!K4_MID) k4_epi<2, 0, P - 2>(c); k4_epi<3, 1, P - 2>(c); }
      else if (!K4_MID) k4_epi<TL - 1, 0, P - 2>(c);
    }
  }
  SBAR();
}
template <int B, int... Fs> DI void k4_run(K4C& c, std::integer_sequence<int, Fs...>) { (k4_frag<B + Fs>(c), ...); }
DI void k4_last(K4C& c) {
  float a[8], t[8];
#pragma unroll
  for (int e = 0; e < 8; ++e) { a[e] = c.acc[0][e >> 2][e & 3] * c.c1[e >> 2]; t[e] = a[e] * c.acc[0][2 + (e >> 2)][e & 3]; }
#pragma unroll
  for (int e = 0; e < 8; ++e) a[e] = __builtin_amdgcn_exp2f(a[e]);
#pragma unroll
  for (int e = 0; e < 8; ++e) a[e] = __builtin_amdgcn_rcpf(__builtin_fmaf(a[e], c.c2[e >> 2], c.c2[e >> 2]));
#pragma unroll
  for (int e = 0; e < 4; ++e) c.pk[0][e] = cvtpk(t[2 * e] * a[2 * e], t[2 * e + 1] * a[2 * e + 1]);
  k4_epi<8, 0, 32>(c); k4_epi<8, 0, 33>(c);
}
DI f32x4 addbf(f32x4 a, unsigned lo, unsigned hi) { return a + f32x4{bflo(lo), bfhi(lo), bflo(hi), bfhi(hi)}; }
struct K5C {
  const char *ab, *ab2;
  LDSP const char *ra[4], *oa[3], *xr;
  LDSP char* xw;
  u32x4 A[9][4], wf[5];
  f32x4 acc[2][7], own[2][2], v[4];
  const char* hb; unsigned ho; int kq; u32x2 hr[3];
  __amdgpu_buffer_rsrc_t wrs; unsigned tw, p8s;
  gu32* cnt;
};
template <int Z, int J> DI void k5_issue(K5C& c, const K4C& k) {
  if constexpr (Z < 7 && J < 4) asm volatile("global_load_dwordx4 %0, %1, %2 sc1"
 : "=v"(c.A[Z + 2][J]) : "v"(k.lane16), "s"(((J >> 1) ? c.ab2 : c.ab) + K5_PERM[2 * (Z + 2) + (J & 1)] * 1024));
  if constexpr (Z < 6 && J >= 4) k5_piece<Z + 3>(k.wd, J - 4, k.w, k.lane16);
  if constexpr (Z == 6 && J >= 4) k5_own<J - 4>(k.wd, k.w, k.lane16);
  if constexpr (Z == 7 && J == 0) k5_own<4>(k.wd, k.w, k.lane16);
  if constexpr (Z == 7 && J == 1) asm volatile("global_load_dwordx2 %0, %1, %2" : "=v"(c.hr[0]) : "v"(c.ho), "s"(c.hb + c.kq * 1024));
  if constexpr (Z == 7 && J == 2) asm volatile("global_load_dwordx2 %0, %1, %2 offset:512" : "=v"(c.hr[1]) : "v"(c.ho), "s"(c.hb + c.kq * 1024));
  if constexpr (Z == 7 && J == 3) asm volatile("global_load_dwordx2 %0, %1, %2" : "=v"(c.hr[2]) : "v"(c.ho), "s"(c.hb + 4096));
  if constexpr (Z == 8 && J < 4) k5_own<5 + J>(k.wd, k.w, k.lane16);
}
template <int N> DI u32x4 k5_read(const K5C& c) {
  if constexpr (N < 126) return ldsr16(c.ra[(N / 14) & 3] + (N % 14) * 1024);
  else { constexpr int o = K5_OC[(N - 126) / 4] + ((N - 126) & 3) * 1024; return ldsr16(c.oa[o >> 16] + (o & 0xffff)); }
}
template <int F> DI void k5_frag(K5C& c, K4C& k) {
  constexpr int N = F + 4;
  const u32x4 a = c.wf[F % 5];
  if constexpr (F < 126) {
    constexpr int Z = F / 14, f = F % 14, st = f / 7, j = f % 7;
    if (F < 7) { G_MFMA0(c.acc[0][j], a, c.A[Z][st]); G_MFMA0(c.acc[1][j], a, c.A[Z][2 + st]); }
    else { G_MFMA(c.acc[1][j], a, c.A[Z][2 + st]); G_MFMA(c.acc[0][j], a, c.A[Z][st]); }
    if constexpr (f == 10 && Z == 8) asm volatile("s_waitcnt vmcnt(4)\n\ts_barrier" : "+v"(c.hr[0]), "+v"(c.hr[1]), "+v"(c.hr[2])::"memory");
    if constexpr (f == 10 && Z < 8) {
      u32x4(&A)[4] = c.A[Z + 1];
      if (Z == 0) asm volatile("s_waitcnt vmcnt(9) lgkmcnt(0)\n\ts_barrier" : "+v"(A[0]), "+v"(A[1]), "+v"(A[2]), "+v"(A[3])::"memory");
      else if (Z == 3) asm volatile("s_waitcnt vmcnt(4) lgkmcnt(0)\n\ts_barrier" : "+v"(A[0]), "+v"(A[1]), "+v"(A[2]), "+v"(A[3]), "+v"(k.poll)::"memory");
      else if (Z < 7) asm volatile("s_waitcnt vmcnt(7) lgkmcnt(0)\n\ts_barrier" : "+v"(A[0]), "+v"(A[1]), "+v"(A[2]), "+v"(A[3])::"memory");
      else asm volatile("s_waitcnt vmcnt(8) lgkmcnt(0)\n\ts_barrier" : "+v"(A[0]), "+v"(A[1]), "+v"(A[2]), "+v"(A[3])::"memory");
      if (Z == 1 && k.w == 0 && k.lane16 == 0) __hip_atomic_fetch_add(k.cntB, 1u, __ATOMIC_RELAXED, __HIP_MEMORY_SCOPE_AGENT);
      if (Z == 3) wait_count(k.cntB, k.poll, 8u);
    }
    c.wf[N % 5] = k5_read<N>(c);
    if constexpr (Z == 3 && f == 1) POLL(k.poll, k.cntB);
    constexpr int slot = f == 0 ? 2 : (f == 2 ? 3 : (f == 4 ? 4 : (f == 5 ? 5 : (f == 7 ? 6 : (f == 9 ? 7 : -1)))));
    if constexpr (F == 0) { k5_issue<0, 0>(c, k); k5_issue<0, 1>(c, k); }
    if constexpr (slot >= 0) k5_issue<Z, slot>(c, k);
    if constexpr (Z < 8 && f > 10 && (f & 1)) k5_issue<Z + 1, (f - 11) / 2>(c, k);
  } else {
    constexpr int B = F - 126, z = B / 4, st = (B >> 1) & 1, t = B & 1;
    if (B < 2) { G_MFMA0(c.own[0][t], a, c.A[z][st]); G_MFMA0(c.own[1][t], a, c.A[z][2 + st]); }
    else { G_MFMA(c.own[1][t], a, c.A[z][2 + st]); G_MFMA(c.own[0][t], a, c.A[z][st]); }
    if constexpr (F == 131) asm volatile("s_waitcnt vmcnt(0) lgkmcnt(0)\n\ts_barrier" ::: "memory");
    if constexpr (F == K5_SIG) {
      asm volatile("s_waitcnt vmcnt(0)\n\ts_barrier" ::: "memory");
      if (k.w == 0 && k.lane16 == 0) __hip_atomic_fetch_add(c.cnt, 1u, __ATOMIC_RELAXED, __HIP_MEMORY_SCOPE_AGENT);
    }
    if constexpr (N < 162) c.wf[N % 5] = k5_read<N>(c);
  }
  if constexpr (F >= 123 && F < 130) { PIN(c.acc[1][F - 123]); *(LDSP f32x4*)(c.xw + (F - 123) * 1024) = c.acc[1][F - 123]; }
  if constexpr (F == 131) { _Pragma("unroll") for (int i = 0; i < 4; ++i) c.v[i] = *(LDSP const f32x4*)(c.xr + i * 1024); }
  if constexpr (F == 133 || F == 135 || F == 138) {
    constexpr int j = F == 133 ? 0 : (F == 135 ? 1 : 2), o = 2 * (j & 1);
    const f32x4 x = c.acc[0][2 * j] + c.v[o], y = c.acc[0][2 * j + 1] + c.v[o + 1];
    bst16<16>(u32x4{cvtpk(x[0], x[1]), cvtpk(x[2], x[3]), cvtpk(y[0], y[1]), cvtpk(y[2], y[3])}, c.wrs, k.lane16,
              (unsigned)WS_PART + ((((c.kq + j + 1) & 3) * 512 + c.tw) * 3 + j) * 1024);
    if constexpr (F == 135) { _Pragma("unroll") for (int i = 0; i < 3; ++i) c.v[i] = *(LDSP const f32x4*)(c.xr + (4 + i) * 1024); }
  }
  if constexpr (F == 140) {
    c.acc[0][6] += c.v[2];
    __builtin_amdgcn_raw_buffer_store_b64(u32x2{cvtpk(c.acc[0][6][0], c.acc[0][6][1]), cvtpk(c.acc[0][6][2], c.acc[0][6][3])}, c.wrs, c.p8s, c.kq * 512, 16);
    PIN(c.acc[0][6]);
  }
  SBAR();
}
template <int B, int... Fs> DI void k5_run(K5C& c, K4C& k, std::integer_sequence<int, Fs...>) { (k5_frag<B + Fs>(c, k), ...); }

extern "C" __global__ __launch_bounds__(512, 2) void k_ffn(char* out, char* ws) {
  extern __shared__ __attribute__((aligned(16))) char smem[];
  asm volatile("" : "+s"(out), "+s"(ws));
  LDSP char* lds = (LDSP char*)smem;
  const int tid = threadIdx.x, lane = tid & 63, w = rfl(tid >> 6);
  const int xcd = blockIdx.x & 7, bi = blockIdx.x >> 3;
  const int m = lane & 15, g = lane >> 4, kq = xcd >> 1;
  STAMP_DECL; STAMP(0);
  K4C c;
  {
    const int rb = 4 * (xcd & 1) + (bi & 3), fs = 8 * kq + (bi >> 2), mt = 16 * rb + 2 * w;
    c.lds0 = (unsigned)(uintptr_t)lds; c.lane16 = lane * 16; c.w = w;
    c.T0 = 9 * fs + 8 * (fs & 1); c.dir = 1 - 2 * (fs & 1);
    c.wb = ws + WS_W1 + (size_t)c.T0 * K4_SLOT;
    c.xb = ws + WS_HC + (size_t)mt * 18432;
    if (K4_WD) { k4_issue_w<0>(c, 0); k4_issue_w<0>(c, 1); }
    k4_issue1<0, 0>(c); k4_issue1<0, 1>(c); k4_issue1<0, 2>(c); k4_issue1<0, 3>(c); k4_issue1<0, 4>(c); k4_issue1<0, 5>(c);
    unsigned sq[8];
#pragma unroll
    for (int i = 0; i < 8; ++i) sq[i] = gld4(ws + WS_SSQ + (i >> 1) * 8192 + (i & 1) * 64, 4 * g * 8192 + (16 * mt + m) * 4);
    k4_issue1<1, 0>(c); k4_issue1<1, 1>(c); k4_issue1<1, 2>(c); k4_issue1<1, 3>(c); k4_issue1<1, 4>(c); k4_issue1<1, 5>(c);
    SBAR();
#pragma unroll
    for (int i = 0; i < 4; ++i) { c.wl[i] = lds + c.lane16 + i * K4_SLOT; PIN(c.wl[i]); }
    c.ars = mkrsrc(ws + WS_ACT, 128u * 144 * 1024);
    c.avo = (mt + (g & 1)) * (144 * 1024) + (g >> 1) * 256 + m * 16;
    c.wd.wb = ws + WS_WD + ((size_t)(bi >> 3) * 144 + 36 * kq) * 9 * 1024;
#pragma unroll
    for (int i = 0; i < 4; ++i) {
      const int p = i < 3 ? 8 * i + w : 24 + (w & 3), st = (p * 37) >> 8, t = p - 7 * st + 2;
      c.wd.q[i] = ((st >> 1) * 162 + (t < 8 ? (t + 2 * kq) & 7 : 8)) * 1024;
      c.wd.qs[i] = st & 1;
      asm volatile("" : "+s"(c.wd.q[i]), "+s"(c.wd.qs[i]));
    }
    c.wd.qo = ((w >> 2) * 162 + 2 * kq + (w & 1)) * 1024; c.wd.qos = (w >> 1) & 1; c.wd.lds0 = c.lds0;
    if (K4_PFWD) {
      c.pfb = ws + WS_ACT;
      c.pfo = (mt + ((lane >> 5) & 1)) * (144 * 1024) + ((lane >> 4) & 1) * 256 + ((lane >> 3) & 1) * 128;
      PIN(c.pfo);
    }
    if (K4_PFW2) {
      c.pfwb = ws + WS_WD + (size_t)kq * 331776 + (size_t)(bi >> 3) * 1327104;
      c.pfw = min(((bi & 7) * 8 + w) * 42 + (lane < 14 ? lane : 13), 2592 - 29) * 128; PIN(c.pfw);
    }
    c.pf0 = 0;
    c.cntA = (gu32*)(ws + WS_CNT + (64 + rb * 4 + kq) * 128);
    STAMP(1);
    asm volatile("s_waitcnt vmcnt(5)\n\ts_barrier" : "+v"(sq[0]), "+v"(sq[1]), "+v"(sq[2]), "+v"(sq[3]), "+v"(sq[4]), "+v"(sq[5]), "+v"(sq[6]), "+v"(sq[7]),
                 "+v"(c.X[0][0]), "+v"(c.X[0][1]), "+v"(c.X[1][0]), "+v"(c.X[1][1])::"memory");
#pragma unroll
    for (int i = 0; i < K4_PF; ++i) c.wf[i] = ldsr16(c.wl[k4_tile(i)] + k4_addr(i));
#pragma unroll
    for (int t = 0; t < 2; ++t) {
      float ss = (__uint_as_float(sq[t]) + __uint_as_float(sq[2 + t])) + (__uint_as_float(sq[4 + t]) + __uint_as_float(sq[6 + t]));
      ss = xor32_sum(xor16_sum(ss));
      const float v = ss * (1.0f / DM) + LN_EPS, rs = __builtin_amdgcn_rsqf(v);
      c.c1[t] = -LOG2E * rs; c.c2[t] = -LOG2E * (v * rs);
      PIN(c.c1[t]); PIN(c.c2[t]);
      if (K4_PK) { c.c1p[t] = f32x2{c.c1[t], c.c1[t]}; c.c2p[t] = f32x2{c.c2[t], c.c2[t]}; PIN(c.c1p[t]); PIN(c.c2p[t]); }
    }
    SBAR();
    k4_run<0>(c, std::make_integer_sequence<int, 288>{});
    c.cntB = c.cntA + 32 * 32;
  }
  const int rb = 8 * (xcd & 1) + (bi & 7), nb = bi >> 3, mg = w & 3, kh = w >> 2, mu = 8 * rb + 2 * mg + kh;
  gu32* prodB = c.cntB;
  c.cntA = (gu32*)(ws + WS_CNT + (64 + (rb >> 1) * 4 + kq) * 128);
  c.ab = ws + WS_ACT + ((size_t)mu * 144 + 36 * kq + 18 * kh) * 1024;
  c.ab2 = ws + WS_ACT + ((size_t)(mu ^ 1) * 144 + 36 * kq + 18 * kh) * 1024;
  k4_run<288>(c, std::make_integer_sequence<int, 36>{});
  STAMP(2);
  K5C d;
  d.ab = c.ab; d.ab2 = c.ab2;
  asm volatile("s_nop 7" : "+v"(c.acc[0][0]), "+v"(c.acc[0][1]), "+v"(c.acc[0][2]), "+v"(c.acc[0][3]));
  k4_last(c);
  SBAR();
  gu32* consB = c.cntA + 32 * 32;
  d.hb = ws + WS_HU + (size_t)mu * 18432 + nb * 4608; d.ho = (g >> 1) * 256 + m * 16 + (g & 1) * 8; d.kq = kq;
#pragma unroll
  for (int i = 0; i < 4; ++i) { d.ra[i] = lds + K5_R[i] + kh * 14336 + c.lane16; PIN(d.ra[i]); }
#pragma unroll
  for (int i = 0; i < 3; ++i) { d.oa[i] = lds + i * 65536 + kh * 4096 + c.lane16; PIN(d.oa[i]); }
  d.xw = lds + (kh ? K5_R[2] : K5_R[1]) + mg * 7168 + c.lane16; PIN(d.xw);
  d.xr = lds + (kh ? K5_R[1] : K5_R[2]) + mg * 7168 + c.lane16; PIN(d.xr);
  const __amdgpu_buffer_rsrc_t wrs = mkrsrc(ws, (unsigned)WS_END), ors = mkrsrc(out, SEQ * DM * 2);
  const int tile = rb * 4 + nb;
  const bool own8 = (mu & 3) == kq;
  const unsigned tw = tile * 8 + w;
  const unsigned p8 = (unsigned)WS_PART8 + tw * 2048 + lane * 8;
  d.cnt = (gu32*)(ws + WS_CNT + tile * 128);
  d.wrs = wrs; d.tw = tw; d.p8s = own8 ? 0x80000000u : p8; PIN(d.p8s);
#pragma unroll
  for (int i = 0; i < 4; ++i) { d.A[0][i] = c.A0[0][i]; d.A[1][i] = c.A0[1][i]; }
  asm volatile("s_waitcnt vmcnt(6) lgkmcnt(0)\n\ts_barrier" : "+v"(d.A[0][0]), "+v"(d.A[0][1]), "+v"(d.A[0][2]), "+v"(d.A[0][3])::"memory");
  STAMP(3);
  d.wf[0] = k5_read<0>(d); d.wf[1] = k5_read<1>(d); d.wf[2] = k5_read<2>(d); d.wf[3] = k5_read<3>(d);
  SBAR();
  c.cntB = prodB;
  k5_run<0>(d, c, std::make_integer_sequence<int, 28>{});
  c.cntB = consB;
  k5_run<28>(d, c, std::make_integer_sequence<int, 134>{});
  asm volatile("s_nop 7" : "+v"(d.own[0][0]), "+v"(d.own[0][1]), "+v"(d.own[1][0]), "+v"(d.own[1][1]));
  STAMP(4);
  LDSP char* xo = lds + K5_R[0] + c.lane16;
  *(LDSP f32x4*)(xo + w * 2048) = d.own[1][0];
  *(LDSP f32x4*)(xo + w * 2048 + 1024) = d.own[1][1];
  unsigned oo = (16 * mu + m) * 1152 + nb * 288;
  unsigned oo8 = own8 ? oo + 256 + g * 8 : 0x80000000u;
  oo += kq * 64 + (g & 1) * 32 + (g >> 1) * 16;
  PIN(oo); PIN(oo8);
  STAMP(5);
  if (w == 0) wait_count(d.cnt, 0u, 4u);
  asm volatile("s_waitcnt lgkmcnt(0)\n\ts_barrier" ::: "memory");
  STAMP(6);
  u32x4 r[3]; u32x2 r8[3];
#pragma unroll
  for (int j = 0; j < 3; ++j) {
    r[j] = __builtin_amdgcn_raw_buffer_load_b128(wrs, c.lane16, (unsigned)WS_PART + ((kq * 512 + tw) * 3 + j) * 1024, 16);
    r8[j] = __builtin_amdgcn_raw_buffer_load_b64(wrs, own8 ? p8 : 0x80000000u, ((kq + 1 + j) & 3) * 512, 16);
  }
  SBAR();
  f32x4 e0 = addbf(d.own[0][0] + *(LDSP const f32x4*)(xo + (w ^ 4) * 2048), d.hr[0][0], d.hr[0][1]);
  f32x4 e1 = addbf(d.own[0][1] + *(LDSP const f32x4*)(xo + (w ^ 4) * 2048 + 1024), d.hr[1][0], d.hr[1][1]);
  f32x4 e8 = addbf(d.acc[0][6], d.hr[2][0], d.hr[2][1]);
#pragma unroll
  for (int j = 0; j < 3; ++j) { e0 = addbf(e0, r[j][0], r[j][1]); e1 = addbf(e1, r[j][2], r[j][3]); e8 = addbf(e8, r8[j][0], r8[j][1]); }
  {
    const auto s0 = __builtin_amdgcn_permlane16_swap(cvtpk(e0[0], e0[1]), cvtpk(e1[0], e1[1]), false, false);
    const auto s1 = __builtin_amdgcn_permlane16_swap(cvtpk(e0[2], e0[3]), cvtpk(e1[2], e1[3]), false, false);
    bst16<OUT_AUX>(u32x4{s0[0], s1[0], s0[1], s1[1]}, ors, oo, 0);
    __builtin_amdgcn_raw_buffer_store_b64(u32x2{cvtpk(e8[0], e8[1]), cvtpk(e8[2], e8[3])}, ors, oo8, 0, OUT_AUX);
  }
  STAMP(7);
  STAMP_OUT(4, 8);
}
extern "C" __global__ void k_end() {}

extern "C" void kernel_launch(void* const* d_in, const int* in_sizes, int n_in, void* d_out, int out_size, void* d_ws, size_t ws_size,
                              hipStream_t stream) {
  const u16* x = (const u16*)d_in[0];
  const u16* wqkv = (const u16*)d_in[1];
  const u16* wo = (const u16*)d_in[2];
  const u16* wg = (const u16*)d_in[4];
  const u16* wu = (const u16*)d_in[5];
  const u16* wd = (const u16*)d_in[6];
  char* ws = (char*)d_ws;
  (void)hipFuncSetAttribute((const void*)k_qkv, hipFuncAttributeMaxDynamicSharedMemorySize, K1_LDS);
  (void)hipFuncSetAttribute((const void*)k_attn, hipFuncAttributeMaxDynamicSharedMemorySize, K2_LDS);
  (void)hipFuncSetAttribute((const void*)k_ffn, hipFuncAttributeMaxDynamicSharedMemorySize, K4_LDS);
  k_qkv<<<256, 768, K1_LDS, stream>>>(x, wqkv, wo, ws);
  k_attn<<<256, 768, K2_LDS, stream>>>(x, wg, wu, wd, ws);
  k_ffn<<<256, 512, K4_LDS, stream>>>((char*)d_out, ws);
}
```

```cpp
#define SEL 0
#include <hip/hip_runtime.h>
#include <hip/hip_bf16.h>
#include <stdint.h>
#include <utility>
#include <type_traits>

typedef __bf16 bf16x8 __attribute__((ext_vector_type(8)));
typedef __bf16 bf16x2 __attribute__((ext_vector_type(2)));
typedef short s16x4 __attribute__((ext_vector_type(4)));
typedef float f32x16 __attribute__((ext_vector_type(16)));
typedef float f32x4 __attribute__((ext_vector_type(4)));
typedef float f32x2 __attribute__((ext_vector_type(2)));
typedef unsigned u32x4 __attribute__((ext_vector_type(4)));
typedef unsigned u32x2 __attribute__((ext_vector_type(2)));
typedef unsigned short u16;
#define LDSP __attribute__((address_space(3)))
#define DI __device__ __forceinline__
#define SBAR() __builtin_amdgcn_sched_barrier(0)

constexpr int SEQ = 2048, DM = 576, NH = 32, DH = 18, FF = 4608;
constexpr float LOG2E = 1.4426950408889634f;
constexpr float QSCALE = 0.23570226039551584f * LOG2E;
constexpr float LN_EPS = 1e-5f;

constexpr size_t CHUNK_B = 37888;
constexpr size_t KV_HEAD_B = 4 * CHUNK_B;
constexpr size_t WS_QM = 0;
constexpr size_t WS_QT = WS_QM + (size_t)NH * 65536;
constexpr size_t WS_KV = WS_QT + (size_t)NH * 8192;
constexpr size_t WS_KMAX = WS_KV + (size_t)NH * KV_HEAD_B;
constexpr size_t WS_XMEAN = WS_KMAX + 65536;
constexpr size_t WS_OFR = WS_XMEAN + 8192;
constexpr size_t WS_WO = WS_OFR + (size_t)128 * 18 * 1024;
constexpr size_t WS_WBAR = WS_WO + (size_t)12 * 54 * 1024;
constexpr size_t WS_HC = WS_WBAR + 3072;
constexpr size_t WS_SSQ = WS_HC + (size_t)64 * 36 * 1024;
constexpr size_t WS_W1 = WS_SSQ + (size_t)16 * 8192;
constexpr size_t WS_WD = WS_W1 + (size_t)288 * 36 * 1024;
constexpr size_t WS_ACT = WS_WD + (size_t)4 * 144 * 9 * 1024;
constexpr size_t UNIT_B = (size_t)128 * 18432;
constexpr size_t WS_PART = WS_ACT + (size_t)128 * 144 * 1024;
constexpr size_t WS_PART8 = WS_PART + (size_t)4 * 64 * 8 * 3 * 1024;
constexpr size_t WS_CNT = WS_PART8 + (size_t)64 * 8 * 4 * 512;
constexpr size_t WS_HU = WS_CNT + 40960;
constexpr size_t WS_END = WS_HU + UNIT_B;

DI unsigned cvtpk(float lo, float hi) {
  f32x2 v = {lo, hi};
  bf16x2 b = __builtin_convertvector(v, bf16x2);
  return __builtin_bit_cast(unsigned, b);
}
DI float bflo(unsigned u) { return __uint_as_float(u << 16); }
DI float bfhi(unsigned u) { return __uint_as_float(u & 0xffff0000u); }
DI float dot2(unsigned a, unsigned b, float c) {
  return __builtin_amdgcn_fdot2_f32_bf16(__builtin_bit_cast(bf16x2, a), __builtin_bit_cast(bf16x2, b), c, false);
}
DI f32x16 mfma32(u32x4 a, u32x4 b, f32x16 c) {
  return __builtin_amdgcn_mfma_f32_32x32x16_bf16(__builtin_bit_cast(bf16x8, a), __builtin_bit_cast(bf16x8, b), c, 0, 0, 0);
}
DI f32x4 mfma16(u32x4 a, u32x4 b, f32x4 c) {
  return __builtin_amdgcn_mfma_f32_16x16x32_bf16(__builtin_bit_cast(bf16x8, a), __builtin_bit_cast(bf16x8, b), c, 0, 0, 0);
}
DI unsigned rfl(unsigned x) { return __builtin_amdgcn_readfirstlane(x); }
DI const char* uniptr(const char* p) {
  uint64_t v = (uint64_t)p;
  unsigned lo = rfl((unsigned)v), hi = rfl((unsigned)(v >> 32));
  return (const char*)(((uint64_t)hi << 32) | lo);
}
DI void dma16(const char* sbase, unsigned voff, unsigned lds) {
  asm volatile("s_mov_b32 m0, %2\n\ts_nop 0\n\tglobal_load_lds_dwordx4 %0, %1" ::"v"(voff), "s"(sbase), "s"(lds));
}
DI u32x4 gld16(const char* sbase, unsigned voff) {
  u32x4 r;
  asm volatile("global_load_dwordx4 %0, %1, %2" : "=v"(r) : "v"(voff), "s"(sbase));
  return r;
}
DI unsigned gld4(const char* sbase, unsigned voff) {
  unsigned r;
  asm volatile("global_load_dword %0, %1, %2" : "=v"(r) : "v"(voff), "s"(sbase));
  return r;
}
#define WAITV(n) asm volatile("s_waitcnt vmcnt(" #n ")" ::: "memory")
#define BARRIER() asm volatile("s_barrier" ::: "memory")
#define PIN(x) asm volatile("" : "+v"(x))
DI u32x4 ldsr16(LDSP const char* p) { return *(LDSP const u32x4*)p; }
DI float xor32_sum(float v) {
  auto r = __builtin_amdgcn_permlane32_swap(__float_as_uint(v), __float_as_uint(v), false, false);
  return __uint_as_float(r[0]) + __uint_as_float(r[1]);
}
DI float xor32_max(float v) {
  auto r = __builtin_amdgcn_permlane32_swap(__float_as_uint(v), __float_as_uint(v), false, false);
  return fmaxf(__uint_as_float(r[0]), __uint_as_float(r[1]));
}
DI float xor16_sum(float v) {
  auto r = __builtin_amdgcn_permlane16_swap(__float_as_uint(v), __float_as_uint(v), false, false);
  return __uint_as_float(r[0]) + __uint_as_float(r[1]);
}

#define STAMP_DECL
#define STAMP(i)
#define STAMP_OUT(kid, n)

DI int wo_row(int step, int g, int j) {
  if (step < 16) return 18 * (2 * step + (g >> 1)) + 4 * (g & 1) + (j & 3) + 8 * (j >> 2);
  return 18 * (4 * (4 * (step - 16) + g) + (j >> 1)) + 16 + (j & 1);
}

#define K4_SPREAD 1
#define K4_PM 1
#define K4_WD 1
#define K3_PF 0
#define K3_SLEEP 32
#define K1_ROT 0
#define AT_PM 9
#define HELP_PRIO 3
#define HELP_EARLY 0
#define PREP_ORD 1
#define GRAY 1
#define K4_POLL 10
#define K4_PK 0
#define K5_ORD 1
#define K5_SIG 154
#define K4_MID 0
#define AT_HFIRST 0
#define K4_PFW2 0
#define K3_REP 16
#define HELP_MID 1
#define K4_ROT 0
#define K5_FLAG 0
#define K5_SPREAD 1
#define K5_PM 0
#define OUT_AUX 16
#define K4_PFWD 1
#define QKV_AUX 16
#define QKT_AUX 16
#define V_AUX 16
#define O_AUX 16
#define H_AUX 16
#define PART_AUX 16
#define ACT_AUX 16
#define PREP_AUX 16
#define CODE_PF(dst, first, last, extra, blk) CODE_PF2(dst, first, last, extra, blk, 128u)
#define CODE_PF2(dst, first, last, extra, blk, stride)                                                                              \
  do {                                                                                                                     \
    uint64_t pc_; int b_, e_;                                                                                              \
    asm volatile("s_getpc_b64 %0\n.Lpf%=:\n\ts_mov_b32 %1, " #first "-.Lpf%=\n\ts_mov_b32 %2, " #last "-.Lpf%=" : "=s"(pc_), "=s"(b_), "=s"(e_)); \
    const unsigned o_ = (blk) * 8192u + (threadIdx.x & 63) * (stride), n_ = (unsigned)(e_ - b_) + (extra) - 4u;                \
    dst = gld4((const char*)(pc_ + b_), o_ < n_ ? o_ : n_);                                                                \
  } while (0)
#define G_MFMA(d, a, b) asm volatile("v_mfma_f32_16x16x32_bf16 %0, %1, %2, %0" : "+v"(d) : "v"(a), "v"(b))
#define G_MFMA0(d, a, b) asm volatile("v_mfma_f32_16x16x32_bf16 %0, %1, %2, 0" : "=&v"(d) : "v"(a), "v"(b))
template <int AUX> DI void bst16(u32x4 v, __amdgpu_buffer_rsrc_t rs, unsigned voff, unsigned soff) {
  __builtin_amdgcn_raw_buffer_store_b128(v, rs, voff, soff, AUX);
  asm volatile("s_nop 1" ::"v"(v) : "memory");
}
DI __amdgpu_buffer_rsrc_t mkrsrc(const void* p, unsigned bytes) {
  return __builtin_amdgcn_make_buffer_rsrc((void*)p, 0, bytes, 0x00020000);
}

typedef __attribute__((address_space(1))) unsigned gu32;
template <int SLEEP = 1> DI void wait_count(gu32* cnt, unsigned first, unsigned want) {
  if (rfl(first) >= want) return;
  for (unsigned spin = 0; spin < (1u << 16); ++spin) {
    if (rfl(__hip_atomic_load(cnt, __ATOMIC_RELAXED, __HIP_MEMORY_SCOPE_AGENT)) >= want) break;
    __builtin_amdgcn_s_sleep(SLEEP);
  }
}
constexpr int NJ_W1 = 72 * 36, NJ_WD = 9 * 144, NJ_WO = 9 * 18, NJ_ALL = NJ_W1 + NJ_WD + NJ_WO;
struct PrepJob { const char* src; unsigned rs, c1, c2, voff, dst[4]; };
DI void prep_setup(PrepJob& j, int J, int lane, const u16* wo, const u16* wg, const u16* wu, const u16* wd) {
  if (J >= NJ_ALL) J = NJ_ALL - 1;
  const int g = (lane >> 2) & 3;
  unsigned lrow = 8 * g;
  j.c1 = 0; j.c2 = 0;
  if (J < NJ_W1) {
    const int sm = J / 72, tg = J - sm * 72;
    j.src = (const char*)((sm & 1) ? wu : wg) + (size_t)(sm >> 1) * (32 * FF * 2) + tg * 128;
    j.rs = FF * 2;
#pragma unroll
    for (int t = 0; t < 4; ++t) j.dst[t] = (unsigned)WS_W1 + (4 * tg + t) * 36864 + sm * 1024;
  } else if (J < NJ_W1 + NJ_WD) {
    const int q = J - NJ_W1, cc = q / 9, tg = q - cc * 9;
    j.src = (const char*)wd + (size_t)cc * (32 * DM * 2) + tg * 128;
    j.rs = DM * 2;
#pragma unroll
    for (int t = 0; t < 4; ++t) { const int nt = 4 * tg + t, nb = nt / 9; j.dst[t] = (unsigned)WS_WD + ((nb * 144 + cc) * 9 + (nt - nb * 9)) * 1024; }
  } else {
    const int q = J - NJ_W1 - NJ_WD, step = q / 9, tg = q - step * 9;
    j.rs = DM * 2;
    if (step < 16) { j.src = (const char*)wo + (size_t)step * (36 * DM * 2) + tg * 128; lrow = 18 * (g >> 1) + 4 * (g & 1); j.c2 = 4; }
    else { j.src = (const char*)wo + (size_t)(288 * (step - 16) + 16) * (DM * 2) + tg * 128; lrow = 72 * g; j.c1 = 16; }
#pragma unroll
    for (int t = 0; t < 4; ++t) j.dst[t] = (unsigned)WS_WO + (step * 36 + 4 * tg + t) * 1024;
  }
#pragma unroll
  for (int t = 0; t < 4; ++t) j.dst[t] = rfl(j.dst[t]);
  j.voff = lrow * j.rs + (lane >> 4) * 32 + (lane & 3) * 8;
}
template <int JJ>
DI void prep_load(const PrepJob& j, u32x2 (&r)[8]) {
  const char* b = j.src + (JJ + j.c1 * (JJ >> 1) + j.c2 * (JJ >> 2)) * j.rs;
  asm volatile("global_load_dwordx2 %0, %1, %2" : "=v"(r[JJ]) : "v"(j.voff), "s"(b));
}
DI void prep_load_all(const PrepJob& j, u32x2 (&r)[8]) {
  prep_load<0>(j, r); prep_load<1>(j, r); prep_load<2>(j, r); prep_load<3>(j, r); prep_load<4>(j, r); prep_load<5>(j, r); prep_load<6>(j, r); prep_load<7>(j, r);
}
DI void prep_store(const PrepJob& j, const u32x2 (&r)[8], __amdgpu_buffer_rsrc_t rs, unsigned so) {
  u32x4 u[4];
#pragma unroll
  for (int i = 0; i < 4; ++i) {
    u[0][i] = __builtin_amdgcn_perm(r[2 * i + 1][0], r[2 * i][0], 0x05040100u);
    u[1][i] = __builtin_amdgcn_perm(r[2 * i + 1][0], r[2 * i][0], 0x07060302u);
    u[2][i] = __builtin_amdgcn_perm(r[2 * i + 1][1], r[2 * i][1], 0x05040100u);
    u[3][i] = __builtin_amdgcn_perm(r[2 * i + 1][1], r[2 * i][1], 0x07060302u);
  }
#pragma unroll
  for (int i = 0; i < 4; ++i) {
    const auto a0 = __builtin_amdgcn_permlane32_swap(u[0][i], u[2][i], false, false);
    const auto a1 = __builtin_amdgcn_permlane32_swap(u[1][i], u[3][i], false, false);
    const auto b0 = __builtin_amdgcn_permlane16_swap(a0[0], a1[0], false, false);
    const auto b1 = __builtin_amdgcn_permlane16_swap(a0[1], a1[1], false, false);
    u[0][i] = b0[0]; u[1][i] = b0[1]; u[2][i] = b1[0]; u[3][i] = b1[1];
  }
#pragma unroll
  for (int t = 0; t < 4; ++t) bst16<PREP_AUX>(u[t], rs, so, j.dst[t]);
}
#define PIN8(r) "+v"(r[0]), "+v"(r[1]), "+v"(r[2]), "+v"(r[3]), "+v"(r[4]), "+v"(r[5]), "+v"(r[6]), "+v"(r[7])

#define K1_PF 4
#define K1_TOUCH 1
#define K1_TSLEEP 16
#define K1_DEPTH 2
constexpr int K1_PITCH = 224, K1_SLOT = 16384 + 64 * K1_PITCH  , K1_NS = K1_DEPTH + 1;
constexpr int K1_SP = 232;
constexpr int K1_STG = K1_NS * K1_SLOT, K1_CS = K1_STG + 128 * K1_SP + 64, K1_ST = K1_CS + 512 + 64, K1_LDS = K1_ST + 2048;
struct K1C {
  LDSP const char *xa[2], *wl, *wc0, *wc1;
  u32x4 X[3][2], wf[K1_PF + 1], ones;
  f32x4 acc[4][2];
  f32x4 sm[2]; float sq[2];
};
DI void k1_loader(const char* xb, const char* wb, const u16* wo_, char* ws, int j, int lane, unsigned lds0) {
  __builtin_amdgcn_s_setprio(3);
  unsigned xo[2], wo[4];
  const int g = lane >> 4;
  xo[0] = (lane >> 3) * 1152 + (((lane & 7) ^ g) * 16);
  xo[1] = (8 + (lane >> 3)) * 1152 + (((lane & 7) ^ (4 + g)) * 16);
  xb += j * (32 * 1152);
  const unsigned xd = lds0 + j * 4096, wd = lds0 + 16384 + j * 1024;
#define K1_X(z, so) do { dma16(xb + (z) * 128, xo[0], xd + (so)); dma16(xb + (z) * 128, xo[1], xd + (so) + 1024);                    \
    dma16(xb + (z) * 128 + 16 * 1152, xo[0], xd + (so) + 2048); dma16(xb + (z) * 128 + 16 * 1152, xo[1], xd + (so) + 3072); } while (0)
#define K1_W(z, so) do { _Pragma("unroll") for (int i = 0; i < 3; ++i) dma16(wb + (z) * (64 * 3456), wo[i], wd + (so) + i * 4096);     \
    if (j < 2) dma16(wb + (z) * (64 * 3456), wo[3], wd + (so) + 3 * 4096); } while (0)
  K1_X(0, 0);
  SBAR();
#pragma unroll
  for (int i = 0; i < 4; ++i) {
    const unsigned u = 64 * (j + 4 * i) + lane, r = u / 14;
    wo[i] = ((r & ~12u) | ((r & 4u) << 1) | ((r & 8u) >> 1)) * 3456 + (u - r * 14) * 16;
  }
  K1_W(0, 0);
#pragma unroll
  for (int z = 1; z < K1_DEPTH; ++z) { K1_X(z, z * K1_SLOT); K1_W(z, z * K1_SLOT); }
  unsigned so = K1_DEPTH * K1_SLOT;
#pragma unroll 1
  for (int z = K1_DEPTH; z < 9; ++z) {
    asm volatile("s_waitcnt vmcnt(%0)\n\ts_barrier" ::"i"(7 * (K1_DEPTH - 1)) : "memory");
    K1_X(z, so); K1_W(z, so);
    so = so == (K1_NS - 1) * K1_SLOT ? 0 : so + K1_SLOT;
  }
  if (K1_DEPTH > 2) asm volatile("s_waitcnt vmcnt(14)\n\ts_barrier" ::: "memory");
  asm volatile("s_waitcnt vmcnt(7)\n\ts_barrier" ::: "memory");
  const int b = blockIdx.x;
  const bool dojob = j == 1 && b < NJ_WO, dorow = j >= 2 || (j == 0 && b < 64);
  const int br = j >= 2 ? 2 * b + j - 2 : 512 + (b & 63);
  PrepJob jb; u32x2 r[8]; unsigned wr[5];
  if (dojob) prep_setup(jb, NJ_W1 + NJ_WD + b, lane, wo_, wo_, wo_, wo_);
  const char* rp = (const char*)wo_ + (size_t)wo_row(br >> 5, (br >> 3) & 3, br & 7) * (DM * 2);
  SBAR();
  asm volatile("s_waitcnt vmcnt(0)\n\ts_barrier" ::: "memory");
  if (dojob) prep_load_all(jb, r);
  if (dorow) {
#pragma unroll
    for (int i = 0; i < 5; ++i) wr[i] = gld4(rp, (i < 4 ? lane + 64 * i : 256 + (lane & 31)) * 4);
  }
  asm volatile("s_barrier");
  asm volatile("s_barrier");
  const __amdgpu_buffer_rsrc_t wrs = mkrsrc(ws, (unsigned)WS_END);
  asm volatile("s_waitcnt vmcnt(0)" : PIN8(r), "+v"(wr[0]), "+v"(wr[1]), "+v"(wr[2]), "+v"(wr[3]), "+v"(wr[4])::"memory");
  if (dojob) {
#pragma unroll
    for (int t = 0; t < 4; ++t) jb.dst[t] = rfl(jb.dst[t]);
    prep_store(jb, r, wrs, ((lane >> 2) & 3) * 256 + ((lane & 3) * 4 + (lane >> 4)) * 16);
  }
  if (dorow) {
    float a = 0.f;
#pragma unroll
    for (int i = 0; i < 4; ++i) a = dot2(wr[i], 0x3f803f80u, a);
    a = dot2(lane >= 32 ? 0u : wr[4], 0x3f803f80u, a);
    a = xor16_sum(xor32_sum(a));
    a += __uint_as_float(__builtin_amdgcn_update_dpp(0u, __float_as_uint(a), 0x128, 0xf, 0xf, false));
    a += __uint_as_float(__builtin_amdgcn_update_dpp(0u, __float_as_uint(a), 0x124, 0xf, 0xf, false));
    a += __uint_as_float(__builtin_amdgcn_update_dpp(0u, __float_as_uint(a), 0x122, 0xf, 0xf, false));
    a += __uint_as_float(__builtin_amdgcn_update_dpp(0u, __float_as_uint(a), 0x121, 0xf, 0xf, false));
    a *= (1.0f / DM);
    const unsigned hi = cvtpk(a, 0.f) & 0xffffu;
    const unsigned lo = cvtpk(a - bflo(hi), 0.f) & 0xffffu;
    __builtin_amdgcn_raw_buffer_store_b16((u16)(lane ? lo : hi), wrs, lane < 2 ? (unsigned)WS_WBAR + (lane * DM + br) * 2 : 0x80000000u, 0, 16);
  }
#undef K1_X
#undef K1_W
}
DI u32x4 tr_frag(LDSP const char* p, int d) {
  s16x4 lo = __builtin_amdgcn_ds_read_tr16_b64_v4i16((LDSP s16x4*)p);
  s16x4 hi = __builtin_amdgcn_ds_read_tr16_b64_v4i16((LDSP s16x4*)(p + d));
  return __builtin_bit_cast(u32x4, __builtin_shufflevector(lo, hi, 0, 1, 2, 3, 4, 5, 6, 7));
}
template <int NHG, int F> DI void k1_read(K1C& c) {
  constexpr int NF = 4 + NHG, S = F / NF, j = F % NF, Z = S >> 1, so = (Z % K1_NS) * K1_SLOT;
  if constexpr (j == 0) {
    c.X[S % 3][0] = ldsr16(c.xa[S & 1] + so);
    c.X[S % 3][1] = ldsr16(c.xa[S & 1] + so + 2048);
  }
  constexpr int wo = so + (S & 1) * 7168;
  if constexpr (NHG == 0 || j < 3) c.wf[F % (K1_PF + 1)] = tr_frag(c.wl + wo + j * 32, 1792);
  else c.wf[F % (K1_PF + 1)] = tr_frag((j == 3 ? c.wc0 : c.wc1) + wo, 1792);
}
template <int NHG, int F> DI void k1_frag(K1C& c) {
  constexpr int NF = 4 + NHG, S = F / NF, j = F % NF, Z = S >> 1, N = F + K1_PF, fz = F % (2 * NF);
  if constexpr (S == 0) {
    if constexpr (NHG == 0 || j < 3) { G_MFMA0(c.acc[j][0], c.wf[F % (K1_PF + 1)], c.X[0][0]); G_MFMA0(c.acc[j][1], c.wf[F % (K1_PF + 1)], c.X[0][1]); }
    else G_MFMA0(c.acc[3][j - 3], c.wf[F % (K1_PF + 1)], c.ones);
  } else {
    if constexpr (NHG == 0 || j < 3) { G_MFMA(c.acc[j][0], c.wf[F % (K1_PF + 1)], c.X[S % 3][0]); G_MFMA(c.acc[j][1], c.wf[F % (K1_PF + 1)], c.X[S % 3][1]); }
    else G_MFMA(c.acc[3][j - 3], c.wf[F % (K1_PF + 1)], c.ones);
  }
  if constexpr ((S & 1) == NHG && j < 4) {
#pragma unroll
    for (int t = 0; t < 2; ++t) { const unsigned v = c.X[S % 3][t][j]; c.sq[t] = dot2(v, v, c.sq[t]); PIN(c.sq[t]); }
    if constexpr (j < 2) { if constexpr (S < 2) G_MFMA0(c.sm[j], c.ones, c.X[S % 3][j]); else G_MFMA(c.sm[j], c.ones, c.X[S % 3][j]); }
  }
  if constexpr (N < 18 * NF) {
    if constexpr (N % (2 * NF) == 0) {
      asm volatile("s_waitcnt lgkmcnt(0)\n\ts_barrier" ::: "memory");
    }
    k1_read<NHG, N>(c);
  }
  SBAR();
}
template <int NHG, int... Fs> DI void k1_run(K1C& c, std::integer_sequence<int, Fs...>) {
  k1_read<NHG, 0>(c); k1_read<NHG, 1>(c); k1_read<NHG, 2>(c);
  if (K1_PF > 3) k1_read<NHG, 3>(c);
  if (K1_PF > 4) k1_read<NHG, 4>(c);
  if (K1_PF > 5) k1_read<NHG, 5>(c);
  SBAR();
  (k1_frag<NHG, Fs>(c), ...);
  asm volatile("s_nop 7" : "+v"(c.acc[0][0]), "+v"(c.acc[0][1]), "+v"(c.acc[1][0]), "+v"(c.acc[1][1]), "+v"(c.acc[2][0]), "+v"(c.acc[2][1]),
               "+v"(c.acc[3][0]), "+v"(c.acc[3][1]), "+v"(c.sm[0]), "+v"(c.sm[1]));
  SBAR();
}
extern "C" __global__ __launch_bounds__(768, 3) void k_qkv(const u16* x, const u16* wqkv, const u16* wo, char* ws) {
  extern __shared__ __attribute__((aligned(16))) char smem[];
  const int tid = threadIdx.x, lane = tid & 63, w = rfl(tid >> 6);
  const int xcd = blockIdx.x & 7, bi = blockIdx.x >> 3;
  unsigned pf;
  CODE_PF2(pf, k_qkv, k_end, 0, bi, (w == 8 && bi < 9) ? 128u : 0u);
  SBAR();
  asm volatile("" : "+s"(x), "+s"(wqkv), "+s"(wo), "+s"(ws));
  LDSP char* lds = (LDSP char*)smem;
  const unsigned lds0 = (unsigned)(uintptr_t)lds;
  const int rb = 4 * (xcd >> 1) + (bi & 3), cb = 8 * (xcd & 1) + (bi >> 2);
  const int c0 = 108 * cb - 4 * (cb & 1);
  if (__builtin_expect(w >= 8, 1)) {
    k1_loader((const char*)x + rb * (128 * 1152), (const char*)wqkv + c0 * 2, wo, ws, w - 8, lane, lds0);
    asm volatile("" ::"v"(pf));
    return;
  }
  const int m = lane & 15, g = lane >> 4, mg = w & 3, nh = w >> 2;
  STAMP_DECL; STAMP(0);
  if (blockIdx.x == 8 && threadIdx.x == 0) ((__attribute__((address_space(1))) unsigned long long*)(ws + (200u << 20)))[0] = __builtin_amdgcn_s_memrealtime();
  K1C c;
  c.ones = u32x4{0x3f803f80u, 0x3f803f80u, 0x3f803f80u, 0x3f803f80u};
  PIN(c.ones);
  c.xa[0] = lds + (32 * mg + m) * 128 + ((g ^ (m >> 1)) * 16);
  c.xa[1] = lds + (32 * mg + m) * 128 + (((4 + g) ^ (m >> 1)) * 16);
  LDSP const char* wbase = lds + 16384 + (16 * (g >> 1) + 4 * (g & 1) + (m >> 2)) * K1_PITCH + (m & 3) * 8;
  c.wl = wbase + nh * 128;
  c.wc0 = wbase + mg * 32;
  c.wc1 = wbase + (mg < 3 ? mg + 4 : 6) * 32;
  c.sq[0] = c.sq[1] = 0.f;
  const int off = 4 * (cb & 1), T0 = 128 * rb;
  LDSP char* stg = lds + K1_STG;
  LDSP char* a_st = lds + K1_ST + (32 * mg + m) * 8;                 PIN(a_st);
  LDSP char* a_cs = lds + K1_CS + (64 * nh + 4 * g) * 4;             PIN(a_cs);
  LDSP char* a_sw = stg + (32 * mg + m) * K1_SP + (64 * nh + 4 * g) * 2;   PIN(a_sw);
  LDSP char* a_tr = stg + (32 * mg + 4 * (g & 1) + (m >> 2)) * K1_SP + (64 * nh + 16 * (g >> 1) + 4 * (m & 3)) * 2;   PIN(a_tr);
  float qs[4];
#pragma unroll
  for (int j = 0; j < 4; ++j) { qs[j] = (c0 + 64 * nh + 16 * j + 4 * g < DM) ? QSCALE : 1.0f; PIN(qs[j]); }
  const __amdgpu_buffer_rsrc_t wrs = mkrsrc(ws, (unsigned)WS_END);
  constexpr unsigned DROP = 0x80000000u;
  const unsigned kvo = (unsigned)WS_KV + (T0 >> 9) * (unsigned)CHUNK_B;
  unsigned vo[2] = {0, 0}, vone = 0, vd[4] = {DROP, DROP, DROP, DROP};
  if (cb >= 10) {
#pragma unroll
  for (int tt = 0; tt < 2; ++tt) {
    const int cc = 64 * nh + 32 * tt + 16 * (g >> 1) + m - off, hd = (cc + 18) / 18 - 1, G = 6 * cb + hd;
    vo[tt] = (cc >= 0 && cc < 108) ? hd * 4864 + 2 * mg * 608 + (g & 1) * 304 + (cc - 18 * hd) * 16 : 29184;
  }
  vone = tid < 96 ? (tid >> 4) * 4864 + (tid & 15) * 304 + 288 : 29184;
#pragma unroll
  for (int i = 0; i < 4; ++i) {
    const unsigned u = i * 512 + tid, hd = u / 304;
    vd[i] = (u < 1824 && 6 * cb + hd >= 64) ? kvo + 18432 + ((T0 >> 4) & 31) * 608 + (6 * cb + hd - 64) * (unsigned)KV_HEAD_B + (u - hd * 304) * 16 : DROP;
  }
  }
  PIN(vone); PIN(vo[0]); PIN(vo[1]); PIN(vd[0]); PIN(vd[1]); PIN(vd[2]); PIN(vd[3]);
  const int tok = tid & 127, T = T0 + tok;
  LDSP const char* a_g = stg + tok * K1_SP + off * 2;   PIN(a_g);
  unsigned um[2], ut[2];
  const unsigned tl = ((T >> 5) & 15) * 1024 + (T & 31) * 16;
#pragma unroll
  for (int it = 0; it < 2; ++it) {
    const int hd = (w >> 1) + 4 * it, G = 6 * cb + hd, head = G & 31;
    if (hd >= 6 || G >= 64) { um[it] = DROP; ut[it] = DROP; }
    else if (G < 32) { um[it] = (unsigned)WS_QM + head * 65536 + (T >> 9) * 16384 + tl; ut[it] = (unsigned)WS_QT + head * 8192 + T * 4; }
    else { um[it] = kvo + head * (unsigned)KV_HEAD_B + tl; ut[it] = kvo + head * (unsigned)KV_HEAD_B + 16384 + ((T >> 6) & 7) * 256 + (T & 31) * 8 + ((T >> 5) & 1) * 4; }
    PIN(um[it]); PIN(ut[it]);
  }
  STAMP(1);
  asm volatile("s_waitcnt vmcnt(0)" : "+v"(pf));
  if (blockIdx.x < 5 && w == 7) bst16<16>(u32x4{0, 0, 0, 0}, wrs, (unsigned)WS_CNT + (blockIdx.x * 64 + lane) * 128, 0);
  asm volatile("s_barrier" ::: "memory");
  STAMP(2);
  if (nh) k1_run<1>(c, std::make_integer_sequence<int, 90>{});
  else k1_run<0>(c, std::make_integer_sequence<int, 72>{});
  STAMP(3);
#pragma unroll
  for (int t = 0; t < 2; ++t) {
    const f32x2 st = {c.sm[t][0], xor32_sum(xor16_sum(c.sq[t]))};
    *(LDSP f32x2*)(a_st + nh * 1024 + t * 128) = st;
  }
  if (nh) {
    *(LDSP f32x4*)(lds + K1_CS + (16 * mg + 4 * g) * 4) = c.acc[3][0];
    *(LDSP f32x4*)(lds + K1_CS + (16 * (mg + 4) + 4 * g) * 4) = c.acc[3][1];
  }
  asm volatile("s_waitcnt lgkmcnt(0)\n\ts_barrier" ::: "memory");
  {
    f32x2 p[2][2]; f32x4 cc[4];
#pragma unroll
    for (int t = 0; t < 2; ++t) { p[t][0] = *(LDSP const f32x2*)(a_st + t * 128); p[t][1] = *(LDSP const f32x2*)(a_st + 1024 + t * 128); }
#pragma unroll
    for (int j = 0; j < 4; ++j) cc[j] = *(LDSP const f32x4*)(a_cs + j * 64);
    SBAR();
#pragma unroll
    for (int t = 0; t < 2; ++t) {
      const float mean = (p[t][0][0] + p[t][1][0]) * (1.0f / DM);
      const float var = fmaxf((p[t][0][1] + p[t][1][1]) * (1.0f / DM) - mean * mean, 0.f);
      const float rstd = __builtin_amdgcn_rsqf(var + LN_EPS);
      if (cb == 0 && nh == 0) __builtin_amdgcn_raw_buffer_store_b32(__float_as_uint(mean), wrs, (unsigned)WS_XMEAN + (T0 + 32 * mg + 16 * t + m) * 4, 0, QKV_AUX);
#pragma unroll
      for (int j = 0; j < 4; ++j) {
        if (j < 3 || nh == 0) {
          const f32x4 a = (c.acc[j][t] - mean * cc[j]) * (rstd * qs[j]);
          *(LDSP u32x2*)(a_sw + t * (16 * K1_SP) + j * 32) = u32x2{cvtpk(a[0], a[1]), cvtpk(a[2], a[3])};
        }
      }
    }
  }
  if (cb >= 10) {
    asm volatile("s_waitcnt lgkmcnt(0)" ::: "memory");
    u32x4 v[2][2];
#pragma unroll
    for (int tt = 0; tt < 2; ++tt)
#pragma unroll
      for (int t = 0; t < 2; ++t) v[tt][t] = tr_frag(a_tr + t * (16 * K1_SP) + tt * 64, 8 * K1_SP);
    *(LDSP u32x4*)(lds + vone) = c.ones;
#pragma unroll
    for (int tt = 0; tt < 2; ++tt)
#pragma unroll
      for (int t = 0; t < 2; ++t) *(LDSP u32x4*)(lds + vo[tt] + t * 608) = v[tt][t];
  }
  STAMP(4);
  asm volatile("s_waitcnt lgkmcnt(0)\n\ts_barrier" ::: "memory");
  if (cb >= 10) {
    u32x4 v[4];
#pragma unroll
    for (int i = 0; i < 4; ++i) v[i] = ldsr16(lds + i * 8192 + tid * 16);
#pragma unroll
    for (int i = 0; i < 4; ++i) bst16<V_AUX>(v[i], wrs, vd[i], 0);
  }
  if (cb <= 10) {
    unsigned d[2][9];
#pragma unroll
    for (int it = 0; it < 2; ++it) {
      const int hd = (w >> 1) + 4 * it;
      LDSP const unsigned* sp = (LDSP const unsigned*)(a_g + 36 * (hd < 6 ? hd : 0));
#pragma unroll
      for (int i = 0; i < 9; ++i) d[it][i] = sp[i];
    }
    SBAR();
#pragma unroll
    for (int it = 0; it < 2; ++it) {
      bst16<QKV_AUX>(u32x4{d[it][0], d[it][1], d[it][2], d[it][3]}, wrs, um[it], 0);
      bst16<QKV_AUX>(u32x4{d[it][4], d[it][5], d[it][6], d[it][7]}, wrs, um[it] + 512, 0);
      __builtin_amdgcn_raw_buffer_store_b32(d[it][8], wrs, ut[it], 0, QKT_AUX);
    }
#pragma unroll
    for (int it = 0; it < 2; ++it) {
      const int hd = (w >> 1) + 4 * it, G = 6 * cb + hd;
      if (hd < 6 && G >= 32 && G < 64) {
        float n2 = 0.f;
#pragma unroll
        for (int i = 0; i < 9; ++i) n2 = dot2(d[it][i], d[it][i], n2);
        unsigned nb = __float_as_uint(n2);
        { auto r = __builtin_amdgcn_permlane32_swap(nb, nb, false, false); nb = max(r[0], r[1]); }
        { auto r = __builtin_amdgcn_permlane16_swap(nb, nb, false, false); nb = max(r[0], r[1]); }
        __builtin_amdgcn_raw_buffer_store_b32(nb, wrs, (unsigned)WS_KMAX + (((2 * rb + (w & 1)) * 32 + (G & 31)) * 16 + m) * 4, 0, QKV_AUX);
      }
    }
  }
  STAMP(5); STAMP_OUT(1, 6);
}

constexpr int K2_LDS = (int)KV_HEAD_B, K3_WBAR = 90 * 1024, K3_XC = 96 * 1024;
#define A_MFMA_ACC(d, a, b) asm volatile("v_mfma_f32_32x32x16_bf16 %0, %1, %2, %0" : "+v"(d) : "v"(a), "v"(b))
#define AT_M(m)
#define AT_U
struct AttnC {
  LDSP const char *kmb, *ktb, *vb;
  unsigned kma[3], kta[3], va[3]; unsigned long long mk, mv;
  u32x4 qm, qt, kf, v0, v1, p1; u32x2 ktl;
  f32x16 negm, o, sa, sb;
};
constexpr int k_off(int T) { return (T >> 4) * (int)CHUNK_B + (T & 15) * 1024; }
constexpr int kt_off(int T) { return (T >> 4) * (int)CHUNK_B + ((T >> 1) & 7) * 256; }
constexpr int v_off(int T) { return (T >> 4) * (int)CHUNK_B + (T & 15) * 1216; }
#define AT_STMT(TXT)                                                                                                              \
  asm volatile(TXT : "+{v[2:17]}"(c.o), "+{v[34:49]}"(c.sa), "+{v[50:65]}"(c.sb), "+{v[66:69]}"(c.kf), "+{v[70:73]}"(c.p1),        \
               "+{v[84:87]}"(c.v1), "+{v[88:91]}"(c.v0), "+{v[92:93]}"(c.ktl)                                                      \
               : "{v[18:33]}"(c.negm), "{v[98:101]}"(c.qm), "{v[102:105]}"(c.qt), [ka] "v"(c.kma[KO >> 16]), [kt] "v"(c.kta[KT >> 16]), \
                 [va] "v"(c.va[V1 >> 16]), [vb] "v"(c.va[V0 >> 16]), [ko] "i"(KO & 0xffff), [kto] "i"(KT & 0xffff), [vo1] "i"(V1 & 0xffff),  \
                 [vo0] "i"(V0 & 0xffff), [mk] "s"(c.mk), [mv] "s"(c.mv)                                                            \
               : "v74", "v75", "v76", "v77")
template <int T> DI void attn_tile(AttnC& c) {
  constexpr int KO = k_off(T < 62 ? T + 2 : 0), KT = kt_off(T < 62 ? T + 2 : 0), V1 = v_off(T) + 608, V0 = v_off(T < 63 ? T + 1 : 0);
  if constexpr (T == 0)
    AT_STMT("v_mfma_f32_32x32x8_bf16 v[50:65], v[92:93], v[104:105], v[18:33]\n\t" "v_exp_f32 v34, v34\n\t" "v_exp_f32 v35, v35\n\t" "v_exp_f32 v36, v36\n\t" "v_exp_f32 v37, v37\n\t" AT_M("mk") "ds_read_b64 v[92:93], %[kt] offset:%[kto]\n\t" AT_U AT_M("mv") "ds_read_b128 v[84:87], %[va] offset:%[vo1]\n\t" AT_U "v_cvt_pk_bf16_f32 v74, v34, v35\n\t" "v_exp_f32 v38, v38\n\t" "v_cvt_pk_bf16_f32 v75, v36, v37\n\t" "v_exp_f32 v39, v39\n\t" "v_exp_f32 v40, v40\n\t" "v_exp_f32 v41, v41\n\t" "s_waitcnt lgkmcnt(2)\n\t" "v_mfma_f32_32x32x16_bf16 v[50:65], v[66:69], v[98:101], v[50:65]\n\t" "ds_read_b128 v[66:69], %[ka] offset:%[ko]\n\t" "v_cvt_pk_bf16_f32 v76, v38, v39\n\t" "v_exp_f32 v42, v42\n\t" "v_cvt_pk_bf16_f32 v77, v40, v41\n\t" "v_exp_f32 v43, v43\n\t" "v_exp_f32 v44, v44\n\t" "v_exp_f32 v45, v45\n\t" "s_waitcnt lgkmcnt(2)\n\t" "v_mfma_f32_32x32x16_bf16 v[2:17], v[88:91], v[74:77], v[2:17]\n\t" AT_M("mv") "ds_read_b128 v[88:91], %[vb] offset:%[vo0]\n\t" AT_U "v_cvt_pk_bf16_f32 v70, v42, v43\n\t" "v_exp_f32 v46, v46\n\t" "v_cvt_pk_bf16_f32 v71, v44, v45\n\t" "v_exp_f32 v47, v47\n\t" "v_exp_f32 v48, v48\n\t" "v_exp_f32 v49, v49\n\t" "v_cvt_pk_bf16_f32 v72, v46, v47\n\t" "v_cvt_pk_bf16_f32 v73, v48, v49\n\t");
  else if constexpr (T == 62)
    AT_STMT("v_mfma_f32_32x32x8_bf16 v[50:65], v[92:93], v[104:105], v[18:33]\n\t" "v_exp_f32 v34, v34\n\t" "v_exp_f32 v35, v35\n\t" "v_exp_f32 v36, v36\n\t" "v_exp_f32 v37, v37\n\t" "s_waitcnt lgkmcnt(2)\n\t" "v_mfma_f32_32x32x16_bf16 v[2:17], v[84:87], v[70:73], v[2:17]\n\t" AT_M("mv") "ds_read_b128 v[84:87], %[va] offset:%[vo1]\n\t" AT_U "v_cvt_pk_bf16_f32 v74, v34, v35\n\t" "v_exp_f32 v38, v38\n\t" "v_cvt_pk_bf16_f32 v75, v36, v37\n\t" "v_exp_f32 v39, v39\n\t" "v_exp_f32 v40, v40\n\t" "v_exp_f32 v41, v41\n\t" "s_waitcnt lgkmcnt(2)\n\t" "v_mfma_f32_32x32x16_bf16 v[50:65], v[66:69], v[98:101], v[50:65]\n\t" "v_cvt_pk_bf16_f32 v76, v38, v39\n\t" "v_exp_f32 v42, v42\n\t" "v_cvt_pk_bf16_f32 v77, v40, v41\n\t" "v_exp_f32 v43, v43\n\t" "v_exp_f32 v44, v44\n\t" "v_exp_f32 v45, v45\n\t" "s_waitcnt lgkmcnt(1)\n\t" "v_mfma_f32_32x32x16_bf16 v[2:17], v[88:91], v[74:77], v[2:17]\n\t" AT_M("mv") "ds_read_b128 v[88:91], %[vb] offset:%[vo0]\n\t" AT_U "v_cvt_pk_bf16_f32 v70, v42, v43\n\t" "v_exp_f32 v46, v46\n\t" "v_cvt_pk_bf16_f32 v71, v44, v45\n\t" "v_exp_f32 v47, v47\n\t" "v_exp_f32 v48, v48\n\t" "v_exp_f32 v49, v49\n\t" "v_cvt_pk_bf16_f32 v72, v46, v47\n\t" "v_cvt_pk_bf16_f32 v73, v48, v49\n\t");
  else if constexpr (T == 63)
    AT_STMT("v_exp_f32 v50, v50\n\t" "v_exp_f32 v51, v51\n\t" "v_exp_f32 v52, v52\n\t" "v_exp_f32 v53, v53\n\t" "s_waitcnt lgkmcnt(0)\n\t" "v_mfma_f32_32x32x16_bf16 v[2:17], v[84:87], v[70:73], v[2:17]\n\t" AT_M("mv") "ds_read_b128 v[84:87], %[va] offset:%[vo1]\n\t" AT_U "v_cvt_pk_bf16_f32 v74, v50, v51\n\t" "v_exp_f32 v54, v54\n\t" "v_cvt_pk_bf16_f32 v75, v52, v53\n\t" "v_exp_f32 v55, v55\n\t" "v_exp_f32 v56, v56\n\t" "v_exp_f32 v57, v57\n\t" "v_cvt_pk_bf16_f32 v76, v54, v55\n\t" "v_exp_f32 v58, v58\n\t" "v_cvt_pk_bf16_f32 v77, v56, v57\n\t" "v_exp_f32 v59, v59\n\t" "v_exp_f32 v60, v60\n\t" "v_exp_f32 v61, v61\n\t" "s_waitcnt lgkmcnt(1)\n\t" "v_mfma_f32_32x32x16_bf16 v[2:17], v[88:91], v[74:77], v[2:17]\n\t" "v_cvt_pk_bf16_f32 v70, v58, v59\n\t" "v_exp_f32 v62, v62\n\t" "v_cvt_pk_bf16_f32 v71, v60, v61\n\t" "v_exp_f32 v63, v63\n\t" "v_exp_f32 v64, v64\n\t" "v_exp_f32 v65, v65\n\t" "v_cvt_pk_bf16_f32 v72, v62, v63\n\t" "v_cvt_pk_bf16_f32 v73, v64, v65\n\t");
  else if constexpr ((T & 3) == 0)
    AT_STMT("v_mfma_f32_32x32x8_bf16 v[50:65], v[92:93], v[104:105], v[18:33]\n\t" "v_exp_f32 v34, v34\n\t" "v_exp_f32 v35, v35\n\t" "v_exp_f32 v36, v36\n\t" "v_exp_f32 v37, v37\n\t" "s_waitcnt lgkmcnt(2)\n\t" "v_mfma_f32_32x32x16_bf16 v[2:17], v[84:87], v[70:73], v[2:17]\n\t" AT_M("mk") "ds_read_b64 v[92:93], %[kt] offset:%[kto]\n\t" AT_U AT_M("mv") "ds_read_b128 v[84:87], %[va] offset:%[vo1]\n\t" AT_U "v_cvt_pk_bf16_f32 v74, v34, v35\n\t" "v_exp_f32 v38, v38\n\t" "v_cvt_pk_bf16_f32 v75, v36, v37\n\t" "v_exp_f32 v39, v39\n\t" "v_exp_f32 v40, v40\n\t" "v_exp_f32 v41, v41\n\t" "s_waitcnt lgkmcnt(2)\n\t" "v_mfma_f32_32x32x16_bf16 v[50:65], v[66:69], v[98:101], v[50:65]\n\t" "ds_read_b128 v[66:69], %[ka] offset:%[ko]\n\t" "v_cvt_pk_bf16_f32 v76, v38, v39\n\t" "v_exp_f32 v42, v42\n\t" "v_cvt_pk_bf16_f32 v77, v40, v41\n\t" "v_exp_f32 v43, v43\n\t" "v_exp_f32 v44, v44\n\t" "v_exp_f32 v45, v45\n\t" "s_waitcnt lgkmcnt(2)\n\t" "v_mfma_f32_32x32x16_bf16 v[2:17], v[88:91], v[74:77], v[2:17]\n\t" AT_M("mv") "ds_read_b128 v[88:91], %[vb] offset:%[vo0]\n\t" AT_U "v_cvt_pk_bf16_f32 v70, v42, v43\n\t" "v_exp_f32 v46, v46\n\t" "v_cvt_pk_bf16_f32 v71, v44, v45\n\t" "v_exp_f32 v47, v47\n\t" "v_exp_f32 v48, v48\n\t" "v_exp_f32 v49, v49\n\t" "v_cvt_pk_bf16_f32 v72, v46, v47\n\t" "v_cvt_pk_bf16_f32 v73, v48, v49\n\t");
  else if constexpr ((T & 3) == 1)
    AT_STMT("v_mfma_f32_32x32x8_bf16 v[34:49], v[92:93], v[102:103], v[18:33]\n\t" "v_exp_f32 v50, v50\n\t" "v_exp_f32 v51, v51\n\t" "v_exp_f32 v52, v52\n\t" "v_exp_f32 v53, v53\n\t" "s_waitcnt lgkmcnt(2)\n\t" "v_mfma_f32_32x32x16_bf16 v[2:17], v[84:87], v[70:73], v[2:17]\n\t" AT_M("mv") "ds_read_b128 v[84:87], %[va] offset:%[vo1]\n\t" AT_U "v_cvt_pk_bf16_f32 v74, v50, v51\n\t" "v_exp_f32 v54, v54\n\t" "v_cvt_pk_bf16_f32 v75, v52, v53\n\t" "v_exp_f32 v55, v55\n\t" "v_exp_f32 v56, v56\n\t" "v_exp_f32 v57, v57\n\t" "s_waitcnt lgkmcnt(2)\n\t" "v_mfma_f32_32x32x16_bf16 v[34:49], v[66:69], v[98:101], v[34:49]\n\t" "ds_read_b128 v[66:69], %[ka] offset:%[ko]\n\t" "v_cvt_pk_bf16_f32 v76, v54, v55\n\t" "v_exp_f32 v58, v58\n\t" "v_cvt_pk_bf16_f32 v77, v56, v57\n\t" "v_exp_f32 v59, v59\n\t" "v_exp_f32 v60, v60\n\t" "v_exp_f32 v61, v61\n\t" "s_waitcnt lgkmcnt(2)\n\t" "v_mfma_f32_32x32x16_bf16 v[2:17], v[88:91], v[74:77], v[2:17]\n\t" AT_M("mv") "ds_read_b128 v[88:91], %[vb] offset:%[vo0]\n\t" AT_U "v_cvt_pk_bf16_f32 v70, v58, v59\n\t" "v_exp_f32 v62, v62\n\t" "v_cvt_pk_bf16_f32 v71, v60, v61\n\t" "v_exp_f32 v63, v63\n\t" "v_exp_f32 v64, v64\n\t" "v_exp_f32 v65, v65\n\t" "v_cvt_pk_bf16_f32 v72, v62, v63\n\t" "v_cvt_pk_bf16_f32 v73, v64, v65\n\t");
  else if constexpr ((T & 3) == 2)
    AT_STMT("v_mfma_f32_32x32x8_bf16 v[50:65], v[92:93], v[104:105], v[18:33]\n\t" "v_exp_f32 v34, v34\n\t" "v_exp_f32 v35, v35\n\t" "v_exp_f32 v36, v36\n\t" "v_exp_f32 v37, v37\n\t" "s_waitcnt lgkmcnt(2)\n\t" "v_mfma_f32_32x32x16_bf16 v[2:17], v[84:87], v[70:73], v[2:17]\n\t" AT_M("mk") "ds_read_b64 v[92:93], %[kt] offset:%[kto]\n\t" AT_U AT_M("mv") "ds_read_b128 v[84:87], %[va] offset:%[vo1]\n\t" AT_U "v_cvt_pk_bf16_f32 v74, v34, v35\n\t" "v_exp_f32 v38, v38\n\t" "v_cvt_pk_bf16_f32 v75, v36, v37\n\t" "v_exp_f32 v39, v39\n\t" "v_exp_f32 v40, v40\n\t" "v_exp_f32 v41, v41\n\t" "s_waitcnt lgkmcnt(2)\n\t" "v_mfma_f32_32x32x16_bf16 v[50:65], v[66:69], v[98:101], v[50:65]\n\t" "ds_read_b128 v[66:69], %[ka] offset:%[ko]\n\t" "v_cvt_pk_bf16_f32 v76, v38, v39\n\t" "v_exp_f32 v42, v42\n\t" "v_cvt_pk_bf16_f32 v77, v40, v41\n\t" "v_exp_f32 v43, v43\n\t" "v_exp_f32 v44, v44\n\t" "v_exp_f32 v45, v45\n\t" "s_waitcnt lgkmcnt(2)\n\t" "v_mfma_f32_32x32x16_bf16 v[2:17], v[88:91], v[74:77], v[2:17]\n\t" AT_M("mv") "ds_read_b128 v[88:91], %[vb] offset:%[vo0]\n\t" AT_U "v_cvt_pk_bf16_f32 v70, v42, v43\n\t" "v_exp_f32 v46, v46\n\t" "v_cvt_pk_bf16_f32 v71, v44, v45\n\t" "v_exp_f32 v47, v47\n\t" "v_exp_f32 v48, v48\n\t" "v_exp_f32 v49, v49\n\t" "v_cvt_pk_bf16_f32 v72, v46, v47\n\t" "v_cvt_pk_bf16_f32 v73, v48, v49\n\t");
  else if constexpr ((T & 3) == 3)
    AT_STMT("v_mfma_f32_32x32x8_bf16 v[34:49], v[92:93], v[102:103], v[18:33]\n\t" "v_exp_f32 v50, v50\n\t" "v_exp_f32 v51, v51\n\t" "v_exp_f32 v52, v52\n\t" "v_exp_f32 v53, v53\n\t" "s_waitcnt lgkmcnt(2)\n\t" "v_mfma_f32_32x32x16_bf16 v[2:17], v[84:87], v[70:73], v[2:17]\n\t" AT_M("mv") "ds_read_b128 v[84:87], %[va] offset:%[vo1]\n\t" AT_U "v_cvt_pk_bf16_f32 v74, v50, v51\n\t" "v_exp_f32 v54, v54\n\t" "v_cvt_pk_bf16_f32 v75, v52, v53\n\t" "v_exp_f32 v55, v55\n\t" "v_exp_f32 v56, v56\n\t" "v_exp_f32 v57, v57\n\t" "s_waitcnt lgkmcnt(2)\n\t" "v_mfma_f32_32x32x16_bf16 v[34:49], v[66:69], v[98:101], v[34:49]\n\t" "ds_read_b128 v[66:69], %[ka] offset:%[ko]\n\t" "v_cvt_pk_bf16_f32 v76, v54, v55\n\t" "v_exp_f32 v58, v58\n\t" "v_cvt_pk_bf16_f32 v77, v56, v57\n\t" "v_exp_f32 v59, v59\n\t" "v_exp_f32 v60, v60\n\t" "v_exp_f32 v61, v61\n\t" "s_waitcnt lgkmcnt(2)\n\t" "v_mfma_f32_32x32x16_bf16 v[2:17], v[88:91], v[74:77], v[2:17]\n\t" AT_M("mv") "ds_read_b128 v[88:91], %[vb] offset:%[vo0]\n\t" AT_U "v_cvt_pk_bf16_f32 v70, v58, v59\n\t" "v_exp_f32 v62, v62\n\t" "v_cvt_pk_bf16_f32 v71, v60, v61\n\t" "v_exp_f32 v63, v63\n\t" "v_exp_f32 v64, v64\n\t" "v_exp_f32 v65, v65\n\t" "v_cvt_pk_bf16_f32 v72, v62, v63\n\t" "v_cvt_pk_bf16_f32 v73, v64, v65\n\t");
}
DI void attn_group_slow(LDSP const char* km, LDSP const char* kt, LDSP const char* vp, const u32x4& qm, unsigned qtl, const f32x16& negm, f32x16& o) {
#pragma unroll
  for (int t = 0; t < 4; ++t) {
    const u32x2 k2 = *(LDSP const u32x2*)(kt + (t >> 1) * 256);
    const u32x4 ktl = {k2[0], k2[1], 0, 0};
    const u32x4 kf = ldsr16(km + t * 1024);
    const u32x4 v0 = ldsr16(vp + t * 1216), v1 = ldsr16(vp + t * 1216 + 608);
    u32x4 qq = {0, 0, 0, 0}; qq[t & 1] = qtl;
    f32x16 s = mfma32(ktl, qq, negm);
    s = mfma32(kf, qm, s);
    u32x4 p0, p1;
#pragma unroll
    for (int i = 0; i < 4; ++i) {
      p0[i] = cvtpk(__builtin_amdgcn_exp2f(s[2 * i]), __builtin_amdgcn_exp2f(s[2 * i + 1]));
      p1[i] = cvtpk(__builtin_amdgcn_exp2f(s[8 + 2 * i]), __builtin_amdgcn_exp2f(s[8 + 2 * i + 1]));
    }
    o = mfma32(v0, p0, o);
    o = mfma32(v1, p1, o);
  }
}
template <int G> DI void attn_group(AttnC& c) {
  __builtin_amdgcn_s_setprio(G < 5 ? 3 - (G & 3) : (G < 8 ? 3 : (G < 11 ? 2 : (G < 14 ? 1 : 0))));
  attn_tile<4 * G + 0>(c); attn_tile<4 * G + 1>(c); attn_tile<4 * G + 2>(c); attn_tile<4 * G + 3>(c);
}
DI void attn_helper(const u16* wg, const u16* wu, const u16* wd, char* ws, int head, int j, int lane, unsigned lds0) {
  __builtin_amdgcn_s_setprio(HELP_PRIO);
  const char* kv = ws + WS_KV + (size_t)head * KV_HEAD_B;
  const unsigned lane16 = lane * 16;
#define KV_DMA(o) dma16(kv + (o), lane16, lds0 + (o))
#pragma unroll
  for (int i = 0; i < 4; ++i) KV_DMA(((i >> 1) * 16 + (i & 1) * 4 + j) * 1024);
#pragma unroll
  for (int i = 0; i < 6; ++i) { const int p = i < 2 ? 8 + 4 * i + j : min(16 + 4 * i + j, 36); KV_DMA(p * 1024); }
  const int hw = blockIdx.x * 4 + j;
  PrepJob jb[4];
#pragma unroll
  for (int i = 0; i < 4; ++i) { prep_setup(jb[i], min(4 * hw + i, NJ_W1 + NJ_WD - 1), lane, wd, wg, wu, wd); PIN(jb[i].voff); }
  SBAR();
  asm volatile("s_waitcnt vmcnt(6)\n\ts_barrier" ::: "memory");
#pragma unroll
  for (int i = 0; i < 10; ++i) KV_DMA((unsigned)CHUNK_B + min(4 * i + j, 36) * 1024);
  asm volatile("s_waitcnt vmcnt(10)\n\ts_barrier" ::: "memory");
#pragma unroll
  for (int i = 0; i < 19; ++i) KV_DMA(2 * (unsigned)CHUNK_B + min(4 * i + j, 73) * 1024);
#undef KV_DMA
  u32x2 r[4][8];
  unsigned pf;
  asm volatile("s_waitcnt vmcnt(19)\n\ts_barrier" ::: "memory");
  prep_load_all(jb[0], r[0]); prep_load_all(jb[1], r[1]);
  asm volatile("s_waitcnt vmcnt(16)\n\ts_barrier" ::: "memory");
  const __amdgpu_buffer_rsrc_t wrs = mkrsrc(ws, (unsigned)WS_END);
  const unsigned pso = ((lane >> 2) & 3) * 256 + ((lane & 3) * 4 + (lane >> 4)) * 16;
  prep_load_all(jb[2], r[2]); prep_load_all(jb[3], r[3]);
  CODE_PF2(pf, k_ffn, k_end, 0, j, (blockIdx.x >> 3) == 0 ? 128u : 0u);
  asm volatile("s_waitcnt vmcnt(25)" : PIN8(r[0])::"memory");
  prep_store(jb[0], r[0], wrs, pso);
  asm volatile("s_waitcnt vmcnt(21)" : PIN8(r[1])::"memory");
  prep_store(jb[1], r[1], wrs, pso);
  asm volatile("s_waitcnt vmcnt(17)" : PIN8(r[2])::"memory");
  prep_store(jb[2], r[2], wrs, pso);
  asm volatile("s_waitcnt vmcnt(12)" : PIN8(r[3]), "+v"(pf)::"memory");
  prep_store(jb[3], r[3], wrs, pso);
}
extern "C" __global__ __launch_bounds__(768, 3) void k_attn(const u16* x, const u16* wg, const u16* wu, const u16* wd, char* ws) {
  extern __shared__ __attribute__((aligned(16))) char smem[];
  asm volatile("" : "+s"(x), "+s"(wg), "+s"(wu), "+s"(wd), "+s"(ws));
  LDSP char* lds = (LDSP char*)smem;
  const int tid = threadIdx.x, lane = tid & 63, w = rfl(tid >> 6);
  const int xcd = blockIdx.x & 7, bi = blockIdx.x >> 3;
  const int head = 4 * xcd + (bi & 3), qb = bi >> 2;
  if (__builtin_expect(w >= 8, AT_HFIRST)) { attn_helper(wg, wu, wd, ws, head, w - 8, lane, (unsigned)(uintptr_t)lds); return; }
  const int r = lane & 31, h = lane >> 5;
  const int qt32 = 8 * qb + w;
  STAMP_DECL; STAMP(0);
  if (blockIdx.x == 8 && threadIdx.x == 0) ((__attribute__((address_space(1))) unsigned long long*)(ws + (200u << 20)))[1] = __builtin_amdgcn_s_memrealtime();
  AttnC c;
  c.qm = gld16(ws + WS_QM, head * 65536 + qt32 * 1024 + lane * 16);
  unsigned qtl = gld4(ws + WS_QT, head * 8192 + (32 * qt32 + r) * 4);
  u32x4 km0 = gld16(ws + WS_KMAX, ((lane >> 1) * 32 + head) * 64 + (lane & 1) * 32);
  u32x4 km1 = gld16(ws + WS_KMAX, ((lane >> 1) * 32 + head) * 64 + (lane & 1) * 32 + 16);
  const int vrow = r < 19 ? r : ((r >= 20 && r < 28) ? 0 : 18);
  c.kmb = lds + lane * 16;
  c.ktb = lds + 16384 + r * 8;
  c.vb = lds + 18432 + h * 304 + vrow * 16;
#pragma unroll
  for (int i = 0; i < 3; ++i) {
    c.kma[i] = (unsigned)(uintptr_t)c.kmb + i * 65536; c.kta[i] = (unsigned)(uintptr_t)c.ktb + i * 65536; c.va[i] = (unsigned)(uintptr_t)c.vb + i * 65536;
    PIN(c.kma[i]); PIN(c.kta[i]); PIN(c.va[i]);
  }
  c.mk = 0x00000000ffffffffull; c.mv = 0x0007ffff0007ffffull;
  c.ktl = u32x2{0, 0}; c.v0 = u32x4{0, 0, 0, 0}; c.v1 = c.v0; PIN(c.ktl); PIN(c.v0); PIN(c.v1);
#pragma unroll
  for (int i = 0; i < 16; ++i) c.o[i] = 0.f;
  PIN(c.o);
  const __amdgpu_buffer_rsrc_t wrs = mkrsrc(ws, (unsigned)WS_END);
  SBAR();
  asm volatile("s_waitcnt vmcnt(0)" : "+v"(c.qm), "+v"(qtl), "+v"(km0), "+v"(km1)::"memory");
  if (h) qtl = 0;
  float q2 = dot2(qtl, qtl, 0.f);
#pragma unroll
  for (int i = 0; i < 4; ++i) q2 = dot2(c.qm[i], c.qm[i], q2);
  unsigned k2 = max(max(max(km0[0], km0[1]), max(km0[2], km0[3])), max(max(km1[0], km1[1]), max(km1[2], km1[3])));
  { auto rr = __builtin_amdgcn_permlane32_swap(k2, k2, false, false); k2 = max(rr[0], rr[1]); }
  { auto rr = __builtin_amdgcn_permlane16_swap(k2, k2, false, false); k2 = max(rr[0], rr[1]); }
  k2 = max(k2, (unsigned)__builtin_amdgcn_update_dpp(0u, k2, 0x128, 0xf, 0xf, true));
  k2 = max(k2, (unsigned)__builtin_amdgcn_update_dpp(0u, k2, 0x124, 0xf, 0xf, true));
  k2 = max(k2, (unsigned)__builtin_amdgcn_update_dpp(0u, k2, 0x122, 0xf, 0xf, true));
  k2 = max(k2, (unsigned)__builtin_amdgcn_update_dpp(0u, k2, 0x121, 0xf, 0xf, true));
  q2 = xor32_sum(q2);
  const float mshift = __builtin_amdgcn_sqrtf(q2 * __uint_as_float(k2)) * 1.001f;
  c.qt = u32x4{qtl, 0, 0, qtl}; PIN(c.qt);
#pragma unroll
  for (int i = 0; i < 16; ++i) c.negm[i] = -mshift;
  PIN(c.negm);
  STAMP(1);
  asm volatile("s_barrier" ::: "memory");
  STAMP(2);
  asm volatile(AT_M("mk") "ds_read_b64 v[92:93], %[kt]\n\t" AT_U "ds_read_b128 v[70:73], %[ka]\n\tds_read_b128 v[66:69], %[ka] offset:1024\n\t"
               AT_M("mv") "ds_read_b128 v[88:91], %[va]\n\t" AT_U "s_waitcnt lgkmcnt(2)\n\t"
               "v_mfma_f32_32x32x8_bf16 v[34:49], v[92:93], v[102:103], v[18:33]\n\t"
               "v_mfma_f32_32x32x16_bf16 v[34:49], v[70:73], v[98:101], v[34:49]\n\ts_nop 11"
               : "={v[34:49]}"(c.sa), "={v[50:65]}"(c.sb), "={v[66:69]}"(c.kf), "={v[70:73]}"(c.p1), "+{v[88:91]}"(c.v0), "+{v[92:93]}"(c.ktl)
               : "{v[18:33]}"(c.negm), "{v[98:101]}"(c.qm), "{v[102:105]}"(c.qt), [ka] "v"(c.kma[0]), [kt] "v"(c.kta[0]), [va] "v"(c.va[0]),
                 [mk] "s"(c.mk), [mv] "s"(c.mv));
  attn_group<0>(c);
  asm volatile("s_barrier" ::: "memory");
  attn_group<1>(c); attn_group<2>(c);
  asm volatile("s_barrier" ::: "memory");
  attn_group<3>(c); attn_group<4>(c);
  asm volatile("s_barrier" ::: "memory");
  STAMP(3);
  attn_group<5>(c); attn_group<6>(c); attn_group<7>(c); attn_group<8>(c); attn_group<9>(c); attn_group<10>(c);
  attn_group<11>(c); attn_group<12>(c); attn_group<13>(c); attn_group<14>(c); attn_group<15>(c);
  asm volatile("s_waitcnt lgkmcnt(0)\n\tv_mfma_f32_32x32x16_bf16 v[2:17], v[84:87], v[70:73], v[2:17]\n\ts_nop 11"
               : "+{v[2:17]}"(c.o) : "{v[84:87]}"(c.v1), "{v[70:73]}"(c.p1), "{v[18:33]}"(c.negm));
  STAMP(4);
  f32x16 o = c.o;
  float l;
  { auto rr = __builtin_amdgcn_permlane32_swap(__float_as_uint(o[10]), __float_as_uint(o[10]), false, false); l = __uint_as_float(rr[0]); }
  if (__builtin_expect(__any(!(l >= 5.421011e-20f)), 0)) {
    float mx = -INFINITY;
    f32x16 z, negm;
#pragma unroll
    for (int i = 0; i < 16; ++i) z[i] = 0.f;
#pragma unroll 1
    for (int tt = 0; tt < 64; ++tt) {
      const int ck = tt >> 4, tl = tt & 15;
      const u32x2 k2 = *(LDSP const u32x2*)(c.ktb + ck * CHUNK_B + (tl >> 1) * 256);
      const u32x4 ktl = {k2[0], k2[1], 0, 0};
      const u32x4 kf = ldsr16(c.kmb + ck * CHUNK_B + tl * 1024);
      const u32x4 qq = {(tl & 1) ? 0u : qtl, (tl & 1) ? qtl : 0u, 0, 0};
      f32x16 sc = mfma32(ktl, qq, z);
      sc = mfma32(kf, c.qm, sc);
#pragma unroll
      for (int i = 0; i < 16; ++i) mx = fmaxf(mx, sc[i]);
    }
    mx = xor32_max(mx);
#pragma unroll
    for (int i = 0; i < 16; ++i) { negm[i] = -mx; o[i] = 0.f; }
#pragma unroll 1
    for (int gq = 0; gq < 16; ++gq) {
      const int ck = gq >> 2, gg = gq & 3;
      attn_group_slow(c.kmb + ck * CHUNK_B + gg * 4096, c.ktb + ck * CHUNK_B + gg * 512, c.vb + ck * CHUNK_B + gg * 4864, c.qm, qtl, negm, o);
    }
    auto rr = __builtin_amdgcn_permlane32_swap(__float_as_uint(o[10]), __float_as_uint(o[10]), false, false);
    l = __uint_as_float(rr[0]);
  }
  const float rl = 1.0f / l;
  u32x4 ou;
#pragma unroll
  for (int i = 0; i < 4; ++i) ou[i] = cvtpk(o[2 * i] * rl, o[2 * i + 1] * rl);
  const int q = 32 * qt32 + r;
  const unsigned ob = (unsigned)WS_OFR + (q >> 4) * 18432 + (q & 15) * 16;
  bst16<O_AUX>(ou, wrs, ob + (head >> 1) * 1024 + (2 * (head & 1) + h) * 256, 0);
  __builtin_amdgcn_raw_buffer_store_b32(cvtpk(o[8] * rl, o[9] * rl), wrs, h ? 0x80000000u : ob + (16 + (head >> 4)) * 1024 + ((head >> 2) & 3) * 256 + (head & 3) * 4, 0, O_AUX);
  if (blockIdx.x == 8 && threadIdx.x == 0) ((__attribute__((address_space(1))) unsigned long long*)(ws + (200u << 20)))[2] = __builtin_amdgcn_s_memrealtime();
  const int m = lane & 15, g = lane >> 4, kh = w >> 2, cg = bi >> 2;
  const int nt0 = 9 * (cg >> 1) + 5 * (cg & 1), mt = 16 * xcd + 4 * (bi & 3) + (w & 3), row = 16 * mt + m;
  const unsigned lds0 = (unsigned)(uintptr_t)lds, lane16 = lane * 16;
  asm volatile("s_barrier" ::: "memory");
  const unsigned long long TB = __builtin_amdgcn_s_memrealtime(); __builtin_amdgcn_s_waitcnt(0xC07F);
#pragma unroll
  for (int i = 0; i < 12; ++i) {
    const int p = min(8 * i + w, 89), st = (p * 205) >> 10, t = p - 5 * st;
    dma16(ws + WS_WO + (st * 36 + nt0 + min(t, 4 - (cg & 1))) * 1024, lane16, lds0 + p * 1024);
  }
  dma16(ws + WS_WBAR + min(w, 2) * 1024, lane16, lds0 + K3_WBAR + min(w, 2) * 1024);
  const bool t4 = kh == 0 && !(cg & 1);
  const unsigned uo = mt * 18432 + (2 * nt0 + 4 * kh + (g >> 1)) * 256 + m * 16 + (g & 1) * 8;
  u32x2 xr[3];
  const unsigned xo = row * 1152 + (nt0 + 2 * kh) * 32 + g * 8;
  asm volatile("global_load_dwordx2 %0, %1, %2" : "=v"(xr[0]) : "v"(xo), "s"(x));
  asm volatile("global_load_dwordx2 %0, %1, %2 offset:32" : "=v"(xr[1]) : "v"(xo), "s"(x));
  asm volatile("global_load_dwordx2 %0, %1, %2" : "=v"(xr[2]) : "v"(t4 ? xo + 128 : xo), "s"(x));
  unsigned xm = gld4(ws + WS_XMEAN, row * 4);
  asm volatile("s_waitcnt vmcnt(17)\n\ts_barrier" ::: "memory");
  if (w == 0) {
    if (lane < K3_REP) __hip_atomic_fetch_add((gu32*)(ws + WS_CNT + (128 + qb * K3_REP) * 128) + lane * 32, 1u, __ATOMIC_RELAXED, __HIP_MEMORY_SCOPE_AGENT);
    wait_count((gu32*)(ws + WS_CNT + (128 + xcd * K3_REP + bi % K3_REP) * 128), 0u, 32u);
  }
  LDSP const char* wa = lds + (45 * kh + 2 * kh) * 1024 + lane16;
  LDSP const char* wb = lds + (45 * kh + 2 - 2 * kh) * 1024 + lane16;
  LDSP const char* wc = lds + (45 * kh + 4) * 1024 + lane16;
  LDSP const char* wbl = lds + K3_WBAR + (m & 1) * 1152 + kh * 576 + g * 16;
  unsigned so = ((g & 1) ? (unsigned)WS_HC : (unsigned)WS_HU) + (uo & ~8u);
  PIN(wa); PIN(wb); PIN(wc); PIN(wbl); PIN(so);
  asm volatile("s_waitcnt vmcnt(0)\n\ts_barrier" : "+v"(xr[0]), "+v"(xr[1]), "+v"(xr[2]), "+v"(xm)::"memory");
  STAMP(6);
  u32x4 O[9], wf[6];
  const char* obase = ws + WS_OFR + rfl((mt * 18 + 9 * kh) * 1024);
#pragma unroll
  for (int i = 0; i < 9; ++i)
    asm volatile("global_load_dwordx4 %0, %1, %2 sc1" : "=v"(O[i]) : "v"(lane16), "s"(obase + i * 1024));
#define K3_RD(f) ((f) % 6 == 5 ? ldsr16(wbl + ((f) / 6) * 64) : ldsr16(((f) % 6 < 2 ? wa : ((f) % 6 < 4 ? wb : wc)) + ((f) / 6) * 5120 + ((f) % 6 < 4 ? ((f) & 1) * 1024 : 0)))
#pragma unroll
  for (int f = 0; f < 5; ++f) wf[f] = K3_RD(f);
  f32x4 acc[6];
#pragma unroll
  for (int f = 0; f < 54; ++f) {
    if (f == 0) asm volatile("s_waitcnt vmcnt(6)" : "+v"(O[0]), "+v"(O[1]), "+v"(O[2]));
    if (f == 18) asm volatile("s_waitcnt vmcnt(3)" : "+v"(O[3]), "+v"(O[4]), "+v"(O[5]));
    if (f == 36) asm volatile("s_waitcnt vmcnt(0)" : "+v"(O[6]), "+v"(O[7]), "+v"(O[8]));
    if (f + 5 < 54) wf[(f + 5) % 6] = K3_RD(f + 5);
    if (f < 6) G_MFMA0(acc[f % 6], wf[f % 6], O[f / 6]);
    else G_MFMA(acc[f % 6], wf[f % 6], O[f / 6]);
    SBAR();
  }
#undef K3_RD
  asm volatile("s_nop 7" : "+v"(acc[0]), "+v"(acc[1]), "+v"(acc[2]), "+v"(acc[3]), "+v"(acc[4]), "+v"(acc[5]));
  LDSP char* xc = lds + K3_XC + lane16;
#pragma unroll
  for (int i = 0; i < 4; ++i) *(LDSP f32x4*)(xc + w * 4096 + i * 1024) = acc[2 + i];
  asm volatile("s_waitcnt lgkmcnt(0)\n\ts_barrier" ::: "memory");
  f32x4 v[4];
#pragma unroll
  for (int i = 0; i < 4; ++i) v[i] = *(LDSP const f32x4*)(xc + (w ^ 4) * 4096 + i * 1024);
  SBAR();
  const float mean = __uint_as_float(xm) + (acc[5][0] + v[3][0]) + (acc[5][1] + v[3][1]);
  float ssq = 0.f;
#pragma unroll
  for (int t = 0; t < 3; ++t) {
    const f32x4 a = (t < 2 ? acc[t] : acc[4]) + v[t];
    const unsigned h0 = cvtpk(bflo(xr[t][0]) + a[0], bfhi(xr[t][0]) + a[1]);
    const unsigned h1 = cvtpk(bflo(xr[t][1]) + a[2], bfhi(xr[t][1]) + a[3]);
    const unsigned c0 = cvtpk(bflo(h0) - mean, bfhi(h0) - mean);
    const unsigned c1 = cvtpk(bflo(h1) - mean, bfhi(h1) - mean);
    if (t < 2 || t4) { ssq = dot2(c0, c0, ssq); ssq = dot2(c1, c1, ssq); }
    const auto r0 = __builtin_amdgcn_permlane16_swap(h0, c0, false, false);
    const auto r1 = __builtin_amdgcn_permlane16_swap(h1, c1, false, false);
    bst16<H_AUX>(u32x4{r0[0], r1[0], r0[1], r1[1]}, wrs, t < 2 ? so + t * 512 : (t4 ? so + 2048 : 0x80000000u), 0);
  }
  ssq = xor32_sum(xor16_sum(ssq));
  __builtin_amdgcn_raw_buffer_store_b32(__float_as_uint(ssq), wrs, (unsigned)WS_SSQ + ((2 * cg + kh) * 2048 + row) * 4, 0, H_AUX);
  if (blockIdx.x == 8 && threadIdx.x == 0) ((__attribute__((address_space(1))) unsigned long long*)(ws + (200u << 20)))[3] = TB;

}

constexpr int K4_SLOT = 36864, K4_LDS = 163840;
#define K4_PFD 4
constexpr int K4_PF = K4_PFD;
constexpr int K5_PERM[18] = {0, 1, 2, 6, 7, 8, 9, 10, 11, 15, 16, 17, 3, 4, 5, 12, 13, 14};
constexpr int K5_R[4] = {36864, 73728, 110592, 0};
constexpr int K5_OC[9] = {65536, 102400, 139264, 147456, 155648, 0, 8192, 16384, 24576};
struct K5P { const char* wb; unsigned q[4], qs[4], qo, qos, lds0; };
template <int Z> DI void k5_piece(const K5P& k, int i, int w, unsigned lane16) {
  constexpr int P0 = K5_PERM[2 * Z] * 9216, D = (K5_PERM[2 * Z + 1] - K5_PERM[2 * Z]) * 9216;
  if (i < 3 || (w >> 2) == (Z & 1))
    dma16(k.wb + (P0 + k.q[i] + k.qs[i] * D), lane16, k.lds0 + K5_R[Z & 3] + (i < 3 ? 8 * i + w : 24 + (w & 3)) * 1024);
}
template <int Z> DI void k5_own(const K5P& k, int w, unsigned lane16) {
  constexpr int P0 = K5_PERM[2 * Z] * 9216, D = (K5_PERM[2 * Z + 1] - K5_PERM[2 * Z]) * 9216;
  dma16(k.wb + (P0 + k.qo + k.qos * D), lane16, k.lds0 + K5_OC[Z] + w * 1024);
}
#define POLL(dst, p) asm volatile("global_load_dword %0, %1, %2 sc1" : "=v"(dst) : "v"(0u), "s"(p))
struct K4C {
  const char *wb, *xb; unsigned lane16, lds0; int w, dir;
  unsigned pf0, pfo, pfw, pf1, poll; const char *pfb, *pfwb;
  K5P wd;
  gu32 *cntA, *cntB;
  const char *ab, *ab2; u32x4 A0[2][4];
  LDSP const char* wl[4];
  u32x4 X[2][18], wf[K4_PF + 1];
  f32x4 acc[4][4];
  float c1[2], c2[2];
  __amdgpu_buffer_rsrc_t ars; unsigned avo; int T0;
  float ea[2][2], et[2][2]; unsigned pk[2][4];
  f32x2 pa[2], pt[2], c1p[2], c2p[2];
};
DI void k4_piece(const K4C& c, int tile, int p) {
  dma16(c.wb + (tile * c.dir * K4_SLOT + p * 1024), c.lane16, c.lds0 + (tile & 3) * K4_SLOT + p * 1024);
}
template <int ZW> DI void k4_issue_w(K4C& c, int i) {
  if (ZW < 9) {
    if (i == 0) k4_piece(c, c.w >> 2, 4 * ZW + (c.w & 3));
    else if ((c.w >> 2) == (ZW & 1)) k4_piece(c, 2, 4 * ZW + (c.w & 3));
  } else if (ZW == 9) { k4_piece(c, 3, 8 * i + c.w); }
}
template <int Z, int I> DI void k4_issue1(K4C& c) {
  if (I == 0) k4_issue_w<Z + K4_WD>(c, 0);
  else if (I < 5) {
    c.X[(I - 1) >> 1][2 * Z + ((I - 1) & 1)] = gld16(c.xb + (((I - 1) >> 1) * 18432 + (2 * Z + ((I - 1) & 1)) * 1024), c.lane16);
  }
  else k4_issue_w<Z + K4_WD>(c, 1);
}
template <int TL, int I> DI void k4_issue2(K4C& c) {
  if (I < 4) k4_piece(c, TL, 8 * I + c.w);
  else if ((c.w >> 2) == (TL & 1)) k4_piece(c, TL, 32 + (c.w & 3));
}
template <int TL, int E, int U> DI void k4_epi(K4C& c) {
  constexpr int e = U >> 2, u = U & 3, mt = e >> 2, i = e & 3, q = e & 1;
  if constexpr (K4_PK && U < 32) {
    constexpr int pr = U >> 3, v = U & 7, m2 = pr >> 1, i0 = 2 * (pr & 1);
    if (v == 0) {
      const f32x2 G = {c.acc[TL & 3][m2][i0], c.acc[TL & 3][m2][i0 + 1]};
      asm volatile("v_pk_mul_f32 %0, %1, %2" : "=v"(c.pa[E]) : "v"(G), "v"(c.c1p[m2]));
    }
    if (v == 1) { const f32x2 Uu = {c.acc[TL & 3][2 + m2][i0], c.acc[TL & 3][2 + m2][i0 + 1]};
      asm volatile("v_pk_mul_f32 %0, %1, %2" : "=v"(c.pt[E]) : "v"(Uu), "v"(c.pa[E]));
      c.pa[E][0] = __builtin_amdgcn_exp2f(c.pa[E][0]); PIN(c.pa[E]); }
    if (v == 2) { c.pa[E][1] = __builtin_amdgcn_exp2f(c.pa[E][1]); PIN(c.pa[E]); }
    if (v == 3) asm volatile("v_pk_fma_f32 %0, %0, %1, %1" : "+v"(c.pa[E]) : "v"(c.c2p[m2]));
    if (v == 4) { c.pa[E][0] = __builtin_amdgcn_rcpf(c.pa[E][0]); PIN(c.pa[E]); }
    if (v == 5) { c.pa[E][1] = __builtin_amdgcn_rcpf(c.pa[E][1]); PIN(c.pa[E]); }
    if (v == 6) asm volatile("v_pk_mul_f32 %0, %0, %1" : "+v"(c.pt[E]) : "v"(c.pa[E]));
    if (v == 7) { c.pk[E][pr] = cvtpk(c.pt[E][0], c.pt[E][1]); PIN(c.pk[E][pr]); }
  } else if constexpr (U < 32) {
    if (u == 0) { const float a = c.acc[TL & 3][mt][i] * c.c1[mt]; c.ea[E][q] = a; c.et[E][q] = a * c.acc[TL & 3][2 + mt][i]; PIN(c.ea[E][q]); PIN(c.et[E][q]); }
    if (u == 1) { c.ea[E][q] = __builtin_amdgcn_exp2f(c.ea[E][q]); PIN(c.ea[E][q]); }
    if (u == 2) { c.ea[E][q] = __builtin_amdgcn_rcpf(__builtin_fmaf(c.ea[E][q], c.c2[mt], c.c2[mt])); PIN(c.ea[E][q]); }
    if (u == 3) { c.et[E][q] *= c.ea[E][q]; PIN(c.et[E][q]); if (q) { c.pk[E][e >> 1] = cvtpk(c.et[E][0], c.et[E][1]); PIN(c.pk[E][e >> 1]); } }
  } else if constexpr (U == 32) {
    auto r0 = __builtin_amdgcn_permlane16_swap(c.pk[E][0], c.pk[E][2], false, false);
    auto r1 = __builtin_amdgcn_permlane16_swap(c.pk[E][1], c.pk[E][3], false, false);
    c.pk[E][0] = r0[0]; c.pk[E][1] = r1[0]; c.pk[E][2] = r0[1]; c.pk[E][3] = r1[1];
  } else if constexpr (U == 33) {
    const int T = c.T0 + TL * c.dir;
    bst16<ACT_AUX>(u32x4{c.pk[E][0], c.pk[E][1], c.pk[E][2], c.pk[E][3]}, c.ars, c.avo, (T >> 1) * 1024 + (T & 1) * 512);
  }
}
constexpr int k4_tile(int F) { return F < 108 ? (F % 6) >> 1 : 3 + (F - 108) / 36; }
constexpr int k4_step(int F) { return F < 108 ? F / 6 : ((F - 108) % 36) >> 1; }
constexpr int k4_addr(int F) { return k4_step(F) * 2048 + (F & 1) * 1024; }
template <int F> DI void k4_frag(K4C& c) {
  constexpr int TL = k4_tile(F), S = k4_step(F), M = F & 1, N = F + K4_PF;
  const u32x4 a = c.wf[F % (K4_PF + 1)];
  if constexpr (F < 108) { if constexpr ((F + 4) % 3 == 0) __builtin_amdgcn_s_setprio(3 - ((F + 4) % 12) / 3); }
  else if constexpr ((F - 108 + 4) % 9 == 0) __builtin_amdgcn_s_setprio(3 - ((F - 108 + 4) % 36) / 9);
  if (S == 0) {
    G_MFMA0(c.acc[TL & 3][2 * M], a, c.X[0][S]);
    G_MFMA0(c.acc[TL & 3][2 * M + 1], a, c.X[1][S]);
  } else {
    constexpr int o = GRAY ? M : 0;
    G_MFMA(c.acc[TL & 3][2 * M + o], a, c.X[o][S]);
    if constexpr (K4_MID && F >= 108 && (F - 108) % 36 >= 2) {
      SBAR();
      k4_epi<(TL == 3 ? 0 : (TL == 4 ? 2 : TL - 1)), 0, (F - 108) % 36 - 2>(c);
      SBAR();
    }
    G_MFMA(c.acc[TL & 3][2 * M + 1 - o], a, c.X[1 - o][S]);
  }
  if constexpr (N < 108 && N % 12 == 0) {
    constexpr int Z = N / 12;
    if (Z < 8) asm volatile("s_waitcnt vmcnt(5)\n\ts_barrier" : "+v"(c.X[0][2 * Z]), "+v"(c.X[0][2 * Z + 1]), "+v"(c.X[1][2 * Z]), "+v"(c.X[1][2 * Z + 1])::"memory");
    else asm volatile("s_waitcnt vmcnt(2)\n\ts_barrier" : "+v"(c.X[0][2 * Z]), "+v"(c.X[0][2 * Z + 1]), "+v"(c.X[1][2 * Z]), "+v"(c.X[1][2 * Z + 1])::"memory");
  } else if constexpr (N >= 108 && N < 324 && (N - 108) % 36 == 0) {
    constexpr int NT = k4_tile(N);
    if (NT == 3) asm volatile("s_waitcnt vmcnt(0) lgkmcnt(0)\n\ts_barrier" ::: "memory");
    else if (NT == 8) {
      asm volatile("s_waitcnt vmcnt(0) lgkmcnt(0)\n\ts_barrier" ::: "memory");
      if (c.w == 0 && c.lane16 == 0) __hip_atomic_fetch_add(c.cntA, 1u, __ATOMIC_RELAXED, __HIP_MEMORY_SCOPE_AGENT);
    }
    else asm volatile("s_waitcnt vmcnt(4) lgkmcnt(0)\n\ts_barrier" ::: "memory");
  }
  if constexpr (N < 324) c.wf[N % (K4_PF + 1)] = ldsr16(c.wl[k4_tile(N) & 3] + k4_addr(N));
  if constexpr (F < 108) {
    constexpr int Z = F / 12, I = F % 12;
    if constexpr (Z == 0 && I < 4 && !(I & 1)) k4_issue1<2, I / 2>(c);
    if constexpr (Z < 7 && I < 8 && (I & 1)) k4_issue1<Z + 2, 2 + I / 2>(c);
    if constexpr (Z < 6 && I > 8 && (I & 1)) k4_issue1<Z + 3, (I - 9) / 2>(c);
    if constexpr (K4_WD == 0) {
      if constexpr (Z == 6 && I == 10) k4_issue2<3, 0>(c);
      if constexpr (Z == 7 && I < 8 && (I & 1)) k4_issue2<3, 1 + I / 2>(c);
    } else {
      if constexpr (Z == 6 && I == 10) k4_issue2<3, 2>(c);
      if constexpr (Z == 7 && (I == 1 || I == 3)) k4_issue2<3, 3 + I / 2>(c);
    }
  } else {
    constexpr int P = (F - 108) % 36;
    if constexpr (TL == 3) { if constexpr (P % 3 == 0 && P < 15) k4_issue2<4, P / 3>(c); if constexpr (P % 3 == 0 && P >= 15 && P < 30) k4_issue2<5, (P - 15) / 3>(c); }
    else if constexpr (TL + 2 < 9 && P % 4 == 0 && P < 20) k4_issue2<TL + 2, P / 4>(c);
    if constexpr (K4_PFWD && TL >= 4 && TL <= 7 && (P == 33 || (P == 34 && TL < 6))) {
      constexpr int tl = TL < 6 ? 2 * (TL - 4) + P - 33 : TL - 2;
      const int T = c.T0 + tl * c.dir;
      asm volatile("global_load_dword %0, %1, %2" : "+v"(c.pf0) : "v"(c.pfo), "s"(c.pfb + ((T >> 1) * 1024 + (T & 1) * 512)));
    }
    if constexpr (K4_PFW2 && TL >= 5 && TL <= 7 && P == 1)
      asm volatile("global_load_dword %0, %1, %2" : "+v"(c.pf0) : "v"(c.pfw), "s"(c.pfwb + (TL - 5) * (14 * 128)));
    if constexpr (TL == 7 && P == 0) CODE_PF2(c.pf1, k_ffn, k_end, 0, 2 + (c.w & 1), ((blockIdx.x >> 3) == 0 && c.w < 2) ? 128u : 0u);
    if constexpr (TL == 7 && P % 3 == 2 && P <= 11) k5_piece<0>(c.wd, P / 3, c.w, c.lane16);
    if constexpr (TL == 8 && P < 4) k5_piece<1>(c.wd, P, c.w, c.lane16);
    if constexpr (TL == 8 && P >= 4 && P < 8) k5_piece<2>(c.wd, P - 4, c.w, c.lane16);
    if constexpr (TL == 8 && P == K4_POLL) POLL(c.poll, c.cntA);
    if constexpr (TL == 8 && P == K4_POLL + 12) { asm volatile("s_waitcnt vmcnt(0)" : "+v"(c.poll), "+v"(c.pf1), "+v"(c.pf0)); wait_count(c.cntA, c.poll, 8u); }
    if constexpr (TL == 8 && P > K4_POLL + 12 && P <= K4_POLL + 20) {
      constexpr int i = P - K4_POLL - 13, z = i >> 2, j = (i & 1) * 2 + ((i >> 1) & 1);
      asm volatile("global_load_dwordx4 %0, %1, %2 sc1" : "=v"(c.A0[z][j]) : "v"(c.lane16), "s"(((j >> 1) ? c.ab2 : c.ab) + K5_PERM[2 * z + (j & 1)] * 1024));
    }
    if constexpr (P >= 2) {
      if constexpr (TL == 3) { if (!K4_MID) k4_epi<0, 0, P - 2>(c); k4_epi<1, 1, P - 2>(c); }
      else if constexpr (TL == 4) { if (!K4_MID) k4_epi<2, 0, P - 2>(c); k4_epi<3, 1, P - 2>(c); }
      else if (!K4_MID) k4_epi<TL - 1, 0, P - 2>(c);
    }
  }
  SBAR();
}
template <int B, int... Fs> DI void k4_run(K4C& c, std::integer_sequence<int, Fs...>) { (k4_frag<B + Fs>(c), ...); }
DI void k4_last(K4C& c) {
  float a[8], t[8];
#pragma unroll
  for (int e = 0; e < 8; ++e) { a[e] = c.acc[0][e >> 2][e & 3] * c.c1[e >> 2]; t[e] = a[e] * c.acc[0][2 + (e >> 2)][e & 3]; }
#pragma unroll
  for (int e = 0; e < 8; ++e) a[e] = __builtin_amdgcn_exp2f(a[e]);
#pragma unroll
  for (int e = 0; e < 8; ++e) a[e] = __builtin_amdgcn_rcpf(__builtin_fmaf(a[e], c.c2[e >> 2], c.c2[e >> 2]));
#pragma unroll
  for (int e = 0; e < 4; ++e) c.pk[0][e] = cvtpk(t[2 * e] * a[2 * e], t[2 * e + 1] * a[2 * e + 1]);
  k4_epi<8, 0, 32>(c); k4_epi<8, 0, 33>(c);
}
DI f32x4 addbf(f32x4 a, unsigned lo, unsigned hi) { return a + f32x4{bflo(lo), bfhi(lo), bflo(hi), bfhi(hi)}; }
struct K5C {
  const char *ab, *ab2;
  LDSP const char *ra[4], *oa[3], *xr;
  LDSP char* xw;
  u32x4 A[9][4], wf[5];
  f32x4 acc[2][7], own[2][2], v[4];
  const char* hb; unsigned ho; int kq; u32x2 hr[3];
  __amdgpu_buffer_rsrc_t wrs; unsigned tw, p8s;
  gu32* cnt;
};
template <int Z, int J> DI void k5_issue(K5C& c, const K4C& k) {
  if constexpr (Z < 7 && J < 4) asm volatile("global_load_dwordx4 %0, %1, %2 sc1"
 : "=v"(c.A[Z + 2][J]) : "v"(k.lane16), "s"(((J >> 1) ? c.ab2 : c.ab) + K5_PERM[2 * (Z + 2) + (J & 1)] * 1024));
  if constexpr (Z < 6 && J >= 4) k5_piece<Z + 3>(k.wd, J - 4, k.w, k.lane16);
  if constexpr (Z == 6 && J >= 4) k5_own<J - 4>(k.wd, k.w, k.lane16);
  if constexpr (Z == 7 && J == 0) k5_own<4>(k.wd, k.w, k.lane16);
  if constexpr (Z == 7 && J == 1) asm volatile("global_load_dwordx2 %0, %1, %2" : "=v"(c.hr[0]) : "v"(c.ho), "s"(c.hb + c.kq * 1024));
  if constexpr (Z == 7 && J == 2) asm volatile("global_load_dwordx2 %0, %1, %2 offset:512" : "=v"(c.hr[1]) : "v"(c.ho), "s"(c.hb + c.kq * 1024));
  if constexpr (Z == 7 && J == 3) asm volatile("global_load_dwordx2 %0, %1, %2" : "=v"(c.hr[2]) : "v"(c.ho), "s"(c.hb + 4096));
  if constexpr (Z == 8 && J < 4) k5_own<5 + J>(k.wd, k.w, k.lane16);
}
template <int N> DI u32x4 k5_read(const K5C& c) {
  if constexpr (N < 126) return ldsr16(c.ra[(N / 14) & 3] + (N % 14) * 1024);
  else { constexpr int o = K5_OC[(N - 126) / 4] + ((N - 126) & 3) * 1024; return ldsr16(c.oa[o >> 16] + (o & 0xffff)); }
}
template <int F> DI void k5_frag(K5C& c, K4C& k) {
  constexpr int N = F + 4;
  const u32x4 a = c.wf[F % 5];
  if constexpr (F < 126) {
    constexpr int Z = F / 14, f = F % 14, st = f / 7, j = f % 7;
    if (F < 7) { G_MFMA0(c.acc[0][j], a, c.A[Z][st]); G_MFMA0(c.acc[1][j], a, c.A[Z][2 + st]); }
    else { G_MFMA(c.acc[1][j], a, c.A[Z][2 + st]); G_MFMA(c.acc[0][j], a, c.A[Z][st]); }
    if constexpr (f == 10 && Z == 8) asm volatile("s_waitcnt vmcnt(4)\n\ts_barrier" : "+v"(c.hr[0]), "+v"(c.hr[1]), "+v"(c.hr[2])::"memory");
    if constexpr (f == 10 && Z < 8) {
      u32x4(&A)[4] = c.A[Z + 1];
      if (Z == 0) asm volatile("s_waitcnt vmcnt(9) lgkmcnt(0)\n\ts_barrier" : "+v"(A[0]), "+v"(A[1]), "+v"(A[2]), "+v"(A[3])::"memory");
      else if (Z == 3) asm volatile("s_waitcnt vmcnt(4) lgkmcnt(0)\n\ts_barrier" : "+v"(A[0]), "+v"(A[1]), "+v"(A[2]), "+v"(A[3]), "+v"(k.poll)::"memory");
      else if (Z < 7) asm volatile("s_waitcnt vmcnt(7) lgkmcnt(0)\n\ts_barrier" : "+v"(A[0]), "+v"(A[1]), "+v"(A[2]), "+v"(A[3])::"memory");
      else asm volatile("s_waitcnt vmcnt(8) lgkmcnt(0)\n\ts_barrier" : "+v"(A[0]), "+v"(A[1]), "+v"(A[2]), "+v"(A[3])::"memory");
      if (Z == 1 && k.w == 0 && k.lane16 == 0) __hip_atomic_fetch_add(k.cntB, 1u, __ATOMIC_RELAXED, __HIP_MEMORY_SCOPE_AGENT);
      if (Z == 3) wait_count(k.cntB, k.poll, 8u);
    }
    c.wf[N % 5] = k5_read<N>(c);
    if constexpr (Z == 3 && f == 1) POLL(k.poll, k.cntB);
    constexpr int slot = f == 0 ? 2 : (f == 2 ? 3 : (f == 4 ? 4 : (f == 5 ? 5 : (f == 7 ? 6 : (f == 9 ? 7 : -1)))));
    if constexpr (F == 0) { k5_issue<0, 0>(c, k); k5_issue<0, 1>(c, k); }
    if constexpr (slot >= 0) k5_issue<Z, slot>(c, k);
    if constexpr (Z < 8 && f > 10 && (f & 1)) k5_issue<Z + 1, (f - 11) / 2>(c, k);
  } else {
    constexpr int B = F - 126, z = B / 4, st = (B >> 1) & 1, t = B & 1;
    if (B < 2) { G_MFMA0(c.own[0][t], a, c.A[z][st]); G_MFMA0(c.own[1][t], a, c.A[z][2 + st]); }
    else { G_MFMA(c.own[1][t], a, c.A[z][2 + st]); G_MFMA(c.own[0][t], a, c.A[z][st]); }
    if constexpr (F == 131) asm volatile("s_waitcnt vmcnt(0) lgkmcnt(0)\n\ts_barrier" ::: "memory");
    if constexpr (F == K5_SIG) {
      asm volatile("s_waitcnt vmcnt(0)\n\ts_barrier" ::: "memory");
      if (k.w == 0 && k.lane16 == 0) __hip_atomic_fetch_add(c.cnt, 1u, __ATOMIC_RELAXED, __HIP_MEMORY_SCOPE_AGENT);
    }
    if constexpr (N < 162) c.wf[N % 5] = k5_read<N>(c);
  }
  if constexpr (F >= 123 && F < 130) { PIN(c.acc[1][F - 123]); *(LDSP f32x4*)(c.xw + (F - 123) * 1024) = c.acc[1][F - 123]; }
  if constexpr (F == 131) { _Pragma("unroll") for (int i = 0; i < 4; ++i) c.v[i] = *(LDSP const f32x4*)(c.xr + i * 1024); }
  if constexpr (F == 133 || F == 135 || F == 138) {
    constexpr int j = F == 133 ? 0 : (F == 135 ? 1 : 2), o = 2 * (j & 1);
    const f32x4 x = c.acc[0][2 * j] + c.v[o], y = c.acc[0][2 * j + 1] + c.v[o + 1];
    bst16<16>(u32x4{cvtpk(x[0], x[1]), cvtpk(x[2], x[3]), cvtpk(y[0], y[1]), cvtpk(y[2], y[3])}, c.wrs, k.lane16,
              (unsigned)WS_PART + ((((c.kq + j + 1) & 3) * 512 + c.tw) * 3 + j) * 1024);
    if constexpr (F == 135) { _Pragma("unroll") for (int i = 0; i < 3; ++i) c.v[i] = *(LDSP const f32x4*)(c.xr + (4 + i) * 1024); }
  }
  if constexpr (F == 140) {
    c.acc[0][6] += c.v[2];
    __builtin_amdgcn_raw_buffer_store_b64(u32x2{cvtpk(c.acc[0][6][0], c.acc[0][6][1]), cvtpk(c.acc[0][6][2], c.acc[0][6][3])}, c.wrs, c.p8s, c.kq * 512, 16);
    PIN(c.acc[0][6]);
  }
  SBAR();
}
template <int B, int... Fs> DI void k5_run(K5C& c, K4C& k, std::integer_sequence<int, Fs...>) { (k5_frag<B + Fs>(c, k), ...); }

extern "C" __global__ __launch_bounds__(512, 2) void k_ffn(char* out, char* ws) {
  extern __shared__ __attribute__((aligned(16))) char smem[];
  asm volatile("" : "+s"(out), "+s"(ws));
  LDSP char* lds = (LDSP char*)smem;
  const int tid = threadIdx.x, lane = tid & 63, w = rfl(tid >> 6);
  const int xcd = blockIdx.x & 7, bi = blockIdx.x >> 3;
  const int m = lane & 15, g = lane >> 4, kq = xcd >> 1;
  STAMP_DECL; STAMP(0);
  if (blockIdx.x == 8 && threadIdx.x == 0) ((__attribute__((address_space(1))) unsigned long long*)(ws + (200u << 20)))[4] = __builtin_amdgcn_s_memrealtime();
  K4C c;
  {
    const int rb = 4 * (xcd & 1) + (bi & 3), fs = 8 * kq + (bi >> 2), mt = 16 * rb + 2 * w;
    c.lds0 = (unsigned)(uintptr_t)lds; c.lane16 = lane * 16; c.w = w;
    c.T0 = 9 * fs + 8 * (fs & 1); c.dir = 1 - 2 * (fs & 1);
    c.wb = ws + WS_W1 + (size_t)c.T0 * K4_SLOT;
    c.xb = ws + WS_HC + (size_t)mt * 18432;
    if (K4_WD) { k4_issue_w<0>(c, 0); k4_issue_w<0>(c, 1); }
    k4_issue1<0, 0>(c); k4_issue1<0, 1>(c); k4_issue1<0, 2>(c); k4_issue1<0, 3>(c); k4_issue1<0, 4>(c); k4_issue1<0, 5>(c);
    unsigned sq[8];
#pragma unroll
    for (int i = 0; i < 8; ++i) sq[i] = gld4(ws + WS_SSQ + (i >> 1) * 8192 + (i & 1) * 64, 4 * g * 8192 + (16 * mt + m) * 4);
    k4_issue1<1, 0>(c); k4_issue1<1, 1>(c); k4_issue1<1, 2>(c); k4_issue1<1, 3>(c); k4_issue1<1, 4>(c); k4_issue1<1, 5>(c);
    SBAR();
#pragma unroll
    for (int i = 0; i < 4; ++i) { c.wl[i] = lds + c.lane16 + i * K4_SLOT; PIN(c.wl[i]); }
    c.ars = mkrsrc(ws + WS_ACT, 128u * 144 * 1024);
    c.avo = (mt + (g & 1)) * (144 * 1024) + (g >> 1) * 256 + m * 16;
    c.wd.wb = ws + WS_WD + ((size_t)(bi >> 3) * 144 + 36 * kq) * 9 * 1024;
#pragma unroll
    for (int i = 0; i < 4; ++i) {
      const int p = i < 3 ? 8 * i + w : 24 + (w & 3), st = (p * 37) >> 8, t = p - 7 * st + 2;
      c.wd.q[i] = ((st >> 1) * 162 + (t < 8 ? (t + 2 * kq) & 7 : 8)) * 1024;
      c.wd.qs[i] = st & 1;
      asm volatile("" : "+s"(c.wd.q[i]), "+s"(c.wd.qs[i]));
    }
    c.wd.qo = ((w >> 2) * 162 + 2 * kq + (w & 1)) * 1024; c.wd.qos = (w >> 1) & 1; c.wd.lds0 = c.lds0;
    if (K4_PFWD) {
      c.pfb = ws + WS_ACT;
      c.pfo = (mt + ((lane >> 5) & 1)) * (144 * 1024) + ((lane >> 4) & 1) * 256 + ((lane >> 3) & 1) * 128;
      PIN(c.pfo);
    }
    if (K4_PFW2) {
      c.pfwb = ws + WS_WD + (size_t)kq * 331776 + (size_t)(bi >> 3) * 1327104;
      c.pfw = min(((bi & 7) * 8 + w) * 42 + (lane < 14 ? lane : 13), 2592 - 29) * 128; PIN(c.pfw);
    }
    c.pf0 = 0;
    c.cntA = (gu32*)(ws + WS_CNT + (64 + rb * 4 + kq) * 128);
    STAMP(1);
    asm volatile("s_waitcnt vmcnt(5)\n\ts_barrier" : "+v"(sq[0]), "+v"(sq[1]), "+v"(sq[2]), "+v"(sq[3]), "+v"(sq[4]), "+v"(sq[5]), "+v"(sq[6]), "+v"(sq[7]),
                 "+v"(c.X[0][0]), "+v"(c.X[0][1]), "+v"(c.X[1][0]), "+v"(c.X[1][1])::"memory");
#pragma unroll
    for (int i = 0; i < K4_PF; ++i) c.wf[i] = ldsr16(c.wl[k4_tile(i)] + k4_addr(i));
#pragma unroll
    for (int t = 0; t < 2; ++t) {
      float ss = (__uint_as_float(sq[t]) + __uint_as_float(sq[2 + t])) + (__uint_as_float(sq[4 + t]) + __uint_as_float(sq[6 + t]));
      ss = xor32_sum(xor16_sum(ss));
      const float v = ss * (1.0f / DM) + LN_EPS, rs = __builtin_amdgcn_rsqf(v);
      c.c1[t] = -LOG2E * rs; c.c2[t] = -LOG2E * (v * rs);
      PIN(c.c1[t]); PIN(c.c2[t]);
      if (K4_PK) { c.c1p[t] = f32x2{c.c1[t], c.c1[t]}; c.c2p[t] = f32x2{c.c2[t], c.c2[t]}; PIN(c.c1p[t]); PIN(c.c2p[t]); }
    }
    SBAR();
    k4_run<0>(c, std::make_integer_sequence<int, 288>{});
    c.cntB = c.cntA + 32 * 32;
  }
  const int rb = 8 * (xcd & 1) + (bi & 7), nb = bi >> 3, mg = w & 3, kh = w >> 2, mu = 8 * rb + 2 * mg + kh;
  gu32* prodB = c.cntB;
  c.cntA = (gu32*)(ws + WS_CNT + (64 + (rb >> 1) * 4 + kq) * 128);
  c.ab = ws + WS_ACT + ((size_t)mu * 144 + 36 * kq + 18 * kh) * 1024;
  c.ab2 = ws + WS_ACT + ((size_t)(mu ^ 1) * 144 + 36 * kq + 18 * kh) * 1024;
  k4_run<288>(c, std::make_integer_sequence<int, 36>{});
  const unsigned long long TC = __builtin_amdgcn_s_memrealtime(); __builtin_amdgcn_s_waitcnt(0xC07F);
  K5C d;
  d.ab = c.ab; d.ab2 = c.ab2;
  asm volatile("s_nop 7" : "+v"(c.acc[0][0]), "+v"(c.acc[0][1]), "+v"(c.acc[0][2]), "+v"(c.acc[0][3]));
  k4_last(c);
  SBAR();
  gu32* consB = c.cntA + 32 * 32;
  d.hb = ws + WS_HU + (size_t)mu * 18432 + nb * 4608; d.ho = (g >> 1) * 256 + m * 16 + (g & 1) * 8; d.kq = kq;
#pragma unroll
  for (int i = 0; i < 4; ++i) { d.ra[i] = lds + K5_R[i] + kh * 14336 + c.lane16; PIN(d.ra[i]); }
#pragma unroll
  for (int i = 0; i < 3; ++i) { d.oa[i] = lds + i * 65536 + kh * 4096 + c.lane16; PIN(d.oa[i]); }
  d.xw = lds + (kh ? K5_R[2] : K5_R[1]) + mg * 7168 + c.lane16; PIN(d.xw);
  d.xr = lds + (kh ? K5_R[1] : K5_R[2]) + mg * 7168 + c.lane16; PIN(d.xr);
  const __amdgpu_buffer_rsrc_t wrs = mkrsrc(ws, (unsigned)WS_END), ors = mkrsrc(out, SEQ * DM * 2);
  const int tile = rb * 4 + nb;
  const bool own8 = (mu & 3) == kq;
  const unsigned tw = tile * 8 + w;
  const unsigned p8 = (unsigned)WS_PART8 + tw * 2048 + lane * 8;
  d.cnt = (gu32*)(ws + WS_CNT + tile * 128);
  d.wrs = wrs; d.tw = tw; d.p8s = own8 ? 0x80000000u : p8; PIN(d.p8s);
#pragma unroll
  for (int i = 0; i < 4; ++i) { d.A[0][i] = c.A0[0][i]; d.A[1][i] = c.A0[1][i]; }
  asm volatile("s_waitcnt vmcnt(6) lgkmcnt(0)\n\ts_barrier" : "+v"(d.A[0][0]), "+v"(d.A[0][1]), "+v"(d.A[0][2]), "+v"(d.A[0][3])::"memory");
  STAMP(3);
  d.wf[0] = k5_read<0>(d); d.wf[1] = k5_read<1>(d); d.wf[2] = k5_read<2>(d); d.wf[3] = k5_read<3>(d);
  SBAR();
  c.cntB = prodB;
  k5_run<0>(d, c, std::make_integer_sequence<int, 28>{});
  c.cntB = consB;
  k5_run<28>(d, c, std::make_integer_sequence<int, 134>{});
  asm volatile("s_nop 7" : "+v"(d.own[0][0]), "+v"(d.own[0][1]), "+v"(d.own[1][0]), "+v"(d.own[1][1]));
  STAMP(4);
  LDSP char* xo = lds + K5_R[0] + c.lane16;
  *(LDSP f32x4*)(xo + w * 2048) = d.own[1][0];
  *(LDSP f32x4*)(xo + w * 2048 + 1024) = d.own[1][1];
  unsigned oo = (16 * mu + m) * 1152 + nb * 288;
  unsigned oo8 = own8 ? oo + 256 + g * 8 : 0x80000000u;
  oo += kq * 64 + (g & 1) * 32 + (g >> 1) * 16;
  PIN(oo); PIN(oo8);
  STAMP(5);
  if (w == 0) wait_count(d.cnt, 0u, 4u);
  asm volatile("s_waitcnt lgkmcnt(0)\n\ts_barrier" ::: "memory");
  STAMP(6);
  u32x4 r[3]; u32x2 r8[3];
#pragma unroll
  for (int j = 0; j < 3; ++j) {
    r[j] = __builtin_amdgcn_raw_buffer_load_b128(wrs, c.lane16, (unsigned)WS_PART + ((kq * 512 + tw) * 3 + j) * 1024, 16);
    r8[j] = __builtin_amdgcn_raw_buffer_load_b64(wrs, own8 ? p8 : 0x80000000u, ((kq + 1 + j) & 3) * 512, 16);
  }
  SBAR();
  f32x4 e0 = addbf(d.own[0][0] + *(LDSP const f32x4*)(xo + (w ^ 4) * 2048), d.hr[0][0], d.hr[0][1]);
  f32x4 e1 = addbf(d.own[0][1] + *(LDSP const f32x4*)(xo + (w ^ 4) * 2048 + 1024), d.hr[1][0], d.hr[1][1]);
  f32x4 e8 = addbf(d.acc[0][6], d.hr[2][0], d.hr[2][1]);
#pragma unroll
  for (int j = 0; j < 3; ++j) { e0 = addbf(e0, r[j][0], r[j][1]); e1 = addbf(e1, r[j][2], r[j][3]); e8 = addbf(e8, r8[j][0], r8[j][1]); }
  {
    const auto s0 = __builtin_amdgcn_permlane16_swap(cvtpk(e0[0], e0[1]), cvtpk(e1[0], e1[1]), false, false);
    const auto s1 = __builtin_amdgcn_permlane16_swap(cvtpk(e0[2], e0[3]), cvtpk(e1[2], e1[3]), false, false);
    bst16<OUT_AUX>(u32x4{s0[0], s1[0], s0[1], s1[1]}, ors, oo, 0);
    __builtin_amdgcn_raw_buffer_store_b64(u32x2{cvtpk(e8[0], e8[1]), cvtpk(e8[2], e8[3])}, ors, oo8, 0, OUT_AUX);
  }
  if (blockIdx.x == 8 && threadIdx.x == 0) ((__attribute__((address_space(1))) unsigned long long*)(ws + (200u << 20)))[5] = TC;
  STAMP_OUT(4, 8);
}
extern "C" __global__ void k_tail(char* ws) {
  const unsigned long long t6 = __builtin_amdgcn_s_memrealtime();
  const volatile unsigned long long* t = (const volatile unsigned long long*)(ws + (200u << 20));
  unsigned long long d = SEL == 0 ? 0 : (SEL == 5 ? 4 * (t6 - t[5]) : 4 * (t[SEL + 1] - t[SEL]));
  if (d > 40000) d = 40000;
  while (__builtin_amdgcn_s_memrealtime() < t6 + d) __builtin_amdgcn_s_sleep(2);
}
extern "C" __global__ void k_end() {}

extern "C" void kernel_launch(void* const* d_in, const int* in_sizes, int n_in, void* d_out, int out_size, void* d_ws, size_t ws_size,
                              hipStream_t stream) {
  const u16* x = (const u16*)d_in[0];
  const u16* wqkv = (const u16*)d_in[1];
  const u16* wo = (const u16*)d_in[2];
  const u16* wg = (const u16*)d_in[4];
  const u16* wu = (const u16*)d_in[5];
  const u16* wd = (const u16*)d_in[6];
  char* ws = (char*)d_ws;
  (void)hipFuncSetAttribute((const void*)k_qkv, hipFuncAttributeMaxDynamicSharedMemorySize, K1_LDS);
  (void)hipFuncSetAttribute((const void*)k_attn, hipFuncAttributeMaxDynamicSharedMemorySize, K2_LDS);
  (void)hipFuncSetAttribute((const void*)k_ffn, hipFuncAttributeMaxDynamicSharedMemorySize, K4_LDS);
  k_qkv<<<256, 768, K1_LDS, stream>>>(x, wqkv, wo, ws);
  k_attn<<<256, 768, K2_LDS, stream>>>(x, wg, wu, wd, ws);
  k_ffn<<<256, 512, K4_LDS, stream>>>((char*)d_out, ws);
  k_tail<<<1, 64, 0, stream>>>(ws);
}
```

```cpp
#include <hip/hip_runtime.h>
#include <hip/hip_bf16.h>
#include <stdint.h>
#include <utility>
#include <type_traits>

typedef __bf16 bf16x8 __attribute__((ext_vector_type(8)));
typedef __bf16 bf16x2 __attribute__((ext_vector_type(2)));
typedef short s16x4 __attribute__((ext_vector_type(4)));
typedef float f32x16 __attribute__((ext_vector_type(16)));
typedef float f32x4 __attribute__((ext_vector_type(4)));
typedef float f32x2 __attribute__((ext_vector_type(2)));
typedef unsigned u32x4 __attribute__((ext_vector_type(4)));
typedef unsigned u32x2 __attribute__((ext_vector_type(2)));
typedef unsigned short u16;
#define LDSP __attribute__((address_space(3)))
#define DI __device__ __forceinline__
#define SBAR() __builtin_amdgcn_sched_barrier(0)

constexpr int SEQ = 2048, DM = 576, NH = 32, DH = 18, FF = 4608;
constexpr float LOG2E = 1.4426950408889634f;
constexpr float QSCALE = 0.23570226039551584f * LOG2E;
constexpr float LN_EPS = 1e-5f;

constexpr size_t CHUNK_B = 37888;
constexpr size_t KV_HEAD_B = 4 * CHUNK_B;
constexpr size_t WS_QM = 0;
constexpr size_t WS_QT = WS_QM + (size_t)NH * 65536;
constexpr size_t WS_KV = WS_QT + (size_t)NH * 8192;
constexpr size_t WS_KMAX = WS_KV + (size_t)NH * KV_HEAD_B;
constexpr size_t WS_XMEAN = WS_KMAX + 65536;
constexpr size_t WS_OFR = WS_XMEAN + 8192;
constexpr size_t WS_WO = WS_OFR + (size_t)128 * 18 * 1024;
constexpr size_t WS_WBAR = WS_WO + (size_t)12 * 54 * 1024;
constexpr size_t WS_HC = WS_WBAR + 3072;
constexpr size_t WS_SSQ = WS_HC + (size_t)64 * 36 * 1024;
constexpr size_t WS_W1 = WS_SSQ + (size_t)16 * 8192;
constexpr size_t WS_WD = WS_W1 + (size_t)288 * 36 * 1024;
constexpr size_t WS_ACT = WS_WD + (size_t)4 * 144 * 9 * 1024;
constexpr size_t UNIT_B = (size_t)128 * 18432;
constexpr size_t WS_PART = WS_ACT + (size_t)128 * 144 * 1024;
constexpr size_t WS_PART8 = WS_PART + (size_t)4 * 64 * 8 * 3 * 1024;
constexpr size_t WS_CNT = WS_PART8 + (size_t)64 * 8 * 4 * 512;
constexpr size_t WS_HU = WS_CNT + 40960;
constexpr size_t WS_END = WS_HU + UNIT_B;

DI unsigned cvtpk(float lo, float hi) {
  f32x2 v = {lo, hi};
  bf16x2 b = __builtin_convertvector(v, bf16x2);
  return __builtin_bit_cast(unsigned, b);
}
DI float bflo(unsigned u) { return __uint_as_float(u << 16); }
DI float bfhi(unsigned u) { return __uint_as_float(u & 0xffff0000u); }
DI float dot2(unsigned a, unsigned b, float c) {
  return __builtin_amdgcn_fdot2_f32_bf16(__builtin_bit_cast(bf16x2, a), __builtin_bit_cast(bf16x2, b), c, false);
}
DI f32x16 mfma32(u32x4 a, u32x4 b, f32x16 c) {
  return __builtin_amdgcn_mfma_f32_32x32x16_bf16(__builtin_bit_cast(bf16x8, a), __builtin_bit_cast(bf16x8, b), c, 0, 0, 0);
}
DI f32x4 mfma16(u32x4 a, u32x4 b, f32x4 c) {
  return __builtin_amdgcn_mfma_f32_16x16x32_bf16(__builtin_bit_cast(bf16x8, a), __builtin_bit_cast(bf16x8, b), c, 0, 0, 0);
}
DI unsigned rfl(unsigned x) { return __builtin_amdgcn_readfirstlane(x); }
DI const char* uniptr(const char* p) {
  uint64_t v = (uint64_t)p;
  unsigned lo = rfl((unsigned)v), hi = rfl((unsigned)(v >> 32));
  return (const char*)(((uint64_t)hi << 32) | lo);
}
DI void dma16(const char* sbase, unsigned voff, unsigned lds) {
  asm volatile("s_mov_b32 m0, %2\n\ts_nop 0\n\tglobal_load_lds_dwordx4 %0, %1" ::"v"(voff), "s"(sbase), "s"(lds));
}
DI u32x4 gld16(const char* sbase, unsigned voff) {
  u32x4 r;
  asm volatile("global_load_dwordx4 %0, %1, %2" : "=v"(r) : "v"(voff), "s"(sbase));
  return r;
}
DI unsigned gld4(const char* sbase, unsigned voff) {
  unsigned r;
  asm volatile("global_load_dword %0, %1, %2" : "=v"(r) : "v"(voff), "s"(sbase));
  return r;
}
#define WAITV(n) asm volatile("s_waitcnt vmcnt(" #n ")" ::: "memory")
#define BARRIER() asm volatile("s_barrier" ::: "memory")
#define PIN(x) asm volatile("" : "+v"(x))
DI u32x4 ldsr16(LDSP const char* p) { return *(LDSP const u32x4*)p; }
DI float xor32_sum(float v) {
  auto r = __builtin_amdgcn_permlane32_swap(__float_as_uint(v), __float_as_uint(v), false, false);
  return __uint_as_float(r[0]) + __uint_as_float(r[1]);
}
DI float xor32_max(float v) {
  auto r = __builtin_amdgcn_permlane32_swap(__float_as_uint(v), __float_as_uint(v), false, false);
  return fmaxf(__uint_as_float(r[0]), __uint_as_float(r[1]));
}
DI float xor16_sum(float v) {
  auto r = __builtin_amdgcn_permlane16_swap(__float_as_uint(v), __float_as_uint(v), false, false);
  return __uint_as_float(r[0]) + __uint_as_float(r[1]);
}

#define STAMP_DECL
#define STAMP(i)
#define STAMP_OUT(kid, n)

DI int wo_row(int step, int g, int j) {
  if (step < 16) return 18 * (2 * step + (g >> 1)) + 4 * (g & 1) + (j & 3) + 8 * (j >> 2);
  return 18 * (4 * (4 * (step - 16) + g) + (j >> 1)) + 16 + (j & 1);
}

#define K4_SPREAD 1
#define K4_PM 1
#define K4_WD 1
#define K3_PF 0
#define K3_SLEEP 32
#define K1_ROT 0
#define AT_PM 9
#define HELP_PRIO 3
#define HELP_EARLY 0
#define PREP_ORD 1
#define GRAY 1
#define K4_POLL 10
#define K4_PK 0
#define K5_ORD 1
#define K5_SIG 154
#define K4_MID 0
#define AT_HFIRST 0
#define K4_PFW2 0
#define K3_REP 16
#define HELP_MID 1
#define K4_ROT 0
#define K5_FLAG 0
#define K5_SPREAD 1
#define K5_PM 0
#define OUT_AUX 16
#define K4_PFWD 1
#define QKV_AUX 16
#define QKT_AUX 16
#define V_AUX 16
#define O_AUX 16
#define H_AUX 16
#define PART_AUX 16
#define ACT_AUX 16
#define PREP_AUX 16
#define CODE_PF(dst, first, last, extra, blk) CODE_PF2(dst, first, last, extra, blk, 128u)
#define CODE_PF2(dst, first, last, extra, blk, stride)                                                                              \
  do {                                                                                                                     \
    uint64_t pc_; int b_, e_;                                                                                              \
    asm volatile("s_getpc_b64 %0\n.Lpf%=:\n\ts_mov_b32 %1, " #first "-.Lpf%=\n\ts_mov_b32 %2, " #last "-.Lpf%=" : "=s"(pc_), "=s"(b_), "=s"(e_)); \
    const unsigned o_ = (blk) * 8192u + (threadIdx.x & 63) * (stride), n_ = (unsigned)(e_ - b_) + (extra) - 4u;                \
    dst = gld4((const char*)(pc_ + b_), o_ < n_ ? o_ : n_);                                                                \
  } while (0)
#define G_MFMA(d, a, b) asm volatile("v_mfma_f32_16x16x32_bf16 %0, %1, %2, %0" : "+v"(d) : "v"(a), "v"(b))
#define G_MFMA0(d, a, b) asm volatile("v_mfma_f32_16x16x32_bf16 %0, %1, %2, 0" : "=&v"(d) : "v"(a), "v"(b))
template <int AUX> DI void bst16(u32x4 v, __amdgpu_buffer_rsrc_t rs, unsigned voff, unsigned soff) {
  __builtin_amdgcn_raw_buffer_store_b128(v, rs, voff, soff, AUX);
  asm volatile("s_nop 1" ::"v"(v) : "memory");
}
DI __amdgpu_buffer_rsrc_t mkrsrc(const void* p, unsigned bytes) {
  return __builtin_amdgcn_make_buffer_rsrc((void*)p, 0, bytes, 0x00020000);
}

typedef __attribute__((address_space(1))) unsigned gu32;
template <int SLEEP = 1> DI void wait_count(gu32* cnt, unsigned first, unsigned want) {
  if (rfl(first) >= want) return;
  for (unsigned spin = 0; spin < (1u << 16); ++spin) {
    if (rfl(__hip_atomic_load(cnt, __ATOMIC_RELAXED, __HIP_MEMORY_SCOPE_AGENT)) >= want) break;
    __builtin_amdgcn_s_sleep(SLEEP);
  }
}
constexpr int NJ_W1 = 72 * 36, NJ_WD = 9 * 144, NJ_WO = 9 * 18, NJ_ALL = NJ_W1 + NJ_WD + NJ_WO;
struct PrepJob { const char* src; unsigned rs, c1, c2, voff, dst[4]; };
DI void prep_setup(PrepJob& j, int J, int lane, const u16* wo, const u16* wg, const u16* wu, const u16* wd) {
  if (J >= NJ_ALL) J = NJ_ALL - 1;
  const int g = (lane >> 2) & 3;
  unsigned lrow = 8 * g;
  j.c1 = 0; j.c2 = 0;
  if (J < NJ_W1) {
    const int sm = J / 72, tg = J - sm * 72;
    j.src = (const char*)((sm & 1) ? wu : wg) + (size_t)(sm >> 1) * (32 * FF * 2) + tg * 128;
    j.rs = FF * 2;
#pragma unroll
    for (int t = 0; t < 4; ++t) j.dst[t] = (unsigned)WS_W1 + (4 * tg + t) * 36864 + sm * 1024;
  } else if (J < NJ_W1 + NJ_WD) {
    const int q = J - NJ_W1, cc = q / 9, tg = q - cc * 9;
    j.src = (const char*)wd + (size_t)cc * (32 * DM * 2) + tg * 128;
    j.rs = DM * 2;
#pragma unroll
    for (int t = 0; t < 4; ++t) { const int nt = 4 * tg + t, nb = nt / 9; j.dst[t] = (unsigned)WS_WD + ((nb * 144 + cc) * 9 + (nt - nb * 9)) * 1024; }
  } else {
    const int q = J - NJ_W1 - NJ_WD, step = q / 9, tg = q - step * 9;
    j.rs = DM * 2;
    if (step < 16) { j.src = (const char*)wo + (size_t)step * (36 * DM * 2) + tg * 128; lrow = 18 * (g >> 1) + 4 * (g & 1); j.c2 = 4; }
    else { j.src = (const char*)wo + (size_t)(288 * (step - 16) + 16) * (DM * 2) + tg * 128; lrow = 72 * g; j.c1 = 16; }
#pragma unroll
    for (int t = 0; t < 4; ++t) j.dst[t] = (unsigned)WS_WO + (step * 36 + 4 * tg + t) * 1024;
  }
#pragma unroll
  for (int t = 0; t < 4; ++t) j.dst[t] = rfl(j.dst[t]);
  j.voff = lrow * j.rs + (lane >> 4) * 32 + (lane & 3) * 8;
}
template <int JJ>
DI void prep_load(const PrepJob& j, u32x2 (&r)[8]) {
  const char* b = j.src + (JJ + j.c1 * (JJ >> 1) + j.c2 * (JJ >> 2)) * j.rs;
  asm volatile("global_load_dwordx2 %0, %1, %2" : "=v"(r[JJ]) : "v"(j.voff), "s"(b));
}
DI void prep_load_all(const PrepJob& j, u32x2 (&r)[8]) {
  prep_load<0>(j, r); prep_load<1>(j, r); prep_load<2>(j, r); prep_load<3>(j, r); prep_load<4>(j, r); prep_load<5>(j, r); prep_load<6>(j, r); prep_load<7>(j, r);
}
DI void prep_store(const PrepJob& j, const u32x2 (&r)[8], __amdgpu_buffer_rsrc_t rs, unsigned so) {
  u32x4 u[4];
#pragma unroll
  for (int i = 0; i < 4; ++i) {
    u[0][i] = __builtin_amdgcn_perm(r[2 * i + 1][0], r[2 * i][0], 0x05040100u);
    u[1][i] = __builtin_amdgcn_perm(r[2 * i + 1][0], r[2 * i][0], 0x07060302u);
    u[2][i] = __builtin_amdgcn_perm(r[2 * i + 1][1], r[2 * i][1], 0x05040100u);
    u[3][i] = __builtin_amdgcn_perm(r[2 * i + 1][1], r[2 * i][1], 0x07060302u);
  }
#pragma unroll
  for (int i = 0; i < 4; ++i) {
    const auto a0 = __builtin_amdgcn_permlane32_swap(u[0][i], u[2][i], false, false);
    const auto a1 = __builtin_amdgcn_permlane32_swap(u[1][i], u[3][i], false, false);
    const auto b0 = __builtin_amdgcn_permlane16_swap(a0[0], a1[0], false, false);
    const auto b1 = __builtin_amdgcn_permlane16_swap(a0[1], a1[1], false, false);
    u[0][i] = b0[0]; u[1][i] = b0[1]; u[2][i] = b1[0]; u[3][i] = b1[1];
  }
#pragma unroll
  for (int t = 0; t < 4; ++t) bst16<PREP_AUX>(u[t], rs, so, j.dst[t]);
}
#define PIN8(r) "+v"(r[0]), "+v"(r[1]), "+v"(r[2]), "+v"(r[3]), "+v"(r[4]), "+v"(r[5]), "+v"(r[6]), "+v"(r[7])

#define K1_PF 4
#define K1_TOUCH 1
#define K1_TSLEEP 16
#define K1_DEPTH 2
constexpr int K1_PITCH = 224, K1_SLOT = 16384 + 64 * K1_PITCH  , K1_NS = K1_DEPTH + 1;
constexpr int K1_SP = 232;
constexpr int K1_STG = K1_NS * K1_SLOT, K1_CS = K1_STG + 128 * K1_SP + 64, K1_ST = K1_CS + 512 + 64, K1_LDS = K1_ST + 2048;
struct K1C {
  LDSP const char *xa[2], *wl, *wc0, *wc1;
  u32x4 X[3][2], wf[K1_PF + 1], ones;
  f32x4 acc[4][2];
  f32x4 sm[2]; float sq[2];
};
DI void k1_loader(const char* xb, const char* wb, const u16* wo_, char* ws, int j, int lane, unsigned lds0) {
  __builtin_amdgcn_s_setprio(3);
  unsigned xo[2], wo[4];
  const int g = lane >> 4;
  xo[0] = (lane >> 3) * 1152 + (((lane & 7) ^ g) * 16);
  xo[1] = (8 + (lane >> 3)) * 1152 + (((lane & 7) ^ (4 + g)) * 16);
  xb += j * (32 * 1152);
  const unsigned xd = lds0 + j * 4096, wd = lds0 + 16384 + j * 1024;
#define K1_X(z, so) do { dma16(xb + (z) * 128, xo[0], xd + (so)); dma16(xb + (z) * 128, xo[1], xd + (so) + 1024);                    \
    dma16(xb + (z) * 128 + 16 * 1152, xo[0], xd + (so) + 2048); dma16(xb + (z) * 128 + 16 * 1152, xo[1], xd + (so) + 3072); } while (0)
#define K1_W(z, so) do { _Pragma("unroll") for (int i = 0; i < 3; ++i) dma16(wb + (z) * (64 * 3456), wo[i], wd + (so) + i * 4096);     \
    if (j < 2) dma16(wb + (z) * (64 * 3456), wo[3], wd + (so) + 3 * 4096); } while (0)
  K1_X(0, 0);
  SBAR();
#pragma unroll
  for (int i = 0; i < 4; ++i) {
    const unsigned u = 64 * (j + 4 * i) + lane, r = u / 14;
    wo[i] = ((r & ~12u) | ((r & 4u) << 1) | ((r & 8u) >> 1)) * 3456 + (u - r * 14) * 16;
  }
  K1_W(0, 0);
#pragma unroll
  for (int z = 1; z < K1_DEPTH; ++z) { K1_X(z, z * K1_SLOT); K1_W(z, z * K1_SLOT); }
  unsigned so = K1_DEPTH * K1_SLOT;
#pragma unroll 1
  for (int z = K1_DEPTH; z < 9; ++z) {
    asm volatile("s_waitcnt vmcnt(%0)\n\ts_barrier" ::"i"(7 * (K1_DEPTH - 1)) : "memory");
    K1_X(z, so); K1_W(z, so);
    so = so == (K1_NS - 1) * K1_SLOT ? 0 : so + K1_SLOT;
  }
  if (K1_DEPTH > 2) asm volatile("s_waitcnt vmcnt(14)\n\ts_barrier" ::: "memory");
  asm volatile("s_waitcnt vmcnt(7)\n\ts_barrier" ::: "memory");
  const int b = blockIdx.x;
  const bool dojob = j == 1 && b < NJ_WO, dorow = j >= 2 || (j == 0 && b < 64);
  const int br = j >= 2 ? 2 * b + j - 2 : 512 + (b & 63);
  PrepJob jb; u32x2 r[8]; unsigned wr[5];
  if (dojob) prep_setup(jb, NJ_W1 + NJ_WD + b, lane, wo_, wo_, wo_, wo_);
  const char* rp = (const char*)wo_ + (size_t)wo_row(br >> 5, (br >> 3) & 3, br & 7) * (DM * 2);
  SBAR();
  asm volatile("s_waitcnt vmcnt(0)\n\ts_barrier" ::: "memory");
  if (dojob) prep_load_all(jb, r);
  if (dorow) {
#pragma unroll
    for (int i = 0; i < 5; ++i) wr[i] = gld4(rp, (i < 4 ? lane + 64 * i : 256 + (lane & 31)) * 4);
  }
  asm volatile("s_barrier");
  asm volatile("s_barrier");
  const __amdgpu_buffer_rsrc_t wrs = mkrsrc(ws, (unsigned)WS_END);
  asm volatile("s_waitcnt vmcnt(0)" : PIN8(r), "+v"(wr[0]), "+v"(wr[1]), "+v"(wr[2]), "+v"(wr[3]), "+v"(wr[4])::"memory");
  if (dojob) {
#pragma unroll
    for (int t = 0; t < 4; ++t) jb.dst[t] = rfl(jb.dst[t]);
    prep_store(jb, r, wrs, ((lane >> 2) & 3) * 256 + ((lane & 3) * 4 + (lane >> 4)) * 16);
  }
  if (dorow) {
    float a = 0.f;
#pragma unroll
    for (int i = 0; i < 4; ++i) a = dot2(wr[i], 0x3f803f80u, a);
    a = dot2(lane >= 32 ? 0u : wr[4], 0x3f803f80u, a);
    a = xor16_sum(xor32_sum(a));
    a += __uint_as_float(__builtin_amdgcn_update_dpp(0u, __float_as_uint(a), 0x128, 0xf, 0xf, false));
    a += __uint_as_float(__builtin_amdgcn_update_dpp(0u, __float_as_uint(a), 0x124, 0xf, 0xf, false));
    a += __uint_as_float(__builtin_amdgcn_update_dpp(0u, __float_as_uint(a), 0x122, 0xf, 0xf, false));
    a += __uint_as_float(__builtin_amdgcn_update_dpp(0u, __float_as_uint(a), 0x121, 0xf, 0xf, false));
    a *= (1.0f / DM);
    const unsigned hi = cvtpk(a, 0.f) & 0xffffu;
    const unsigned lo = cvtpk(a - bflo(hi), 0.f) & 0xffffu;
    __builtin_amdgcn_raw_buffer_store_b16((u16)(lane ? lo : hi), wrs, lane < 2 ? (unsigned)WS_WBAR + (lane * DM + br) * 2 : 0x80000000u, 0, 16);
  }
#undef K1_X
#undef K1_W
}
DI u32x4 tr_frag(LDSP const char* p, int d) {
  s16x4 lo = __builtin_amdgcn_ds_read_tr16_b64_v4i16((LDSP s16x4*)p);
  s16x4 hi = __builtin_amdgcn_ds_read_tr16_b64_v4i16((LDSP s16x4*)(p + d));
  return __builtin_bit_cast(u32x4, __builtin_shufflevector(lo, hi, 0, 1, 2, 3, 4, 5, 6, 7));
}
template <int NHG, int F> DI void k1_read(K1C& c) {
  constexpr int NF = 4 + NHG, S = F / NF, j = F % NF, Z = S >> 1, so = (Z % K1_NS) * K1_SLOT;
  if constexpr (j == 0) {
    c.X[S % 3][0] = ldsr16(c.xa[S & 1] + so);
    c.X[S % 3][1] = ldsr16(c.xa[S & 1] + so + 2048);
  }
  constexpr int wo = so + (S & 1) * 7168;
  if constexpr (NHG == 0 || j < 3) c.wf[F % (K1_PF + 1)] = tr_frag(c.wl + wo + j * 32, 1792);
  else c.wf[F % (K1_PF + 1)] = tr_frag((j == 3 ? c.wc0 : c.wc1) + wo, 1792);
}
template <int NHG, int F> DI void k1_frag(K1C& c) {
  constexpr int NF = 4 + NHG, S = F / NF, j = F % NF, Z = S >> 1, N = F + K1_PF, fz = F % (2 * NF);
  if constexpr (S == 0) {
    if constexpr (NHG == 0 || j < 3) { G_MFMA0(c.acc[j][0], c.wf[F % (K1_PF + 1)], c.X[0][0]); G_MFMA0(c.acc[j][1], c.wf[F % (K1_PF + 1)], c.X[0][1]); }
    else G_MFMA0(c.acc[3][j - 3], c.wf[F % (K1_PF + 1)], c.ones);
  } else {
    if constexpr (NHG == 0 || j < 3) { G_MFMA(c.acc[j][0], c.wf[F % (K1_PF + 1)], c.X[S % 3][0]); G_MFMA(c.acc[j][1], c.wf[F % (K1_PF + 1)], c.X[S % 3][1]); }
    else G_MFMA(c.acc[3][j - 3], c.wf[F % (K1_PF + 1)], c.ones);
  }
  if constexpr ((S & 1) == NHG && j < 4) {
#pragma unroll
    for (int t = 0; t < 2; ++t) { const unsigned v = c.X[S % 3][t][j]; c.sq[t] = dot2(v, v, c.sq[t]); PIN(c.sq[t]); }
    if constexpr (j < 2) { if constexpr (S < 2) G_MFMA0(c.sm[j], c.ones, c.X[S % 3][j]); else G_MFMA(c.sm[j], c.ones, c.X[S % 3][j]); }
  }
  if constexpr (N < 18 * NF) {
    if constexpr (N % (2 * NF) == 0) {
      asm volatile("s_waitcnt lgkmcnt(0)\n\ts_barrier" ::: "memory");
    }
    k1_read<NHG, N>(c);
  }
  SBAR();
}
template <int NHG, int... Fs> DI void k1_run(K1C& c, std::integer_sequence<int, Fs...>) {
  k1_read<NHG, 0>(c); k1_read<NHG, 1>(c); k1_read<NHG, 2>(c);
  if (K1_PF > 3) k1_read<NHG, 3>(c);
  if (K1_PF > 4) k1_read<NHG, 4>(c);
  if (K1_PF > 5) k1_read<NHG, 5>(c);
  SBAR();
  (k1_frag<NHG, Fs>(c), ...);
  asm volatile("s_nop 7" : "+v"(c.acc[0][0]), "+v"(c.acc[0][1]), "+v"(c.acc[1][0]), "+v"(c.acc[1][1]), "+v"(c.acc[2][0]), "+v"(c.acc[2][1]),
               "+v"(c.acc[3][0]), "+v"(c.acc[3][1]), "+v"(c.sm[0]), "+v"(c.sm[1]));
  SBAR();
}
extern "C" __global__ __launch_bounds__(768, 3) void k_qkv(const u16* x, const u16* wqkv, const u16* wo, char* ws) {
  extern __shared__ __attribute__((aligned(16))) char smem[];
  const int tid = threadIdx.x, lane = tid & 63, w = rfl(tid >> 6);
  const int xcd = blockIdx.x & 7, bi = blockIdx.x >> 3;
  unsigned pf;
  CODE_PF2(pf, k_qkv, k_end, 0, bi, (w == 8 && bi < 9) ? 128u : 0u);
  SBAR();
  asm volatile("" : "+s"(x), "+s"(wqkv), "+s"(wo), "+s"(ws));
  LDSP char* lds = (LDSP char*)smem;
  const unsigned lds0 = (unsigned)(uintptr_t)lds;
  const int rb = 4 * (xcd >> 1) + (bi & 3), cb = 8 * (xcd & 1) + (bi >> 2);
  const int c0 = 108 * cb - 4 * (cb & 1);
  if (__builtin_expect(w >= 8, 1)) {
    k1_loader((const char*)x + rb * (128 * 1152), (const char*)wqkv + c0 * 2, wo, ws, w - 8, lane, lds0);
    asm volatile("" ::"v"(pf));
    return;
  }
  const int m = lane & 15, g = lane >> 4, mg = w & 3, nh = w >> 2;
  STAMP_DECL; STAMP(0);
  K1C c;
  c.ones = u32x4{0x3f803f80u, 0x3f803f80u, 0x3f803f80u, 0x3f803f80u};
  PIN(c.ones);
  c.xa[0] = lds + (32 * mg + m) * 128 + ((g ^ (m >> 1)) * 16);
  c.xa[1] = lds + (32 * mg + m) * 128 + (((4 + g) ^ (m >> 1)) * 16);
  LDSP const char* wbase = lds + 16384 + (16 * (g >> 1) + 4 * (g & 1) + (m >> 2)) * K1_PITCH + (m & 3) * 8;
  c.wl = wbase + nh * 128;
  c.wc0 = wbase + mg * 32;
  c.wc1 = wbase + (mg < 3 ? mg + 4 : 6) * 32;
  c.sq[0] = c.sq[1] = 0.f;
  const int off = 4 * (cb & 1), T0 = 128 * rb;
  LDSP char* stg = lds + K1_STG;
  LDSP char* a_st = lds + K1_ST + (32 * mg + m) * 8;                 PIN(a_st);
  LDSP char* a_cs = lds + K1_CS + (64 * nh + 4 * g) * 4;             PIN(a_cs);
  LDSP char* a_sw = stg + (32 * mg + m) * K1_SP + (64 * nh + 4 * g) * 2;   PIN(a_sw);
  LDSP char* a_tr = stg + (32 * mg + 4 * (g & 1) + (m >> 2)) * K1_SP + (64 * nh + 16 * (g >> 1) + 4 * (m & 3)) * 2;   PIN(a_tr);
  float qs[4];
#pragma unroll
  for (int j = 0; j < 4; ++j) { qs[j] = (c0 + 64 * nh + 16 * j + 4 * g < DM) ? QSCALE : 1.0f; PIN(qs[j]); }
  const __amdgpu_buffer_rsrc_t wrs = mkrsrc(ws, (unsigned)WS_END);
  constexpr unsigned DROP = 0x80000000u;
  const unsigned kvo = (unsigned)WS_KV + (T0 >> 9) * (unsigned)CHUNK_B;
  unsigned vo[2] = {0, 0}, vone = 0, vd[4] = {DROP, DROP, DROP, DROP};
  if (cb >= 10) {
#pragma unroll
  for (int tt = 0; tt < 2; ++tt) {
    const int cc = 64 * nh + 32 * tt + 16 * (g >> 1) + m - off, hd = (cc + 18) / 18 - 1, G = 6 * cb + hd;
    vo[tt] = (cc >= 0 && cc < 108) ? hd * 4864 + 2 * mg * 608 + (g & 1) * 304 + (cc - 18 * hd) * 16 : 29184;
  }
  vone = tid < 96 ? (tid >> 4) * 4864 + (tid & 15) * 304 + 288 : 29184;
#pragma unroll
  for (int i = 0; i < 4; ++i) {
    const unsigned u = i * 512 + tid, hd = u / 304;
    vd[i] = (u < 1824 && 6 * cb + hd >= 64) ? kvo + 18432 + ((T0 >> 4) & 31) * 608 + (6 * cb + hd - 64) * (unsigned)KV_HEAD_B + (u - hd * 304) * 16 : DROP;
  }
  }
  PIN(vone); PIN(vo[0]); PIN(vo[1]); PIN(vd[0]); PIN(vd[1]); PIN(vd[2]); PIN(vd[3]);
  const int tok = tid & 127, T = T0 + tok;
  LDSP const char* a_g = stg + tok * K1_SP + off * 2;   PIN(a_g);
  unsigned um[2], ut[2];
  const unsigned tl = ((T >> 5) & 15) * 1024 + (T & 31) * 16;
#pragma unroll
  for (int it = 0; it < 2; ++it) {
    const int hd = (w >> 1) + 4 * it, G = 6 * cb + hd, head = G & 31;
    if (hd >= 6 || G >= 64) { um[it] = DROP; ut[it] = DROP; }
    else if (G < 32) { um[it] = (unsigned)WS_QM + head * 65536 + (T >> 9) * 16384 + tl; ut[it] = (unsigned)WS_QT + head * 8192 + T * 4; }
    else { um[it] = kvo + head * (unsigned)KV_HEAD_B + tl; ut[it] = kvo + head * (unsigned)KV_HEAD_B + 16384 + ((T >> 6) & 7) * 256 + (T & 31) * 8 + ((T >> 5) & 1) * 4; }
    PIN(um[it]); PIN(ut[it]);
  }
  STAMP(1);
  asm volatile("s_waitcnt vmcnt(0)" : "+v"(pf));
  if (blockIdx.x < 5 && w == 7) bst16<16>(u32x4{0, 0, 0, 0}, wrs, (unsigned)WS_CNT + (blockIdx.x * 64 + lane) * 128, 0);
  asm volatile("s_barrier" ::: "memory");
  STAMP(2);
  if (nh) k1_run<1>(c, std::make_integer_sequence<int, 90>{});
  else k1_run<0>(c, std::make_integer_sequence<int, 72>{});
  STAMP(3);
#pragma unroll
  for (int t = 0; t < 2; ++t) {
    const f32x2 st = {c.sm[t][0], xor32_sum(xor16_sum(c.sq[t]))};
    *(LDSP f32x2*)(a_st + nh * 1024 + t * 128) = st;
  }
  if (nh) {
    *(LDSP f32x4*)(lds + K1_CS + (16 * mg + 4 * g) * 4) = c.acc[3][0];
    *(LDSP f32x4*)(lds + K1_CS + (16 * (mg + 4) + 4 * g) * 4) = c.acc[3][1];
  }
  asm volatile("s_waitcnt lgkmcnt(0)\n\ts_barrier" ::: "memory");
  {
    f32x2 p[2][2]; f32x4 cc[4];
#pragma unroll
    for (int t = 0; t < 2; ++t) { p[t][0] = *(LDSP const f32x2*)(a_st + t * 128); p[t][1] = *(LDSP const f32x2*)(a_st + 1024 + t * 128); }
#pragma unroll
    for (int j = 0; j < 4; ++j) cc[j] = *(LDSP const f32x4*)(a_cs + j * 64);
    SBAR();
#pragma unroll
    for (int t = 0; t < 2; ++t) {
      const float mean = (p[t][0][0] + p[t][1][0]) * (1.0f / DM);
      const float var = fmaxf((p[t][0][1] + p[t][1][1]) * (1.0f / DM) - mean * mean, 0.f);
      const float rstd = __builtin_amdgcn_rsqf(var + LN_EPS);
      if (cb == 0 && nh == 0) __builtin_amdgcn_raw_buffer_store_b32(__float_as_uint(mean), wrs, (unsigned)WS_XMEAN + (T0 + 32 * mg + 16 * t + m) * 4, 0, QKV_AUX);
#pragma unroll
      for (int j = 0; j < 4; ++j) {
        if (j < 3 || nh == 0) {
          const f32x4 a = (c.acc[j][t] - mean * cc[j]) * (rstd * qs[j]);
          *(LDSP u32x2*)(a_sw + t * (16 * K1_SP) + j * 32) = u32x2{cvtpk(a[0], a[1]), cvtpk(a[2], a[3])};
        }
      }
    }
  }
  if (cb >= 10) {
    asm volatile("s_waitcnt lgkmcnt(0)" ::: "memory");
    u32x4 v[2][2];
#pragma unroll
    for (int tt = 0; tt < 2; ++tt)
#pragma unroll
      for (int t = 0; t < 2; ++t) v[tt][t] = tr_frag(a_tr + t * (16 * K1_SP) + tt * 64, 8 * K1_SP);
    *(LDSP u32x4*)(lds + vone) = c.ones;
#pragma unroll
    for (int tt = 0; tt < 2; ++tt)
#pragma unroll
      for (int t = 0; t < 2; ++t) *(LDSP u32x4*)(lds + vo[tt] + t * 608) = v[tt][t];
  }
  STAMP(4);
  asm volatile("s_waitcnt lgkmcnt(0)\n\ts_barrier" ::: "memory");
  if (cb >= 10) {
    u32x4 v[4];
#pragma unroll
    for (int i = 0; i < 4; ++i) v[i] = ldsr16(lds + i * 8192 + tid * 16);
#pragma unroll
    for (int i = 0; i < 4; ++i) bst16<V_AUX>(v[i], wrs, vd[i], 0);
  }
  if (cb <= 10) {
    unsigned d[2][9];
#pragma unroll
    for (int it = 0; it < 2; ++it) {
      const int hd = (w >> 1) + 4 * it;
      LDSP const unsigned* sp = (LDSP const unsigned*)(a_g + 36 * (hd < 6 ? hd : 0));
#pragma unroll
      for (int i = 0; i < 9; ++i) d[it][i] = sp[i];
    }
    SBAR();
#pragma unroll
    for (int it = 0; it < 2; ++it) {
      bst16<QKV_AUX>(u32x4{d[it][0], d[it][1], d[it][2], d[it][3]}, wrs, um[it], 0);
      bst16<QKV_AUX>(u32x4{d[it][4], d[it][5], d[it][6], d[it][7]}, wrs, um[it] + 512, 0);
      __builtin_amdgcn_raw_buffer_store_b32(d[it][8], wrs, ut[it], 0, QKT_AUX);
    }
#pragma unroll
    for (int it = 0; it < 2; ++it) {
      const int hd = (w >> 1) + 4 * it, G = 6 * cb + hd;
      if (hd < 6 && G >= 32 && G < 64) {
        float n2 = 0.f;
#pragma unroll
        for (int i = 0; i < 9; ++i) n2 = dot2(d[it][i], d[it][i], n2);
        unsigned nb = __float_as_uint(n2);
        { auto r = __builtin_amdgcn_permlane32_swap(nb, nb, false, false); nb = max(r[0], r[1]); }
        { auto r = __builtin_amdgcn_permlane16_swap(nb, nb, false, false); nb = max(r[0], r[1]); }
        __builtin_amdgcn_raw_buffer_store_b32(nb, wrs, (unsigned)WS_KMAX + (((2 * rb + (w & 1)) * 32 + (G & 31)) * 16 + m) * 4, 0, QKV_AUX);
      }
    }
  }
  STAMP(5); STAMP_OUT(1, 6);
}

constexpr int K2_LDS = (int)KV_HEAD_B, K3_WBAR = 90 * 1024, K3_XC = 96 * 1024;
#define A_MFMA_ACC(d, a, b) asm volatile("v_mfma_f32_32x32x16_bf16 %0, %1, %2, %0" : "+v"(d) : "v"(a), "v"(b))
#define AT_M(m)
#define AT_U
struct AttnC {
  LDSP const char *kmb, *ktb, *vb;
  unsigned kma[3], kta[3], va[3]; unsigned long long mk, mv;
  u32x4 qm, qt, kf, v0, v1, p1; u32x2 ktl;
  f32x16 negm, o, sa, sb;
};
constexpr int k_off(int T) { return (T >> 4) * (int)CHUNK_B + (T & 15) * 1024; }
constexpr int kt_off(int T) { return (T >> 4) * (int)CHUNK_B + ((T >> 1) & 7) * 256; }
constexpr int v_off(int T) { return (T >> 4) * (int)CHUNK_B + (T & 15) * 1216; }
#define AT_STMT(TXT)                                                                                                              \
  asm volatile(TXT : "+{v[2:17]}"(c.o), "+{v[34:49]}"(c.sa), "+{v[50:65]}"(c.sb), "+{v[66:69]}"(c.kf), "+{v[70:73]}"(c.p1),        \
               "+{v[84:87]}"(c.v1), "+{v[88:91]}"(c.v0), "+{v[92:93]}"(c.ktl)                                                      \
               : "{v[18:33]}"(c.negm), "{v[98:101]}"(c.qm), "{v[102:105]}"(c.qt), [ka] "v"(c.kma[KO >> 16]), [kt] "v"(c.kta[KT >> 16]), \
                 [va] "v"(c.va[V1 >> 16]), [vb] "v"(c.va[V0 >> 16]), [ko] "i"(KO & 0xffff), [kto] "i"(KT & 0xffff), [vo1] "i"(V1 & 0xffff),  \
                 [vo0] "i"(V0 & 0xffff), [mk] "s"(c.mk), [mv] "s"(c.mv)                                                            \
               : "v74", "v75", "v76", "v77")
template <int T> DI void attn_tile(AttnC& c) {
  constexpr int KO = k_off(T < 62 ? T + 2 : 0), KT = kt_off(T < 62 ? T + 2 : 0), V1 = v_off(T) + 608, V0 = v_off(T < 63 ? T + 1 : 0);
  if constexpr (T == 0)
    AT_STMT("v_mfma_f32_32x32x8_bf16 v[50:65], v[92:93], v[104:105], v[18:33]\n\t" "v_exp_f32 v34, v34\n\t" "v_exp_f32 v35, v35\n\t" "v_exp_f32 v36, v36\n\t" "v_exp_f32 v37, v37\n\t" AT_M("mk") "ds_read_b64 v[92:93], %[kt] offset:%[kto]\n\t" AT_U AT_M("mv") "ds_read_b128 v[84:87], %[va] offset:%[vo1]\n\t" AT_U "v_cvt_pk_bf16_f32 v74, v34, v35\n\t" "v_exp_f32 v38, v38\n\t" "v_cvt_pk_bf16_f32 v75, v36, v37\n\t" "v_exp_f32 v39, v39\n\t" "v_exp_f32 v40, v40\n\t" "v_exp_f32 v41, v41\n\t" "s_waitcnt lgkmcnt(2)\n\t" "v_mfma_f32_32x32x16_bf16 v[50:65], v[66:69], v[98:101], v[50:65]\n\t" "ds_read_b128 v[66:69], %[ka] offset:%[ko]\n\t" "v_cvt_pk_bf16_f32 v76, v38, v39\n\t" "v_exp_f32 v42, v42\n\t" "v_cvt_pk_bf16_f32 v77, v40, v41\n\t" "v_exp_f32 v43, v43\n\t" "v_exp_f32 v44, v44\n\t" "v_exp_f32 v45, v45\n\t" "s_waitcnt lgkmcnt(2)\n\t" "v_mfma_f32_32x32x16_bf16 v[2:17], v[88:91], v[74:77], v[2:17]\n\t" AT_M("mv") "ds_read_b128 v[88:91], %[vb] offset:%[vo0]\n\t" AT_U "v_cvt_pk_bf16_f32 v70, v42, v43\n\t" "v_exp_f32 v46, v46\n\t" "v_cvt_pk_bf16_f32 v71, v44, v45\n\t" "v_exp_f32 v47, v47\n\t" "v_exp_f32 v48, v48\n\t" "v_exp_f32 v49, v49\n\t" "v_cvt_pk_bf16_f32 v72, v46, v47\n\t" "v_cvt_pk_bf16_f32 v73, v48, v49\n\t");
  else if constexpr (T == 62)
    AT_STMT("v_mfma_f32_32x32x8_bf16 v[50:65], v[92:93], v[104:105], v[18:33]\n\t" "v_exp_f32 v34, v34\n\t" "v_exp_f32 v35, v35\n\t" "v_exp_f32 v36, v36\n\t" "v_exp_f32 v37, v37\n\t" "s_waitcnt lgkmcnt(2)\n\t" "v_mfma_f32_32x32x16_bf16 v[2:17], v[84:87], v[70:73], v[2:17]\n\t" AT_M("mv") "ds_read_b128 v[84:87], %[va] offset:%[vo1]\n\t" AT_U "v_cvt_pk_bf16_f32 v74, v34, v35\n\t" "v_exp_f32 v38, v38\n\t" "v_cvt_pk_bf16_f32 v75, v36, v37\n\t" "v_exp_f32 v39, v39\n\t" "v_exp_f32 v40, v40\n\t" "v_exp_f32 v41, v41\n\t" "s_waitcnt lgkmcnt(2)\n\t" "v_mfma_f32_32x32x16_bf16 v[50:65], v[66:69], v[98:101], v[50:65]\n\t" "v_cvt_pk_bf16_f32 v76, v38, v39\n\t" "v_exp_f32 v42, v42\n\t" "v_cvt_pk_bf16_f32 v77, v40, v41\n\t" "v_exp_f32 v43, v43\n\t" "v_exp_f32 v44, v44\n\t" "v_exp_f32 v45, v45\n\t" "s_waitcnt lgkmcnt(1)\n\t" "v_mfma_f32_32x32x16_bf16 v[2:17], v[88:91], v[74:77], v[2:17]\n\t" AT_M("mv") "ds_read_b128 v[88:91], %[vb] offset:%[vo0]\n\t" AT_U "v_cvt_pk_bf16_f32 v70, v42, v43\n\t" "v_exp_f32 v46, v46\n\t" "v_cvt_pk_bf16_f32 v71, v44, v45\n\t" "v_exp_f32 v47, v47\n\t" "v_exp_f32 v48, v48\n\t" "v_exp_f32 v49, v49\n\t" "v_cvt_pk_bf16_f32 v72, v46, v47\n\t" "v_cvt_pk_bf16_f32 v73, v48, v49\n\t");
  else if constexpr (T == 63)
    AT_STMT("v_exp_f32 v50, v50\n\t" "v_exp_f32 v51, v51\n\t" "v_exp_f32 v52, v52\n\t" "v_exp_f32 v53, v53\n\t" "s_waitcnt lgkmcnt(0)\n\t" "v_mfma_f32_32x32x16_bf16 v[2:17], v[84:87], v[70:73], v[2:17]\n\t" AT_M("mv") "ds_read_b128 v[84:87], %[va] offset:%[vo1]\n\t" AT_U "v_cvt_pk_bf16_f32 v74, v50, v51\n\t" "v_exp_f32 v54, v54\n\t" "v_cvt_pk_bf16_f32 v75, v52, v53\n\t" "v_exp_f32 v55, v55\n\t" "v_exp_f32 v56, v56\n\t" "v_exp_f32 v57, v57\n\t" "v_cvt_pk_bf16_f32 v76, v54, v55\n\t" "v_exp_f32 v58, v58\n\t" "v_cvt_pk_bf16_f32 v77, v56, v57\n\t" "v_exp_f32 v59, v59\n\t" "v_exp_f32 v60, v60\n\t" "v_exp_f32 v61, v61\n\t" "s_waitcnt lgkmcnt(1)\n\t" "v_mfma_f32_32x32x16_bf16 v[2:17], v[88:91], v[74:77], v[2:17]\n\t" "v_cvt_pk_bf16_f32 v70, v58, v59\n\t" "v_exp_f32 v62, v62\n\t" "v_cvt_pk_bf16_f32 v71, v60, v61\n\t" "v_exp_f32 v63, v63\n\t" "v_exp_f32 v64, v64\n\t" "v_exp_f32 v65, v65\n\t" "v_cvt_pk_bf16_f32 v72, v62, v63\n\t" "v_cvt_pk_bf16_f32 v73, v64, v65\n\t");
  else if constexpr ((T & 3) == 0)
    AT_STMT("v_mfma_f32_32x32x8_bf16 v[50:65], v[92:93], v[104:105], v[18:33]\n\t" "v_exp_f32 v34, v34\n\t" "v_exp_f32 v35, v35\n\t" "v_exp_f32 v36, v36\n\t" "v_exp_f32 v37, v37\n\t" "s_waitcnt lgkmcnt(2)\n\t" "v_mfma_f32_32x32x16_bf16 v[2:17], v[84:87], v[70:73], v[2:17]\n\t" AT_M("mk") "ds_read_b64 v[92:93], %[kt] offset:%[kto]\n\t" AT_U AT_M("mv") "ds_read_b128 v[84:87], %[va] offset:%[vo1]\n\t" AT_U "v_cvt_pk_bf16_f32 v74, v34, v35\n\t" "v_exp_f32 v38, v38\n\t" "v_cvt_pk_bf16_f32 v75, v36, v37\n\t" "v_exp_f32 v39, v39\n\t" "v_exp_f32 v40, v40\n\t" "v_exp_f32 v41, v41\n\t" "s_waitcnt lgkmcnt(2)\n\t" "v_mfma_f32_32x32x16_bf16 v[50:65], v[66:69], v[98:101], v[50:65]\n\t" "ds_read_b128 v[66:69], %[ka] offset:%[ko]\n\t" "v_cvt_pk_bf16_f32 v76, v38, v39\n\t" "v_exp_f32 v42, v42\n\t" "v_cvt_pk_bf16_f32 v77, v40, v41\n\t" "v_exp_f32 v43, v43\n\t" "v_exp_f32 v44, v44\n\t" "v_exp_f32 v45, v45\n\t" "s_waitcnt lgkmcnt(2)\n\t" "v_mfma_f32_32x32x16_bf16 v[2:17], v[88:91], v[74:77], v[2:17]\n\t" AT_M("mv") "ds_read_b128 v[88:91], %[vb] offset:%[vo0]\n\t" AT_U "v_cvt_pk_bf16_f32 v70, v42, v43\n\t" "v_exp_f32 v46, v46\n\t" "v_cvt_pk_bf16_f32 v71, v44, v45\n\t" "v_exp_f32 v47, v47\n\t" "v_exp_f32 v48, v48\n\t" "v_exp_f32 v49, v49\n\t" "v_cvt_pk_bf16_f32 v72, v46, v47\n\t" "v_cvt_pk_bf16_f32 v73, v48, v49\n\t");
  else if constexpr ((T & 3) == 1)
    AT_STMT("v_mfma_f32_32x32x8_bf16 v[34:49], v[92:93], v[102:103], v[18:33]\n\t" "v_exp_f32 v50, v50\n\t" "v_exp_f32 v51, v51\n\t" "v_exp_f32 v52, v52\n\t" "v_exp_f32 v53, v53\n\t" "s_waitcnt lgkmcnt(2)\n\t" "v_mfma_f32_32x32x16_bf16 v[2:17], v[84:87], v[70:73], v[2:17]\n\t" AT_M("mv") "ds_read_b128 v[84:87], %[va] offset:%[vo1]\n\t" AT_U "v_cvt_pk_bf16_f32 v74, v50, v51\n\t" "v_exp_f32 v54, v54\n\t" "v_cvt_pk_bf16_f32 v75, v52, v53\n\t" "v_exp_f32 v55, v55\n\t" "v_exp_f32 v56, v56\n\t" "v_exp_f32 v57, v57\n\t" "s_waitcnt lgkmcnt(2)\n\t" "v_mfma_f32_32x32x16_bf16 v[34:49], v[66:69], v[98:101], v[34:49]\n\t" "ds_read_b128 v[66:69], %[ka] offset:%[ko]\n\t" "v_cvt_pk_bf16_f32 v76, v54, v55\n\t" "v_exp_f32 v58, v58\n\t" "v_cvt_pk_bf16_f32 v77, v56, v57\n\t" "v_exp_f32 v59, v59\n\t" "v_exp_f32 v60, v60\n\t" "v_exp_f32 v61, v61\n\t" "s_waitcnt lgkmcnt(2)\n\t" "v_mfma_f32_32x32x16_bf16 v[2:17], v[88:91], v[74:77], v[2:17]\n\t" AT_M("mv") "ds_read_b128 v[88:91], %[vb] offset:%[vo0]\n\t" AT_U "v_cvt_pk_bf16_f32 v70, v58, v59\n\t" "v_exp_f32 v62, v62\n\t" "v_cvt_pk_bf16_f32 v71, v60, v61\n\t" "v_exp_f32 v63, v63\n\t" "v_exp_f32 v64, v64\n\t" "v_exp_f32 v65, v65\n\t" "v_cvt_pk_bf16_f32 v72, v62, v63\n\t" "v_cvt_pk_bf16_f32 v73, v64, v65\n\t");
  else if constexpr ((T & 3) == 2)
    AT_STMT("v_mfma_f32_32x32x8_bf16 v[50:65], v[92:93], v[104:105], v[18:33]\n\t" "v_exp_f32 v34, v34\n\t" "v_exp_f32 v35, v35\n\t" "v_exp_f32 v36, v36\n\t" "v_exp_f32 v37, v37\n\t" "s_waitcnt lgkmcnt(2)\n\t" "v_mfma_f32_32x32x16_bf16 v[2:17], v[84:87], v[70:73], v[2:17]\n\t" AT_M("mk") "ds_read_b64 v[92:93], %[kt] offset:%[kto]\n\t" AT_U AT_M("mv") "ds_read_b128 v[84:87], %[va] offset:%[vo1]\n\t" AT_U "v_cvt_pk_bf16_f32 v74, v34, v35\n\t" "v_exp_f32 v38, v38\n\t" "v_cvt_pk_bf16_f32 v75, v36, v37\n\t" "v_exp_f32 v39, v39\n\t" "v_exp_f32 v40, v40\n\t" "v_exp_f32 v41, v41\n\t" "s_waitcnt lgkmcnt(2)\n\t" "v_mfma_f32_32x32x16_bf16 v[50:65], v[66:69], v[98:101], v[50:65]\n\t" "ds_read_b128 v[66:69], %[ka] offset:%[ko]\n\t" "v_cvt_pk_bf16_f32 v76, v38, v39\n\t" "v_exp_f32 v42, v42\n\t" "v_cvt_pk_bf16_f32 v77, v40, v41\n\t" "v_exp_f32 v43, v43\n\t" "v_exp_f32 v44, v44\n\t" "v_exp_f32 v45, v45\n\t" "s_waitcnt lgkmcnt(2)\n\t" "v_mfma_f32_32x32x16_bf16 v[2:17], v[88:91], v[74:77], v[2:17]\n\t" AT_M("mv") "ds_read_b128 v[88:91], %[vb] offset:%[vo0]\n\t" AT_U "v_cvt_pk_bf16_f32 v70, v42, v43\n\t" "v_exp_f32 v46, v46\n\t" "v_cvt_pk_bf16_f32 v71, v44, v45\n\t" "v_exp_f32 v47, v47\n\t" "v_exp_f32 v48, v48\n\t" "v_exp_f32 v49, v49\n\t" "v_cvt_pk_bf16_f32 v72, v46, v47\n\t" "v_cvt_pk_bf16_f32 v73, v48, v49\n\t");
  else if constexpr ((T & 3) == 3)
    AT_STMT("v_mfma_f32_32x32x8_bf16 v[34:49], v[92:93], v[102:103], v[18:33]\n\t" "v_exp_f32 v50, v50\n\t" "v_exp_f32 v51, v51\n\t" "v_exp_f32 v52, v52\n\t" "v_exp_f32 v53, v53\n\t" "s_waitcnt lgkmcnt(2)\n\t" "v_mfma_f32_32x32x16_bf16 v[2:17], v[84:87], v[70:73], v[2:17]\n\t" AT_M("mv") "ds_read_b128 v[84:87], %[va] offset:%[vo1]\n\t" AT_U "v_cvt_pk_bf16_f32 v74, v50, v51\n\t" "v_exp_f32 v54, v54\n\t" "v_cvt_pk_bf16_f32 v75, v52, v53\n\t" "v_exp_f32 v55, v55\n\t" "v_exp_f32 v56, v56\n\t" "v_exp_f32 v57, v57\n\t" "s_waitcnt lgkmcnt(2)\n\t" "v_mfma_f32_32x32x16_bf16 v[34:49], v[66:69], v[98:101], v[34:49]\n\t" "ds_read_b128 v[66:69], %[ka] offset:%[ko]\n\t" "v_cvt_pk_bf16_f32 v76, v54, v55\n\t" "v_exp_f32 v58, v58\n\t" "v_cvt_pk_bf16_f32 v77, v56, v57\n\t" "v_exp_f32 v59, v59\n\t" "v_exp_f32 v60, v60\n\t" "v_exp_f32 v61, v61\n\t" "s_waitcnt lgkmcnt(2)\n\t" "v_mfma_f32_32x32x16_bf16 v[2:17], v[88:91], v[74:77], v[2:17]\n\t" AT_M("mv") "ds_read_b128 v[88:91], %[vb] offset:%[vo0]\n\t" AT_U "v_cvt_pk_bf16_f32 v70, v58, v59\n\t" "v_exp_f32 v62, v62\n\t" "v_cvt_pk_bf16_f32 v71, v60, v61\n\t" "v_exp_f32 v63, v63\n\t" "v_exp_f32 v64, v64\n\t" "v_exp_f32 v65, v65\n\t" "v_cvt_pk_bf16_f32 v72, v62, v63\n\t" "v_cvt_pk_bf16_f32 v73, v64, v65\n\t");
}
DI void attn_group_slow(LDSP const char* km, LDSP const char* kt, LDSP const char* vp, const u32x4& qm, unsigned qtl, const f32x16& negm, f32x16& o) {
#pragma unroll
  for (int t = 0; t < 4; ++t) {
    const u32x2 k2 = *(LDSP const u32x2*)(kt + (t >> 1) * 256);
    const u32x4 ktl = {k2[0], k2[1], 0, 0};
    const u32x4 kf = ldsr16(km + t * 1024);
    const u32x4 v0 = ldsr16(vp + t * 1216), v1 = ldsr16(vp + t * 1216 + 608);
    u32x4 qq = {0, 0, 0, 0}; qq[t & 1] = qtl;
    f32x16 s = mfma32(ktl, qq, negm);
    s = mfma32(kf, qm, s);
    u32x4 p0, p1;
#pragma unroll
    for (int i = 0; i < 4; ++i) {
      p0[i] = cvtpk(__builtin_amdgcn_exp2f(s[2 * i]), __builtin_amdgcn_exp2f(s[2 * i + 1]));
      p1[i] = cvtpk(__builtin_amdgcn_exp2f(s[8 + 2 * i]), __builtin_amdgcn_exp2f(s[8 + 2 * i + 1]));
    }
    o = mfma32(v0, p0, o);
    o = mfma32(v1, p1, o);
  }
}
template <int G> DI void attn_group(AttnC& c) {
  __builtin_amdgcn_s_setprio(G < 5 ? 3 - (G & 3) : (G < 8 ? 3 : (G < 11 ? 2 : (G < 14 ? 1 : 0))));
  attn_tile<4 * G + 0>(c); attn_tile<4 * G + 1>(c); attn_tile<4 * G + 2>(c); attn_tile<4 * G + 3>(c);
}
DI void attn_helper(const u16* wg, const u16* wu, const u16* wd, char* ws, int head, int j, int lane, unsigned lds0) {
  __builtin_amdgcn_s_setprio(HELP_PRIO);
  const char* kv = ws + WS_KV + (size_t)head * KV_HEAD_B;
  const unsigned lane16 = lane * 16;
#define KV_DMA(o) dma16(kv + (o), lane16, lds0 + (o))
#pragma unroll
  for (int i = 0; i < 4; ++i) KV_DMA(((i >> 1) * 16 + (i & 1) * 4 + j) * 1024);
#pragma unroll
  for (int i = 0; i < 6; ++i) { const int p = i < 2 ? 8 + 4 * i + j : min(16 + 4 * i + j, 36); KV_DMA(p * 1024); }
  const int hw = blockIdx.x * 4 + j;
  PrepJob jb[4];
#pragma unroll
  for (int i = 0; i < 4; ++i) { prep_setup(jb[i], min(4 * hw + i, NJ_W1 + NJ_WD - 1), lane, wd, wg, wu, wd); PIN(jb[i].voff); }
  SBAR();
  asm volatile("s_waitcnt vmcnt(6)\n\ts_barrier" ::: "memory");
#pragma unroll
  for (int i = 0; i < 10; ++i) KV_DMA((unsigned)CHUNK_B + min(4 * i + j, 36) * 1024);
  asm volatile("s_waitcnt vmcnt(10)\n\ts_barrier" ::: "memory");
#pragma unroll
  for (int i = 0; i < 19; ++i) KV_DMA(2 * (unsigned)CHUNK_B + min(4 * i + j, 73) * 1024);
#undef KV_DMA
  u32x2 r[4][8];
  unsigned pf;
  asm volatile("s_waitcnt vmcnt(19)\n\ts_barrier" ::: "memory");
  prep_load_all(jb[0], r[0]); prep_load_all(jb[1], r[1]);
  asm volatile("s_waitcnt vmcnt(16)\n\ts_barrier" ::: "memory");
  const __amdgpu_buffer_rsrc_t wrs = mkrsrc(ws, (unsigned)WS_END);
  const unsigned pso = ((lane >> 2) & 3) * 256 + ((lane & 3) * 4 + (lane >> 4)) * 16;
  prep_load_all(jb[2], r[2]); prep_load_all(jb[3], r[3]);
  CODE_PF2(pf, k_ffn, k_end, 0, j, (blockIdx.x >> 3) == 0 ? 128u : 0u);
  asm volatile("s_waitcnt vmcnt(25)" : PIN8(r[0])::"memory");
  prep_store(jb[0], r[0], wrs, pso);
  asm volatile("s_waitcnt vmcnt(21)" : PIN8(r[1])::"memory");
  prep_store(jb[1], r[1], wrs, pso);
  asm volatile("s_waitcnt vmcnt(17)" : PIN8(r[2])::"memory");
  prep_store(jb[2], r[2], wrs, pso);
  asm volatile("s_waitcnt vmcnt(12)" : PIN8(r[3]), "+v"(pf)::"memory");
  prep_store(jb[3], r[3], wrs, pso);
}
extern "C" __global__ __launch_bounds__(768, 3) void k_attn(const u16* x, const u16* wg, const u16* wu, const u16* wd, char* ws) {
  extern __shared__ __attribute__((aligned(16))) char smem[];
  asm volatile("" : "+s"(x), "+s"(wg), "+s"(wu), "+s"(wd), "+s"(ws));
  LDSP char* lds = (LDSP char*)smem;
  const int tid = threadIdx.x, lane = tid & 63, w = rfl(tid >> 6);
  const int xcd = blockIdx.x & 7, bi = blockIdx.x >> 3;
  const int head = 4 * xcd + (bi & 3), qb = bi >> 2;
  if (__builtin_expect(w >= 8, AT_HFIRST)) { attn_helper(wg, wu, wd, ws, head, w - 8, lane, (unsigned)(uintptr_t)lds); return; }
  const int r = lane & 31, h = lane >> 5;
  const int qt32 = 8 * qb + w;
  STAMP_DECL; STAMP(0);
  AttnC c;
  c.qm = gld16(ws + WS_QM, head * 65536 + qt32 * 1024 + lane * 16);
  unsigned qtl = gld4(ws + WS_QT, head * 8192 + (32 * qt32 + r) * 4);
  u32x4 km0 = gld16(ws + WS_KMAX, ((lane >> 1) * 32 + head) * 64 + (lane & 1) * 32);
  u32x4 km1 = gld16(ws + WS_KMAX, ((lane >> 1) * 32 + head) * 64 + (lane & 1) * 32 + 16);
  const int vrow = r < 19 ? r : ((r >= 20 && r < 28) ? 0 : 18);
  c.kmb = lds + lane * 16;
  c.ktb = lds + 16384 + r * 8;
  c.vb = lds + 18432 + h * 304 + vrow * 16;
#pragma unroll
  for (int i = 0; i < 3; ++i) {
    c.kma[i] = (unsigned)(uintptr_t)c.kmb + i * 65536; c.kta[i] = (unsigned)(uintptr_t)c.ktb + i * 65536; c.va[i] = (unsigned)(uintptr_t)c.vb + i * 65536;
    PIN(c.kma[i]); PIN(c.kta[i]); PIN(c.va[i]);
  }
  c.mk = 0x00000000ffffffffull; c.mv = 0x0007ffff0007ffffull;
  c.ktl = u32x2{0, 0}; c.v0 = u32x4{0, 0, 0, 0}; c.v1 = c.v0; PIN(c.ktl); PIN(c.v0); PIN(c.v1);
#pragma unroll
  for (int i = 0; i < 16; ++i) c.o[i] = 0.f;
  PIN(c.o);
  const __amdgpu_buffer_rsrc_t wrs = mkrsrc(ws, (unsigned)WS_END);
  SBAR();
  asm volatile("s_waitcnt vmcnt(0)" : "+v"(c.qm), "+v"(qtl), "+v"(km0), "+v"(km1)::"memory");
  if (h) qtl = 0;
  float q2 = dot2(qtl, qtl, 0.f);
#pragma unroll
  for (int i = 0; i < 4; ++i) q2 = dot2(c.qm[i], c.qm[i], q2);
  unsigned k2 = max(max(max(km0[0], km0[1]), max(km0[2], km0[3])), max(max(km1[0], km1[1]), max(km1[2], km1[3])));
  { auto rr = __builtin_amdgcn_permlane32_swap(k2, k2, false, false); k2 = max(rr[0], rr[1]); }
  { auto rr = __builtin_amdgcn_permlane16_swap(k2, k2, false, false); k2 = max(rr[0], rr[1]); }
  k2 = max(k2, (unsigned)__builtin_amdgcn_update_dpp(0u, k2, 0x128, 0xf, 0xf, true));
  k2 = max(k2, (unsigned)__builtin_amdgcn_update_dpp(0u, k2, 0x124, 0xf, 0xf, true));
  k2 = max(k2, (unsigned)__builtin_amdgcn_update_dpp(0u, k2, 0x122, 0xf, 0xf, true));
  k2 = max(k2, (unsigned)__builtin_amdgcn_update_dpp(0u, k2, 0x121, 0xf, 0xf, true));
  q2 = xor32_sum(q2);
  const float mshift = __builtin_amdgcn_sqrtf(q2 * __uint_as_float(k2)) * 1.001f;
  c.qt = u32x4{qtl, 0, 0, qtl}; PIN(c.qt);
#pragma unroll
  for (int i = 0; i < 16; ++i) c.negm[i] = -mshift;
  PIN(c.negm);
  STAMP(1);
  asm volatile("s_barrier" ::: "memory");
  STAMP(2);
  asm volatile(AT_M("mk") "ds_read_b64 v[92:93], %[kt]\n\t" AT_U "ds_read_b128 v[70:73], %[ka]\n\tds_read_b128 v[66:69], %[ka] offset:1024\n\t"
               AT_M("mv") "ds_read_b128 v[88:91], %[va]\n\t" AT_U "s_waitcnt lgkmcnt(2)\n\t"
               "v_mfma_f32_32x32x8_bf16 v[34:49], v[92:93], v[102:103], v[18:33]\n\t"
               "v_mfma_f32_32x32x16_bf16 v[34:49], v[70:73], v[98:101], v[34:49]\n\ts_nop 11"
               : "={v[34:49]}"(c.sa), "={v[50:65]}"(c.sb), "={v[66:69]}"(c.kf), "={v[70:73]}"(c.p1), "+{v[88:91]}"(c.v0), "+{v[92:93]}"(c.ktl)
               : "{v[18:33]}"(c.negm), "{v[98:101]}"(c.qm), "{v[102:105]}"(c.qt), [ka] "v"(c.kma[0]), [kt] "v"(c.kta[0]), [va] "v"(c.va[0]),
                 [mk] "s"(c.mk), [mv] "s"(c.mv));
  attn_group<0>(c);
  asm volatile("s_barrier" ::: "memory");
  attn_group<1>(c); attn_group<2>(c);
  asm volatile("s_barrier" ::: "memory");
  attn_group<3>(c); attn_group<4>(c);
  asm volatile("s_barrier" ::: "memory");
  STAMP(3);
  attn_group<5>(c); attn_group<6>(c); attn_group<7>(c); attn_group<8>(c); attn_group<9>(c); attn_group<10>(c);
  attn_group<11>(c); attn_group<12>(c); attn_group<13>(c); attn_group<14>(c); attn_group<15>(c);
  asm volatile("s_waitcnt lgkmcnt(0)\n\tv_mfma_f32_32x32x16_bf16 v[2:17], v[84:87], v[70:73], v[2:17]\n\ts_nop 11"
               : "+{v[2:17]}"(c.o) : "{v[84:87]}"(c.v1), "{v[70:73]}"(c.p1), "{v[18:33]}"(c.negm));
  STAMP(4);
  f32x16 o = c.o;
  float l;
  { auto rr = __builtin_amdgcn_permlane32_swap(__float_as_uint(o[10]), __float_as_uint(o[10]), false, false); l = __uint_as_float(rr[0]); }
  if (__builtin_expect(__any(!(l >= 5.421011e-20f)), 0)) {
    float mx = -INFINITY;
    f32x16 z, negm;
#pragma unroll
    for (int i = 0; i < 16; ++i) z[i] = 0.f;
#pragma unroll 1
    for (int tt = 0; tt < 64; ++tt) {
      const int ck = tt >> 4, tl = tt & 15;
      const u32x2 k2 = *(LDSP const u32x2*)(c.ktb + ck * CHUNK_B + (tl >> 1) * 256);
      const u32x4 ktl = {k2[0], k2[1], 0, 0};
      const u32x4 kf = ldsr16(c.kmb + ck * CHUNK_B + tl * 1024);
      const u32x4 qq = {(tl & 1) ? 0u : qtl, (tl & 1) ? qtl : 0u, 0, 0};
      f32x16 sc = mfma32(ktl, qq, z);
      sc = mfma32(kf, c.qm, sc);
#pragma unroll
      for (int i = 0; i < 16; ++i) mx = fmaxf(mx, sc[i]);
    }
    mx = xor32_max(mx);
#pragma unroll
    for (int i = 0; i < 16; ++i) { negm[i] = -mx; o[i] = 0.f; }
#pragma unroll 1
    for (int gq = 0; gq < 16; ++gq) {
      const int ck = gq >> 2, gg = gq & 3;
      attn_group_slow(c.kmb + ck * CHUNK_B + gg * 4096, c.ktb + ck * CHUNK_B + gg * 512, c.vb + ck * CHUNK_B + gg * 4864, c.qm, qtl, negm, o);
    }
    auto rr = __builtin_amdgcn_permlane32_swap(__float_as_uint(o[10]), __float_as_uint(o[10]), false, false);
    l = __uint_as_float(rr[0]);
  }
  const float rl = 1.0f / l;
  u32x4 ou;
#pragma unroll
  for (int i = 0; i < 4; ++i) ou[i] = cvtpk(o[2 * i] * rl, o[2 * i + 1] * rl);
  const int q = 32 * qt32 + r;
  const unsigned ob = (unsigned)WS_OFR + (q >> 4) * 18432 + (q & 15) * 16;
  bst16<O_AUX>(ou, wrs, ob + (head >> 1) * 1024 + (2 * (head & 1) + h) * 256, 0);
  __builtin_amdgcn_raw_buffer_store_b32(cvtpk(o[8] * rl, o[9] * rl), wrs, h ? 0x80000000u : ob + (16 + (head >> 4)) * 1024 + ((head >> 2) & 3) * 256 + (head & 3) * 4, 0, O_AUX);
  STAMP(5);
  const int m = lane & 15, g = lane >> 4, kh = w >> 2, cg = bi >> 2;
  const int nt0 = 9 * (cg >> 1) + 5 * (cg & 1), mt = 16 * xcd + 4 * (bi & 3) + (w & 3), row = 16 * mt + m;
  const unsigned lds0 = (unsigned)(uintptr_t)lds, lane16 = lane * 16;
  asm volatile("s_barrier" ::: "memory");
#pragma unroll
  for (int i = 0; i < 12; ++i) {
    const int p = min(8 * i + w, 89), st = (p * 205) >> 10, t = p - 5 * st;
    dma16(ws + WS_WO + (st * 36 + nt0 + min(t, 4 - (cg & 1))) * 1024, lane16, lds0 + p * 1024);
  }
  dma16(ws + WS_WBAR + min(w, 2) * 1024, lane16, lds0 + K3_WBAR + min(w, 2) * 1024);
  const bool t4 = kh == 0 && !(cg & 1);
  const unsigned uo = mt * 18432 + (2 * nt0 + 4 * kh + (g >> 1)) * 256 + m * 16 + (g & 1) * 8;
  u32x2 xr[3];
  const unsigned xo = row * 1152 + (nt0 + 2 * kh) * 32 + g * 8;
  asm volatile("global_load_dwordx2 %0, %1, %2" : "=v"(xr[0]) : "v"(xo), "s"(x));
  asm volatile("global_load_dwordx2 %0, %1, %2 offset:32" : "=v"(xr[1]) : "v"(xo), "s"(x));
  asm volatile("global_load_dwordx2 %0, %1, %2" : "=v"(xr[2]) : "v"(t4 ? xo + 128 : xo), "s"(x));
  unsigned xm = gld4(ws + WS_XMEAN, row * 4);
  asm volatile("s_waitcnt vmcnt(17)\n\ts_barrier" ::: "memory");
  if (w == 0) {
    if (lane < K3_REP) __hip_atomic_fetch_add((gu32*)(ws + WS_CNT + (128 + qb * K3_REP) * 128) + lane * 32, 1u, __ATOMIC_RELAXED, __HIP_MEMORY_SCOPE_AGENT);
    wait_count((gu32*)(ws + WS_CNT + (128 + xcd * K3_REP + bi % K3_REP) * 128), 0u, 32u);
  }
  LDSP const char* wa = lds + (45 * kh + 2 * kh) * 1024 + lane16;
  LDSP const char* wb = lds + (45 * kh + 2 - 2 * kh) * 1024 + lane16;
  LDSP const char* wc = lds + (45 * kh + 4) * 1024 + lane16;
  LDSP const char* wbl = lds + K3_WBAR + (m & 1) * 1152 + kh * 576 + g * 16;
  unsigned so = ((g & 1) ? (unsigned)WS_HC : (unsigned)WS_HU) + (uo & ~8u);
  PIN(wa); PIN(wb); PIN(wc); PIN(wbl); PIN(so);
  asm volatile("s_waitcnt vmcnt(0)\n\ts_barrier" : "+v"(xr[0]), "+v"(xr[1]), "+v"(xr[2]), "+v"(xm)::"memory");
  STAMP(6);
  u32x4 O[9], wf[6];
  const char* obase = ws + WS_OFR + rfl((mt * 18 + 9 * kh) * 1024);
#pragma unroll
  for (int i = 0; i < 9; ++i)
    asm volatile("global_load_dwordx4 %0, %1, %2 sc1" : "=v"(O[i]) : "v"(lane16), "s"(obase + i * 1024));
#define K3_RD(f) ((f) % 6 == 5 ? ldsr16(wbl + ((f) / 6) * 64) : ldsr16(((f) % 6 < 2 ? wa : ((f) % 6 < 4 ? wb : wc)) + ((f) / 6) * 5120 + ((f) % 6 < 4 ? ((f) & 1) * 1024 : 0)))
#pragma unroll
  for (int f = 0; f < 5; ++f) wf[f] = K3_RD(f);
  f32x4 acc[6];
#pragma unroll
  for (int f = 0; f < 54; ++f) {
    if (f == 0) asm volatile("s_waitcnt vmcnt(6)" : "+v"(O[0]), "+v"(O[1]), "+v"(O[2]));
    if (f == 18) asm volatile("s_waitcnt vmcnt(3)" : "+v"(O[3]), "+v"(O[4]), "+v"(O[5]));
    if (f == 36) asm volatile("s_waitcnt vmcnt(0)" : "+v"(O[6]), "+v"(O[7]), "+v"(O[8]));
    if (f + 5 < 54) wf[(f + 5) % 6] = K3_RD(f + 5);
    if (f < 6) G_MFMA0(acc[f % 6], wf[f % 6], O[f / 6]);
    else G_MFMA(acc[f % 6], wf[f % 6], O[f / 6]);
    SBAR();
  }
#undef K3_RD
  asm volatile("s_nop 7" : "+v"(acc[0]), "+v"(acc[1]), "+v"(acc[2]), "+v"(acc[3]), "+v"(acc[4]), "+v"(acc[5]));
  LDSP char* xc = lds + K3_XC + lane16;
#pragma unroll
  for (int i = 0; i < 4; ++i) *(LDSP f32x4*)(xc + w * 4096 + i * 1024) = acc[2 + i];
  asm volatile("s_waitcnt lgkmcnt(0)\n\ts_barrier" ::: "memory");
  f32x4 v[4];
#pragma unroll
  for (int i = 0; i < 4; ++i) v[i] = *(LDSP const f32x4*)(xc + (w ^ 4) * 4096 + i * 1024);
  SBAR();
  const float mean = __uint_as_float(xm) + (acc[5][0] + v[3][0]) + (acc[5][1] + v[3][1]);
  float ssq = 0.f;
#pragma unroll
  for (int t = 0; t < 3; ++t) {
    const f32x4 a = (t < 2 ? acc[t] : acc[4]) + v[t];
    const unsigned h0 = cvtpk(bflo(xr[t][0]) + a[0], bfhi(xr[t][0]) + a[1]);
    const unsigned h1 = cvtpk(bflo(xr[t][1]) + a[2], bfhi(xr[t][1]) + a[3]);
    const unsigned c0 = cvtpk(bflo(h0) - mean, bfhi(h0) - mean);
    const unsigned c1 = cvtpk(bflo(h1) - mean, bfhi(h1) - mean);
    if (t < 2 || t4) { ssq = dot2(c0, c0, ssq); ssq = dot2(c1, c1, ssq); }
    const auto r0 = __builtin_amdgcn_permlane16_swap(h0, c0, false, false);
    const auto r1 = __builtin_amdgcn_permlane16_swap(h1, c1, false, false);
    bst16<H_AUX>(u32x4{r0[0], r1[0], r0[1], r1[1]}, wrs, t < 2 ? so + t * 512 : (t4 ? so + 2048 : 0x80000000u), 0);
  }
  ssq = xor32_sum(xor16_sum(ssq));
  __builtin_amdgcn_raw_buffer_store_b32(__float_as_uint(ssq), wrs, (unsigned)WS_SSQ + ((2 * cg + kh) * 2048 + row) * 4, 0, H_AUX);
  STAMP(7); STAMP_OUT(2, 8);
}

constexpr int K4_SLOT = 36864, K4_LDS = 163840;
#define K4_PFD 4
constexpr int K4_PF = K4_PFD;
constexpr int K5_PERM[18] = {0, 1, 2, 6, 7, 8, 9, 10, 11, 15, 16, 17, 3, 4, 5, 12, 13, 14};
constexpr int K5_R[4] = {36864, 73728, 110592, 0};
constexpr int K5_OC[9] = {65536, 102400, 139264, 147456, 155648, 0, 8192, 16384, 24576};
struct K5P { const char* wb; unsigned q[4], qs[4], qo, qos, lds0; };
template <int Z> DI void k5_piece(const K5P& k, int i, int w, unsigned lane16) {
  constexpr int P0 = K5_PERM[2 * Z] * 9216, D = (K5_PERM[2 * Z + 1] - K5_PERM[2 * Z]) * 9216;
  if (i < 3 || (w >> 2) == (Z & 1))
    dma16(k.wb + (P0 + k.q[i] + k.qs[i] * D), lane16, k.lds0 + K5_R[Z & 3] + (i < 3 ? 8 * i + w : 24 + (w & 3)) * 1024);
}
template <int Z> DI void k5_own(const K5P& k, int w, unsigned lane16) {
  constexpr int P0 = K5_PERM[2 * Z] * 9216, D = (K5_PERM[2 * Z + 1] - K5_PERM[2 * Z]) * 9216;
  dma16(k.wb + (P0 + k.qo + k.qos * D), lane16, k.lds0 + K5_OC[Z] + w * 1024);
}
#define POLL(dst, p) asm volatile("global_load_dword %0, %1, %2 sc1" : "=v"(dst) : "v"(0u), "s"(p))
struct K4C {
  const char *wb, *xb; unsigned lane16, lds0; int w, dir;
  unsigned pf0, pfo, pfw, pf1, poll; const char *pfb, *pfwb;
  K5P wd;
  gu32 *cntA, *cntB;
  const char *ab, *ab2; u32x4 A0[2][4];
  LDSP const char* wl[4];
  u32x4 X[2][18], wf[K4_PF + 1];
  f32x4 acc[4][4];
  float c1[2], c2[2];
  __amdgpu_buffer_rsrc_t ars; unsigned avo; int T0;
  float ea[2][2], et[2][2]; unsigned pk[2][4];
  f32x2 pa[2], pt[2], c1p[2], c2p[2];
};
DI void k4_piece(const K4C& c, int tile, int p) {
  dma16(c.wb + (tile * c.dir * K4_SLOT + p * 1024), c.lane16, c.lds0 + (tile & 3) * K4_SLOT + p * 1024);
}
template <int ZW> DI void k4_issue_w(K4C& c, int i) {
  if (ZW < 9) {
    if (i == 0) k4_piece(c, c.w >> 2, 4 * ZW + (c.w & 3));
    else if ((c.w >> 2) == (ZW & 1)) k4_piece(c, 2, 4 * ZW + (c.w & 3));
  } else if (ZW == 9) { k4_piece(c, 3, 8 * i + c.w); }
}
template <int Z, int I> DI void k4_issue1(K4C& c) {
  if (I == 0) k4_issue_w<Z + K4_WD>(c, 0);
  else if (I < 5) {
    c.X[(I - 1) >> 1][2 * Z + ((I - 1) & 1)] = gld16(c.xb + (((I - 1) >> 1) * 18432 + (2 * Z + ((I - 1) & 1)) * 1024), c.lane16);
  }
  else k4_issue_w<Z + K4_WD>(c, 1);
}
template <int TL, int I> DI void k4_issue2(K4C& c) {
  if (I < 4) k4_piece(c, TL, 8 * I + c.w);
  else if ((c.w >> 2) == (TL & 1)) k4_piece(c, TL, 32 + (c.w & 3));
}
template <int TL, int E, int U> DI void k4_epi(K4C& c) {
  constexpr int e = U >> 2, u = U & 3, mt = e >> 2, i = e & 3, q = e & 1;
  if constexpr (K4_PK && U < 32) {
    constexpr int pr = U >> 3, v = U & 7, m2 = pr >> 1, i0 = 2 * (pr & 1);
    if (v == 0) {
      const f32x2 G = {c.acc[TL & 3][m2][i0], c.acc[TL & 3][m2][i0 + 1]};
      asm volatile("v_pk_mul_f32 %0, %1, %2" : "=v"(c.pa[E]) : "v"(G), "v"(c.c1p[m2]));
    }
    if (v == 1) { const f32x2 Uu = {c.acc[TL & 3][2 + m2][i0], c.acc[TL & 3][2 + m2][i0 + 1]};
      asm volatile("v_pk_mul_f32 %0, %1, %2" : "=v"(c.pt[E]) : "v"(Uu), "v"(c.pa[E]));
      c.pa[E][0] = __builtin_amdgcn_exp2f(c.pa[E][0]); PIN(c.pa[E]); }
    if (v == 2) { c.pa[E][1] = __builtin_amdgcn_exp2f(c.pa[E][1]); PIN(c.pa[E]); }
    if (v == 3) asm volatile("v_pk_fma_f32 %0, %0, %1, %1" : "+v"(c.pa[E]) : "v"(c.c2p[m2]));
    if (v == 4) { c.pa[E][0] = __builtin_amdgcn_rcpf(c.pa[E][0]); PIN(c.pa[E]); }
    if (v == 5) { c.pa[E][1] = __builtin_amdgcn_rcpf(c.pa[E][1]); PIN(c.pa[E]); }
    if (v == 6) asm volatile("v_pk_mul_f32 %0, %0, %1" : "+v"(c.pt[E]) : "v"(c.pa[E]));
    if (v == 7) { c.pk[E][pr] = cvtpk(c.pt[E][0], c.pt[E][1]); PIN(c.pk[E][pr]); }
  } else if constexpr (U < 32) {
    if (u == 0) { const float a = c.acc[TL & 3][mt][i] * c.c1[mt]; c.ea[E][q] = a; c.et[E][q] = a * c.acc[TL & 3][2 + mt][i]; PIN(c.ea[E][q]); PIN(c.et[E][q]); }
    if (u == 1) { c.ea[E][q] = __builtin_amdgcn_exp2f(c.ea[E][q]); PIN(c.ea[E][q]); }
    if (u == 2) { c.ea[E][q] = __builtin_amdgcn_rcpf(__builtin_fmaf(c.ea[E][q], c.c2[mt], c.c2[mt])); PIN(c.ea[E][q]); }
    if (u == 3) { c.et[E][q] *= c.ea[E][q]; PIN(c.et[E][q]); if (q) { c.pk[E][e >> 1] = cvtpk(c.et[E][0], c.et[E][1]); PIN(c.pk[E][e >> 1]); } }
  } else if constexpr (U == 32) {
    auto r0 = __builtin_amdgcn_permlane16_swap(c.pk[E][0], c.pk[E][2], false, false);
    auto r1 = __builtin_amdgcn_permlane16_swap(c.pk[E][1], c.pk[E][3], false, false);
    c.pk[E][0] = r0[0]; c.pk[E][1] = r1[0]; c.pk[E][2] = r0[1]; c.pk[E][3] = r1[1];
  } else if constexpr (U == 33) {
    const int T = c.T0 + TL * c.dir;
    bst16<ACT_AUX>(u32x4{c.pk[E][0], c.pk[E][1], c.pk[E][2], c.pk[E][3]}, c.ars, c.avo, (T >> 1) * 1024 + (T & 1) * 512);
  }
}
constexpr int k4_tile(int F) { return F < 108 ? (F % 6) >> 1 : 3 + (F - 108) / 36; }
constexpr int k4_step(int F) { return F < 108 ? F / 6 : ((F - 108) % 36) >> 1; }
constexpr int k4_addr(int F) { return k4_step(F) * 2048 + (F & 1) * 1024; }
template <int F> DI void k4_frag(K4C& c) {
  constexpr int TL = k4_tile(F), S = k4_step(F), M = F & 1, N = F + K4_PF;
  const u32x4 a = c.wf[F % (K4_PF + 1)];
  if constexpr (F < 108) { if constexpr ((F + 4) % 3 == 0) __builtin_amdgcn_s_setprio(3 - ((F + 4) % 12) / 3); }
  else if constexpr ((F - 108 + 4) % 9 == 0) __builtin_amdgcn_s_setprio(3 - ((F - 108 + 4) % 36) / 9);
  if (S == 0) {
    G_MFMA0(c.acc[TL & 3][2 * M], a, c.X[0][S]);
    G_MFMA0(c.acc[TL & 3][2 * M + 1], a, c.X[1][S]);
  } else {
    constexpr int o = GRAY ? M : 0;
    G_MFMA(c.acc[TL & 3][2 * M + o], a, c.X[o][S]);
    if constexpr (K4_MID && F >= 108 && (F - 108) % 36 >= 2) {
      SBAR();
      k4_epi<(TL == 3 ? 0 : (TL == 4 ? 2 : TL - 1)), 0, (F - 108) % 36 - 2>(c);
      SBAR();
    }
    G_MFMA(c.acc[TL & 3][2 * M + 1 - o], a, c.X[1 - o][S]);
  }
  if constexpr (N < 108 && N % 12 == 0) {
    constexpr int Z = N / 12;
    if (Z < 8) asm volatile("s_waitcnt vmcnt(5)\n\ts_barrier" : "+v"(c.X[0][2 * Z]), "+v"(c.X[0][2 * Z + 1]), "+v"(c.X[1][2 * Z]), "+v"(c.X[1][2 * Z + 1])::"memory");
    else asm volatile("s_waitcnt vmcnt(2)\n\ts_barrier" : "+v"(c.X[0][2 * Z]), "+v"(c.X[0][2 * Z + 1]), "+v"(c.X[1][2 * Z]), "+v"(c.X[1][2 * Z + 1])::"memory");
  } else if constexpr (N >= 108 && N < 324 && (N - 108) % 36 == 0) {
    constexpr int NT = k4_tile(N);
    if (NT == 3) asm volatile("s_waitcnt vmcnt(0) lgkmcnt(0)\n\ts_barrier" ::: "memory");
    else if (NT == 8) {
      asm volatile("s_waitcnt vmcnt(0) lgkmcnt(0)\n\ts_barrier" ::: "memory");
      if (c.w == 0 && c.lane16 == 0) __hip_atomic_fetch_add(c.cntA, 1u, __ATOMIC_RELAXED, __HIP_MEMORY_SCOPE_AGENT);
    }
    else asm volatile("s_waitcnt vmcnt(4) lgkmcnt(0)\n\ts_barrier" ::: "memory");
  }
  if constexpr (N < 324) c.wf[N % (K4_PF + 1)] = ldsr16(c.wl[k4_tile(N) & 3] + k4_addr(N));
  if constexpr (F < 108) {
    constexpr int Z = F / 12, I = F % 12;
    if constexpr (Z == 0 && I < 4 && !(I & 1)) k4_issue1<2, I / 2>(c);
    if constexpr (Z < 7 && I < 8 && (I & 1)) k4_issue1<Z + 2, 2 + I / 2>(c);
    if constexpr (Z < 6 && I > 8 && (I & 1)) k4_issue1<Z + 3, (I - 9) / 2>(c);
    if constexpr (K4_WD == 0) {
      if constexpr (Z == 6 && I == 10) k4_issue2<3, 0>(c);
      if constexpr (Z == 7 && I < 8 && (I & 1)) k4_issue2<3, 1 + I / 2>(c);
    } else {
      if constexpr (Z == 6 && I == 10) k4_issue2<3, 2>(c);
      if constexpr (Z == 7 && (I == 1 || I == 3)) k4_issue2<3, 3 + I / 2>(c);
    }
  } else {
    constexpr int P = (F - 108) % 36;
    if constexpr (TL == 3) { if constexpr (P % 3 == 0 && P < 15) k4_issue2<4, P / 3>(c); if constexpr (P % 3 == 0 && P >= 15 && P < 30) k4_issue2<5, (P - 15) / 3>(c); }
    else if constexpr (TL + 2 < 9 && P % 4 == 0 && P < 20) k4_issue2<TL + 2, P / 4>(c);
    if constexpr (K4_PFWD && TL >= 4 && TL <= 7 && (P == 33 || (P == 34 && TL < 6))) {
      constexpr int tl = TL < 6 ? 2 * (TL - 4) + P - 33 : TL - 2;
      const int T = c.T0 + tl * c.dir;
      asm volatile("global_load_dword %0, %1, %2" : "+v"(c.pf0) : "v"(c.pfo), "s"(c.pfb + ((T >> 1) * 1024 + (T & 1) * 512)));
    }
    if constexpr (K4_PFW2 && TL >= 5 && TL <= 7 && P == 1)
      asm volatile("global_load_dword %0, %1, %2" : "+v"(c.pf0) : "v"(c.pfw), "s"(c.pfwb + (TL - 5) * (14 * 128)));
    if constexpr (TL == 7 && P == 0) CODE_PF2(c.pf1, k_ffn, k_end, 0, 2 + (c.w & 1), ((blockIdx.x >> 3) == 0 && c.w < 2) ? 128u : 0u);
    if constexpr (TL == 7 && P % 3 == 2 && P <= 11) k5_piece<0>(c.wd, P / 3, c.w, c.lane16);
    if constexpr (TL == 8 && P < 4) k5_piece<1>(c.wd, P, c.w, c.lane16);
    if constexpr (TL == 8 && P >= 4 && P < 8) k5_piece<2>(c.wd, P - 4, c.w, c.lane16);
    if constexpr (TL == 8 && P == K4_POLL) POLL(c.poll, c.cntA);
    if constexpr (TL == 8 && P == K4_POLL + 12) { asm volatile("s_waitcnt vmcnt(0)" : "+v"(c.poll), "+v"(c.pf1), "+v"(c.pf0)); wait_count(c.cntA, c.poll, 8u); }
    if constexpr (TL == 8 && P > K4_POLL + 12 && P <= K4_POLL + 20) {
      constexpr int i = P - K4_POLL - 13, z = i >> 2, j = (i & 1) * 2 + ((i >> 1) & 1);
      asm volatile("global_load_dwordx4 %0, %1, %2 sc1" : "=v"(c.A0[z][j]) : "v"(c.lane16), "s"(((j >> 1) ? c.ab2 : c.ab) + K5_PERM[2 * z + (j & 1)] * 1024));
    }
    if constexpr (P >= 2) {
      if constexpr (TL == 3) { if (!K4_MID) k4_epi<0, 0, P - 2>(c); k4_epi<1, 1, P - 2>(c); }
      else if constexpr (TL == 4) { if (!K4_MID) k4_epi<2, 0, P - 2>(c); k4_epi<3, 1, P - 2>(c); }
      else if (!K4_MID) k4_epi<TL - 1, 0, P - 2>(c);
    }
  }
  SBAR();
}
template <int B, int... Fs> DI void k4_run(K4C& c, std::integer_sequence<int, Fs...>) { (k4_frag<B + Fs>(c), ...); }
DI void k4_last(K4C& c) {
  float a[8], t[8];
#pragma unroll
  for (int e = 0; e < 8; ++e) { a[e] = c.acc[0][e >> 2][e & 3] * c.c1[e >> 2]; t[e] = a[e] * c.acc[0][2 + (e >> 2)][e & 3]; }
#pragma unroll
  for (int e = 0; e < 8; ++e) a[e] = __builtin_amdgcn_exp2f(a[e]);
#pragma unroll
  for (int e = 0; e < 8; ++e) a[e] = __builtin_amdgcn_rcpf(__builtin_fmaf(a[e], c.c2[e >> 2], c.c2[e >> 2]));
#pragma unroll
  for (int e = 0; e < 4; ++e) c.pk[0][e] = cvtpk(t[2 * e] * a[2 * e], t[2 * e + 1] * a[2 * e + 1]);
  k4_epi<8, 0, 32>(c); k4_epi<8, 0, 33>(c);
}
DI f32x4 addbf(f32x4 a, unsigned lo, unsigned hi) { return a + f32x4{bflo(lo), bfhi(lo), bflo(hi), bfhi(hi)}; }
struct K5C {
  const char *ab, *ab2;
  LDSP const char *ra[4], *oa[3], *xr;
  LDSP char* xw;
  u32x4 A[9][4], wf[5];
  f32x4 acc[2][7], own[2][2], v[4];
  const char* hb; unsigned ho; int kq; u32x2 hr[3];
  __amdgpu_buffer_rsrc_t wrs; unsigned tw, p8s;
  gu32* cnt;
};
template <int Z, int J> DI void k5_issue(K5C& c, const K4C& k) {
  if constexpr (Z < 7 && J < 4) asm volatile("global_load_dwordx4 %0, %1, %2 sc1"
 : "=v"(c.A[Z + 2][J]) : "v"(k.lane16), "s"(((J >> 1) ? c.ab2 : c.ab) + K5_PERM[2 * (Z + 2) + (J & 1)] * 1024));
  if constexpr (Z < 6 && J >= 4) k5_piece<Z + 3>(k.wd, J - 4, k.w, k.lane16);
  if constexpr (Z == 6 && J >= 4) k5_own<J - 4>(k.wd, k.w, k.lane16);
  if constexpr (Z == 7 && J == 0) k5_own<4>(k.wd, k.w, k.lane16);
  if constexpr (Z == 7 && J == 1) asm volatile("global_load_dwordx2 %0, %1, %2" : "=v"(c.hr[0]) : "v"(c.ho), "s"(c.hb + c.kq * 1024));
  if constexpr (Z == 7 && J == 2) asm volatile("global_load_dwordx2 %0, %1, %2 offset:512" : "=v"(c.hr[1]) : "v"(c.ho), "s"(c.hb + c.kq * 1024));
  if constexpr (Z == 7 && J == 3) asm volatile("global_load_dwordx2 %0, %1, %2" : "=v"(c.hr[2]) : "v"(c.ho), "s"(c.hb + 4096));
  if constexpr (Z == 8 && J < 4) k5_own<5 + J>(k.wd, k.w, k.lane16);
}
template <int N> DI u32x4 k5_read(const K5C& c) {
  if constexpr (N < 126) return ldsr16(c.ra[(N / 14) & 3] + (N % 14) * 1024);
  else { constexpr int o = K5_OC[(N - 126) / 4] + ((N - 126) & 3) * 1024; return ldsr16(c.oa[o >> 16] + (o & 0xffff)); }
}
template <int F> DI void k5_frag(K5C& c, K4C& k) {
  constexpr int N = F + 4;
  const u32x4 a = c.wf[F % 5];
  if constexpr (F < 126) {
    constexpr int Z = F / 14, f = F % 14, st = f / 7, j = f % 7;
    if (F < 7) { G_MFMA0(c.acc[0][j], a, c.A[Z][st]); G_MFMA0(c.acc[1][j], a, c.A[Z][2 + st]); }
    else { G_MFMA(c.acc[1][j], a, c.A[Z][2 + st]); G_MFMA(c.acc[0][j], a, c.A[Z][st]); }
    if constexpr (f == 10 && Z == 8) asm volatile("s_waitcnt vmcnt(4)\n\ts_barrier" : "+v"(c.hr[0]), "+v"(c.hr[1]), "+v"(c.hr[2])::"memory");
    if constexpr (f == 10 && Z < 8) {
      u32x4(&A)[4] = c.A[Z + 1];
      if (Z == 0) asm volatile("s_waitcnt vmcnt(9) lgkmcnt(0)\n\ts_barrier" : "+v"(A[0]), "+v"(A[1]), "+v"(A[2]), "+v"(A[3])::"memory");
      else if (Z == 3) asm volatile("s_waitcnt vmcnt(4) lgkmcnt(0)\n\ts_barrier" : "+v"(A[0]), "+v"(A[1]), "+v"(A[2]), "+v"(A[3]), "+v"(k.poll)::"memory");
      else if (Z < 7) asm volatile("s_waitcnt vmcnt(7) lgkmcnt(0)\n\ts_barrier" : "+v"(A[0]), "+v"(A[1]), "+v"(A[2]), "+v"(A[3])::"memory");
      else asm volatile("s_waitcnt vmcnt(8) lgkmcnt(0)\n\ts_barrier" : "+v"(A[0]), "+v"(A[1]), "+v"(A[2]), "+v"(A[3])::"memory");
      if (Z == 1 && k.w == 0 && k.lane16 == 0) __hip_atomic_fetch_add(k.cntB, 1u, __ATOMIC_RELAXED, __HIP_MEMORY_SCOPE_AGENT);
      if (Z == 3) wait_count(k.cntB, k.poll, 8u);
    }
    c.wf[N % 5] = k5_read<N>(c);
    if constexpr (Z == 3 && f == 1) POLL(k.poll, k.cntB);
    constexpr int slot = f == 0 ? 2 : (f == 2 ? 3 : (f == 4 ? 4 : (f == 5 ? 5 : (f == 7 ? 6 : (f == 9 ? 7 : -1)))));
    if constexpr (F == 0) { k5_issue<0, 0>(c, k); k5_issue<0, 1>(c, k); }
    if constexpr (slot >= 0) k5_issue<Z, slot>(c, k);
    if constexpr (Z < 8 && f > 10 && (f & 1)) k5_issue<Z + 1, (f - 11) / 2>(c, k);
  } else {
    constexpr int B = F - 126, z = B / 4, st = (B >> 1) & 1, t = B & 1;
    if (B < 2) { G_MFMA0(c.own[0][t], a, c.A[z][st]); G_MFMA0(c.own[1][t], a, c.A[z][2 + st]); }
    else { G_MFMA(c.own[1][t], a, c.A[z][2 + st]); G_MFMA(c.own[0][t], a, c.A[z][st]); }
    if constexpr (F == 131) asm volatile("s_waitcnt vmcnt(0) lgkmcnt(0)\n\ts_barrier" ::: "memory");
    if constexpr (F == K5_SIG) {
      asm volatile("s_waitcnt vmcnt(0)\n\ts_barrier" ::: "memory");
      if (k.w == 0 && k.lane16 == 0) __hip_atomic_fetch_add(c.cnt, 1u, __ATOMIC_RELAXED, __HIP_MEMORY_SCOPE_AGENT);
    }
    if constexpr (N < 162) c.wf[N % 5] = k5_read<N>(c);
  }
  if constexpr (F >= 123 && F < 130) { PIN(c.acc[1][F - 123]); *(LDSP f32x4*)(c.xw + (F - 123) * 1024) = c.acc[1][F - 123]; }
  if constexpr (F == 131) { _Pragma("unroll") for (int i = 0; i < 4; ++i) c.v[i] = *(LDSP const f32x4*)(c.xr + i * 1024); }
  if constexpr (F == 133 || F == 135 || F == 138) {
    constexpr int j = F == 133 ? 0 : (F == 135 ? 1 : 2), o = 2 * (j & 1);
    const f32x4 x = c.acc[0][2 * j] + c.v[o], y = c.acc[0][2 * j + 1] + c.v[o + 1];
    bst16<16>(u32x4{cvtpk(x[0], x[1]), cvtpk(x[2], x[3]), cvtpk(y[0], y[1]), cvtpk(y[2], y[3])}, c.wrs, k.lane16,
              (unsigned)WS_PART + ((((c.kq + j + 1) & 3) * 512 + c.tw) * 3 + j) * 1024);
    if constexpr (F == 135) { _Pragma("unroll") for (int i = 0; i < 3; ++i) c.v[i] = *(LDSP const f32x4*)(c.xr + (4 + i) * 1024); }
  }
  if constexpr (F == 140) {
    c.acc[0][6] += c.v[2];
    __builtin_amdgcn_raw_buffer_store_b64(u32x2{cvtpk(c.acc[0][6][0], c.acc[0][6][1]), cvtpk(c.acc[0][6][2], c.acc[0][6][3])}, c.wrs, c.p8s, c.kq * 512, 16);
    PIN(c.acc[0][6]);
  }
  SBAR();
}
template <int B, int... Fs> DI void k5_run(K5C& c, K4C& k, std::integer_sequence<int, Fs...>) { (k5_frag<B + Fs>(c, k), ...); }

extern "C" __global__ __launch_bounds__(512, 2) void k_ffn(char* out, char* ws) {
  extern __shared__ __attribute__((aligned(16))) char smem[];
  asm volatile("" : "+s"(out), "+s"(ws));
  LDSP char* lds = (LDSP char*)smem;
  const int tid = threadIdx.x, lane = tid & 63, w = rfl(tid >> 6);
  const int xcd = blockIdx.x & 7, bi = blockIdx.x >> 3;
  const int m = lane & 15, g = lane >> 4, kq = xcd >> 1;
  STAMP_DECL; STAMP(0);
  K4C c;
  {
    const int rb = 4 * (xcd & 1) + (bi & 3), fs = 8 * kq + (bi >> 2), mt = 16 * rb + 2 * w;
    c.lds0 = (unsigned)(uintptr_t)lds; c.lane16 = lane * 16; c.w = w;
    c.T0 = 9 * fs + 8 * (fs & 1); c.dir = 1 - 2 * (fs & 1);
    c.wb = ws + WS_W1 + (size_t)c.T0 * K4_SLOT;
    c.xb = ws + WS_HC + (size_t)mt * 18432;
    if (K4_WD) { k4_issue_w<0>(c, 0); k4_issue_w<0>(c, 1); }
    k4_issue1<0, 0>(c); k4_issue1<0, 1>(c); k4_issue1<0, 2>(c); k4_issue1<0, 3>(c); k4_issue1<0, 4>(c); k4_issue1<0, 5>(c);
    unsigned sq[8];
#pragma unroll
    for (int i = 0; i < 8; ++i) sq[i] = gld4(ws + WS_SSQ + (i >> 1) * 8192 + (i & 1) * 64, 4 * g * 8192 + (16 * mt + m) * 4);
    k4_issue1<1, 0>(c); k4_issue1<1, 1>(c); k4_issue1<1, 2>(c); k4_issue1<1, 3>(c); k4_issue1<1, 4>(c); k4_issue1<1, 5>(c);
    SBAR();
#pragma unroll
    for (int i = 0; i < 4; ++i) { c.wl[i] = lds + c.lane16 + i * K4_SLOT; PIN(c.wl[i]); }
    c.ars = mkrsrc(ws + WS_ACT, 128u * 144 * 1024);
    c.avo = (mt + (g & 1)) * (144 * 1024) + (g >> 1) * 256 + m * 16;
    c.wd.wb = ws + WS_WD + ((size_t)(bi >> 3) * 144 + 36 * kq) * 9 * 1024;
#pragma unroll
    for (int i = 0; i < 4; ++i) {
      const int p = i < 3 ? 8 * i + w : 24 + (w & 3), st = (p * 37) >> 8, t = p - 7 * st + 2;
      c.wd.q[i] = ((st >> 1) * 162 + (t < 8 ? (t + 2 * kq) & 7 : 8)) * 1024;
      c.wd.qs[i] = st & 1;
      asm volatile("" : "+s"(c.wd.q[i]), "+s"(c.wd.qs[i]));
    }
    c.wd.qo = ((w >> 2) * 162 + 2 * kq + (w & 1)) * 1024; c.wd.qos = (w >> 1) & 1; c.wd.lds0 = c.lds0;
    if (K4_PFWD) {
      c.pfb = ws + WS_ACT;
      c.pfo = (mt + ((lane >> 5) & 1)) * (144 * 1024) + ((lane >> 4) & 1) * 256 + ((lane >> 3) & 1) * 128;
      PIN(c.pfo);
    }
    if (K4_PFW2) {
      c.pfwb = ws + WS_WD + (size_t)kq * 331776 + (size_t)(bi >> 3) * 1327104;
      c.pfw = min(((bi & 7) * 8 + w) * 42 + (lane < 14 ? lane : 13), 2592 - 29) * 128; PIN(c.pfw);
    }
    c.pf0 = 0;
    c.cntA = (gu32*)(ws + WS_CNT + (64 + rb * 4 + kq) * 128);
    STAMP(1);
    asm volatile("s_waitcnt vmcnt(5)\n\ts_barrier" : "+v"(sq[0]), "+v"(sq[1]), "+v"(sq[2]), "+v"(sq[3]), "+v"(sq[4]), "+v"(sq[5]), "+v"(sq[6]), "+v"(sq[7]),
                 "+v"(c.X[0][0]), "+v"(c.X[0][1]), "+v"(c.X[1][0]), "+v"(c.X[1][1])::"memory");
#pragma unroll
    for (int i = 0; i < K4_PF; ++i) c.wf[i] = ldsr16(c.wl[k4_tile(i)] + k4_addr(i));
#pragma unroll
    for (int t = 0; t < 2; ++t) {
      float ss = (__uint_as_float(sq[t]) + __uint_as_float(sq[2 + t])) + (__uint_as_float(sq[4 + t]) + __uint_as_float(sq[6 + t]));
      ss = xor32_sum(xor16_sum(ss));
      const float v = ss * (1.0f / DM) + LN_EPS, rs = __builtin_amdgcn_rsqf(v);
      c.c1[t] = -LOG2E * rs; c.c2[t] = -LOG2E * (v * rs);
      PIN(c.c1[t]); PIN(c.c2[t]);
      if (K4_PK) { c.c1p[t] = f32x2{c.c1[t], c.c1[t]}; c.c2p[t] = f32x2{c.c2[t], c.c2[t]}; PIN(c.c1p[t]); PIN(c.c2p[t]); }
    }
    SBAR();
    k4_run<0>(c, std::make_integer_sequence<int, 288>{});
    c.cntB = c.cntA + 32 * 32;
  }
  const int rb = 8 * (xcd & 1) + (bi & 7), nb = bi >> 3, mg = w & 3, kh = w >> 2, mu = 8 * rb + 2 * mg + kh;
  gu32* prodB = c.cntB;
  c.cntA = (gu32*)(ws + WS_CNT + (64 + (rb >> 1) * 4 + kq) * 128);
  c.ab = ws + WS_ACT + ((size_t)mu * 144 + 36 * kq + 18 * kh) * 1024;
  c.ab2 = ws + WS_ACT + ((size_t)(mu ^ 1) * 144 + 36 * kq + 18 * kh) * 1024;
  k4_run<288>(c, std::make_integer_sequence<int, 36>{});
  STAMP(2);
  K5C d;
  d.ab = c.ab; d.ab2 = c.ab2;
  asm volatile("s_nop 7" : "+v"(c.acc[0][0]), "+v"(c.acc[0][1]), "+v"(c.acc[0][2]), "+v"(c.acc[0][3]));
  k4_last(c);
  SBAR();
  gu32* consB = c.cntA + 32 * 32;
  d.hb = ws + WS_HU + (size_t)mu * 18432 + nb * 4608; d.ho = (g >> 1) * 256 + m * 16 + (g & 1) * 8; d.kq = kq;
#pragma unroll
  for (int i = 0; i < 4; ++i) { d.ra[i] = lds + K5_R[i] + kh * 14336 + c.lane16; PIN(d.ra[i]); }
#pragma unroll
  for (int i = 0; i < 3; ++i) { d.oa[i] = lds + i * 65536 + kh * 4096 + c.lane16; PIN(d.oa[i]); }
  d.xw = lds + (kh ? K5_R[2] : K5_R[1]) + mg * 7168 + c.lane16; PIN(d.xw);
  d.xr = lds + (kh ? K5_R[1] : K5_R[2]) + mg * 7168 + c.lane16; PIN(d.xr);
  const __amdgpu_buffer_rsrc_t wrs = mkrsrc(ws, (unsigned)WS_END), ors = mkrsrc(out, SEQ * DM * 2);
  const int tile = rb * 4 + nb;
  const bool own8 = (mu & 3) == kq;
  const unsigned tw = tile * 8 + w;
  const unsigned p8 = (unsigned)WS_PART8 + tw * 2048 + lane * 8;
  d.cnt = (gu32*)(ws + WS_CNT + tile * 128);
  d.wrs = wrs; d.tw = tw; d.p8s = own8 ? 0x80000000u : p8; PIN(d.p8s);
#pragma unroll
  for (int i = 0; i < 4; ++i) { d.A[0][i] = c.A0[0][i]; d.A[1][i] = c.A0[1][i]; }
  asm volatile("s_waitcnt vmcnt(6) lgkmcnt(0)\n\ts_barrier" : "+v"(d.A[0][0]), "+v"(d.A[0][1]), "+v"(d.A[0][2]), "+v"(d.A[0][3])::"memory");
  STAMP(3);
  d.wf[0] = k5_read<0>(d); d.wf[1] = k5_read<1>(d); d.wf[2] = k5_read<2>(d); d.wf[3] = k5_read<3>(d);
  SBAR();
  c.cntB = prodB;
  k5_run<0>(d, c, std::make_integer_sequence<int, 28>{});
  c.cntB = consB;
  k5_run<28>(d, c, std::make_integer_sequence<int, 134>{});
  asm volatile("s_nop 7" : "+v"(d.own[0][0]), "+v"(d.own[0][1]), "+v"(d.own[1][0]), "+v"(d.own[1][1]));
  STAMP(4);
  LDSP char* xo = lds + K5_R[0] + c.lane16;
  *(LDSP f32x4*)(xo + w * 2048) = d.own[1][0];
  *(LDSP f32x4*)(xo + w * 2048 + 1024) = d.own[1][1];
  unsigned oo = (16 * mu + m) * 1152 + nb * 288;
  unsigned oo8 = own8 ? oo + 256 + g * 8 : 0x80000000u;
  oo += kq * 64 + (g & 1) * 32 + (g >> 1) * 16;
  PIN(oo); PIN(oo8);
  STAMP(5);
  if (w == 0) wait_count(d.cnt, 0u, 4u);
  asm volatile("s_waitcnt lgkmcnt(0)\n\ts_barrier" ::: "memory");
  STAMP(6);
  u32x4 r[3]; u32x2 r8[3];
#pragma unroll
  for (int j = 0; j < 3; ++j) {
    r[j] = __builtin_amdgcn_raw_buffer_load_b128(wrs, c.lane16, (unsigned)WS_PART + ((kq * 512 + tw) * 3 + j) * 1024, 16);
    r8[j] = __builtin_amdgcn_raw_buffer_load_b64(wrs, own8 ? p8 : 0x80000000u, ((kq + 1 + j) & 3) * 512, 16);
  }
  SBAR();
  f32x4 e0 = addbf(d.own[0][0] + *(LDSP const f32x4*)(xo + (w ^ 4) * 2048), d.hr[0][0], d.hr[0][1]);
  f32x4 e1 = addbf(d.own[0][1] + *(LDSP const f32x4*)(xo + (w ^ 4) * 2048 + 1024), d.hr[1][0], d.hr[1][1]);
  f32x4 e8 = addbf(d.acc[0][6], d.hr[2][0], d.hr[2][1]);
#pragma unroll
  for (int j = 0; j < 3; ++j) { e0 = addbf(e0, r[j][0], r[j][1]); e1 = addbf(e1, r[j][2], r[j][3]); e8 = addbf(e8, r8[j][0], r8[j][1]); }
  {
    const auto s0 = __builtin_amdgcn_permlane16_swap(cvtpk(e0[0], e0[1]), cvtpk(e1[0], e1[1]), false, false);
    const auto s1 = __builtin_amdgcn_permlane16_swap(cvtpk(e0[2], e0[3]), cvtpk(e1[2], e1[3]), false, false);
    bst16<OUT_AUX>(u32x4{s0[0], s1[0], s0[1], s1[1]}, ors, oo, 0);
    __builtin_amdgcn_raw_buffer_store_b64(u32x2{cvtpk(e8[0], e8[1]), cvtpk(e8[2], e8[3])}, ors, oo8, 0, OUT_AUX);
  }
  STAMP(7);
  STAMP_OUT(4, 8);
}
extern "C" __global__ void k_end() {}

extern "C" void kernel_launch(void* const* d_in, const int* in_sizes, int n_in, void* d_out, int out_size, void* d_ws, size_t ws_size,
                              hipStream_t stream) {
  const u16* x = (const u16*)d_in[0];
  const u16* wqkv = (const u16*)d_in[1];
  const u16* wo = (const u16*)d_in[2];
  const u16* wg = (const u16*)d_in[4];
  const u16* wu = (const u16*)d_in[5];
  const u16* wd = (const u16*)d_in[6];
  char* ws = (char*)d_ws;
  (void)hipFuncSetAttribute((const void*)k_qkv, hipFuncAttributeMaxDynamicSharedMemorySize, K1_LDS);
  (void)hipFuncSetAttribute((const void*)k_attn, hipFuncAttributeMaxDynamicSharedMemorySize, K2_LDS);
  (void)hipFuncSetAttribute((const void*)k_ffn, hipFuncAttributeMaxDynamicSharedMemorySize, K4_LDS);
  k_qkv<<<256, 768, K1_LDS, stream>>>(x, wqkv, wo, ws);
  k_attn<<<256, 768, K2_LDS, stream>>>(x, wg, wu, wd, ws);
  k_ffn<<<256, 512, K4_LDS, stream>>>((char*)d_out, ws);
  k_end<<<1, 64, 0, stream>>>();
}
```

```cpp
#include <hip/hip_runtime.h>
#include <hip/hip_bf16.h>
#include <stdint.h>
#include <utility>
#include <type_traits>

typedef __bf16 bf16x8 __attribute__((ext_vector_type(8)));
typedef __bf16 bf16x2 __attribute__((ext_vector_type(2)));
typedef short s16x4 __attribute__((ext_vector_type(4)));
typedef float f32x16 __attribute__((ext_vector_type(16)));
typedef float f32x4 __attribute__((ext_vector_type(4)));
typedef float f32x2 __attribute__((ext_vector_type(2)));
typedef unsigned u32x4 __attribute__((ext_vector_type(4)));
typedef unsigned u32x2 __attribute__((ext_vector_type(2)));
typedef unsigned short u16;
#define LDSP __attribute__((address_space(3)))
#define DI __device__ __forceinline__
#define SBAR() __builtin_amdgcn_sched_barrier(0)

constexpr int SEQ = 2048, DM = 576, NH = 32, DH = 18, FF = 4608;
constexpr float LOG2E = 1.4426950408889634f;
constexpr float QSCALE = 0.23570226039551584f * LOG2E;
constexpr float LN_EPS = 1e-5f;

constexpr size_t CHUNK_B = 37888;
constexpr size_t KV_HEAD_B = 4 * CHUNK_B;
constexpr size_t WS_QM = 0;
constexpr size_t WS_QT = WS_QM + (size_t)NH * 65536;
constexpr size_t WS_KV = WS_QT + (size_t)NH * 8192;
constexpr size_t WS_KMAX = WS_KV + (size_t)NH * KV_HEAD_B;
constexpr size_t WS_XMEAN = WS_KMAX + 65536;
constexpr size_t WS_OFR = WS_XMEAN + 8192;
constexpr size_t WS_WO = WS_OFR + (size_t)128 * 18 * 1024;
constexpr size_t WS_WBAR = WS_WO + (size_t)12 * 54 * 1024;
constexpr size_t WS_HC = WS_WBAR + 3072;
constexpr size_t WS_SSQ = WS_HC + (size_t)64 * 36 * 1024;
constexpr size_t WS_W1 = WS_SSQ + (size_t)16 * 8192;
constexpr size_t WS_WD = WS_W1 + (size_t)288 * 36 * 1024;
constexpr size_t WS_ACT = WS_WD + (size_t)4 * 144 * 9 * 1024;
constexpr size_t UNIT_B = (size_t)128 * 18432;
constexpr size_t WS_PART = WS_ACT + (size_t)128 * 144 * 1024;
constexpr size_t WS_PART8 = WS_PART + (size_t)4 * 64 * 8 * 3 * 1024;
constexpr size_t WS_CNT = WS_PART8 + (size_t)64 * 8 * 4 * 512;
constexpr size_t WS_HU = WS_CNT + 40960;
constexpr size_t WS_END = WS_HU + UNIT_B;

DI unsigned cvtpk(float lo, float hi) {
  f32x2 v = {lo, hi};
  bf16x2 b = __builtin_convertvector(v, bf16x2);
  return __builtin_bit_cast(unsigned, b);
}
DI float bflo(unsigned u) { return __uint_as_float(u << 16); }
DI float bfhi(unsigned u) { return __uint_as_float(u & 0xffff0000u); }
DI float dot2(unsigned a, unsigned b, float c) {
  return __builtin_amdgcn_fdot2_f32_bf16(__builtin_bit_cast(bf16x2, a), __builtin_bit_cast(bf16x2, b), c, false);
}
DI f32x16 mfma32(u32x4 a, u32x4 b, f32x16 c) {
  return __builtin_amdgcn_mfma_f32_32x32x16_bf16(__builtin_bit_cast(bf16x8, a), __builtin_bit_cast(bf16x8, b), c, 0, 0, 0);
}
DI f32x4 mfma16(u32x4 a, u32x4 b, f32x4 c) {
  return __builtin_amdgcn_mfma_f32_16x16x32_bf16(__builtin_bit_cast(bf16x8, a), __builtin_bit_cast(bf16x8, b), c, 0, 0, 0);
}
DI unsigned rfl(unsigned x) { return __builtin_amdgcn_readfirstlane(x); }
DI const char* uniptr(const char* p) {
  uint64_t v = (uint64_t)p;
  unsigned lo = rfl((unsigned)v), hi = rfl((unsigned)(v >> 32));
  return (const char*)(((uint64_t)hi << 32) | lo);
}
DI void dma16(const char* sbase, unsigned voff, unsigned lds) {
  asm volatile("s_mov_b32 m0, %2\n\ts_nop 0\n\tglobal_load_lds_dwordx4 %0, %1" ::"v"(voff), "s"(sbase), "s"(lds));
}
DI void dma16nt(const char* sbase, unsigned voff, unsigned lds) {
  asm volatile("s_mov_b32 m0, %2\n\ts_nop 0\n\tglobal_load_lds_dwordx4 %0, %1 nt" ::"v"(voff), "s"(sbase), "s"(lds));
}
DI u32x4 gld16(const char* sbase, unsigned voff) {
  u32x4 r;
  asm volatile("global_load_dwordx4 %0, %1, %2" : "=v"(r) : "v"(voff), "s"(sbase));
  return r;
}
DI unsigned gld4(const char* sbase, unsigned voff) {
  unsigned r;
  asm volatile("global_load_dword %0, %1, %2" : "=v"(r) : "v"(voff), "s"(sbase));
  return r;
}
#define WAITV(n) asm volatile("s_waitcnt vmcnt(" #n ")" ::: "memory")
#define BARRIER() asm volatile("s_barrier" ::: "memory")
#define PIN(x) asm volatile("" : "+v"(x))
DI u32x4 ldsr16(LDSP const char* p) { return *(LDSP const u32x4*)p; }
DI float xor32_sum(float v) {
  auto r = __builtin_amdgcn_permlane32_swap(__float_as_uint(v), __float_as_uint(v), false, false);
  return __uint_as_float(r[0]) + __uint_as_float(r[1]);
}
DI float xor32_max(float v) {
  auto r = __builtin_amdgcn_permlane32_swap(__float_as_uint(v), __float_as_uint(v), false, false);
  return fmaxf(__uint_as_float(r[0]), __uint_as_float(r[1]));
}
DI float xor16_sum(float v) {
  auto r = __builtin_amdgcn_permlane16_swap(__float_as_uint(v), __float_as_uint(v), false, false);
  return __uint_as_float(r[0]) + __uint_as_float(r[1]);
}

#define STAMP_DECL
#define STAMP(i)
#define STAMP_OUT(kid, n)

DI int wo_row(int step, int g, int j) {
  if (step < 16) return 18 * (2 * step + (g >> 1)) + 4 * (g & 1) + (j & 3) + 8 * (j >> 2);
  return 18 * (4 * (4 * (step - 16) + g) + (j >> 1)) + 16 + (j & 1);
}

#define K4_SPREAD 1
#define K4_PM 1
#define K4_WD 1
#define K3_PF 0
#define K3_SLEEP 32
#define K1_ROT 0
#define AT_PM 9
#define HELP_PRIO 3
#define HELP_EARLY 0
#define PREP_ORD 1
#define GRAY 1
#define K4_POLL 10
#define K4_PK 0
#define K5_ORD 1
#define K5_SIG 154
#define K4_MID 0
#define AT_HFIRST 0
#define K4_PFW2 0
#define K3_REP 16
#define HELP_MID 1
#define K4_NT 0
#define K4_ROT 0
#define K5_FLAG 0
#define K5_SPREAD 1
#define K5_PM 0
#define OUT_AUX 16
#define K4_PFWD 1
#define QKV_AUX 16
#define QKT_AUX 16
#define V_AUX 16
#define O_AUX 16
#define H_AUX 16
#define PART_AUX 16
#define ACT_AUX 16
#define PREP_AUX 16
#define CODE_PF(dst, first, last, extra, blk) CODE_PF2(dst, first, last, extra, blk, 128u)
#define CODE_PF2(dst, first, last, extra, blk, stride)                                                                              \
  do {                                                                                                                     \
    uint64_t pc_; int b_, e_;                                                                                              \
    asm volatile("s_getpc_b64 %0\n.Lpf%=:\n\ts_mov_b32 %1, " #first "-.Lpf%=\n\ts_mov_b32 %2, " #last "-.Lpf%=" : "=s"(pc_), "=s"(b_), "=s"(e_)); \
    const unsigned o_ = (blk) * 8192u + (threadIdx.x & 63) * (stride), n_ = (unsigned)(e_ - b_) + (extra) - 4u;                \
    dst = gld4((const char*)(pc_ + b_), o_ < n_ ? o_ : n_);                                                                \
  } while (0)
#define G_MFMA(d, a, b) asm volatile("v_mfma_f32_16x16x32_bf16 %0, %1, %2, %0" : "+v"(d) : "v"(a), "v"(b))
#define G_MFMA0(d, a, b) asm volatile("v_mfma_f32_16x16x32_bf16 %0, %1, %2, 0" : "=&v"(d) : "v"(a), "v"(b))
template <int AUX> DI void bst16(u32x4 v, __amdgpu_buffer_rsrc_t rs, unsigned voff, unsigned soff) {
  __builtin_amdgcn_raw_buffer_store_b128(v, rs, voff, soff, AUX);
  asm volatile("s_nop 1" ::"v"(v) : "memory");
}
DI __amdgpu_buffer_rsrc_t mkrsrc(const void* p, unsigned bytes) {
  return __builtin_amdgcn_make_buffer_rsrc((void*)p, 0, bytes, 0x00020000);
}

typedef __attribute__((address_space(1))) unsigned gu32;
template <int SLEEP = 1> DI void wait_count(gu32* cnt, unsigned first, unsigned want) {
  if (rfl(first) >= want) return;
  for (unsigned spin = 0; spin < (1u << 16); ++spin) {
    if (rfl(__hip_atomic_load(cnt, __ATOMIC_RELAXED, __HIP_MEMORY_SCOPE_AGENT)) >= want) break;
    __builtin_amdgcn_s_sleep(SLEEP);
  }
}
constexpr int NJ_W1 = 72 * 36, NJ_WD = 9 * 144, NJ_WO = 9 * 18, NJ_ALL = NJ_W1 + NJ_WD + NJ_WO;
struct PrepJob { const char* src; unsigned rs, c1, c2, voff, dst[4]; };
DI void prep_setup(PrepJob& j, int J, int lane, const u16* wo, const u16* wg, const u16* wu, const u16* wd) {
  if (J >= NJ_ALL) J = NJ_ALL - 1;
  const int g = (lane >> 2) & 3;
  unsigned lrow = 8 * g;
  j.c1 = 0; j.c2 = 0;
  if (J < NJ_W1) {
    const int sm = J / 72, tg = J - sm * 72;
    j.src = (const char*)((sm & 1) ? wu : wg) + (size_t)(sm >> 1) * (32 * FF * 2) + tg * 128;
    j.rs = FF * 2;
#pragma unroll
    for (int t = 0; t < 4; ++t) j.dst[t] = (unsigned)WS_W1 + (4 * tg + t) * 36864 + sm * 1024;
  } else if (J < NJ_W1 + NJ_WD) {
    const int q = J - NJ_W1, cc = q / 9, tg = q - cc * 9;
    j.src = (const char*)wd + (size_t)cc * (32 * DM * 2) + tg * 128;
    j.rs = DM * 2;
#pragma unroll
    for (int t = 0; t < 4; ++t) { const int nt = 4 * tg + t, nb = nt / 9; j.dst[t] = (unsigned)WS_WD + ((nb * 144 + cc) * 9 + (nt - nb * 9)) * 1024; }
  } else {
    const int q = J - NJ_W1 - NJ_WD, step = q / 9, tg = q - step * 9;
    j.rs = DM * 2;
    if (step < 16) { j.src = (const char*)wo + (size_t)step * (36 * DM * 2) + tg * 128; lrow = 18 * (g >> 1) + 4 * (g & 1); j.c2 = 4; }
    else { j.src = (const char*)wo + (size_t)(288 * (step - 16) + 16) * (DM * 2) + tg * 128; lrow = 72 * g; j.c1 = 16; }
#pragma unroll
    for (int t = 0; t < 4; ++t) j.dst[t] = (unsigned)WS_WO + (step * 36 + 4 * tg + t) * 1024;
  }
#pragma unroll
  for (int t = 0; t < 4; ++t) j.dst[t] = rfl(j.dst[t]);
  j.voff = lrow * j.rs + (lane >> 4) * 32 + (lane & 3) * 8;
}
template <int JJ>
DI void prep_load(const PrepJob& j, u32x2 (&r)[8]) {
  const char* b = j.src + (JJ + j.c1 * (JJ >> 1) + j.c2 * (JJ >> 2)) * j.rs;
  asm volatile("global_load_dwordx2 %0, %1, %2" : "=v"(r[JJ]) : "v"(j.voff), "s"(b));
}
DI void prep_load_all(const PrepJob& j, u32x2 (&r)[8]) {
  prep_load<0>(j, r); prep_load<1>(j, r); prep_load<2>(j, r); prep_load<3>(j, r); prep_load<4>(j, r); prep_load<5>(j, r); prep_load<6>(j, r); prep_load<7>(j, r);
}
DI void prep_store(const PrepJob& j, const u32x2 (&r)[8], __amdgpu_buffer_rsrc_t rs, unsigned so) {
  u32x4 u[4];
#pragma unroll
  for (int i = 0; i < 4; ++i) {
    u[0][i] = __builtin_amdgcn_perm(r[2 * i + 1][0], r[2 * i][0], 0x05040100u);
    u[1][i] = __builtin_amdgcn_perm(r[2 * i + 1][0], r[2 * i][0], 0x07060302u);
    u[2][i] = __builtin_amdgcn_perm(r[2 * i + 1][1], r[2 * i][1], 0x05040100u);
    u[3][i] = __builtin_amdgcn_perm(r[2 * i + 1][1], r[2 * i][1], 0x07060302u);
  }
#pragma unroll
  for (int i = 0; i < 4; ++i) {
    const auto a0 = __builtin_amdgcn_permlane32_swap(u[0][i], u[2][i], false, false);
    const auto a1 = __builtin_amdgcn_permlane32_swap(u[1][i], u[3][i], false, false);
    const auto b0 = __builtin_amdgcn_permlane16_swap(a0[0], a1[0], false, false);
    const auto b1 = __builtin_amdgcn_permlane16_swap(a0[1], a1[1], false, false);
    u[0][i] = b0[0]; u[1][i] = b0[1]; u[2][i] = b1[0]; u[3][i] = b1[1];
  }
#pragma unroll
  for (int t = 0; t < 4; ++t) bst16<PREP_AUX>(u[t], rs, so, j.dst[t]);
}
#define PIN8(r) "+v"(r[0]), "+v"(r[1]), "+v"(r[2]), "+v"(r[3]), "+v"(r[4]), "+v"(r[5]), "+v"(r[6]), "+v"(r[7])

#define K1_PF 4
#define K1_TOUCH 1
#define K1_TSLEEP 16
#define K1_DEPTH 2
constexpr int K1_PITCH = 224, K1_SLOT = 16384 + 64 * K1_PITCH  , K1_NS = K1_DEPTH + 1;
constexpr int K1_SP = 232;
constexpr int K1_STG = K1_NS * K1_SLOT, K1_CS = K1_STG + 128 * K1_SP + 64, K1_ST = K1_CS + 512 + 64, K1_LDS = K1_ST + 2048;
struct K1C {
  LDSP const char *xa[2], *wl, *wc0, *wc1;
  u32x4 X[3][2], wf[K1_PF + 1], ones;
  f32x4 acc[4][2];
  f32x4 sm[2]; float sq[2];
};
DI void k1_loader(const char* xb, const char* wb, const u16* wo_, char* ws, int j, int lane, unsigned lds0) {
  __builtin_amdgcn_s_setprio(3);
  unsigned xo[2], wo[4];
  const int g = lane >> 4;
  xo[0] = (lane >> 3) * 1152 + (((lane & 7) ^ g) * 16);
  xo[1] = (8 + (lane >> 3)) * 1152 + (((lane & 7) ^ (4 + g)) * 16);
  xb += j * (32 * 1152);
  const unsigned xd = lds0 + j * 4096, wd = lds0 + 16384 + j * 1024;
#define K1_X(z, so) do { dma16(xb + (z) * 128, xo[0], xd + (so)); dma16(xb + (z) * 128, xo[1], xd + (so) + 1024);                    \
    dma16(xb + (z) * 128 + 16 * 1152, xo[0], xd + (so) + 2048); dma16(xb + (z) * 128 + 16 * 1152, xo[1], xd + (so) + 3072); } while (0)
#define K1_W(z, so) do { _Pragma("unroll") for (int i = 0; i < 3; ++i) dma16(wb + (z) * (64 * 3456), wo[i], wd + (so) + i * 4096);     \
    if (j < 2) dma16(wb + (z) * (64 * 3456), wo[3], wd + (so) + 3 * 4096); } while (0)
  K1_X(0, 0);
  SBAR();
#pragma unroll
  for (int i = 0; i < 4; ++i) {
    const unsigned u = 64 * (j + 4 * i) + lane, r = u / 14;
    wo[i] = ((r & ~12u) | ((r & 4u) << 1) | ((r & 8u) >> 1)) * 3456 + (u - r * 14) * 16;
  }
  K1_W(0, 0);
#pragma unroll
  for (int z = 1; z < K1_DEPTH; ++z) { K1_X(z, z * K1_SLOT); K1_W(z, z * K1_SLOT); }
  unsigned so = K1_DEPTH * K1_SLOT;
#pragma unroll 1
  for (int z = K1_DEPTH; z < 9; ++z) {
    asm volatile("s_waitcnt vmcnt(%0)\n\ts_barrier" ::"i"(7 * (K1_DEPTH - 1)) : "memory");
    K1_X(z, so); K1_W(z, so);
    so = so == (K1_NS - 1) * K1_SLOT ? 0 : so + K1_SLOT;
  }
  if (K1_DEPTH > 2) asm volatile("s_waitcnt vmcnt(14)\n\ts_barrier" ::: "memory");
  asm volatile("s_waitcnt vmcnt(7)\n\ts_barrier" ::: "memory");
  const int b = blockIdx.x;
  const bool dojob = j == 1 && b < NJ_WO, dorow = j >= 2 || (j == 0 && b < 64);
  const int br = j >= 2 ? 2 * b + j - 2 : 512 + (b & 63);
  PrepJob jb; u32x2 r[8]; unsigned wr[5];
  if (dojob) prep_setup(jb, NJ_W1 + NJ_WD + b, lane, wo_, wo_, wo_, wo_);
  const char* rp = (const char*)wo_ + (size_t)wo_row(br >> 5, (br >> 3) & 3, br & 7) * (DM * 2);
  SBAR();
  asm volatile("s_waitcnt vmcnt(0)\n\ts_barrier" ::: "memory");
  if (dojob) prep_load_all(jb, r);
  if (dorow) {
#pragma unroll
    for (int i = 0; i < 5; ++i) wr[i] = gld4(rp, (i < 4 ? lane + 64 * i : 256 + (lane & 31)) * 4);
  }
  asm volatile("s_barrier");
  asm volatile("s_barrier");
  const __amdgpu_buffer_rsrc_t wrs = mkrsrc(ws, (unsigned)WS_END);
  asm volatile("s_waitcnt vmcnt(0)" : PIN8(r), "+v"(wr[0]), "+v"(wr[1]), "+v"(wr[2]), "+v"(wr[3]), "+v"(wr[4])::"memory");
  if (dojob) {
#pragma unroll
    for (int t = 0; t < 4; ++t) jb.dst[t] = rfl(jb.dst[t]);
    prep_store(jb, r, wrs, ((lane >> 2) & 3) * 256 + ((lane & 3) * 4 + (lane >> 4)) * 16);
  }
  if (dorow) {
    float a = 0.f;
#pragma unroll
    for (int i = 0; i < 4; ++i) a = dot2(wr[i], 0x3f803f80u, a);
    a = dot2(lane >= 32 ? 0u : wr[4], 0x3f803f80u, a);
    a = xor16_sum(xor32_sum(a));
    a += __uint_as_float(__builtin_amdgcn_update_dpp(0u, __float_as_uint(a), 0x128, 0xf, 0xf, false));
    a += __uint_as_float(__builtin_amdgcn_update_dpp(0u, __float_as_uint(a), 0x124, 0xf, 0xf, false));
    a += __uint_as_float(__builtin_amdgcn_update_dpp(0u, __float_as_uint(a), 0x122, 0xf, 0xf, false));
    a += __uint_as_float(__builtin_amdgcn_update_dpp(0u, __float_as_uint(a), 0x121, 0xf, 0xf, false));
    a *= (1.0f / DM);
    const unsigned hi = cvtpk(a, 0.f) & 0xffffu;
    const unsigned lo = cvtpk(a - bflo(hi), 0.f) & 0xffffu;
    __builtin_amdgcn_raw_buffer_store_b16((u16)(lane ? lo : hi), wrs, lane < 2 ? (unsigned)WS_WBAR + (lane * DM + br) * 2 : 0x80000000u, 0, 16);
  }
#undef K1_X
#undef K1_W
}
DI u32x4 tr_frag(LDSP const char* p, int d) {
  s16x4 lo = __builtin_amdgcn_ds_read_tr16_b64_v4i16((LDSP s16x4*)p);
  s16x4 hi = __builtin_amdgcn_ds_read_tr16_b64_v4i16((LDSP s16x4*)(p + d));
  return __builtin_bit_cast(u32x4, __builtin_shufflevector(lo, hi, 0, 1, 2, 3, 4, 5, 6, 7));
}
template <int NHG, int F> DI void k1_read(K1C& c) {
  constexpr int NF = 4 + NHG, S = F / NF, j = F % NF, Z = S >> 1, so = (Z % K1_NS) * K1_SLOT;
  if constexpr (j == 0) {
    c.X[S % 3][0] = ldsr16(c.xa[S & 1] + so);
    c.X[S % 3][1] = ldsr16(c.xa[S & 1] + so + 2048);
  }
  constexpr int wo = so + (S & 1) * 7168;
  if constexpr (NHG == 0 || j < 3) c.wf[F % (K1_PF + 1)] = tr_frag(c.wl + wo + j * 32, 1792);
  else c.wf[F % (K1_PF + 1)] = tr_frag((j == 3 ? c.wc0 : c.wc1) + wo, 1792);
}
template <int NHG, int F> DI void k1_frag(K1C& c) {
  constexpr int NF = 4 + NHG, S = F / NF, j = F % NF, Z = S >> 1, N = F + K1_PF, fz = F % (2 * NF);
  if constexpr (S == 0) {
    if constexpr (NHG == 0 || j < 3) { G_MFMA0(c.acc[j][0], c.wf[F % (K1_PF + 1)], c.X[0][0]); G_MFMA0(c.acc[j][1], c.wf[F % (K1_PF + 1)], c.X[0][1]); }
    else G_MFMA0(c.acc[3][j - 3], c.wf[F % (K1_PF + 1)], c.ones);
  } else {
    if constexpr (NHG == 0 || j < 3) { G_MFMA(c.acc[j][0], c.wf[F % (K1_PF + 1)], c.X[S % 3][0]); G_MFMA(c.acc[j][1], c.wf[F % (K1_PF + 1)], c.X[S % 3][1]); }
    else G_MFMA(c.acc[3][j - 3], c.wf[F % (K1_PF + 1)], c.ones);
  }
  if constexpr ((S & 1) == NHG && j < 4) {
#pragma unroll
    for (int t = 0; t < 2; ++t) { const unsigned v = c.X[S % 3][t][j]; c.sq[t] = dot2(v, v, c.sq[t]); PIN(c.sq[t]); }
    if constexpr (j < 2) { if constexpr (S < 2) G_MFMA0(c.sm[j], c.ones, c.X[S % 3][j]); else G_MFMA(c.sm[j], c.ones, c.X[S % 3][j]); }
  }
  if constexpr (N < 18 * NF) {
    if constexpr (N % (2 * NF) == 0) {
      asm volatile("s_waitcnt lgkmcnt(0)\n\ts_barrier" ::: "memory");
    }
    k1_read<NHG, N>(c);
  }
  SBAR();
}
template <int NHG, int... Fs> DI void k1_run(K1C& c, std::integer_sequence<int, Fs...>) {
  k1_read<NHG, 0>(c); k1_read<NHG, 1>(c); k1_read<NHG, 2>(c);
  if (K1_PF > 3) k1_read<NHG, 3>(c);
  if (K1_PF > 4) k1_read<NHG, 4>(c);
  if (K1_PF > 5) k1_read<NHG, 5>(c);
  SBAR();
  (k1_frag<NHG, Fs>(c), ...);
  asm volatile("s_nop 7" : "+v"(c.acc[0][0]), "+v"(c.acc[0][1]), "+v"(c.acc[1][0]), "+v"(c.acc[1][1]), "+v"(c.acc[2][0]), "+v"(c.acc[2][1]),
               "+v"(c.acc[3][0]), "+v"(c.acc[3][1]), "+v"(c.sm[0]), "+v"(c.sm[1]));
  SBAR();
}
extern "C" __global__ __launch_bounds__(768, 3) void k_qkv(const u16* x, const u16* wqkv, const u16* wo, char* ws) {
  extern __shared__ __attribute__((aligned(16))) char smem[];
  const int tid = threadIdx.x, lane = tid & 63, w = rfl(tid >> 6);
  const int xcd = blockIdx.x & 7, bi = blockIdx.x >> 3;
  unsigned pf;
  CODE_PF2(pf, k_qkv, k_end, 0, bi, (w == 8 && bi < 9) ? 128u : 0u);
  SBAR();
  asm volatile("" : "+s"(x), "+s"(wqkv), "+s"(wo), "+s"(ws));
  LDSP char* lds = (LDSP char*)smem;
  const unsigned lds0 = (unsigned)(uintptr_t)lds;
  const int rb = 4 * (xcd >> 1) + (bi & 3), cb = 8 * (xcd & 1) + (bi >> 2);
  const int c0 = 108 * cb - 4 * (cb & 1);
  if (__builtin_expect(w >= 8, 1)) {
    k1_loader((const char*)x + rb * (128 * 1152), (const char*)wqkv + c0 * 2, wo, ws, w - 8, lane, lds0);
    asm volatile("" ::"v"(pf));
    return;
  }
  const int m = lane & 15, g = lane >> 4, mg = w & 3, nh = w >> 2;
  STAMP_DECL; STAMP(0);
  K1C c;
  c.ones = u32x4{0x3f803f80u, 0x3f803f80u, 0x3f803f80u, 0x3f803f80u};
  PIN(c.ones);
  c.xa[0] = lds + (32 * mg + m) * 128 + ((g ^ (m >> 1)) * 16);
  c.xa[1] = lds + (32 * mg + m) * 128 + (((4 + g) ^ (m >> 1)) * 16);
  LDSP const char* wbase = lds + 16384 + (16 * (g >> 1) + 4 * (g & 1) + (m >> 2)) * K1_PITCH + (m & 3) * 8;
  c.wl = wbase + nh * 128;
  c.wc0 = wbase + mg * 32;
  c.wc1 = wbase + (mg < 3 ? mg + 4 : 6) * 32;
  c.sq[0] = c.sq[1] = 0.f;
  const int off = 4 * (cb & 1), T0 = 128 * rb;
  LDSP char* stg = lds + K1_STG;
  LDSP char* a_st = lds + K1_ST + (32 * mg + m) * 8;                 PIN(a_st);
  LDSP char* a_cs = lds + K1_CS + (64 * nh + 4 * g) * 4;             PIN(a_cs);
  LDSP char* a_sw = stg + (32 * mg + m) * K1_SP + (64 * nh + 4 * g) * 2;   PIN(a_sw);
  LDSP char* a_tr = stg + (32 * mg + 4 * (g & 1) + (m >> 2)) * K1_SP + (64 * nh + 16 * (g >> 1) + 4 * (m & 3)) * 2;   PIN(a_tr);
  float qs[4];
#pragma unroll
  for (int j = 0; j < 4; ++j) { qs[j] = (c0 + 64 * nh + 16 * j + 4 * g < DM) ? QSCALE : 1.0f; PIN(qs[j]); }
  const __amdgpu_buffer_rsrc_t wrs = mkrsrc(ws, (unsigned)WS_END);
  constexpr unsigned DROP = 0x80000000u;
  const unsigned kvo = (unsigned)WS_KV + (T0 >> 9) * (unsigned)CHUNK_B;
  unsigned vo[2] = {0, 0}, vone = 0, vd[4] = {DROP, DROP, DROP, DROP};
  if (cb >= 10) {
#pragma unroll
  for (int tt = 0; tt < 2; ++tt) {
    const int cc = 64 * nh + 32 * tt + 16 * (g >> 1) + m - off, hd = (cc + 18) / 18 - 1, G = 6 * cb + hd;
    vo[tt] = (cc >= 0 && cc < 108) ? hd * 4864 + 2 * mg * 608 + (g & 1) * 304 + (cc - 18 * hd) * 16 : 29184;
  }
  vone = tid < 96 ? (tid >> 4) * 4864 + (tid & 15) * 304 + 288 : 29184;
#pragma unroll
  for (int i = 0; i < 4; ++i) {
    const unsigned u = i * 512 + tid, hd = u / 304;
    vd[i] = (u < 1824 && 6 * cb + hd >= 64) ? kvo + 18432 + ((T0 >> 4) & 31) * 608 + (6 * cb + hd - 64) * (unsigned)KV_HEAD_B + (u - hd * 304) * 16 : DROP;
  }
  }
  PIN(vone); PIN(vo[0]); PIN(vo[1]); PIN(vd[0]); PIN(vd[1]); PIN(vd[2]); PIN(vd[3]);
  const int tok = tid & 127, T = T0 + tok;
  LDSP const char* a_g = stg + tok * K1_SP + off * 2;   PIN(a_g);
  unsigned um[2], ut[2];
  const unsigned tl = ((T >> 5) & 15) * 1024 + (T & 31) * 16;
#pragma unroll
  for (int it = 0; it < 2; ++it) {
    const int hd = (w >> 1) + 4 * it, G = 6 * cb + hd, head = G & 31;
    if (hd >= 6 || G >= 64) { um[it] = DROP; ut[it] = DROP; }
    else if (G < 32) { um[it] = (unsigned)WS_QM + head * 65536 + (T >> 9) * 16384 + tl; ut[it] = (unsigned)WS_QT + head * 8192 + T * 4; }
    else { um[it] = kvo + head * (unsigned)KV_HEAD_B + tl; ut[it] = kvo + head * (unsigned)KV_HEAD_B + 16384 + ((T >> 6) & 7) * 256 + (T & 31) * 8 + ((T >> 5) & 1) * 4; }
    PIN(um[it]); PIN(ut[it]);
  }
  STAMP(1);
  asm volatile("s_waitcnt vmcnt(0)" : "+v"(pf));
  if (blockIdx.x < 5 && w == 7) bst16<16>(u32x4{0, 0, 0, 0}, wrs, (unsigned)WS_CNT + (blockIdx.x * 64 + lane) * 128, 0);
  asm volatile("s_barrier" ::: "memory");
  STAMP(2);
  if (nh) k1_run<1>(c, std::make_integer_sequence<int, 90>{});
  else k1_run<0>(c, std::make_integer_sequence<int, 72>{});
  STAMP(3);
#pragma unroll
  for (int t = 0; t < 2; ++t) {
    const f32x2 st = {c.sm[t][0], xor32_sum(xor16_sum(c.sq[t]))};
    *(LDSP f32x2*)(a_st + nh * 1024 + t * 128) = st;
  }
  if (nh) {
    *(LDSP f32x4*)(lds + K1_CS + (16 * mg + 4 * g) * 4) = c.acc[3][0];
    *(LDSP f32x4*)(lds + K1_CS + (16 * (mg + 4) + 4 * g) * 4) = c.acc[3][1];
  }
  asm volatile("s_waitcnt lgkmcnt(0)\n\ts_barrier" ::: "memory");
  {
    f32x2 p[2][2]; f32x4 cc[4];
#pragma unroll
    for (int t = 0; t < 2; ++t) { p[t][0] = *(LDSP const f32x2*)(a_st + t * 128); p[t][1] = *(LDSP const f32x2*)(a_st + 1024 + t * 128); }
#pragma unroll
    for (int j = 0; j < 4; ++j) cc[j] = *(LDSP const f32x4*)(a_cs + j * 64);
    SBAR();
#pragma unroll
    for (int t = 0; t < 2; ++t) {
      const float mean = (p[t][0][0] + p[t][1][0]) * (1.0f / DM);
      const float var = fmaxf((p[t][0][1] + p[t][1][1]) * (1.0f / DM) - mean * mean, 0.f);
      const float rstd = __builtin_amdgcn_rsqf(var + LN_EPS);
      if (cb == 0 && nh == 0) __builtin_amdgcn_raw_buffer_store_b32(__float_as_uint(mean), wrs, (unsigned)WS_XMEAN + (T0 + 32 * mg + 16 * t + m) * 4, 0, QKV_AUX);
#pragma unroll
      for (int j = 0; j < 4; ++j) {
        if (j < 3 || nh == 0) {
          const f32x4 a = (c.acc[j][t] - mean * cc[j]) * (rstd * qs[j]);
          *(LDSP u32x2*)(a_sw + t * (16 * K1_SP) + j * 32) = u32x2{cvtpk(a[0], a[1]), cvtpk(a[2], a[3])};
        }
      }
    }
  }
  if (cb >= 10) {
    asm volatile("s_waitcnt lgkmcnt(0)" ::: "memory");
    u32x4 v[2][2];
#pragma unroll
    for (int tt = 0; tt < 2; ++tt)
#pragma unroll
      for (int t = 0; t < 2; ++t) v[tt][t] = tr_frag(a_tr + t * (16 * K1_SP) + tt * 64, 8 * K1_SP);
    *(LDSP u32x4*)(lds + vone) = c.ones;
#pragma unroll
    for (int tt = 0; tt < 2; ++tt)
#pragma unroll
      for (int t = 0; t < 2; ++t) *(LDSP u32x4*)(lds + vo[tt] + t * 608) = v[tt][t];
  }
  STAMP(4);
  asm volatile("s_waitcnt lgkmcnt(0)\n\ts_barrier" ::: "memory");
  if (cb >= 10) {
    u32x4 v[4];
#pragma unroll
    for (int i = 0; i < 4; ++i) v[i] = ldsr16(lds + i * 8192 + tid * 16);
#pragma unroll
    for (int i = 0; i < 4; ++i) bst16<V_AUX>(v[i], wrs, vd[i], 0);
  }
  if (cb <= 10) {
    unsigned d[2][9];
#pragma unroll
    for (int it = 0; it < 2; ++it) {
      const int hd = (w >> 1) + 4 * it;
      LDSP const unsigned* sp = (LDSP const unsigned*)(a_g + 36 * (hd < 6 ? hd : 0));
#pragma unroll
      for (int i = 0; i < 9; ++i) d[it][i] = sp[i];
    }
    SBAR();
#pragma unroll
    for (int it = 0; it < 2; ++it) {
      bst16<QKV_AUX>(u32x4{d[it][0], d[it][1], d[it][2], d[it][3]}, wrs, um[it], 0);
      bst16<QKV_AUX>(u32x4{d[it][4], d[it][5], d[it][6], d[it][7]}, wrs, um[it] + 512, 0);
      __builtin_amdgcn_raw_buffer_store_b32(d[it][8], wrs, ut[it], 0, QKT_AUX);
    }
#pragma unroll
    for (int it = 0; it < 2; ++it) {
      const int hd = (w >> 1) + 4 * it, G = 6 * cb + hd;
      if (hd < 6 && G >= 32 && G < 64) {
        float n2 = 0.f;
#pragma unroll
        for (int i = 0; i < 9; ++i) n2 = dot2(d[it][i], d[it][i], n2);
        unsigned nb = __float_as_uint(n2);
        { auto r = __builtin_amdgcn_permlane32_swap(nb, nb, false, false); nb = max(r[0], r[1]); }
        { auto r = __builtin_amdgcn_permlane16_swap(nb, nb, false, false); nb = max(r[0], r[1]); }
        __builtin_amdgcn_raw_buffer_store_b32(nb, wrs, (unsigned)WS_KMAX + (((2 * rb + (w & 1)) * 32 + (G & 31)) * 16 + m) * 4, 0, QKV_AUX);
      }
    }
  }
  STAMP(5); STAMP_OUT(1, 6);
}

constexpr int K2_LDS = (int)KV_HEAD_B, K3_WBAR = 90 * 1024, K3_XC = 96 * 1024;
#define A_MFMA_ACC(d, a, b) asm volatile("v_mfma_f32_32x32x16_bf16 %0, %1, %2, %0" : "+v"(d) : "v"(a), "v"(b))
#define AT_M(m)
#define AT_U
struct AttnC {
  LDSP const char *kmb, *ktb, *vb;
  unsigned kma[3], kta[3], va[3]; unsigned long long mk, mv;
  u32x4 qm, qt, kf, v0, v1, p1; u32x2 ktl;
  f32x16 negm, o, sa, sb;
};
constexpr int k_off(int T) { return (T >> 4) * (int)CHUNK_B + (T & 15) * 1024; }
constexpr int kt_off(int T) { return (T >> 4) * (int)CHUNK_B + ((T >> 1) & 7) * 256; }
constexpr int v_off(int T) { return (T >> 4) * (int)CHUNK_B + (T & 15) * 1216; }
#define AT_STMT(TXT)                                                                                                              \
  asm volatile(TXT : "+{v[2:17]}"(c.o), "+{v[34:49]}"(c.sa), "+{v[50:65]}"(c.sb), "+{v[66:69]}"(c.kf), "+{v[70:73]}"(c.p1),        \
               "+{v[84:87]}"(c.v1), "+{v[88:91]}"(c.v0), "+{v[92:93]}"(c.ktl)                                                      \
               : "{v[18:33]}"(c.negm), "{v[98:101]}"(c.qm), "{v[102:105]}"(c.qt), [ka] "v"(c.kma[KO >> 16]), [kt] "v"(c.kta[KT >> 16]), \
                 [va] "v"(c.va[V1 >> 16]), [vb] "v"(c.va[V0 >> 16]), [ko] "i"(KO & 0xffff), [kto] "i"(KT & 0xffff), [vo1] "i"(V1 & 0xffff),  \
                 [vo0] "i"(V0 & 0xffff), [mk] "s"(c.mk), [mv] "s"(c.mv)                                                            \
               : "v74", "v75", "v76", "v77")
template <int T> DI void attn_tile(AttnC& c) {
  constexpr int KO = k_off(T < 62 ? T + 2 : 0), KT = kt_off(T < 62 ? T + 2 : 0), V1 = v_off(T) + 608, V0 = v_off(T < 63 ? T + 1 : 0);
  if constexpr (T == 0)
    AT_STMT("v_mfma_f32_32x32x8_bf16 v[50:65], v[92:93], v[104:105], v[18:33]\n\t" "v_exp_f32 v34, v34\n\t" "v_exp_f32 v35, v35\n\t" "v_exp_f32 v36, v36\n\t" "v_exp_f32 v37, v37\n\t" AT_M("mk") "ds_read_b64 v[92:93], %[kt] offset:%[kto]\n\t" AT_U AT_M("mv") "ds_read_b128 v[84:87], %[va] offset:%[vo1]\n\t" AT_U "v_cvt_pk_bf16_f32 v74, v34, v35\n\t" "v_exp_f32 v38, v38\n\t" "v_cvt_pk_bf16_f32 v75, v36, v37\n\t" "v_exp_f32 v39, v39\n\t" "v_exp_f32 v40, v40\n\t" "v_exp_f32 v41, v41\n\t" "s_waitcnt lgkmcnt(2)\n\t" "v_mfma_f32_32x32x16_bf16 v[50:65], v[66:69], v[98:101], v[50:65]\n\t" "ds_read_b128 v[66:69], %[ka] offset:%[ko]\n\t" "v_cvt_pk_bf16_f32 v76, v38, v39\n\t" "v_exp_f32 v42, v42\n\t" "v_cvt_pk_bf16_f32 v77, v40, v41\n\t" "v_exp_f32 v43, v43\n\t" "v_exp_f32 v44, v44\n\t" "v_exp_f32 v45, v45\n\t" "s_waitcnt lgkmcnt(2)\n\t" "v_mfma_f32_32x32x16_bf16 v[2:17], v[88:91], v[74:77], v[2:17]\n\t" AT_M("mv") "ds_read_b128 v[88:91], %[vb] offset:%[vo0]\n\t" AT_U "v_cvt_pk_bf16_f32 v70, v42, v43\n\t" "v_exp_f32 v46, v46\n\t" "v_cvt_pk_bf16_f32 v71, v44, v45\n\t" "v_exp_f32 v47, v47\n\t" "v_exp_f32 v48, v48\n\t" "v_exp_f32 v49, v49\n\t" "v_cvt_pk_bf16_f32 v72, v46, v47\n\t" "v_cvt_pk_bf16_f32 v73, v48, v49\n\t");
  else if constexpr (T == 62)
    AT_STMT("v_mfma_f32_32x32x8_bf16 v[50:65], v[92:93], v[104:105], v[18:33]\n\t" "v_exp_f32 v34, v34\n\t" "v_exp_f32 v35, v35\n\t" "v_exp_f32 v36, v36\n\t" "v_exp_f32 v37, v37\n\t" "s_waitcnt lgkmcnt(2)\n\t" "v_mfma_f32_32x32x16_bf16 v[2:17], v[84:87], v[70:73], v[2:17]\n\t" AT_M("mv") "ds_read_b128 v[84:87], %[va] offset:%[vo1]\n\t" AT_U "v_cvt_pk_bf16_f32 v74, v34, v35\n\t" "v_exp_f32 v38, v38\n\t" "v_cvt_pk_bf16_f32 v75, v36, v37\n\t" "v_exp_f32 v39, v39\n\t" "v_exp_f32 v40, v40\n\t" "v_exp_f32 v41, v41\n\t" "s_waitcnt lgkmcnt(2)\n\t" "v_mfma_f32_32x32x16_bf16 v[50:65], v[66:69], v[98:101], v[50:65]\n\t" "v_cvt_pk_bf16_f32 v76, v38, v39\n\t" "v_exp_f32 v42, v42\n\t" "v_cvt_pk_bf16_f32 v77, v40, v41\n\t" "v_exp_f32 v43, v43\n\t" "v_exp_f32 v44, v44\n\t" "v_exp_f32 v45, v45\n\t" "s_waitcnt lgkmcnt(1)\n\t" "v_mfma_f32_32x32x16_bf16 v[2:17], v[88:91], v[74:77], v[2:17]\n\t" AT_M("mv") "ds_read_b128 v[88:91], %[vb] offset:%[vo0]\n\t" AT_U "v_cvt_pk_bf16_f32 v70, v42, v43\n\t" "v_exp_f32 v46, v46\n\t" "v_cvt_pk_bf16_f32 v71, v44, v45\n\t" "v_exp_f32 v47, v47\n\t" "v_exp_f32 v48, v48\n\t" "v_exp_f32 v49, v49\n\t" "v_cvt_pk_bf16_f32 v72, v46, v47\n\t" "v_cvt_pk_bf16_f32 v73, v48, v49\n\t");
  else if constexpr (T == 63)
    AT_STMT("v_exp_f32 v50, v50\n\t" "v_exp_f32 v51, v51\n\t" "v_exp_f32 v52, v52\n\t" "v_exp_f32 v53, v53\n\t" "s_waitcnt lgkmcnt(0)\n\t" "v_mfma_f32_32x32x16_bf16 v[2:17], v[84:87], v[70:73], v[2:17]\n\t" AT_M("mv") "ds_read_b128 v[84:87], %[va] offset:%[vo1]\n\t" AT_U "v_cvt_pk_bf16_f32 v74, v50, v51\n\t" "v_exp_f32 v54, v54\n\t" "v_cvt_pk_bf16_f32 v75, v52, v53\n\t" "v_exp_f32 v55, v55\n\t" "v_exp_f32 v56, v56\n\t" "v_exp_f32 v57, v57\n\t" "v_cvt_pk_bf16_f32 v76, v54, v55\n\t" "v_exp_f32 v58, v58\n\t" "v_cvt_pk_bf16_f32 v77, v56, v57\n\t" "v_exp_f32 v59, v59\n\t" "v_exp_f32 v60, v60\n\t" "v_exp_f32 v61, v61\n\t" "s_waitcnt lgkmcnt(1)\n\t" "v_mfma_f32_32x32x16_bf16 v[2:17], v[88:91], v[74:77], v[2:17]\n\t" "v_cvt_pk_bf16_f32 v70, v58, v59\n\t" "v_exp_f32 v62, v62\n\t" "v_cvt_pk_bf16_f32 v71, v60, v61\n\t" "v_exp_f32 v63, v63\n\t" "v_exp_f32 v64, v64\n\t" "v_exp_f32 v65, v65\n\t" "v_cvt_pk_bf16_f32 v72, v62, v63\n\t" "v_cvt_pk_bf16_f32 v73, v64, v65\n\t");
  else if constexpr ((T & 3) == 0)
    AT_STMT("v_mfma_f32_32x32x8_bf16 v[50:65], v[92:93], v[104:105], v[18:33]\n\t" "v_exp_f32 v34, v34\n\t" "v_exp_f32 v35, v35\n\t" "v_exp_f32 v36, v36\n\t" "v_exp_f32 v37, v37\n\t" "s_waitcnt lgkmcnt(2)\n\t" "v_mfma_f32_32x32x16_bf16 v[2:17], v[84:87], v[70:73], v[2:17]\n\t" AT_M("mk") "ds_read_b64 v[92:93], %[kt] offset:%[kto]\n\t" AT_U AT_M("mv") "ds_read_b128 v[84:87], %[va] offset:%[vo1]\n\t" AT_U "v_cvt_pk_bf16_f32 v74, v34, v35\n\t" "v_exp_f32 v38, v38\n\t" "v_cvt_pk_bf16_f32 v75, v36, v37\n\t" "v_exp_f32 v39, v39\n\t" "v_exp_f32 v40, v40\n\t" "v_exp_f32 v41, v41\n\t" "s_waitcnt lgkmcnt(2)\n\t" "v_mfma_f32_32x32x16_bf16 v[50:65], v[66:69], v[98:101], v[50:65]\n\t" "ds_read_b128 v[66:69], %[ka] offset:%[ko]\n\t" "v_cvt_pk_bf16_f32 v76, v38, v39\n\t" "v_exp_f32 v42, v42\n\t" "v_cvt_pk_bf16_f32 v77, v40, v41\n\t" "v_exp_f32 v43, v43\n\t" "v_exp_f32 v44, v44\n\t" "v_exp_f32 v45, v45\n\t" "s_waitcnt lgkmcnt(2)\n\t" "v_mfma_f32_32x32x16_bf16 v[2:17], v[88:91], v[74:77], v[2:17]\n\t" AT_M("mv") "ds_read_b128 v[88:91], %[vb] offset:%[vo0]\n\t" AT_U "v_cvt_pk_bf16_f32 v70, v42, v43\n\t" "v_exp_f32 v46, v46\n\t" "v_cvt_pk_bf16_f32 v71, v44, v45\n\t" "v_exp_f32 v47, v47\n\t" "v_exp_f32 v48, v48\n\t" "v_exp_f32 v49, v49\n\t" "v_cvt_pk_bf16_f32 v72, v46, v47\n\t" "v_cvt_pk_bf16_f32 v73, v48, v49\n\t");
  else if constexpr ((T & 3) == 1)
    AT_STMT("v_mfma_f32_32x32x8_bf16 v[34:49], v[92:93], v[102:103], v[18:33]\n\t" "v_exp_f32 v50, v50\n\t" "v_exp_f32 v51, v51\n\t" "v_exp_f32 v52, v52\n\t" "v_exp_f32 v53, v53\n\t" "s_waitcnt lgkmcnt(2)\n\t" "v_mfma_f32_32x32x16_bf16 v[2:17], v[84:87], v[70:73], v[2:17]\n\t" AT_M("mv") "ds_read_b128 v[84:87], %[va] offset:%[vo1]\n\t" AT_U "v_cvt_pk_bf16_f32 v74, v50, v51\n\t" "v_exp_f32 v54, v54\n\t" "v_cvt_pk_bf16_f32 v75, v52, v53\n\t" "v_exp_f32 v55, v55\n\t" "v_exp_f32 v56, v56\n\t" "v_exp_f32 v57, v57\n\t" "s_waitcnt lgkmcnt(2)\n\t" "v_mfma_f32_32x32x16_bf16 v[34:49], v[66:69], v[98:101], v[34:49]\n\t" "ds_read_b128 v[66:69], %[ka] offset:%[ko]\n\t" "v_cvt_pk_bf16_f32 v76, v54, v55\n\t" "v_exp_f32 v58, v58\n\t" "v_cvt_pk_bf16_f32 v77, v56, v57\n\t" "v_exp_f32 v59, v59\n\t" "v_exp_f32 v60, v60\n\t" "v_exp_f32 v61, v61\n\t" "s_waitcnt lgkmcnt(2)\n\t" "v_mfma_f32_32x32x16_bf16 v[2:17], v[88:91], v[74:77], v[2:17]\n\t" AT_M("mv") "ds_read_b128 v[88:91], %[vb] offset:%[vo0]\n\t" AT_U "v_cvt_pk_bf16_f32 v70, v58, v59\n\t" "v_exp_f32 v62, v62\n\t" "v_cvt_pk_bf16_f32 v71, v60, v61\n\t" "v_exp_f32 v63, v63\n\t" "v_exp_f32 v64, v64\n\t" "v_exp_f32 v65, v65\n\t" "v_cvt_pk_bf16_f32 v72, v62, v63\n\t" "v_cvt_pk_bf16_f32 v73, v64, v65\n\t");
  else if constexpr ((T & 3) == 2)
    AT_STMT("v_mfma_f32_32x32x8_bf16 v[50:65], v[92:93], v[104:105], v[18:33]\n\t" "v_exp_f32 v34, v34\n\t" "v_exp_f32 v35, v35\n\t" "v_exp_f32 v36, v36\n\t" "v_exp_f32 v37, v37\n\t" "s_waitcnt lgkmcnt(2)\n\t" "v_mfma_f32_32x32x16_bf16 v[2:17], v[84:87], v[70:73], v[2:17]\n\t" AT_M("mk") "ds_read_b64 v[92:93], %[kt] offset:%[kto]\n\t" AT_U AT_M("mv") "ds_read_b128 v[84:87], %[va] offset:%[vo1]\n\t" AT_U "v_cvt_pk_bf16_f32 v74, v34, v35\n\t" "v_exp_f32 v38, v38\n\t" "v_cvt_pk_bf16_f32 v75, v36, v37\n\t" "v_exp_f32 v39, v39\n\t" "v_exp_f32 v40, v40\n\t" "v_exp_f32 v41, v41\n\t" "s_waitcnt lgkmcnt(2)\n\t" "v_mfma_f32_32x32x16_bf16 v[50:65], v[66:69], v[98:101], v[50:65]\n\t" "ds_read_b128 v[66:69], %[ka] offset:%[ko]\n\t" "v_cvt_pk_bf16_f32 v76, v38, v39\n\t" "v_exp_f32 v42, v42\n\t" "v_cvt_pk_bf16_f32 v77, v40, v41\n\t" "v_exp_f32 v43, v43\n\t" "v_exp_f32 v44, v44\n\t" "v_exp_f32 v45, v45\n\t" "s_waitcnt lgkmcnt(2)\n\t" "v_mfma_f32_32x32x16_bf16 v[2:17], v[88:91], v[74:77], v[2:17]\n\t" AT_M("mv") "ds_read_b128 v[88:91], %[vb] offset:%[vo0]\n\t" AT_U "v_cvt_pk_bf16_f32 v70, v42, v43\n\t" "v_exp_f32 v46, v46\n\t" "v_cvt_pk_bf16_f32 v71, v44, v45\n\t" "v_exp_f32 v47, v47\n\t" "v_exp_f32 v48, v48\n\t" "v_exp_f32 v49, v49\n\t" "v_cvt_pk_bf16_f32 v72, v46, v47\n\t" "v_cvt_pk_bf16_f32 v73, v48, v49\n\t");
  else if constexpr ((T & 3) == 3)
    AT_STMT("v_mfma_f32_32x32x8_bf16 v[34:49], v[92:93], v[102:103], v[18:33]\n\t" "v_exp_f32 v50, v50\n\t" "v_exp_f32 v51, v51\n\t" "v_exp_f32 v52, v52\n\t" "v_exp_f32 v53, v53\n\t" "s_waitcnt lgkmcnt(2)\n\t" "v_mfma_f32_32x32x16_bf16 v[2:17], v[84:87], v[70:73], v[2:17]\n\t" AT_M("mv") "ds_read_b128 v[84:87], %[va] offset:%[vo1]\n\t" AT_U "v_cvt_pk_bf16_f32 v74, v50, v51\n\t" "v_exp_f32 v54, v54\n\t" "v_cvt_pk_bf16_f32 v75, v52, v53\n\t" "v_exp_f32 v55, v55\n\t" "v_exp_f32 v56, v56\n\t" "v_exp_f32 v57, v57\n\t" "s_waitcnt lgkmcnt(2)\n\t" "v_mfma_f32_32x32x16_bf16 v[34:49], v[66:69], v[98:101], v[34:49]\n\t" "ds_read_b128 v[66:69], %[ka] offset:%[ko]\n\t" "v_cvt_pk_bf16_f32 v76, v54, v55\n\t" "v_exp_f32 v58, v58\n\t" "v_cvt_pk_bf16_f32 v77, v56, v57\n\t" "v_exp_f32 v59, v59\n\t" "v_exp_f32 v60, v60\n\t" "v_exp_f32 v61, v61\n\t" "s_waitcnt lgkmcnt(2)\n\t" "v_mfma_f32_32x32x16_bf16 v[2:17], v[88:91], v[74:77], v[2:17]\n\t" AT_M("mv") "ds_read_b128 v[88:91], %[vb] offset:%[vo0]\n\t" AT_U "v_cvt_pk_bf16_f32 v70, v58, v59\n\t" "v_exp_f32 v62, v62\n\t" "v_cvt_pk_bf16_f32 v71, v60, v61\n\t" "v_exp_f32 v63, v63\n\t" "v_exp_f32 v64, v64\n\t" "v_exp_f32 v65, v65\n\t" "v_cvt_pk_bf16_f32 v72, v62, v63\n\t" "v_cvt_pk_bf16_f32 v73, v64, v65\n\t");
}
DI void attn_group_slow(LDSP const char* km, LDSP const char* kt, LDSP const char* vp, const u32x4& qm, unsigned qtl, const f32x16& negm, f32x16& o) {
#pragma unroll
  for (int t = 0; t < 4; ++t) {
    const u32x2 k2 = *(LDSP const u32x2*)(kt + (t >> 1) * 256);
    const u32x4 ktl = {k2[0], k2[1], 0, 0};
    const u32x4 kf = ldsr16(km + t * 1024);
    const u32x4 v0 = ldsr16(vp + t * 1216), v1 = ldsr16(vp + t * 1216 + 608);
    u32x4 qq = {0, 0, 0, 0}; qq[t & 1] = qtl;
    f32x16 s = mfma32(ktl, qq, negm);
    s = mfma32(kf, qm, s);
    u32x4 p0, p1;
#pragma unroll
    for (int i = 0; i < 4; ++i) {
      p0[i] = cvtpk(__builtin_amdgcn_exp2f(s[2 * i]), __builtin_amdgcn_exp2f(s[2 * i + 1]));
      p1[i] = cvtpk(__builtin_amdgcn_exp2f(s[8 + 2 * i]), __builtin_amdgcn_exp2f(s[8 + 2 * i + 1]));
    }
    o = mfma32(v0, p0, o);
    o = mfma32(v1, p1, o);
  }
}
template <int G> DI void attn_group(AttnC& c) {
  __builtin_amdgcn_s_setprio(G < 5 ? 3 - (G & 3) : (G < 8 ? 3 : (G < 11 ? 2 : (G < 14 ? 1 : 0))));
  attn_tile<4 * G + 0>(c); attn_tile<4 * G + 1>(c); attn_tile<4 * G + 2>(c); attn_tile<4 * G + 3>(c);
}
DI void attn_helper(const u16* wg, const u16* wu, const u16* wd, char* ws, int head, int j, int lane, unsigned lds0) {
  __builtin_amdgcn_s_setprio(HELP_PRIO);
  const char* kv = ws + WS_KV + (size_t)head * KV_HEAD_B;
  const unsigned lane16 = lane * 16;
#define KV_DMA(o) dma16(kv + (o), lane16, lds0 + (o))
#pragma unroll
  for (int i = 0; i < 4; ++i) KV_DMA(((i >> 1) * 16 + (i & 1) * 4 + j) * 1024);
#pragma unroll
  for (int i = 0; i < 6; ++i) { const int p = i < 2 ? 8 + 4 * i + j : min(16 + 4 * i + j, 36); KV_DMA(p * 1024); }
  const int hw = blockIdx.x * 4 + j;
  PrepJob jb[4];
#pragma unroll
  for (int i = 0; i < 4; ++i) { prep_setup(jb[i], min(4 * hw + i, NJ_W1 + NJ_WD - 1), lane, wd, wg, wu, wd); PIN(jb[i].voff); }
  SBAR();
  asm volatile("s_waitcnt vmcnt(6)\n\ts_barrier" ::: "memory");
#pragma unroll
  for (int i = 0; i < 10; ++i) KV_DMA((unsigned)CHUNK_B + min(4 * i + j, 36) * 1024);
  asm volatile("s_waitcnt vmcnt(10)\n\ts_barrier" ::: "memory");
#pragma unroll
  for (int i = 0; i < 19; ++i) KV_DMA(2 * (unsigned)CHUNK_B + min(4 * i + j, 73) * 1024);
#undef KV_DMA
  u32x2 r[4][8];
  unsigned pf;
  asm volatile("s_waitcnt vmcnt(19)\n\ts_barrier" ::: "memory");
  prep_load_all(jb[0], r[0]); prep_load_all(jb[1], r[1]);
  asm volatile("s_waitcnt vmcnt(16)\n\ts_barrier" ::: "memory");
  const __amdgpu_buffer_rsrc_t wrs = mkrsrc(ws, (unsigned)WS_END);
  const unsigned pso = ((lane >> 2) & 3) * 256 + ((lane & 3) * 4 + (lane >> 4)) * 16;
  prep_load_all(jb[2], r[2]); prep_load_all(jb[3], r[3]);
  CODE_PF2(pf, k_ffn, k_end, 0, j, (blockIdx.x >> 3) == 0 ? 128u : 0u);
  asm volatile("s_waitcnt vmcnt(25)" : PIN8(r[0])::"memory");
  prep_store(jb[0], r[0], wrs, pso);
  asm volatile("s_waitcnt vmcnt(21)" : PIN8(r[1])::"memory");
  prep_store(jb[1], r[1], wrs, pso);
  asm volatile("s_waitcnt vmcnt(17)" : PIN8(r[2])::"memory");
  prep_store(jb[2], r[2], wrs, pso);
  asm volatile("s_waitcnt vmcnt(12)" : PIN8(r[3]), "+v"(pf)::"memory");
  prep_store(jb[3], r[3], wrs, pso);
}
extern "C" __global__ __launch_bounds__(768, 3) void k_attn(const u16* x, const u16* wg, const u16* wu, const u16* wd, char* ws) {
  extern __shared__ __attribute__((aligned(16))) char smem[];
  asm volatile("" : "+s"(x), "+s"(wg), "+s"(wu), "+s"(wd), "+s"(ws));
  LDSP char* lds = (LDSP char*)smem;
  const int tid = threadIdx.x, lane = tid & 63, w = rfl(tid >> 6);
  const int xcd = blockIdx.x & 7, bi = blockIdx.x >> 3;
  const int head = 4 * xcd + (bi & 3), qb = bi >> 2;
  if (__builtin_expect(w >= 8, AT_HFIRST)) { attn_helper(wg, wu, wd, ws, head, w - 8, lane, (unsigned)(uintptr_t)lds); return; }
  const int r = lane & 31, h = lane >> 5;
  const int qt32 = 8 * qb + w;
  STAMP_DECL; STAMP(0);
  AttnC c;
  c.qm = gld16(ws + WS_QM, head * 65536 + qt32 * 1024 + lane * 16);
  unsigned qtl = gld4(ws + WS_QT, head * 8192 + (32 * qt32 + r) * 4);
  u32x4 km0 = gld16(ws + WS_KMAX, ((lane >> 1) * 32 + head) * 64 + (lane & 1) * 32);
  u32x4 km1 = gld16(ws + WS_KMAX, ((lane >> 1) * 32 + head) * 64 + (lane & 1) * 32 + 16);
  const int vrow = r < 19 ? r : ((r >= 20 && r < 28) ? 0 : 18);
  c.kmb = lds + lane * 16;
  c.ktb = lds + 16384 + r * 8;
  c.vb = lds + 18432 + h * 304 + vrow * 16;
#pragma unroll
  for (int i = 0; i < 3; ++i) {
    c.kma[i] = (unsigned)(uintptr_t)c.kmb + i * 65536; c.kta[i] = (unsigned)(uintptr_t)c.ktb + i * 65536; c.va[i] = (unsigned)(uintptr_t)c.vb + i * 65536;
    PIN(c.kma[i]); PIN(c.kta[i]); PIN(c.va[i]);
  }
  c.mk = 0x00000000ffffffffull; c.mv = 0x0007ffff0007ffffull;
  c.ktl = u32x2{0, 0}; c.v0 = u32x4{0, 0, 0, 0}; c.v1 = c.v0; PIN(c.ktl); PIN(c.v0); PIN(c.v1);
#pragma unroll
  for (int i = 0; i < 16; ++i) c.o[i] = 0.f;
  PIN(c.o);
  const __amdgpu_buffer_rsrc_t wrs = mkrsrc(ws, (unsigned)WS_END);
  SBAR();
  asm volatile("s_waitcnt vmcnt(0)" : "+v"(c.qm), "+v"(qtl), "+v"(km0), "+v"(km1)::"memory");
  if (h) qtl = 0;
  float q2 = dot2(qtl, qtl, 0.f);
#pragma unroll
  for (int i = 0; i < 4; ++i) q2 = dot2(c.qm[i], c.qm[i], q2);
  unsigned k2 = max(max(max(km0[0], km0[1]), max(km0[2], km0[3])), max(max(km1[0], km1[1]), max(km1[2], km1[3])));
  { auto rr = __builtin_amdgcn_permlane32_swap(k2, k2, false, false); k2 = max(rr[0], rr[1]); }
  { auto rr = __builtin_amdgcn_permlane16_swap(k2, k2, false, false); k2 = max(rr[0], rr[1]); }
  k2 = max(k2, (unsigned)__builtin_amdgcn_update_dpp(0u, k2, 0x128, 0xf, 0xf, true));
  k2 = max(k2, (unsigned)__builtin_amdgcn_update_dpp(0u, k2, 0x124, 0xf, 0xf, true));
  k2 = max(k2, (unsigned)__builtin_amdgcn_update_dpp(0u, k2, 0x122, 0xf, 0xf, true));
  k2 = max(k2, (unsigned)__builtin_amdgcn_update_dpp(0u, k2, 0x121, 0xf, 0xf, true));
  q2 = xor32_sum(q2);
  const float mshift = __builtin_amdgcn_sqrtf(q2 * __uint_as_float(k2)) * 1.001f;
  c.qt = u32x4{qtl, 0, 0, qtl}; PIN(c.qt);
#pragma unroll
  for (int i = 0; i < 16; ++i) c.negm[i] = -mshift;
  PIN(c.negm);
  STAMP(1);
  asm volatile("s_barrier" ::: "memory");
  STAMP(2);
  asm volatile(AT_M("mk") "ds_read_b64 v[92:93], %[kt]\n\t" AT_U "ds_read_b128 v[70:73], %[ka]\n\tds_read_b128 v[66:69], %[ka] offset:1024\n\t"
               AT_M("mv") "ds_read_b128 v[88:91], %[va]\n\t" AT_U "s_waitcnt lgkmcnt(2)\n\t"
               "v_mfma_f32_32x32x8_bf16 v[34:49], v[92:93], v[102:103], v[18:33]\n\t"
               "v_mfma_f32_32x32x16_bf16 v[34:49], v[70:73], v[98:101], v[34:49]\n\ts_nop 11"
               : "={v[34:49]}"(c.sa), "={v[50:65]}"(c.sb), "={v[66:69]}"(c.kf), "={v[70:73]}"(c.p1), "+{v[88:91]}"(c.v0), "+{v[92:93]}"(c.ktl)
               : "{v[18:33]}"(c.negm), "{v[98:101]}"(c.qm), "{v[102:105]}"(c.qt), [ka] "v"(c.kma[0]), [kt] "v"(c.kta[0]), [va] "v"(c.va[0]),
                 [mk] "s"(c.mk), [mv] "s"(c.mv));
  attn_group<0>(c);
  asm volatile("s_barrier" ::: "memory");
  attn_group<1>(c); attn_group<2>(c);
  asm volatile("s_barrier" ::: "memory");
  attn_group<3>(c); attn_group<4>(c);
  asm volatile("s_barrier" ::: "memory");
  STAMP(3);
  attn_group<5>(c); attn_group<6>(c); attn_group<7>(c); attn_group<8>(c); attn_group<9>(c); attn_group<10>(c);
  attn_group<11>(c); attn_group<12>(c); attn_group<13>(c); attn_group<14>(c); attn_group<15>(c);
  asm volatile("s_waitcnt lgkmcnt(0)\n\tv_mfma_f32_32x32x16_bf16 v[2:17], v[84:87], v[70:73], v[2:17]\n\ts_nop 11"
               : "+{v[2:17]}"(c.o) : "{v[84:87]}"(c.v1), "{v[70:73]}"(c.p1), "{v[18:33]}"(c.negm));
  STAMP(4);
  f32x16 o = c.o;
  float l;
  { auto rr = __builtin_amdgcn_permlane32_swap(__float_as_uint(o[10]), __float_as_uint(o[10]), false, false); l = __uint_as_float(rr[0]); }
  if (__builtin_expect(__any(!(l >= 5.421011e-20f)), 0)) {
    float mx = -INFINITY;
    f32x16 z, negm;
#pragma unroll
    for (int i = 0; i < 16; ++i) z[i] = 0.f;
#pragma unroll 1
    for (int tt = 0; tt < 64; ++tt) {
      const int ck = tt >> 4, tl = tt & 15;
      const u32x2 k2 = *(LDSP const u32x2*)(c.ktb + ck * CHUNK_B + (tl >> 1) * 256);
      const u32x4 ktl = {k2[0], k2[1], 0, 0};
      const u32x4 kf = ldsr16(c.kmb + ck * CHUNK_B + tl * 1024);
      const u32x4 qq = {(tl & 1) ? 0u : qtl, (tl & 1) ? qtl : 0u, 0, 0};
      f32x16 sc = mfma32(ktl, qq, z);
      sc = mfma32(kf, c.qm, sc);
#pragma unroll
      for (int i = 0; i < 16; ++i) mx = fmaxf(mx, sc[i]);
    }
    mx = xor32_max(mx);
#pragma unroll
    for (int i = 0; i < 16; ++i) { negm[i] = -mx; o[i] = 0.f; }
#pragma unroll 1
    for (int gq = 0; gq < 16; ++gq) {
      const int ck = gq >> 2, gg = gq & 3;
      attn_group_slow(c.kmb + ck * CHUNK_B + gg * 4096, c.ktb + ck * CHUNK_B + gg * 512, c.vb + ck * CHUNK_B + gg * 4864, c.qm, qtl, negm, o);
    }
    auto rr = __builtin_amdgcn_permlane32_swap(__float_as_uint(o[10]), __float_as_uint(o[10]), false, false);
    l = __uint_as_float(rr[0]);
  }
  const float rl = 1.0f / l;
  u32x4 ou;
#pragma unroll
  for (int i = 0; i < 4; ++i) ou[i] = cvtpk(o[2 * i] * rl, o[2 * i + 1] * rl);
  const int q = 32 * qt32 + r;
  const unsigned ob = (unsigned)WS_OFR + (q >> 4) * 18432 + (q & 15) * 16;
  bst16<O_AUX>(ou, wrs, ob + (head >> 1) * 1024 + (2 * (head & 1) + h) * 256, 0);
  __builtin_amdgcn_raw_buffer_store_b32(cvtpk(o[8] * rl, o[9] * rl), wrs, h ? 0x80000000u : ob + (16 + (head >> 4)) * 1024 + ((head >> 2) & 3) * 256 + (head & 3) * 4, 0, O_AUX);
  STAMP(5);
  const int m = lane & 15, g = lane >> 4, kh = w >> 2, cg = bi >> 2;
  const int nt0 = 9 * (cg >> 1) + 5 * (cg & 1), mt = 16 * xcd + 4 * (bi & 3) + (w & 3), row = 16 * mt + m;
  const unsigned lds0 = (unsigned)(uintptr_t)lds, lane16 = lane * 16;
  asm volatile("s_barrier" ::: "memory");
#pragma unroll
  for (int i = 0; i < 12; ++i) {
    const int p = min(8 * i + w, 89), st = (p * 205) >> 10, t = p - 5 * st;
    dma16(ws + WS_WO + (st * 36 + nt0 + min(t, 4 - (cg & 1))) * 1024, lane16, lds0 + p * 1024);
  }
  dma16(ws + WS_WBAR + min(w, 2) * 1024, lane16, lds0 + K3_WBAR + min(w, 2) * 1024);
  const bool t4 = kh == 0 && !(cg & 1);
  const unsigned uo = mt * 18432 + (2 * nt0 + 4 * kh + (g >> 1)) * 256 + m * 16 + (g & 1) * 8;
  u32x2 xr[3];
  const unsigned xo = row * 1152 + (nt0 + 2 * kh) * 32 + g * 8;
  asm volatile("global_load_dwordx2 %0, %1, %2" : "=v"(xr[0]) : "v"(xo), "s"(x));
  asm volatile("global_load_dwordx2 %0, %1, %2 offset:32" : "=v"(xr[1]) : "v"(xo), "s"(x));
  asm volatile("global_load_dwordx2 %0, %1, %2" : "=v"(xr[2]) : "v"(t4 ? xo + 128 : xo), "s"(x));
  unsigned xm = gld4(ws + WS_XMEAN, row * 4);
  asm volatile("s_waitcnt vmcnt(17)\n\ts_barrier" ::: "memory");
  if (w == 0) {
    if (lane < K3_REP) __hip_atomic_fetch_add((gu32*)(ws + WS_CNT + (128 + qb * K3_REP) * 128) + lane * 32, 1u, __ATOMIC_RELAXED, __HIP_MEMORY_SCOPE_AGENT);
    wait_count((gu32*)(ws + WS_CNT + (128 + xcd * K3_REP + bi % K3_REP) * 128), 0u, 32u);
  }
  LDSP const char* wa = lds + (45 * kh + 2 * kh) * 1024 + lane16;
  LDSP const char* wb = lds + (45 * kh + 2 - 2 * kh) * 1024 + lane16;
  LDSP const char* wc = lds + (45 * kh + 4) * 1024 + lane16;
  LDSP const char* wbl = lds + K3_WBAR + (m & 1) * 1152 + kh * 576 + g * 16;
  unsigned so = ((g & 1) ? (unsigned)WS_HC : (unsigned)WS_HU) + (uo & ~8u);
  PIN(wa); PIN(wb); PIN(wc); PIN(wbl); PIN(so);
  asm volatile("s_waitcnt vmcnt(0)\n\ts_barrier" : "+v"(xr[0]), "+v"(xr[1]), "+v"(xr[2]), "+v"(xm)::"memory");
  STAMP(6);
  u32x4 O[9], wf[6];
  const char* obase = ws + WS_OFR + rfl((mt * 18 + 9 * kh) * 1024);
#pragma unroll
  for (int i = 0; i < 9; ++i)
    asm volatile("global_load_dwordx4 %0, %1, %2 sc1" : "=v"(O[i]) : "v"(lane16), "s"(obase + i * 1024));
#define K3_RD(f) ((f) % 6 == 5 ? ldsr16(wbl + ((f) / 6) * 64) : ldsr16(((f) % 6 < 2 ? wa : ((f) % 6 < 4 ? wb : wc)) + ((f) / 6) * 5120 + ((f) % 6 < 4 ? ((f) & 1) * 1024 : 0)))
#pragma unroll
  for (int f = 0; f < 5; ++f) wf[f] = K3_RD(f);
  f32x4 acc[6];
#pragma unroll
  for (int f = 0; f < 54; ++f) {
    if (f == 0) asm volatile("s_waitcnt vmcnt(6)" : "+v"(O[0]), "+v"(O[1]), "+v"(O[2]));
    if (f == 18) asm volatile("s_waitcnt vmcnt(3)" : "+v"(O[3]), "+v"(O[4]), "+v"(O[5]));
    if (f == 36) asm volatile("s_waitcnt vmcnt(0)" : "+v"(O[6]), "+v"(O[7]), "+v"(O[8]));
    if (f + 5 < 54) wf[(f + 5) % 6] = K3_RD(f + 5);
    if (f < 6) G_MFMA0(acc[f % 6], wf[f % 6], O[f / 6]);
    else G_MFMA(acc[f % 6], wf[f % 6], O[f / 6]);
    SBAR();
  }
#undef K3_RD
  asm volatile("s_nop 7" : "+v"(acc[0]), "+v"(acc[1]), "+v"(acc[2]), "+v"(acc[3]), "+v"(acc[4]), "+v"(acc[5]));
  LDSP char* xc = lds + K3_XC + lane16;
#pragma unroll
  for (int i = 0; i < 4; ++i) *(LDSP f32x4*)(xc + w * 4096 + i * 1024) = acc[2 + i];
  asm volatile("s_waitcnt lgkmcnt(0)\n\ts_barrier" ::: "memory");
  f32x4 v[4];
#pragma unroll
  for (int i = 0; i < 4; ++i) v[i] = *(LDSP const f32x4*)(xc + (w ^ 4) * 4096 + i * 1024);
  SBAR();
  const float mean = __uint_as_float(xm) + (acc[5][0] + v[3][0]) + (acc[5][1] + v[3][1]);
  float ssq = 0.f;
#pragma unroll
  for (int t = 0; t < 3; ++t) {
    const f32x4 a = (t < 2 ? acc[t] : acc[4]) + v[t];
    const unsigned h0 = cvtpk(bflo(xr[t][0]) + a[0], bfhi(xr[t][0]) + a[1]);
    const unsigned h1 = cvtpk(bflo(xr[t][1]) + a[2], bfhi(xr[t][1]) + a[3]);
    const unsigned c0 = cvtpk(bflo(h0) - mean, bfhi(h0) - mean);
    const unsigned c1 = cvtpk(bflo(h1) - mean, bfhi(h1) - mean);
    if (t < 2 || t4) { ssq = dot2(c0, c0, ssq); ssq = dot2(c1, c1, ssq); }
    const auto r0 = __builtin_amdgcn_permlane16_swap(h0, c0, false, false);
    const auto r1 = __builtin_amdgcn_permlane16_swap(h1, c1, false, false);
    bst16<H_AUX>(u32x4{r0[0], r1[0], r0[1], r1[1]}, wrs, t < 2 ? so + t * 512 : (t4 ? so + 2048 : 0x80000000u), 0);
  }
  ssq = xor32_sum(xor16_sum(ssq));
  __builtin_amdgcn_raw_buffer_store_b32(__float_as_uint(ssq), wrs, (unsigned)WS_SSQ + ((2 * cg + kh) * 2048 + row) * 4, 0, H_AUX);
  STAMP(7); STAMP_OUT(2, 8);
}

constexpr int K4_SLOT = 36864, K4_LDS = 163840;
#define K4_PFD 4
constexpr int K4_PF = K4_PFD;
constexpr int K5_PERM[18] = {0, 1, 2, 6, 7, 8, 9, 10, 11, 15, 16, 17, 3, 4, 5, 12, 13, 14};
constexpr int K5_R[4] = {36864, 73728, 110592, 0};
constexpr int K5_OC[9] = {65536, 102400, 139264, 147456, 155648, 0, 8192, 16384, 24576};
struct K5P { const char* wb; unsigned q[4], qs[4], qo, qos, lds0; };
template <int Z> DI void k5_piece(const K5P& k, int i, int w, unsigned lane16) {
  constexpr int P0 = K5_PERM[2 * Z] * 9216, D = (K5_PERM[2 * Z + 1] - K5_PERM[2 * Z]) * 9216;
  if (i < 3 || (w >> 2) == (Z & 1))
    dma16(k.wb + (P0 + k.q[i] + k.qs[i] * D), lane16, k.lds0 + K5_R[Z & 3] + (i < 3 ? 8 * i + w : 24 + (w & 3)) * 1024);
}
template <int Z> DI void k5_own(const K5P& k, int w, unsigned lane16) {
  constexpr int P0 = K5_PERM[2 * Z] * 9216, D = (K5_PERM[2 * Z + 1] - K5_PERM[2 * Z]) * 9216;
  dma16(k.wb + (P0 + k.qo + k.qos * D), lane16, k.lds0 + K5_OC[Z] + w * 1024);
}
#define POLL(dst, p) asm volatile("global_load_dword %0, %1, %2 sc1" : "=v"(dst) : "v"(0u), "s"(p))
struct K4C {
  const char *wb, *xb; unsigned lane16, lds0; int w, dir;
  unsigned pf0, pfo, pfw, pf1, poll; const char *pfb, *pfwb;
  K5P wd;
  gu32 *cntA, *cntB;
  const char *ab, *ab2; u32x4 A0[2][4];
  LDSP const char* wl[4];
  u32x4 X[2][18], wf[K4_PF + 1];
  f32x4 acc[4][4];
  float c1[2], c2[2];
  __amdgpu_buffer_rsrc_t ars; unsigned avo; int T0;
  float ea[2][2], et[2][2]; unsigned pk[2][4];
  f32x2 pa[2], pt[2], c1p[2], c2p[2];
};
DI void k4_piece(const K4C& c, int tile, int p) {
  if (K4_NT) dma16nt(c.wb + (tile * c.dir * K4_SLOT + p * 1024), c.lane16, c.lds0 + (tile & 3) * K4_SLOT + p * 1024);
  else dma16(c.wb + (tile * c.dir * K4_SLOT + p * 1024), c.lane16, c.lds0 + (tile & 3) * K4_SLOT + p * 1024);
}
template <int ZW> DI void k4_issue_w(K4C& c, int i) {
  if (ZW < 9) {
    if (i == 0) k4_piece(c, c.w >> 2, 4 * ZW + (c.w & 3));
    else if ((c.w >> 2) == (ZW & 1)) k4_piece(c, 2, 4 * ZW + (c.w & 3));
  } else if (ZW == 9) { k4_piece(c, 3, 8 * i + c.w); }
}
template <int Z, int I> DI void k4_issue1(K4C& c) {
  if (I == 0) k4_issue_w<Z + K4_WD>(c, 0);
  else if (I < 5) {
    c.X[(I - 1) >> 1][2 * Z + ((I - 1) & 1)] = gld16(c.xb + (((I - 1) >> 1) * 18432 + (2 * Z + ((I - 1) & 1)) * 1024), c.lane16);
  }
  else k4_issue_w<Z + K4_WD>(c, 1);
}
template <int TL, int I> DI void k4_issue2(K4C& c) {
  if (I < 4) k4_piece(c, TL, 8 * I + c.w);
  else if ((c.w >> 2) == (TL & 1)) k4_piece(c, TL, 32 + (c.w & 3));
}
template <int TL, int E, int U> DI void k4_epi(K4C& c) {
  constexpr int e = U >> 2, u = U & 3, mt = e >> 2, i = e & 3, q = e & 1;
  if constexpr (K4_PK && U < 32) {
    constexpr int pr = U >> 3, v = U & 7, m2 = pr >> 1, i0 = 2 * (pr & 1);
    if (v == 0) {
      const f32x2 G = {c.acc[TL & 3][m2][i0], c.acc[TL & 3][m2][i0 + 1]};
      asm volatile("v_pk_mul_f32 %0, %1, %2" : "=v"(c.pa[E]) : "v"(G), "v"(c.c1p[m2]));
    }
    if (v == 1) { const f32x2 Uu = {c.acc[TL & 3][2 + m2][i0], c.acc[TL & 3][2 + m2][i0 + 1]};
      asm volatile("v_pk_mul_f32 %0, %1, %2" : "=v"(c.pt[E]) : "v"(Uu), "v"(c.pa[E]));
      c.pa[E][0] = __builtin_amdgcn_exp2f(c.pa[E][0]); PIN(c.pa[E]); }
    if (v == 2) { c.pa[E][1] = __builtin_amdgcn_exp2f(c.pa[E][1]); PIN(c.pa[E]); }
    if (v == 3) asm volatile("v_pk_fma_f32 %0, %0, %1, %1" : "+v"(c.pa[E]) : "v"(c.c2p[m2]));
    if (v == 4) { c.pa[E][0] = __builtin_amdgcn_rcpf(c.pa[E][0]); PIN(c.pa[E]); }
    if (v == 5) { c.pa[E][1] = __builtin_amdgcn_rcpf(c.pa[E][1]); PIN(c.pa[E]); }
    if (v == 6) asm volatile("v_pk_mul_f32 %0, %0, %1" : "+v"(c.pt[E]) : "v"(c.pa[E]));
    if (v == 7) { c.pk[E][pr] = cvtpk(c.pt[E][0], c.pt[E][1]); PIN(c.pk[E][pr]); }
  } else if constexpr (U < 32) {
    if (u == 0) { const float a = c.acc[TL & 3][mt][i] * c.c1[mt]; c.ea[E][q] = a; c.et[E][q] = a * c.acc[TL & 3][2 + mt][i]; PIN(c.ea[E][q]); PIN(c.et[E][q]); }
    if (u == 1) { c.ea[E][q] = __builtin_amdgcn_exp2f(c.ea[E][q]); PIN(c.ea[E][q]); }
    if (u == 2) { c.ea[E][q] = __builtin_amdgcn_rcpf(__builtin_fmaf(c.ea[E][q], c.c2[mt], c.c2[mt])); PIN(c.ea[E][q]); }
    if (u == 3) { c.et[E][q] *= c.ea[E][q]; PIN(c.et[E][q]); if (q) { c.pk[E][e >> 1] = cvtpk(c.et[E][0], c.et[E][1]); PIN(c.pk[E][e >> 1]); } }
  } else if constexpr (U == 32) {
    auto r0 = __builtin_amdgcn_permlane16_swap(c.pk[E][0], c.pk[E][2], false, false);
    auto r1 = __builtin_amdgcn_permlane16_swap(c.pk[E][1], c.pk[E][3], false, false);
    c.pk[E][0] = r0[0]; c.pk[E][1] = r1[0]; c.pk[E][2] = r0[1]; c.pk[E][3] = r1[1];
  } else if constexpr (U == 33) {
    const int T = c.T0 + TL * c.dir;
    bst16<ACT_AUX>(u32x4{c.pk[E][0], c.pk[E][1], c.pk[E][2], c.pk[E][3]}, c.ars, c.avo, (T >> 1) * 1024 + (T & 1) * 512);
  }
}
constexpr int k4_tile(int F) { return F < 108 ? (F % 6) >> 1 : 3 + (F - 108) / 36; }
constexpr int k4_step(int F) { return F < 108 ? F / 6 : ((F - 108) % 36) >> 1; }
constexpr int k4_addr(int F) { return k4_step(F) * 2048 + (F & 1) * 1024; }
template <int F> DI void k4_frag(K4C& c) {
  constexpr int TL = k4_tile(F), S = k4_step(F), M = F & 1, N = F + K4_PF;
  const u32x4 a = c.wf[F % (K4_PF + 1)];
  if constexpr (F < 108) { if constexpr ((F + 4) % 3 == 0) __builtin_amdgcn_s_setprio(3 - ((F + 4) % 12) / 3); }
  else if constexpr ((F - 108 + 4) % 9 == 0) __builtin_amdgcn_s_setprio(3 - ((F - 108 + 4) % 36) / 9);
  if (S == 0) {
    G_MFMA0(c.acc[TL & 3][2 * M], a, c.X[0][S]);
    G_MFMA0(c.acc[TL & 3][2 * M + 1], a, c.X[1][S]);
  } else {
    constexpr int o = GRAY ? M : 0;
    G_MFMA(c.acc[TL & 3][2 * M + o], a, c.X[o][S]);
    if constexpr (K4_MID && F >= 108 && (F - 108) % 36 >= 2) {
      SBAR();
      k4_epi<(TL == 3 ? 0 : (TL == 4 ? 2 : TL - 1)), 0, (F - 108) % 36 - 2>(c);
      SBAR();
    }
    G_MFMA(c.acc[TL & 3][2 * M + 1 - o], a, c.X[1 - o][S]);
  }
  if constexpr (N < 108 && N % 12 == 0) {
    constexpr int Z = N / 12;
    if (Z < 8) asm volatile("s_waitcnt vmcnt(5)\n\ts_barrier" : "+v"(c.X[0][2 * Z]), "+v"(c.X[0][2 * Z + 1]), "+v"(c.X[1][2 * Z]), "+v"(c.X[1][2 * Z + 1])::"memory");
    else asm volatile("s_waitcnt vmcnt(2)\n\ts_barrier" : "+v"(c.X[0][2 * Z]), "+v"(c.X[0][2 * Z + 1]), "+v"(c.X[1][2 * Z]), "+v"(c.X[1][2 * Z + 1])::"memory");
  } else if constexpr (N >= 108 && N < 324 && (N - 108) % 36 == 0) {
    constexpr int NT = k4_tile(N);
    if (NT == 3) asm volatile("s_waitcnt vmcnt(0) lgkmcnt(0)\n\ts_barrier" ::: "memory");
    else if (NT == 8) {
      asm volatile("s_waitcnt vmcnt(0) lgkmcnt(0)\n\ts_barrier" ::: "memory");
      if (c.w == 0 && c.lane16 == 0) __hip_atomic_fetch_add(c.cntA, 1u, __ATOMIC_RELAXED, __HIP_MEMORY_SCOPE_AGENT);
    }
    else asm volatile("s_waitcnt vmcnt(4) lgkmcnt(0)\n\ts_barrier" ::: "memory");
  }
  if constexpr (N < 324) c.wf[N % (K4_PF + 1)] = ldsr16(c.wl[k4_tile(N) & 3] + k4_addr(N));
  if constexpr (F < 108) {
    constexpr int Z = F / 12, I = F % 12;
    if constexpr (Z == 0 && I < 4 && !(I & 1)) k4_issue1<2, I / 2>(c);
    if constexpr (Z < 7 && I < 8 && (I & 1)) k4_issue1<Z + 2, 2 + I / 2>(c);
    if constexpr (Z < 6 && I > 8 && (I & 1)) k4_issue1<Z + 3, (I - 9) / 2>(c);
    if constexpr (K4_WD == 0) {
      if constexpr (Z == 6 && I == 10) k4_issue2<3, 0>(c);
      if constexpr (Z == 7 && I < 8 && (I & 1)) k4_issue2<3, 1 + I / 2>(c);
    } else {
      if constexpr (Z == 6 && I == 10) k4_issue2<3, 2>(c);
      if constexpr (Z == 7 && (I == 1 || I == 3)) k4_issue2<3, 3 + I / 2>(c);
    }
  } else {
    constexpr int P = (F - 108) % 36;
    if constexpr (TL == 3) { if constexpr (P % 3 == 0 && P < 15) k4_issue2<4, P / 3>(c); if constexpr (P % 3 == 0 && P >= 15 && P < 30) k4_issue2<5, (P - 15) / 3>(c); }
    else if constexpr (TL + 2 < 9 && P % 4 == 0 && P < 20) k4_issue2<TL + 2, P / 4>(c);
    if constexpr (K4_PFWD && TL >= 4 && TL <= 7 && (P == 33 || (P == 34 && TL < 6))) {
      constexpr int tl = TL < 6 ? 2 * (TL - 4) + P - 33 : TL - 2;
      const int T = c.T0 + tl * c.dir;
      asm volatile("global_load_dword %0, %1, %2" : "+v"(c.pf0) : "v"(c.pfo), "s"(c.pfb + ((T >> 1) * 1024 + (T & 1) * 512)));
    }
    if constexpr (K4_PFW2 && TL >= 5 && TL <= 7 && P == 1)
      asm volatile("global_load_dword %0, %1, %2" : "+v"(c.pf0) : "v"(c.pfw), "s"(c.pfwb + (TL - 5) * (14 * 128)));
    if constexpr (TL == 7 && P == 0) CODE_PF2(c.pf1, k_ffn, k_end, 0, 2 + (c.w & 1), ((blockIdx.x >> 3) == 0 && c.w < 2) ? 128u : 0u);
    if constexpr (TL == 7 && P % 3 == 2 && P <= 11) k5_piece<0>(c.wd, P / 3, c.w, c.lane16);
    if constexpr (TL == 8 && P < 4) k5_piece<1>(c.wd, P, c.w, c.lane16);
    if constexpr (TL == 8 && P >= 4 && P < 8) k5_piece<2>(c.wd, P - 4, c.w, c.lane16);
    if constexpr (TL == 8 && P == K4_POLL) POLL(c.poll, c.cntA);
    if constexpr (TL == 8 && P == K4_POLL + 12) { asm volatile("s_waitcnt vmcnt(0)" : "+v"(c.poll), "+v"(c.pf1), "+v"(c.pf0)); wait_count(c.cntA, c.poll, 8u); }
    if constexpr (TL == 8 && P > K4_POLL + 12 && P <= K4_POLL + 20) {
      constexpr int i = P - K4_POLL - 13, z = i >> 2, j = (i & 1) * 2 + ((i >> 1) & 1);
      asm volatile("global_load_dwordx4 %0, %1, %2 sc1" : "=v"(c.A0[z][j]) : "v"(c.lane16), "s"(((j >> 1) ? c.ab2 : c.ab) + K5_PERM[2 * z + (j & 1)] * 1024));
    }
    if constexpr (P >= 2) {
      if constexpr (TL == 3) { if (!K4_MID) k4_epi<0, 0, P - 2>(c); k4_epi<1, 1, P - 2>(c); }
      else if constexpr (TL == 4) { if (!K4_MID) k4_epi<2, 0, P - 2>(c); k4_epi<3, 1, P - 2>(c); }
      else if (!K4_MID) k4_epi<TL - 1, 0, P - 2>(c);
    }
  }
  SBAR();
}
template <int B, int... Fs> DI void k4_run(K4C& c, std::integer_sequence<int, Fs...>) { (k4_frag<B + Fs>(c), ...); }
DI void k4_last(K4C& c) {
  float a[8], t[8];
#pragma unroll
  for (int e = 0; e < 8; ++e) { a[e] = c.acc[0][e >> 2][e & 3] * c.c1[e >> 2]; t[e] = a[e] * c.acc[0][2 + (e >> 2)][e & 3]; }
#pragma unroll
  for (int e = 0; e < 8; ++e) a[e] = __builtin_amdgcn_exp2f(a[e]);
#pragma unroll
  for (int e = 0; e < 8; ++e) a[e] = __builtin_amdgcn_rcpf(__builtin_fmaf(a[e], c.c2[e >> 2], c.c2[e >> 2]));
#pragma unroll
  for (int e = 0; e < 4; ++e) c.pk[0][e] = cvtpk(t[2 * e] * a[2 * e], t[2 * e + 1] * a[2 * e + 1]);
  k4_epi<8, 0, 32>(c); k4_epi<8, 0, 33>(c);
}
DI f32x4 addbf(f32x4 a, unsigned lo, unsigned hi) { return a + f32x4{bflo(lo), bfhi(lo), bflo(hi), bfhi(hi)}; }
struct K5C {
  const char *ab, *ab2;
  LDSP const char *ra[4], *oa[3], *xr;
  LDSP char* xw;
  u32x4 A[9][4], wf[5];
  f32x4 acc[2][7], own[2][2], v[4];
  const char* hb; unsigned ho; int kq; u32x2 hr[3];
  __amdgpu_buffer_rsrc_t wrs; unsigned tw, p8s;
  gu32* cnt;
};
template <int Z, int J> DI void k5_issue(K5C& c, const K4C& k) {
  if constexpr (Z < 7 && J < 4) asm volatile("global_load_dwordx4 %0, %1, %2 sc1"
 : "=v"(c.A[Z + 2][J]) : "v"(k.lane16), "s"(((J >> 1) ? c.ab2 : c.ab) + K5_PERM[2 * (Z + 2) + (J & 1)] * 1024));
  if constexpr (Z < 6 && J >= 4) k5_piece<Z + 3>(k.wd, J - 4, k.w, k.lane16);
  if constexpr (Z == 6 && J >= 4) k5_own<J - 4>(k.wd, k.w, k.lane16);
  if constexpr (Z == 7 && J == 0) k5_own<4>(k.wd, k.w, k.lane16);
  if constexpr (Z == 7 && J == 1) asm volatile("global_load_dwordx2 %0, %1, %2" : "=v"(c.hr[0]) : "v"(c.ho), "s"(c.hb + c.kq * 1024));
  if constexpr (Z == 7 && J == 2) asm volatile("global_load_dwordx2 %0, %1, %2 offset:512" : "=v"(c.hr[1]) : "v"(c.ho), "s"(c.hb + c.kq * 1024));
  if constexpr (Z == 7 && J == 3) asm volatile("global_load_dwordx2 %0, %1, %2" : "=v"(c.hr[2]) : "v"(c.ho), "s"(c.hb + 4096));
  if constexpr (Z == 8 && J < 4) k5_own<5 + J>(k.wd, k.w, k.lane16);
}
template <int N> DI u32x4 k5_read(const K5C& c) {
  if constexpr (N < 126) return ldsr16(c.ra[(N / 14) & 3] + (N % 14) * 1024);
  else { constexpr int o = K5_OC[(N - 126) / 4] + ((N - 126) & 3) * 1024; return ldsr16(c.oa[o >> 16] + (o & 0xffff)); }
}
template <int F> DI void k5_frag(K5C& c, K4C& k) {
  constexpr int N = F + 4;
  const u32x4 a = c.wf[F % 5];
  if constexpr (F < 126) {
    constexpr int Z = F / 14, f = F % 14, st = f / 7, j = f % 7;
    if (F < 7) { G_MFMA0(c.acc[0][j], a, c.A[Z][st]); G_MFMA0(c.acc[1][j], a, c.A[Z][2 + st]); }
    else { G_MFMA(c.acc[1][j], a, c.A[Z][2 + st]); G_MFMA(c.acc[0][j], a, c.A[Z][st]); }
    if constexpr (f == 10 && Z == 8) asm volatile("s_waitcnt vmcnt(4)\n\ts_barrier" : "+v"(c.hr[0]), "+v"(c.hr[1]), "+v"(c.hr[2])::"memory");
    if constexpr (f == 10 && Z < 8) {
      u32x4(&A)[4] = c.A[Z + 1];
      if (Z == 0) asm volatile("s_waitcnt vmcnt(9) lgkmcnt(0)\n\ts_barrier" : "+v"(A[0]), "+v"(A[1]), "+v"(A[2]), "+v"(A[3])::"memory");
      else if (Z == 3) asm volatile("s_waitcnt vmcnt(4) lgkmcnt(0)\n\ts_barrier" : "+v"(A[0]), "+v"(A[1]), "+v"(A[2]), "+v"(A[3]), "+v"(k.poll)::"memory");
      else if (Z < 7) asm volatile("s_waitcnt vmcnt(7) lgkmcnt(0)\n\ts_barrier" : "+v"(A[0]), "+v"(A[1]), "+v"(A[2]), "+v"(A[3])::"memory");
      else asm volatile("s_waitcnt vmcnt(8) lgkmcnt(0)\n\ts_barrier" : "+v"(A[0]), "+v"(A[1]), "+v"(A[2]), "+v"(A[3])::"memory");
      if (Z == 1 && k.w == 0 && k.lane16 == 0) __hip_atomic_fetch_add(k.cntB, 1u, __ATOMIC_RELAXED, __HIP_MEMORY_SCOPE_AGENT);
      if (Z == 3) wait_count(k.cntB, k.poll, 8u);
    }
    c.wf[N % 5] = k5_read<N>(c);
    if constexpr (Z == 3 && f == 1) POLL(k.poll, k.cntB);
    constexpr int slot = f == 0 ? 2 : (f == 2 ? 3 : (f == 4 ? 4 : (f == 5 ? 5 : (f == 7 ? 6 : (f == 9 ? 7 : -1)))));
    if constexpr (F == 0) { k5_issue<0, 0>(c, k); k5_issue<0, 1>(c, k); }
    if constexpr (slot >= 0) k5_issue<Z, slot>(c, k);
    if constexpr (Z < 8 && f > 10 && (f & 1)) k5_issue<Z + 1, (f - 11) / 2>(c, k);
  } else {
    constexpr int B = F - 126, z = B / 4, st = (B >> 1) & 1, t = B & 1;
    if (B < 2) { G_MFMA0(c.own[0][t], a, c.A[z][st]); G_MFMA0(c.own[1][t], a, c.A[z][2 + st]); }
    else { G_MFMA(c.own[1][t], a, c.A[z][2 + st]); G_MFMA(c.own[0][t], a, c.A[z][st]); }
    if constexpr (F == 131) asm volatile("s_waitcnt vmcnt(0) lgkmcnt(0)\n\ts_barrier" ::: "memory");
    if constexpr (F == K5_SIG) {
      asm volatile("s_waitcnt vmcnt(0)\n\ts_barrier" ::: "memory");
      if (k.w == 0 && k.lane16 == 0) __hip_atomic_fetch_add(c.cnt, 1u, __ATOMIC_RELAXED, __HIP_MEMORY_SCOPE_AGENT);
    }
    if constexpr (N < 162) c.wf[N % 5] = k5_read<N>(c);
  }
  if constexpr (F >= 123 && F < 130) { PIN(c.acc[1][F - 123]); *(LDSP f32x4*)(c.xw + (F - 123) * 1024) = c.acc[1][F - 123]; }
  if constexpr (F == 131) { _Pragma("unroll") for (int i = 0; i < 4; ++i) c.v[i] = *(LDSP const f32x4*)(c.xr + i * 1024); }
  if constexpr (F == 133 || F == 135 || F == 138) {
    constexpr int j = F == 133 ? 0 : (F == 135 ? 1 : 2), o = 2 * (j & 1);
    const f32x4 x = c.acc[0][2 * j] + c.v[o], y = c.acc[0][2 * j + 1] + c.v[o + 1];
    bst16<16>(u32x4{cvtpk(x[0], x[1]), cvtpk(x[2], x[3]), cvtpk(y[0], y[1]), cvtpk(y[2], y[3])}, c.wrs, k.lane16,
              (unsigned)WS_PART + ((((c.kq + j + 1) & 3) * 512 + c.tw) * 3 + j) * 1024);
    if constexpr (F == 135) { _Pragma("unroll") for (int i = 0; i < 3; ++i) c.v[i] = *(LDSP const f32x4*)(c.xr + (4 + i) * 1024); }
  }
  if constexpr (F == 140) {
    c.acc[0][6] += c.v[2];
    __builtin_amdgcn_raw_buffer_store_b64(u32x2{cvtpk(c.acc[0][6][0], c.acc[0][6][1]), cvtpk(c.acc[0][6][2], c.acc[0][6][3])}, c.wrs, c.p8s, c.kq * 512, 16);
    PIN(c.acc[0][6]);
  }
  SBAR();
}
template <int B, int... Fs> DI void k5_run(K5C& c, K4C& k, std::integer_sequence<int, Fs...>) { (k5_frag<B + Fs>(c, k), ...); }

extern "C" __global__ __launch_bounds__(512, 2) void k_ffn(char* out, char* ws) {
  extern __shared__ __attribute__((aligned(16))) char smem[];
  asm volatile("" : "+s"(out), "+s"(ws));
  LDSP char* lds = (LDSP char*)smem;
  const int tid = threadIdx.x, lane = tid & 63, w = rfl(tid >> 6);
  const int xcd = blockIdx.x & 7, bi = blockIdx.x >> 3;
  const int m = lane & 15, g = lane >> 4, kq = xcd >> 1;
  STAMP_DECL; STAMP(0);
  K4C c;
  {
    const int rb = 4 * (xcd & 1) + (bi & 3), fs = 8 * kq + (bi >> 2), mt = 16 * rb + 2 * w;
    c.lds0 = (unsigned)(uintptr_t)lds; c.lane16 = lane * 16; c.w = w;
    c.T0 = 9 * fs + 8 * (fs & 1); c.dir = 1 - 2 * (fs & 1);
    c.wb = ws + WS_W1 + (size_t)c.T0 * K4_SLOT;
    c.xb = ws + WS_HC + (size_t)mt * 18432;
    if (K4_WD) { k4_issue_w<0>(c, 0); k4_issue_w<0>(c, 1); }
    k4_issue1<0, 0>(c); k4_issue1<0, 1>(c); k4_issue1<0, 2>(c); k4_issue1<0, 3>(c); k4_issue1<0, 4>(c); k4_issue1<0, 5>(c);
    unsigned sq[8];
#pragma unroll
    for (int i = 0; i < 8; ++i) sq[i] = gld4(ws + WS_SSQ + (i >> 1) * 8192 + (i & 1) * 64, 4 * g * 8192 + (16 * mt + m) * 4);
    k4_issue1<1, 0>(c); k4_issue1<1, 1>(c); k4_issue1<1, 2>(c); k4_issue1<1, 3>(c); k4_issue1<1, 4>(c); k4_issue1<1, 5>(c);
    SBAR();
#pragma unroll
    for (int i = 0; i < 4; ++i) { c.wl[i] = lds + c.lane16 + i * K4_SLOT; PIN(c.wl[i]); }
    c.ars = mkrsrc(ws + WS_ACT, 128u * 144 * 1024);
    c.avo = (mt + (g & 1)) * (144 * 1024) + (g >> 1) * 256 + m * 16;
    c.wd.wb = ws + WS_WD + ((size_t)(bi >> 3) * 144 + 36 * kq) * 9 * 1024;
#pragma unroll
    for (int i = 0; i < 4; ++i) {
      const int p = i < 3 ? 8 * i + w : 24 + (w & 3), st = (p * 37) >> 8, t = p - 7 * st + 2;
      c.wd.q[i] = ((st >> 1) * 162 + (t < 8 ? (t + 2 * kq) & 7 : 8)) * 1024;
      c.wd.qs[i] = st & 1;
      asm volatile("" : "+s"(c.wd.q[i]), "+s"(c.wd.qs[i]));
    }
    c.wd.qo = ((w >> 2) * 162 + 2 * kq + (w & 1)) * 1024; c.wd.qos = (w >> 1) & 1; c.wd.lds0 = c.lds0;
    if (K4_PFWD) {
      c.pfb = ws + WS_ACT;
      c.pfo = (mt + ((lane >> 5) & 1)) * (144 * 1024) + ((lane >> 4) & 1) * 256 + ((lane >> 3) & 1) * 128;
      PIN(c.pfo);
    }
    if (K4_PFW2) {
      c.pfwb = ws + WS_WD + (size_t)kq * 331776 + (size_t)(bi >> 3) * 1327104;
      c.pfw = min(((bi & 7) * 8 + w) * 42 + (lane < 14 ? lane : 13), 2592 - 29) * 128; PIN(c.pfw);
    }
    c.pf0 = 0;
    c.cntA = (gu32*)(ws + WS_CNT + (64 + rb * 4 + kq) * 128);
    STAMP(1);
    asm volatile("s_waitcnt vmcnt(5)\n\ts_barrier" : "+v"(sq[0]), "+v"(sq[1]), "+v"(sq[2]), "+v"(sq[3]), "+v"(sq[4]), "+v"(sq[5]), "+v"(sq[6]), "+v"(sq[7]),
                 "+v"(c.X[0][0]), "+v"(c.X[0][1]), "+v"(c.X[1][0]), "+v"(c.X[1][1])::"memory");
#pragma unroll
    for (int i = 0; i < K4_PF; ++i) c.wf[i] = ldsr16(c.wl[k4_tile(i)] + k4_addr(i));
#pragma unroll
    for (int t = 0; t < 2; ++t) {
      float ss = (__uint_as_float(sq[t]) + __uint_as_float(sq[2 + t])) + (__uint_as_float(sq[4 + t]) + __uint_as_float(sq[6 + t]));
      ss = xor32_sum(xor16_sum(ss));
      const float v = ss * (1.0f / DM) + LN_EPS, rs = __builtin_amdgcn_rsqf(v);
      c.c1[t] = -LOG2E * rs; c.c2[t] = -LOG2E * (v * rs);
      PIN(c.c1[t]); PIN(c.c2[t]);
      if (K4_PK) { c.c1p[t] = f32x2{c.c1[t], c.c1[t]}; c.c2p[t] = f32x2{c.c2[t], c.c2[t]}; PIN(c.c1p[t]); PIN(c.c2p[t]); }
    }
    SBAR();
    k4_run<0>(c, std::make_integer_sequence<int, 288>{});
    c.cntB = c.cntA + 32 * 32;
  }
  const int rb = 8 * (xcd & 1) + (bi & 7), nb = bi >> 3, mg = w & 3, kh = w >> 2, mu = 8 * rb + 2 * mg + kh;
  gu32* prodB = c.cntB;
  c.cntA = (gu32*)(ws + WS_CNT + (64 + (rb >> 1) * 4 + kq) * 128);
  c.ab = ws + WS_ACT + ((size_t)mu * 144 + 36 * kq + 18 * kh) * 1024;
  c.ab2 = ws + WS_ACT + ((size_t)(mu ^ 1) * 144 + 36 * kq + 18 * kh) * 1024;
  k4_run<288>(c, std::make_integer_sequence<int, 36>{});
  STAMP(2);
  K5C d;
  d.ab = c.ab; d.ab2 = c.ab2;
  asm volatile("s_nop 7" : "+v"(c.acc[0][0]), "+v"(c.acc[0][1]), "+v"(c.acc[0][2]), "+v"(c.acc[0][3]));
  k4_last(c);
  SBAR();
  gu32* consB = c.cntA + 32 * 32;
  d.hb = ws + WS_HU + (size_t)mu * 18432 + nb * 4608; d.ho = (g >> 1) * 256 + m * 16 + (g & 1) * 8; d.kq = kq;
#pragma unroll
  for (int i = 0; i < 4; ++i) { d.ra[i] = lds + K5_R[i] + kh * 14336 + c.lane16; PIN(d.ra[i]); }
#pragma unroll
  for (int i = 0; i < 3; ++i) { d.oa[i] = lds + i * 65536 + kh * 4096 + c.lane16; PIN(d.oa[i]); }
  d.xw = lds + (kh ? K5_R[2] : K5_R[1]) + mg * 7168 + c.lane16; PIN(d.xw);
  d.xr = lds + (kh ? K5_R[1] : K5_R[2]) + mg * 7168 + c.lane16; PIN(d.xr);
  const __amdgpu_buffer_rsrc_t wrs = mkrsrc(ws, (unsigned)WS_END), ors = mkrsrc(out, SEQ * DM * 2);
  const int tile = rb * 4 + nb;
  const bool own8 = (mu & 3) == kq;
  const unsigned tw = tile * 8 + w;
  const unsigned p8 = (unsigned)WS_PART8 + tw * 2048 + lane * 8;
  d.cnt = (gu32*)(ws + WS_CNT + tile * 128);
  d.wrs = wrs; d.tw = tw; d.p8s = own8 ? 0x80000000u : p8; PIN(d.p8s);
#pragma unroll
  for (int i = 0; i < 4; ++i) { d.A[0][i] = c.A0[0][i]; d.A[1][i] = c.A0[1][i]; }
  asm volatile("s_waitcnt vmcnt(6) lgkmcnt(0)\n\ts_barrier" : "+v"(d.A[0][0]), "+v"(d.A[0][1]), "+v"(d.A[0][2]), "+v"(d.A[0][3])::"memory");
  STAMP(3);
  d.wf[0] = k5_read<0>(d); d.wf[1] = k5_read<1>(d); d.wf[2] = k5_read<2>(d); d.wf[3] = k5_read<3>(d);
  SBAR();
  c.cntB = prodB;
  k5_run<0>(d, c, std::make_integer_sequence<int, 28>{});
  c.cntB = consB;
  k5_run<28>(d, c, std::make_integer_sequence<int, 134>{});
  asm volatile("s_nop 7" : "+v"(d.own[0][0]), "+v"(d.own[0][1]), "+v"(d.own[1][0]), "+v"(d.own[1][1]));
  STAMP(4);
  LDSP char* xo = lds + K5_R[0] + c.lane16;
  *(LDSP f32x4*)(xo + w * 2048) = d.own[1][0];
  *(LDSP f32x4*)(xo + w * 2048 + 1024) = d.own[1][1];
  unsigned oo = (16 * mu + m) * 1152 + nb * 288;
  unsigned oo8 = own8 ? oo + 256 + g * 8 : 0x80000000u;
  oo += kq * 64 + (g & 1) * 32 + (g >> 1) * 16;
  PIN(oo); PIN(oo8);
  STAMP(5);
  if (w == 0) wait_count(d.cnt, 0u, 4u);
  asm volatile("s_waitcnt lgkmcnt(0)\n\ts_barrier" ::: "memory");
  STAMP(6);
  u32x4 r[3]; u32x2 r8[3];
#pragma unroll
  for (int j = 0; j < 3; ++j) {
    r[j] = __builtin_amdgcn_raw_buffer_load_b128(wrs, c.lane16, (unsigned)WS_PART + ((kq * 512 + tw) * 3 + j) * 1024, 16);
    r8[j] = __builtin_amdgcn_raw_buffer_load_b64(wrs, own8 ? p8 : 0x80000000u, ((kq + 1 + j) & 3) * 512, 16);
  }
  SBAR();
  f32x4 e0 = addbf(d.own[0][0] + *(LDSP const f32x4*)(xo + (w ^ 4) * 2048), d.hr[0][0], d.hr[0][1]);
  f32x4 e1 = addbf(d.own[0][1] + *(LDSP const f32x4*)(xo + (w ^ 4) * 2048 + 1024), d.hr[1][0], d.hr[1][1]);
  f32x4 e8 = addbf(d.acc[0][6], d.hr[2][0], d.hr[2][1]);
#pragma unroll
  for (int j = 0; j < 3; ++j) { e0 = addbf(e0, r[j][0], r[j][1]); e1 = addbf(e1, r[j][2], r[j][3]); e8 = addbf(e8, r8[j][0], r8[j][1]); }
  {
    const auto s0 = __builtin_amdgcn_permlane16_swap(cvtpk(e0[0], e0[1]), cvtpk(e1[0], e1[1]), false, false);
    const auto s1 = __builtin_amdgcn_permlane16_swap(cvtpk(e0[2], e0[3]), cvtpk(e1[2], e1[3]), false, false);
    bst16<OUT_AUX>(u32x4{s0[0], s1[0], s0[1], s1[1]}, ors, oo, 0);
    __builtin_amdgcn_raw_buffer_store_b64(u32x2{cvtpk(e8[0], e8[1]), cvtpk(e8[2], e8[3])}, ors, oo8, 0, OUT_AUX);
  }
  STAMP(7);
  STAMP_OUT(4, 8);
}
extern "C" __global__ void k_end() {}

extern "C" void kernel_launch(void* const* d_in, const int* in_sizes, int n_in, void* d_out, int out_size, void* d_ws, size_t ws_size,
                              hipStream_t stream) {
  const u16* x = (const u16*)d_in[0];
  const u16* wqkv = (const u16*)d_in[1];
  const u16* wo = (const u16*)d_in[2];
  const u16* wg = (const u16*)d_in[4];
  const u16* wu = (const u16*)d_in[5];
  const u16* wd = (const u16*)d_in[6];
  char* ws = (char*)d_ws;
  (void)hipFuncSetAttribute((const void*)k_qkv, hipFuncAttributeMaxDynamicSharedMemorySize, K1_LDS);
  (void)hipFuncSetAttribute((const void*)k_attn, hipFuncAttributeMaxDynamicSharedMemorySize, K2_LDS);
  (void)hipFuncSetAttribute((const void*)k_ffn, hipFuncAttributeMaxDynamicSharedMemorySize, K4_LDS);
  k_qkv<<<256, 768, K1_LDS, stream>>>(x, wqkv, wo, ws);
  k_attn<<<256, 768, K2_LDS, stream>>>(x, wg, wu, wd, ws);
  k_ffn<<<256, 512, K4_LDS, stream>>>((char*)d_out, ws);
}
```

```cpp
#define SEL 3
#include <hip/hip_runtime.h>
#include <hip/hip_bf16.h>
#include <stdint.h>
#include <utility>
#include <type_traits>

typedef __bf16 bf16x8 __attribute__((ext_vector_type(8)));
typedef __bf16 bf16x2 __attribute__((ext_vector_type(2)));
typedef short s16x4 __attribute__((ext_vector_type(4)));
typedef float f32x16 __attribute__((ext_vector_type(16)));
typedef float f32x4 __attribute__((ext_vector_type(4)));
typedef float f32x2 __attribute__((ext_vector_type(2)));
typedef unsigned u32x4 __attribute__((ext_vector_type(4)));
typedef unsigned u32x2 __attribute__((ext_vector_type(2)));
typedef unsigned short u16;
#define LDSP __attribute__((address_space(3)))
#define DI __device__ __forceinline__
#define SBAR() __builtin_amdgcn_sched_barrier(0)

constexpr int SEQ = 2048, DM = 576, NH = 32, DH = 18, FF = 4608;
constexpr float LOG2E = 1.4426950408889634f;
constexpr float QSCALE = 0.23570226039551584f * LOG2E;
constexpr float LN_EPS = 1e-5f;

constexpr size_t CHUNK_B = 37888;
constexpr size_t KV_HEAD_B = 4 * CHUNK_B;
constexpr size_t WS_QM = 0;
constexpr size_t WS_QT = WS_QM + (size_t)NH * 65536;
constexpr size_t WS_KV = WS_QT + (size_t)NH * 8192;
constexpr size_t WS_KMAX = WS_KV + (size_t)NH * KV_HEAD_B;
constexpr size_t WS_XMEAN = WS_KMAX + 65536;
constexpr size_t WS_OFR = WS_XMEAN + 8192;
constexpr size_t WS_WO = WS_OFR + (size_t)128 * 18 * 1024;
constexpr size_t WS_WBAR = WS_WO + (size_t)12 * 54 * 1024;
constexpr size_t WS_HC = WS_WBAR + 3072;
constexpr size_t WS_SSQ = WS_HC + (size_t)64 * 36 * 1024;
constexpr size_t WS_W1 = WS_SSQ + (size_t)16 * 8192;
constexpr size_t WS_WD = WS_W1 + (size_t)288 * 36 * 1024;
constexpr size_t WS_ACT = WS_WD + (size_t)4 * 144 * 9 * 1024;
constexpr size_t UNIT_B = (size_t)128 * 18432;
constexpr size_t WS_PART = WS_ACT + (size_t)128 * 144 * 1024;
constexpr size_t WS_PART8 = WS_PART + (size_t)4 * 64 * 8 * 3 * 1024;
constexpr size_t WS_CNT = WS_PART8 + (size_t)64 * 8 * 4 * 512;
constexpr size_t WS_HU = WS_CNT + 40960;
constexpr size_t WS_END = WS_HU + UNIT_B;

DI unsigned cvtpk(float lo, float hi) {
  f32x2 v = {lo, hi};
  bf16x2 b = __builtin_convertvector(v, bf16x2);
  return __builtin_bit_cast(unsigned, b);
}
DI float bflo(unsigned u) { return __uint_as_float(u << 16); }
DI float bfhi(unsigned u) { return __uint_as_float(u & 0xffff0000u); }
DI float dot2(unsigned a, unsigned b, float c) {
  return __builtin_amdgcn_fdot2_f32_bf16(__builtin_bit_cast(bf16x2, a), __builtin_bit_cast(bf16x2, b), c, false);
}
DI f32x16 mfma32(u32x4 a, u32x4 b, f32x16 c) {
  return __builtin_amdgcn_mfma_f32_32x32x16_bf16(__builtin_bit_cast(bf16x8, a), __builtin_bit_cast(bf16x8, b), c, 0, 0, 0);
}
DI f32x4 mfma16(u32x4 a, u32x4 b, f32x4 c) {
  return __builtin_amdgcn_mfma_f32_16x16x32_bf16(__builtin_bit_cast(bf16x8, a), __builtin_bit_cast(bf16x8, b), c, 0, 0, 0);
}
DI unsigned rfl(unsigned x) { return __builtin_amdgcn_readfirstlane(x); }
DI const char* uniptr(const char* p) {
  uint64_t v = (uint64_t)p;
  unsigned lo = rfl((unsigned)v), hi = rfl((unsigned)(v >> 32));
  return (const char*)(((uint64_t)hi << 32) | lo);
}
DI void dma16(const char* sbase, unsigned voff, unsigned lds) {
  asm volatile("s_mov_b32 m0, %2\n\ts_nop 0\n\tglobal_load_lds_dwordx4 %0, %1" ::"v"(voff), "s"(sbase), "s"(lds));
}
DI void dma16nt(const char* sbase, unsigned voff, unsigned lds) {
  asm volatile("s_mov_b32 m0, %2\n\ts_nop 0\n\tglobal_load_lds_dwordx4 %0, %1 nt" ::"v"(voff), "s"(sbase), "s"(lds));
}
DI u32x4 gld16(const char* sbase, unsigned voff) {
  u32x4 r;
  asm volatile("global_load_dwordx4 %0, %1, %2" : "=v"(r) : "v"(voff), "s"(sbase));
  return r;
}
DI unsigned gld4(const char* sbase, unsigned voff) {
  unsigned r;
  asm volatile("global_load_dword %0, %1, %2" : "=v"(r) : "v"(voff), "s"(sbase));
  return r;
}
#define WAITV(n) asm volatile("s_waitcnt vmcnt(" #n ")" ::: "memory")
#define BARRIER() asm volatile("s_barrier" ::: "memory")
#define PIN(x) asm volatile("" : "+v"(x))
DI u32x4 ldsr16(LDSP const char* p) { return *(LDSP const u32x4*)p; }
DI float xor32_sum(float v) {
  auto r = __builtin_amdgcn_permlane32_swap(__float_as_uint(v), __float_as_uint(v), false, false);
  return __uint_as_float(r[0]) + __uint_as_float(r[1]);
}
DI float xor32_max(float v) {
  auto r = __builtin_amdgcn_permlane32_swap(__float_as_uint(v), __float_as_uint(v), false, false);
  return fmaxf(__uint_as_float(r[0]), __uint_as_float(r[1]));
}
DI float xor16_sum(float v) {
  auto r = __builtin_amdgcn_permlane16_swap(__float_as_uint(v), __float_as_uint(v), false, false);
  return __uint_as_float(r[0]) + __uint_as_float(r[1]);
}

#define STAMP_DECL
#define STAMP(i)
#define STAMP_OUT(kid, n)

DI int wo_row(int step, int g, int j) {
  if (step < 16) return 18 * (2 * step + (g >> 1)) + 4 * (g & 1) + (j & 3) + 8 * (j >> 2);
  return 18 * (4 * (4 * (step - 16) + g) + (j >> 1)) + 16 + (j & 1);
}

#define K4_SPREAD 1
#define K4_PM 1
#define K4_WD 1
#define K3_PF 0
#define K3_SLEEP 32
#define K1_ROT 0
#define AT_PM 9
#define HELP_PRIO 3
#define HELP_EARLY 0
#define PREP_ORD 1
#define GRAY 1
#define K4_POLL 10
#define K4_PK 0
#define K5_ORD 1
#define K5_SIG 154
#define K4_MID 0
#define AT_HFIRST 0
#define K4_PFW2 0
#define K3_REP 16
#define HELP_MID 1
#define K4_NT 0
#define K4_ROT 0
#define K5_FLAG 0
#define K5_SPREAD 1
#define K5_PM 0
#define OUT_AUX 16
#define K4_PFWD 0
#define QKV_AUX 16
#define QKT_AUX 16
#define V_AUX 16
#define O_AUX 16
#define H_AUX 16
#define PART_AUX 16
#define ACT_AUX 16
#define PREP_AUX 16
#define CODE_PF(dst, first, last, extra, blk) CODE_PF2(dst, first, last, extra, blk, 128u)
#define CODE_PF2(dst, first, last, extra, blk, stride)                                                                              \
  do {                                                                                                                     \
    uint64_t pc_; int b_, e_;                                                                                              \
    asm volatile("s_getpc_b64 %0\n.Lpf%=:\n\ts_mov_b32 %1, " #first "-.Lpf%=\n\ts_mov_b32 %2, " #last "-.Lpf%=" : "=s"(pc_), "=s"(b_), "=s"(e_)); \
    const unsigned o_ = (blk) * 8192u + (threadIdx.x & 63) * (stride), n_ = (unsigned)(e_ - b_) + (extra) - 4u;                \
    dst = gld4((const char*)(pc_ + b_), o_ < n_ ? o_ : n_);                                                                \
  } while (0)
#define G_MFMA(d, a, b) asm volatile("v_mfma_f32_16x16x32_bf16 %0, %1, %2, %0" : "+v"(d) : "v"(a), "v"(b))
#define G_MFMA0(d, a, b) asm volatile("v_mfma_f32_16x16x32_bf16 %0, %1, %2, 0" : "=&v"(d) : "v"(a), "v"(b))
template <int AUX> DI void bst16(u32x4 v, __amdgpu_buffer_rsrc_t rs, unsigned voff, unsigned soff) {
  __builtin_amdgcn_raw_buffer_store_b128(v, rs, voff, soff, AUX);
  asm volatile("s_nop 1" ::"v"(v) : "memory");
}
DI __amdgpu_buffer_rsrc_t mkrsrc(const void* p, unsigned bytes) {
  return __builtin_amdgcn_make_buffer_rsrc((void*)p, 0, bytes, 0x00020000);
}

typedef __attribute__((address_space(1))) unsigned gu32;
template <int SLEEP = 1> DI void wait_count(gu32* cnt, unsigned first, unsigned want) {
  if (rfl(first) >= want) return;
  for (unsigned spin = 0; spin < (1u << 16); ++spin) {
    if (rfl(__hip_atomic_load(cnt, __ATOMIC_RELAXED, __HIP_MEMORY_SCOPE_AGENT)) >= want) break;
    __builtin_amdgcn_s_sleep(SLEEP);
  }
}
constexpr int NJ_W1 = 72 * 36, NJ_WD = 9 * 144, NJ_WO = 9 * 18, NJ_ALL = NJ_W1 + NJ_WD + NJ_WO;
struct PrepJob { const char* src; unsigned rs, c1, c2, voff, dst[4]; };
DI void prep_setup(PrepJob& j, int J, int lane, const u16* wo, const u16* wg, const u16* wu, const u16* wd) {
  if (J >= NJ_ALL) J = NJ_ALL - 1;
  const int g = (lane >> 2) & 3;
  unsigned lrow = 8 * g;
  j.c1 = 0; j.c2 = 0;
  if (J < NJ_W1) {
    const int sm = J / 72, tg = J - sm * 72;
    j.src = (const char*)((sm & 1) ? wu : wg) + (size_t)(sm >> 1) * (32 * FF * 2) + tg * 128;
    j.rs = FF * 2;
#pragma unroll
    for (int t = 0; t < 4; ++t) j.dst[t] = (unsigned)WS_W1 + (4 * tg + t) * 36864 + sm * 1024;
  } else if (J < NJ_W1 + NJ_WD) {
    const int q = J - NJ_W1, cc = q / 9, tg = q - cc * 9;
    j.src = (const char*)wd + (size_t)cc * (32 * DM * 2) + tg * 128;
    j.rs = DM * 2;
#pragma unroll
    for (int t = 0; t < 4; ++t) { const int nt = 4 * tg + t, nb = nt / 9; j.dst[t] = (unsigned)WS_WD + ((nb * 144 + cc) * 9 + (nt - nb * 9)) * 1024; }
  } else {
    const int q = J - NJ_W1 - NJ_WD, step = q / 9, tg = q - step * 9;
    j.rs = DM * 2;
    if (step < 16) { j.src = (const char*)wo + (size_t)step * (36 * DM * 2) + tg * 128; lrow = 18 * (g >> 1) + 4 * (g & 1); j.c2 = 4; }
    else { j.src = (const char*)wo + (size_t)(288 * (step - 16) + 16) * (DM * 2) + tg * 128; lrow = 72 * g; j.c1 = 16; }
#pragma unroll
    for (int t = 0; t < 4; ++t) j.dst[t] = (unsigned)WS_WO + (step * 36 + 4 * tg + t) * 1024;
  }
#pragma unroll
  for (int t = 0; t < 4; ++t) j.dst[t] = rfl(j.dst[t]);
  j.voff = lrow * j.rs + (lane >> 4) * 32 + (lane & 3) * 8;
}
template <int JJ>
DI void prep_load(const PrepJob& j, u32x2 (&r)[8]) {
  const char* b = j.src + (JJ + j.c1 * (JJ >> 1) + j.c2 * (JJ >> 2)) * j.rs;
  asm volatile("global_load_dwordx2 %0, %1, %2" : "=v"(r[JJ]) : "v"(j.voff), "s"(b));
}
DI void prep_load_all(const PrepJob& j, u32x2 (&r)[8]) {
  prep_load<0>(j, r); prep_load<1>(j, r); prep_load<2>(j, r); prep_load<3>(j, r); prep_load<4>(j, r); prep_load<5>(j, r); prep_load<6>(j, r); prep_load<7>(j, r);
}
DI void prep_store(const PrepJob& j, const u32x2 (&r)[8], __amdgpu_buffer_rsrc_t rs, unsigned so) {
  u32x4 u[4];
#pragma unroll
  for (int i = 0; i < 4; ++i) {
    u[0][i] = __builtin_amdgcn_perm(r[2 * i + 1][0], r[2 * i][0], 0x05040100u);
    u[1][i] = __builtin_amdgcn_perm(r[2 * i + 1][0], r[2 * i][0], 0x07060302u);
    u[2][i] = __builtin_amdgcn_perm(r[2 * i + 1][1], r[2 * i][1], 0x05040100u);
    u[3][i] = __builtin_amdgcn_perm(r[2 * i + 1][1], r[2 * i][1], 0x07060302u);
  }
#pragma unroll
  for (int i = 0; i < 4; ++i) {
    const auto a0 = __builtin_amdgcn_permlane32_swap(u[0][i], u[2][i], false, false);
    const auto a1 = __builtin_amdgcn_permlane32_swap(u[1][i], u[3][i], false, false);
    const auto b0 = __builtin_amdgcn_permlane16_swap(a0[0], a1[0], false, false);
    const auto b1 = __builtin_amdgcn_permlane16_swap(a0[1], a1[1], false, false);
    u[0][i] = b0[0]; u[1][i] = b0[1]; u[2][i] = b1[0]; u[3][i] = b1[1];
  }
#pragma unroll
  for (int t = 0; t < 4; ++t) bst16<PREP_AUX>(u[t], rs, so, j.dst[t]);
}
#define PIN8(r) "+v"(r[0]), "+v"(r[1]), "+v"(r[2]), "+v"(r[3]), "+v"(r[4]), "+v"(r[5]), "+v"(r[6]), "+v"(r[7])

#define K1_PF 4
#define K1_TOUCH 1
#define K1_TSLEEP 16
#define K1_DEPTH 2
constexpr int K1_PITCH = 224, K1_SLOT = 16384 + 64 * K1_PITCH  , K1_NS = K1_DEPTH + 1;
constexpr int K1_SP = 232;
constexpr int K1_STG = K1_NS * K1_SLOT, K1_CS = K1_STG + 128 * K1_SP + 64, K1_ST = K1_CS + 512 + 64, K1_LDS = K1_ST + 2048;
struct K1C {
  LDSP const char *xa[2], *wl, *wc0, *wc1;
  u32x4 X[3][2], wf[K1_PF + 1], ones;
  f32x4 acc[4][2];
  f32x4 sm[2]; float sq[2];
};
DI void k1_loader(const char* xb, const char* wb, const u16* wo_, char* ws, int j, int lane, unsigned lds0) {
  __builtin_amdgcn_s_setprio(3);
  unsigned xo[2], wo[4];
  const int g = lane >> 4;
  xo[0] = (lane >> 3) * 1152 + (((lane & 7) ^ g) * 16);
  xo[1] = (8 + (lane >> 3)) * 1152 + (((lane & 7) ^ (4 + g)) * 16);
  xb += j * (32 * 1152);
  const unsigned xd = lds0 + j * 4096, wd = lds0 + 16384 + j * 1024;
#define K1_X(z, so) do { dma16(xb + (z) * 128, xo[0], xd + (so)); dma16(xb + (z) * 128, xo[1], xd + (so) + 1024);                    \
    dma16(xb + (z) * 128 + 16 * 1152, xo[0], xd + (so) + 2048); dma16(xb + (z) * 128 + 16 * 1152, xo[1], xd + (so) + 3072); } while (0)
#define K1_W(z, so) do { _Pragma("unroll") for (int i = 0; i < 3; ++i) dma16(wb + (z) * (64 * 3456), wo[i], wd + (so) + i * 4096);     \
    if (j < 2) dma16(wb + (z) * (64 * 3456), wo[3], wd + (so) + 3 * 4096); } while (0)
  K1_X(0, 0);
  SBAR();
#pragma unroll
  for (int i = 0; i < 4; ++i) {
    const unsigned u = 64 * (j + 4 * i) + lane, r = u / 14;
    wo[i] = ((r & ~12u) | ((r & 4u) << 1) | ((r & 8u) >> 1)) * 3456 + (u - r * 14) * 16;
  }
  K1_W(0, 0);
#pragma unroll
  for (int z = 1; z < K1_DEPTH; ++z) { K1_X(z, z * K1_SLOT); K1_W(z, z * K1_SLOT); }
  unsigned so = K1_DEPTH * K1_SLOT;
#pragma unroll 1
  for (int z = K1_DEPTH; z < 9; ++z) {
    asm volatile("s_waitcnt vmcnt(%0)\n\ts_barrier" ::"i"(7 * (K1_DEPTH - 1)) : "memory");
    K1_X(z, so); K1_W(z, so);
    so = so == (K1_NS - 1) * K1_SLOT ? 0 : so + K1_SLOT;
  }
  if (K1_DEPTH > 2) asm volatile("s_waitcnt vmcnt(14)\n\ts_barrier" ::: "memory");
  asm volatile("s_waitcnt vmcnt(7)\n\ts_barrier" ::: "memory");
  const int b = blockIdx.x;
  const bool dojob = j == 1 && b < NJ_WO, dorow = j >= 2 || (j == 0 && b < 64);
  const int br = j >= 2 ? 2 * b + j - 2 : 512 + (b & 63);
  PrepJob jb; u32x2 r[8]; unsigned wr[5];
  if (dojob) prep_setup(jb, NJ_W1 + NJ_WD + b, lane, wo_, wo_, wo_, wo_);
  const char* rp = (const char*)wo_ + (size_t)wo_row(br >> 5, (br >> 3) & 3, br & 7) * (DM * 2);
  SBAR();
  asm volatile("s_waitcnt vmcnt(0)\n\ts_barrier" ::: "memory");
  if (dojob) prep_load_all(jb, r);
  if (dorow) {
#pragma unroll
    for (int i = 0; i < 5; ++i) wr[i] = gld4(rp, (i < 4 ? lane + 64 * i : 256 + (lane & 31)) * 4);
  }
  asm volatile("s_barrier");
  asm volatile("s_barrier");
  const __amdgpu_buffer_rsrc_t wrs = mkrsrc(ws, (unsigned)WS_END);
  asm volatile("s_waitcnt vmcnt(0)" : PIN8(r), "+v"(wr[0]), "+v"(wr[1]), "+v"(wr[2]), "+v"(wr[3]), "+v"(wr[4])::"memory");
  if (dojob) {
#pragma unroll
    for (int t = 0; t < 4; ++t) jb.dst[t] = rfl(jb.dst[t]);
    prep_store(jb, r, wrs, ((lane >> 2) & 3) * 256 + ((lane & 3) * 4 + (lane >> 4)) * 16);
  }
  if (dorow) {
    float a = 0.f;
#pragma unroll
    for (int i = 0; i < 4; ++i) a = dot2(wr[i], 0x3f803f80u, a);
    a = dot2(lane >= 32 ? 0u : wr[4], 0x3f803f80u, a);
    a = xor16_sum(xor32_sum(a));
    a += __uint_as_float(__builtin_amdgcn_update_dpp(0u, __float_as_uint(a), 0x128, 0xf, 0xf, false));
    a += __uint_as_float(__builtin_amdgcn_update_dpp(0u, __float_as_uint(a), 0x124, 0xf, 0xf, false));
    a += __uint_as_float(__builtin_amdgcn_update_dpp(0u, __float_as_uint(a), 0x122, 0xf, 0xf, false));
    a += __uint_as_float(__builtin_amdgcn_update_dpp(0u, __float_as_uint(a), 0x121, 0xf, 0xf, false));
    a *= (1.0f / DM);
    const unsigned hi = cvtpk(a, 0.f) & 0xffffu;
    const unsigned lo = cvtpk(a - bflo(hi), 0.f) & 0xffffu;
    __builtin_amdgcn_raw_buffer_store_b16((u16)(lane ? lo : hi), wrs, lane < 2 ? (unsigned)WS_WBAR + (lane * DM + br) * 2 : 0x80000000u, 0, 16);
  }
#undef K1_X
#undef K1_W
}
DI u32x4 tr_frag(LDSP const char* p, int d) {
  s16x4 lo = __builtin_amdgcn_ds_read_tr16_b64_v4i16((LDSP s16x4*)p);
  s16x4 hi = __builtin_amdgcn_ds_read_tr16_b64_v4i16((LDSP s16x4*)(p + d));
  return __builtin_bit_cast(u32x4, __builtin_shufflevector(lo, hi, 0, 1, 2, 3, 4, 5, 6, 7));
}
template <int NHG, int F> DI void k1_read(K1C& c) {
  constexpr int NF = 4 + NHG, S = F / NF, j = F % NF, Z = S >> 1, so = (Z % K1_NS) * K1_SLOT;
  if constexpr (j == 0) {
    c.X[S % 3][0] = ldsr16(c.xa[S & 1] + so);
    c.X[S % 3][1] = ldsr16(c.xa[S & 1] + so + 2048);
  }
  constexpr int wo = so + (S & 1) * 7168;
  if constexpr (NHG == 0 || j < 3) c.wf[F % (K1_PF + 1)] = tr_frag(c.wl + wo + j * 32, 1792);
  else c.wf[F % (K1_PF + 1)] = tr_frag((j == 3 ? c.wc0 : c.wc1) + wo, 1792);
}
template <int NHG, int F> DI void k1_frag(K1C& c) {
  constexpr int NF = 4 + NHG, S = F / NF, j = F % NF, Z = S >> 1, N = F + K1_PF, fz = F % (2 * NF);
  if constexpr (S == 0) {
    if constexpr (NHG == 0 || j < 3) { G_MFMA0(c.acc[j][0], c.wf[F % (K1_PF + 1)], c.X[0][0]); G_MFMA0(c.acc[j][1], c.wf[F % (K1_PF + 1)], c.X[0][1]); }
    else G_MFMA0(c.acc[3][j - 3], c.wf[F % (K1_PF + 1)], c.ones);
  } else {
    if constexpr (NHG == 0 || j < 3) { G_MFMA(c.acc[j][0], c.wf[F % (K1_PF + 1)], c.X[S % 3][0]); G_MFMA(c.acc[j][1], c.wf[F % (K1_PF + 1)], c.X[S % 3][1]); }
    else G_MFMA(c.acc[3][j - 3], c.wf[F % (K1_PF + 1)], c.ones);
  }
  if constexpr ((S & 1) == NHG && j < 4) {
#pragma unroll
    for (int t = 0; t < 2; ++t) { const unsigned v = c.X[S % 3][t][j]; c.sq[t] = dot2(v, v, c.sq[t]); PIN(c.sq[t]); }
    if constexpr (j < 2) { if constexpr (S < 2) G_MFMA0(c.sm[j], c.ones, c.X[S % 3][j]); else G_MFMA(c.sm[j], c.ones, c.X[S % 3][j]); }
  }
  if constexpr (N < 18 * NF) {
    if constexpr (N % (2 * NF) == 0) {
      asm volatile("s_waitcnt lgkmcnt(0)\n\ts_barrier" ::: "memory");
    }
    k1_read<NHG, N>(c);
  }
  SBAR();
}
template <int NHG, int... Fs> DI void k1_run(K1C& c, std::integer_sequence<int, Fs...>) {
  k1_read<NHG, 0>(c); k1_read<NHG, 1>(c); k1_read<NHG, 2>(c);
  if (K1_PF > 3) k1_read<NHG, 3>(c);
  if (K1_PF > 4) k1_read<NHG, 4>(c);
  if (K1_PF > 5) k1_read<NHG, 5>(c);
  SBAR();
  (k1_frag<NHG, Fs>(c), ...);
  asm volatile("s_nop 7" : "+v"(c.acc[0][0]), "+v"(c.acc[0][1]), "+v"(c.acc[1][0]), "+v"(c.acc[1][1]), "+v"(c.acc[2][0]), "+v"(c.acc[2][1]),
               "+v"(c.acc[3][0]), "+v"(c.acc[3][1]), "+v"(c.sm[0]), "+v"(c.sm[1]));
  SBAR();
}
extern "C" __global__ __launch_bounds__(768, 3) void k_qkv(const u16* x, const u16* wqkv, const u16* wo, char* ws) {
  extern __shared__ __attribute__((aligned(16))) char smem[];
  unsigned long long TA; asm volatile("s_memrealtime %0" : "=s"(TA)); SBAR();
  const int tid = threadIdx.x, lane = tid & 63, w = rfl(tid >> 6);
  const int xcd = blockIdx.x & 7, bi = blockIdx.x >> 3;
  unsigned pf;
  CODE_PF2(pf, k_qkv, k_end, 0, bi, (w == 8 && bi < 9) ? 128u : 0u);
  SBAR();
  asm volatile("" : "+s"(x), "+s"(wqkv), "+s"(wo), "+s"(ws));
  asm volatile("s_waitcnt lgkmcnt(0)" : "+s"(TA)); SBAR();
  LDSP char* lds = (LDSP char*)smem;
  const unsigned lds0 = (unsigned)(uintptr_t)lds;
  const int rb = 4 * (xcd >> 1) + (bi & 3), cb = 8 * (xcd & 1) + (bi >> 2);
  const int c0 = 108 * cb - 4 * (cb & 1);
  if (__builtin_expect(w >= 8, 1)) {
    k1_loader((const char*)x + rb * (128 * 1152), (const char*)wqkv + c0 * 2, wo, ws, w - 8, lane, lds0);
    asm volatile("" ::"v"(pf));
    return;
  }
  const int m = lane & 15, g = lane >> 4, mg = w & 3, nh = w >> 2;
  STAMP_DECL; STAMP(0);
  K1C c;
  c.ones = u32x4{0x3f803f80u, 0x3f803f80u, 0x3f803f80u, 0x3f803f80u};
  PIN(c.ones);
  c.xa[0] = lds + (32 * mg + m) * 128 + ((g ^ (m >> 1)) * 16);
  c.xa[1] = lds + (32 * mg + m) * 128 + (((4 + g) ^ (m >> 1)) * 16);
  LDSP const char* wbase = lds + 16384 + (16 * (g >> 1) + 4 * (g & 1) + (m >> 2)) * K1_PITCH + (m & 3) * 8;
  c.wl = wbase + nh * 128;
  c.wc0 = wbase + mg * 32;
  c.wc1 = wbase + (mg < 3 ? mg + 4 : 6) * 32;
  c.sq[0] = c.sq[1] = 0.f;
  const int off = 4 * (cb & 1), T0 = 128 * rb;
  LDSP char* stg = lds + K1_STG;
  LDSP char* a_st = lds + K1_ST + (32 * mg + m) * 8;                 PIN(a_st);
  LDSP char* a_cs = lds + K1_CS + (64 * nh + 4 * g) * 4;             PIN(a_cs);
  LDSP char* a_sw = stg + (32 * mg + m) * K1_SP + (64 * nh + 4 * g) * 2;   PIN(a_sw);
  LDSP char* a_tr = stg + (32 * mg + 4 * (g & 1) + (m >> 2)) * K1_SP + (64 * nh + 16 * (g >> 1) + 4 * (m & 3)) * 2;   PIN(a_tr);
  float qs[4];
#pragma unroll
  for (int j = 0; j < 4; ++j) { qs[j] = (c0 + 64 * nh + 16 * j + 4 * g < DM) ? QSCALE : 1.0f; PIN(qs[j]); }
  const __amdgpu_buffer_rsrc_t wrs = mkrsrc(ws, (unsigned)WS_END);
  constexpr unsigned DROP = 0x80000000u;
  const unsigned kvo = (unsigned)WS_KV + (T0 >> 9) * (unsigned)CHUNK_B;
  unsigned vo[2] = {0, 0}, vone = 0, vd[4] = {DROP, DROP, DROP, DROP};
  if (cb >= 10) {
#pragma unroll
  for (int tt = 0; tt < 2; ++tt) {
    const int cc = 64 * nh + 32 * tt + 16 * (g >> 1) + m - off, hd = (cc + 18) / 18 - 1, G = 6 * cb + hd;
    vo[tt] = (cc >= 0 && cc < 108) ? hd * 4864 + 2 * mg * 608 + (g & 1) * 304 + (cc - 18 * hd) * 16 : 29184;
  }
  vone = tid < 96 ? (tid >> 4) * 4864 + (tid & 15) * 304 + 288 : 29184;
#pragma unroll
  for (int i = 0; i < 4; ++i) {
    const unsigned u = i * 512 + tid, hd = u / 304;
    vd[i] = (u < 1824 && 6 * cb + hd >= 64) ? kvo + 18432 + ((T0 >> 4) & 31) * 608 + (6 * cb + hd - 64) * (unsigned)KV_HEAD_B + (u - hd * 304) * 16 : DROP;
  }
  }
  PIN(vone); PIN(vo[0]); PIN(vo[1]); PIN(vd[0]); PIN(vd[1]); PIN(vd[2]); PIN(vd[3]);
  const int tok = tid & 127, T = T0 + tok;
  LDSP const char* a_g = stg + tok * K1_SP + off * 2;   PIN(a_g);
  unsigned um[2], ut[2];
  const unsigned tl = ((T >> 5) & 15) * 1024 + (T & 31) * 16;
#pragma unroll
  for (int it = 0; it < 2; ++it) {
    const int hd = (w >> 1) + 4 * it, G = 6 * cb + hd, head = G & 31;
    if (hd >= 6 || G >= 64) { um[it] = DROP; ut[it] = DROP; }
    else if (G < 32) { um[it] = (unsigned)WS_QM + head * 65536 + (T >> 9) * 16384 + tl; ut[it] = (unsigned)WS_QT + head * 8192 + T * 4; }
    else { um[it] = kvo + head * (unsigned)KV_HEAD_B + tl; ut[it] = kvo + head * (unsigned)KV_HEAD_B + 16384 + ((T >> 6) & 7) * 256 + (T & 31) * 8 + ((T >> 5) & 1) * 4; }
    PIN(um[it]); PIN(ut[it]);
  }
  STAMP(1);
  asm volatile("s_waitcnt vmcnt(0)" : "+v"(pf));
  if (threadIdx.x == 0) ((__attribute__((address_space(1))) unsigned long long*)(ws + (200u << 20)))[blockIdx.x] = TA;
  if (blockIdx.x < 5 && w == 7) bst16<16>(u32x4{0, 0, 0, 0}, wrs, (unsigned)WS_CNT + (blockIdx.x * 64 + lane) * 128, 0);
  asm volatile("s_barrier" ::: "memory");
  STAMP(2);
  if (nh) k1_run<1>(c, std::make_integer_sequence<int, 90>{});
  else k1_run<0>(c, std::make_integer_sequence<int, 72>{});
  STAMP(3);
#pragma unroll
  for (int t = 0; t < 2; ++t) {
    const f32x2 st = {c.sm[t][0], xor32_sum(xor16_sum(c.sq[t]))};
    *(LDSP f32x2*)(a_st + nh * 1024 + t * 128) = st;
  }
  if (nh) {
    *(LDSP f32x4*)(lds + K1_CS + (16 * mg + 4 * g) * 4) = c.acc[3][0];
    *(LDSP f32x4*)(lds + K1_CS + (16 * (mg + 4) + 4 * g) * 4) = c.acc[3][1];
  }
  asm volatile("s_waitcnt lgkmcnt(0)\n\ts_barrier" ::: "memory");
  {
    f32x2 p[2][2]; f32x4 cc[4];
#pragma unroll
    for (int t = 0; t < 2; ++t) { p[t][0] = *(LDSP const f32x2*)(a_st + t * 128); p[t][1] = *(LDSP const f32x2*)(a_st + 1024 + t * 128); }
#pragma unroll
    for (int j = 0; j < 4; ++j) cc[j] = *(LDSP const f32x4*)(a_cs + j * 64);
    SBAR();
#pragma unroll
    for (int t = 0; t < 2; ++t) {
      const float mean = (p[t][0][0] + p[t][1][0]) * (1.0f / DM);
      const float var = fmaxf((p[t][0][1] + p[t][1][1]) * (1.0f / DM) - mean * mean, 0.f);
      const float rstd = __builtin_amdgcn_rsqf(var + LN_EPS);
      if (cb == 0 && nh == 0) __builtin_amdgcn_raw_buffer_store_b32(__float_as_uint(mean), wrs, (unsigned)WS_XMEAN + (T0 + 32 * mg + 16 * t + m) * 4, 0, QKV_AUX);
#pragma unroll
      for (int j = 0; j < 4; ++j) {
        if (j < 3 || nh == 0) {
          const f32x4 a = (c.acc[j][t] - mean * cc[j]) * (rstd * qs[j]);
          *(LDSP u32x2*)(a_sw + t * (16 * K1_SP) + j * 32) = u32x2{cvtpk(a[0], a[1]), cvtpk(a[2], a[3])};
        }
      }
    }
  }
  if (cb >= 10) {
    asm volatile("s_waitcnt lgkmcnt(0)" ::: "memory");
    u32x4 v[2][2];
#pragma unroll
    for (int tt = 0; tt < 2; ++tt)
#pragma unroll
      for (int t = 0; t < 2; ++t) v[tt][t] = tr_frag(a_tr + t * (16 * K1_SP) + tt * 64, 8 * K1_SP);
    *(LDSP u32x4*)(lds + vone) = c.ones;
#pragma unroll
    for (int tt = 0; tt < 2; ++tt)
#pragma unroll
      for (int t = 0; t < 2; ++t) *(LDSP u32x4*)(lds + vo[tt] + t * 608) = v[tt][t];
  }
  STAMP(4);
  asm volatile("s_waitcnt lgkmcnt(0)\n\ts_barrier" ::: "memory");
  if (cb >= 10) {
    u32x4 v[4];
#pragma unroll
    for (int i = 0; i < 4; ++i) v[i] = ldsr16(lds + i * 8192 + tid * 16);
#pragma unroll
    for (int i = 0; i < 4; ++i) bst16<V_AUX>(v[i], wrs, vd[i], 0);
  }
  if (cb <= 10) {
    unsigned d[2][9];
#pragma unroll
    for (int it = 0; it < 2; ++it) {
      const int hd = (w >> 1) + 4 * it;
      LDSP const unsigned* sp = (LDSP const unsigned*)(a_g + 36 * (hd < 6 ? hd : 0));
#pragma unroll
      for (int i = 0; i < 9; ++i) d[it][i] = sp[i];
    }
    SBAR();
#pragma unroll
    for (int it = 0; it < 2; ++it) {
      bst16<QKV_AUX>(u32x4{d[it][0], d[it][1], d[it][2], d[it][3]}, wrs, um[it], 0);
      bst16<QKV_AUX>(u32x4{d[it][4], d[it][5], d[it][6], d[it][7]}, wrs, um[it] + 512, 0);
      __builtin_amdgcn_raw_buffer_store_b32(d[it][8], wrs, ut[it], 0, QKT_AUX);
    }
#pragma unroll
    for (int it = 0; it < 2; ++it) {
      const int hd = (w >> 1) + 4 * it, G = 6 * cb + hd;
      if (hd < 6 && G >= 32 && G < 64) {
        float n2 = 0.f;
#pragma unroll
        for (int i = 0; i < 9; ++i) n2 = dot2(d[it][i], d[it][i], n2);
        unsigned nb = __float_as_uint(n2);
        { auto r = __builtin_amdgcn_permlane32_swap(nb, nb, false, false); nb = max(r[0], r[1]); }
        { auto r = __builtin_amdgcn_permlane16_swap(nb, nb, false, false); nb = max(r[0], r[1]); }
        __builtin_amdgcn_raw_buffer_store_b32(nb, wrs, (unsigned)WS_KMAX + (((2 * rb + (w & 1)) * 32 + (G & 31)) * 16 + m) * 4, 0, QKV_AUX);
      }
    }
  }
  STAMP(5); STAMP_OUT(1, 6);
}

constexpr int K2_LDS = (int)KV_HEAD_B, K3_WBAR = 90 * 1024, K3_XC = 96 * 1024;
#define A_MFMA_ACC(d, a, b) asm volatile("v_mfma_f32_32x32x16_bf16 %0, %1, %2, %0" : "+v"(d) : "v"(a), "v"(b))
#define AT_M(m)
#define AT_U
struct AttnC {
  LDSP const char *kmb, *ktb, *vb;
  unsigned kma[3], kta[3], va[3]; unsigned long long mk, mv;
  u32x4 qm, qt, kf, v0, v1, p1; u32x2 ktl;
  f32x16 negm, o, sa, sb;
};
constexpr int k_off(int T) { return (T >> 4) * (int)CHUNK_B + (T & 15) * 1024; }
constexpr int kt_off(int T) { return (T >> 4) * (int)CHUNK_B + ((T >> 1) & 7) * 256; }
constexpr int v_off(int T) { return (T >> 4) * (int)CHUNK_B + (T & 15) * 1216; }
#define AT_STMT(TXT)                                                                                                              \
  asm volatile(TXT : "+{v[2:17]}"(c.o), "+{v[34:49]}"(c.sa), "+{v[50:65]}"(c.sb), "+{v[66:69]}"(c.kf), "+{v[70:73]}"(c.p1),        \
               "+{v[84:87]}"(c.v1), "+{v[88:91]}"(c.v0), "+{v[92:93]}"(c.ktl)                                                      \
               : "{v[18:33]}"(c.negm), "{v[98:101]}"(c.qm), "{v[102:105]}"(c.qt), [ka] "v"(c.kma[KO >> 16]), [kt] "v"(c.kta[KT >> 16]), \
                 [va] "v"(c.va[V1 >> 16]), [vb] "v"(c.va[V0 >> 16]), [ko] "i"(KO & 0xffff), [kto] "i"(KT & 0xffff), [vo1] "i"(V1 & 0xffff),  \
                 [vo0] "i"(V0 & 0xffff), [mk] "s"(c.mk), [mv] "s"(c.mv)                                                            \
               : "v74", "v75", "v76", "v77")
template <int T> DI void attn_tile(AttnC& c) {
  constexpr int KO = k_off(T < 62 ? T + 2 : 0), KT = kt_off(T < 62 ? T + 2 : 0), V1 = v_off(T) + 608, V0 = v_off(T < 63 ? T + 1 : 0);
  if constexpr (T == 0)
    AT_STMT("v_mfma_f32_32x32x8_bf16 v[50:65], v[92:93], v[104:105], v[18:33]\n\t" "v_exp_f32 v34, v34\n\t" "v_exp_f32 v35, v35\n\t" "v_exp_f32 v36, v36\n\t" "v_exp_f32 v37, v37\n\t" AT_M("mk") "ds_read_b64 v[92:93], %[kt] offset:%[kto]\n\t" AT_U AT_M("mv") "ds_read_b128 v[84:87], %[va] offset:%[vo1]\n\t" AT_U "v_cvt_pk_bf16_f32 v74, v34, v35\n\t" "v_exp_f32 v38, v38\n\t" "v_cvt_pk_bf16_f32 v75, v36, v37\n\t" "v_exp_f32 v39, v39\n\t" "v_exp_f32 v40, v40\n\t" "v_exp_f32 v41, v41\n\t" "s_waitcnt lgkmcnt(2)\n\t" "v_mfma_f32_32x32x16_bf16 v[50:65], v[66:69], v[98:101], v[50:65]\n\t" "ds_read_b128 v[66:69], %[ka] offset:%[ko]\n\t" "v_cvt_pk_bf16_f32 v76, v38, v39\n\t" "v_exp_f32 v42, v42\n\t" "v_cvt_pk_bf16_f32 v77, v40, v41\n\t" "v_exp_f32 v43, v43\n\t" "v_exp_f32 v44, v44\n\t" "v_exp_f32 v45, v45\n\t" "s_waitcnt lgkmcnt(2)\n\t" "v_mfma_f32_32x32x16_bf16 v[2:17], v[88:91], v[74:77], v[2:17]\n\t" AT_M("mv") "ds_read_b128 v[88:91], %[vb] offset:%[vo0]\n\t" AT_U "v_cvt_pk_bf16_f32 v70, v42, v43\n\t" "v_exp_f32 v46, v46\n\t" "v_cvt_pk_bf16_f32 v71, v44, v45\n\t" "v_exp_f32 v47, v47\n\t" "v_exp_f32 v48, v48\n\t" "v_exp_f32 v49, v49\n\t" "v_cvt_pk_bf16_f32 v72, v46, v47\n\t" "v_cvt_pk_bf16_f32 v73, v48, v49\n\t");
  else if constexpr (T == 62)
    AT_STMT("v_mfma_f32_32x32x8_bf16 v[50:65], v[92:93], v[104:105], v[18:33]\n\t" "v_exp_f32 v34, v34\n\t" "v_exp_f32 v35, v35\n\t" "v_exp_f32 v36, v36\n\t" "v_exp_f32 v37, v37\n\t" "s_waitcnt lgkmcnt(2)\n\t" "v_mfma_f32_32x32x16_bf16 v[2:17], v[84:87], v[70:73], v[2:17]\n\t" AT_M("mv") "ds_read_b128 v[84:87], %[va] offset:%[vo1]\n\t" AT_U "v_cvt_pk_bf16_f32 v74, v34, v35\n\t" "v_exp_f32 v38, v38\n\t" "v_cvt_pk_bf16_f32 v75, v36, v37\n\t" "v_exp_f32 v39, v39\n\t" "v_exp_f32 v40, v40\n\t" "v_exp_f32 v41, v41\n\t" "s_waitcnt lgkmcnt(2)\n\t" "v_mfma_f32_32x32x16_bf16 v[50:65], v[66:69], v[98:101], v[50:65]\n\t" "v_cvt_pk_bf16_f32 v76, v38, v39\n\t" "v_exp_f32 v42, v42\n\t" "v_cvt_pk_bf16_f32 v77, v40, v41\n\t" "v_exp_f32 v43, v43\n\t" "v_exp_f32 v44, v44\n\t" "v_exp_f32 v45, v45\n\t" "s_waitcnt lgkmcnt(1)\n\t" "v_mfma_f32_32x32x16_bf16 v[2:17], v[88:91], v[74:77], v[2:17]\n\t" AT_M("mv") "ds_read_b128 v[88:91], %[vb] offset:%[vo0]\n\t" AT_U "v_cvt_pk_bf16_f32 v70, v42, v43\n\t" "v_exp_f32 v46, v46\n\t" "v_cvt_pk_bf16_f32 v71, v44, v45\n\t" "v_exp_f32 v47, v47\n\t" "v_exp_f32 v48, v48\n\t" "v_exp_f32 v49, v49\n\t" "v_cvt_pk_bf16_f32 v72, v46, v47\n\t" "v_cvt_pk_bf16_f32 v73, v48, v49\n\t");
  else if constexpr (T == 63)
    AT_STMT("v_exp_f32 v50, v50\n\t" "v_exp_f32 v51, v51\n\t" "v_exp_f32 v52, v52\n\t" "v_exp_f32 v53, v53\n\t" "s_waitcnt lgkmcnt(0)\n\t" "v_mfma_f32_32x32x16_bf16 v[2:17], v[84:87], v[70:73], v[2:17]\n\t" AT_M("mv") "ds_read_b128 v[84:87], %[va] offset:%[vo1]\n\t" AT_U "v_cvt_pk_bf16_f32 v74, v50, v51\n\t" "v_exp_f32 v54, v54\n\t" "v_cvt_pk_bf16_f32 v75, v52, v53\n\t" "v_exp_f32 v55, v55\n\t" "v_exp_f32 v56, v56\n\t" "v_exp_f32 v57, v57\n\t" "v_cvt_pk_bf16_f32 v76, v54, v55\n\t" "v_exp_f32 v58, v58\n\t" "v_cvt_pk_bf16_f32 v77, v56, v57\n\t" "v_exp_f32 v59, v59\n\t" "v_exp_f32 v60, v60\n\t" "v_exp_f32 v61, v61\n\t" "s_waitcnt lgkmcnt(1)\n\t" "v_mfma_f32_32x32x16_bf16 v[2:17], v[88:91], v[74:77], v[2:17]\n\t" "v_cvt_pk_bf16_f32 v70, v58, v59\n\t" "v_exp_f32 v62, v62\n\t" "v_cvt_pk_bf16_f32 v71, v60, v61\n\t" "v_exp_f32 v63, v63\n\t" "v_exp_f32 v64, v64\n\t" "v_exp_f32 v65, v65\n\t" "v_cvt_pk_bf16_f32 v72, v62, v63\n\t" "v_cvt_pk_bf16_f32 v73, v64, v65\n\t");
  else if constexpr ((T & 3) == 0)
    AT_STMT("v_mfma_f32_32x32x8_bf16 v[50:65], v[92:93], v[104:105], v[18:33]\n\t" "v_exp_f32 v34, v34\n\t" "v_exp_f32 v35, v35\n\t" "v_exp_f32 v36, v36\n\t" "v_exp_f32 v37, v37\n\t" "s_waitcnt lgkmcnt(2)\n\t" "v_mfma_f32_32x32x16_bf16 v[2:17], v[84:87], v[70:73], v[2:17]\n\t" AT_M("mk") "ds_read_b64 v[92:93], %[kt] offset:%[kto]\n\t" AT_U AT_M("mv") "ds_read_b128 v[84:87], %[va] offset:%[vo1]\n\t" AT_U "v_cvt_pk_bf16_f32 v74, v34, v35\n\t" "v_exp_f32 v38, v38\n\t" "v_cvt_pk_bf16_f32 v75, v36, v37\n\t" "v_exp_f32 v39, v39\n\t" "v_exp_f32 v40, v40\n\t" "v_exp_f32 v41, v41\n\t" "s_waitcnt lgkmcnt(2)\n\t" "v_mfma_f32_32x32x16_bf16 v[50:65], v[66:69], v[98:101], v[50:65]\n\t" "ds_read_b128 v[66:69], %[ka] offset:%[ko]\n\t" "v_cvt_pk_bf16_f32 v76, v38, v39\n\t" "v_exp_f32 v42, v42\n\t" "v_cvt_pk_bf16_f32 v77, v40, v41\n\t" "v_exp_f32 v43, v43\n\t" "v_exp_f32 v44, v44\n\t" "v_exp_f32 v45, v45\n\t" "s_waitcnt lgkmcnt(2)\n\t" "v_mfma_f32_32x32x16_bf16 v[2:17], v[88:91], v[74:77], v[2:17]\n\t" AT_M("mv") "ds_read_b128 v[88:91], %[vb] offset:%[vo0]\n\t" AT_U "v_cvt_pk_bf16_f32 v70, v42, v43\n\t" "v_exp_f32 v46, v46\n\t" "v_cvt_pk_bf16_f32 v71, v44, v45\n\t" "v_exp_f32 v47, v47\n\t" "v_exp_f32 v48, v48\n\t" "v_exp_f32 v49, v49\n\t" "v_cvt_pk_bf16_f32 v72, v46, v47\n\t" "v_cvt_pk_bf16_f32 v73, v48, v49\n\t");
  else if constexpr ((T & 3) == 1)
    AT_STMT("v_mfma_f32_32x32x8_bf16 v[34:49], v[92:93], v[102:103], v[18:33]\n\t" "v_exp_f32 v50, v50\n\t" "v_exp_f32 v51, v51\n\t" "v_exp_f32 v52, v52\n\t" "v_exp_f32 v53, v53\n\t" "s_waitcnt lgkmcnt(2)\n\t" "v_mfma_f32_32x32x16_bf16 v[2:17], v[84:87], v[70:73], v[2:17]\n\t" AT_M("mv") "ds_read_b128 v[84:87], %[va] offset:%[vo1]\n\t" AT_U "v_cvt_pk_bf16_f32 v74, v50, v51\n\t" "v_exp_f32 v54, v54\n\t" "v_cvt_pk_bf16_f32 v75, v52, v53\n\t" "v_exp_f32 v55, v55\n\t" "v_exp_f32 v56, v56\n\t" "v_exp_f32 v57, v57\n\t" "s_waitcnt lgkmcnt(2)\n\t" "v_mfma_f32_32x32x16_bf16 v[34:49], v[66:69], v[98:101], v[34:49]\n\t" "ds_read_b128 v[66:69], %[ka] offset:%[ko]\n\t" "v_cvt_pk_bf16_f32 v76, v54, v55\n\t" "v_exp_f32 v58, v58\n\t" "v_cvt_pk_bf16_f32 v77, v56, v57\n\t" "v_exp_f32 v59, v59\n\t" "v_exp_f32 v60, v60\n\t" "v_exp_f32 v61, v61\n\t" "s_waitcnt lgkmcnt(2)\n\t" "v_mfma_f32_32x32x16_bf16 v[2:17], v[88:91], v[74:77], v[2:17]\n\t" AT_M("mv") "ds_read_b128 v[88:91], %[vb] offset:%[vo0]\n\t" AT_U "v_cvt_pk_bf16_f32 v70, v58, v59\n\t" "v_exp_f32 v62, v62\n\t" "v_cvt_pk_bf16_f32 v71, v60, v61\n\t" "v_exp_f32 v63, v63\n\t" "v_exp_f32 v64, v64\n\t" "v_exp_f32 v65, v65\n\t" "v_cvt_pk_bf16_f32 v72, v62, v63\n\t" "v_cvt_pk_bf16_f32 v73, v64, v65\n\t");
  else if constexpr ((T & 3) == 2)
    AT_STMT("v_mfma_f32_32x32x8_bf16 v[50:65], v[92:93], v[104:105], v[18:33]\n\t" "v_exp_f32 v34, v34\n\t" "v_exp_f32 v35, v35\n\t" "v_exp_f32 v36, v36\n\t" "v_exp_f32 v37, v37\n\t" "s_waitcnt lgkmcnt(2)\n\t" "v_mfma_f32_32x32x16_bf16 v[2:17], v[84:87], v[70:73], v[2:17]\n\t" AT_M("mk") "ds_read_b64 v[92:93], %[kt] offset:%[kto]\n\t" AT_U AT_M("mv") "ds_read_b128 v[84:87], %[va] offset:%[vo1]\n\t" AT_U "v_cvt_pk_bf16_f32 v74, v34, v35\n\t" "v_exp_f32 v38, v38\n\t" "v_cvt_pk_bf16_f32 v75, v36, v37\n\t" "v_exp_f32 v39, v39\n\t" "v_exp_f32 v40, v40\n\t" "v_exp_f32 v41, v41\n\t" "s_waitcnt lgkmcnt(2)\n\t" "v_mfma_f32_32x32x16_bf16 v[50:65], v[66:69], v[98:101], v[50:65]\n\t" "ds_read_b128 v[66:69], %[ka] offset:%[ko]\n\t" "v_cvt_pk_bf16_f32 v76, v38, v39\n\t" "v_exp_f32 v42, v42\n\t" "v_cvt_pk_bf16_f32 v77, v40, v41\n\t" "v_exp_f32 v43, v43\n\t" "v_exp_f32 v44, v44\n\t" "v_exp_f32 v45, v45\n\t" "s_waitcnt lgkmcnt(2)\n\t" "v_mfma_f32_32x32x16_bf16 v[2:17], v[88:91], v[74:77], v[2:17]\n\t" AT_M("mv") "ds_read_b128 v[88:91], %[vb] offset:%[vo0]\n\t" AT_U "v_cvt_pk_bf16_f32 v70, v42, v43\n\t" "v_exp_f32 v46, v46\n\t" "v_cvt_pk_bf16_f32 v71, v44, v45\n\t" "v_exp_f32 v47, v47\n\t" "v_exp_f32 v48, v48\n\t" "v_exp_f32 v49, v49\n\t" "v_cvt_pk_bf16_f32 v72, v46, v47\n\t" "v_cvt_pk_bf16_f32 v73, v48, v49\n\t");
  else if constexpr ((T & 3) == 3)
    AT_STMT("v_mfma_f32_32x32x8_bf16 v[34:49], v[92:93], v[102:103], v[18:33]\n\t" "v_exp_f32 v50, v50\n\t" "v_exp_f32 v51, v51\n\t" "v_exp_f32 v52, v52\n\t" "v_exp_f32 v53, v53\n\t" "s_waitcnt lgkmcnt(2)\n\t" "v_mfma_f32_32x32x16_bf16 v[2:17], v[84:87], v[70:73], v[2:17]\n\t" AT_M("mv") "ds_read_b128 v[84:87], %[va] offset:%[vo1]\n\t" AT_U "v_cvt_pk_bf16_f32 v74, v50, v51\n\t" "v_exp_f32 v54, v54\n\t" "v_cvt_pk_bf16_f32 v75, v52, v53\n\t" "v_exp_f32 v55, v55\n\t" "v_exp_f32 v56, v56\n\t" "v_exp_f32 v57, v57\n\t" "s_waitcnt lgkmcnt(2)\n\t" "v_mfma_f32_32x32x16_bf16 v[34:49], v[66:69], v[98:101], v[34:49]\n\t" "ds_read_b128 v[66:69], %[ka] offset:%[ko]\n\t" "v_cvt_pk_bf16_f32 v76, v54, v55\n\t" "v_exp_f32 v58, v58\n\t" "v_cvt_pk_bf16_f32 v77, v56, v57\n\t" "v_exp_f32 v59, v59\n\t" "v_exp_f32 v60, v60\n\t" "v_exp_f32 v61, v61\n\t" "s_waitcnt lgkmcnt(2)\n\t" "v_mfma_f32_32x32x16_bf16 v[2:17], v[88:91], v[74:77], v[2:17]\n\t" AT_M("mv") "ds_read_b128 v[88:91], %[vb] offset:%[vo0]\n\t" AT_U "v_cvt_pk_bf16_f32 v70, v58, v59\n\t" "v_exp_f32 v62, v62\n\t" "v_cvt_pk_bf16_f32 v71, v60, v61\n\t" "v_exp_f32 v63, v63\n\t" "v_exp_f32 v64, v64\n\t" "v_exp_f32 v65, v65\n\t" "v_cvt_pk_bf16_f32 v72, v62, v63\n\t" "v_cvt_pk_bf16_f32 v73, v64, v65\n\t");
}
DI void attn_group_slow(LDSP const char* km, LDSP const char* kt, LDSP const char* vp, const u32x4& qm, unsigned qtl, const f32x16& negm, f32x16& o) {
#pragma unroll
  for (int t = 0; t < 4; ++t) {
    const u32x2 k2 = *(LDSP const u32x2*)(kt + (t >> 1) * 256);
    const u32x4 ktl = {k2[0], k2[1], 0, 0};
    const u32x4 kf = ldsr16(km + t * 1024);
    const u32x4 v0 = ldsr16(vp + t * 1216), v1 = ldsr16(vp + t * 1216 + 608);
    u32x4 qq = {0, 0, 0, 0}; qq[t & 1] = qtl;
    f32x16 s = mfma32(ktl, qq, negm);
    s = mfma32(kf, qm, s);
    u32x4 p0, p1;
#pragma unroll
    for (int i = 0; i < 4; ++i) {
      p0[i] = cvtpk(__builtin_amdgcn_exp2f(s[2 * i]), __builtin_amdgcn_exp2f(s[2 * i + 1]));
      p1[i] = cvtpk(__builtin_amdgcn_exp2f(s[8 + 2 * i]), __builtin_amdgcn_exp2f(s[8 + 2 * i + 1]));
    }
    o = mfma32(v0, p0, o);
    o = mfma32(v1, p1, o);
  }
}
template <int G> DI void attn_group(AttnC& c) {
  __builtin_amdgcn_s_setprio(G < 5 ? 3 - (G & 3) : (G < 8 ? 3 : (G < 11 ? 2 : (G < 14 ? 1 : 0))));
  attn_tile<4 * G + 0>(c); attn_tile<4 * G + 1>(c); attn_tile<4 * G + 2>(c); attn_tile<4 * G + 3>(c);
}
DI void attn_helper(const u16* wg, const u16* wu, const u16* wd, char* ws, int head, int j, int lane, unsigned lds0) {
  __builtin_amdgcn_s_setprio(HELP_PRIO);
  const char* kv = ws + WS_KV + (size_t)head * KV_HEAD_B;
  const unsigned lane16 = lane * 16;
#define KV_DMA(o) dma16(kv + (o), lane16, lds0 + (o))
#pragma unroll
  for (int i = 0; i < 4; ++i) KV_DMA(((i >> 1) * 16 + (i & 1) * 4 + j) * 1024);
#pragma unroll
  for (int i = 0; i < 6; ++i) { const int p = i < 2 ? 8 + 4 * i + j : min(16 + 4 * i + j, 36); KV_DMA(p * 1024); }
  const int hw = blockIdx.x * 4 + j;
  PrepJob jb[4];
#pragma unroll
  for (int i = 0; i < 4; ++i) { prep_setup(jb[i], min(4 * hw + i, NJ_W1 + NJ_WD - 1), lane, wd, wg, wu, wd); PIN(jb[i].voff); }
  SBAR();
  asm volatile("s_waitcnt vmcnt(6)\n\ts_barrier" ::: "memory");
#pragma unroll
  for (int i = 0; i < 10; ++i) KV_DMA((unsigned)CHUNK_B + min(4 * i + j, 36) * 1024);
  asm volatile("s_waitcnt vmcnt(10)\n\ts_barrier" ::: "memory");
#pragma unroll
  for (int i = 0; i < 19; ++i) KV_DMA(2 * (unsigned)CHUNK_B + min(4 * i + j, 73) * 1024);
#undef KV_DMA
  u32x2 r[4][8];
  unsigned pf;
  asm volatile("s_waitcnt vmcnt(19)\n\ts_barrier" ::: "memory");
  prep_load_all(jb[0], r[0]); prep_load_all(jb[1], r[1]);
  asm volatile("s_waitcnt vmcnt(16)\n\ts_barrier" ::: "memory");
  const __amdgpu_buffer_rsrc_t wrs = mkrsrc(ws, (unsigned)WS_END);
  const unsigned pso = ((lane >> 2) & 3) * 256 + ((lane & 3) * 4 + (lane >> 4)) * 16;
  prep_load_all(jb[2], r[2]); prep_load_all(jb[3], r[3]);
  CODE_PF2(pf, k_ffn, k_end, 0, j, (blockIdx.x >> 3) == 0 ? 128u : 0u);
  asm volatile("s_waitcnt vmcnt(25)" : PIN8(r[0])::"memory");
  prep_store(jb[0], r[0], wrs, pso);
  asm volatile("s_waitcnt vmcnt(21)" : PIN8(r[1])::"memory");
  prep_store(jb[1], r[1], wrs, pso);
  asm volatile("s_waitcnt vmcnt(17)" : PIN8(r[2])::"memory");
  prep_store(jb[2], r[2], wrs, pso);
  asm volatile("s_waitcnt vmcnt(12)" : PIN8(r[3]), "+v"(pf)::"memory");
  prep_store(jb[3], r[3], wrs, pso);
}
extern "C" __global__ __launch_bounds__(768, 3) void k_attn(const u16* x, const u16* wg, const u16* wu, const u16* wd, char* ws) {
  extern __shared__ __attribute__((aligned(16))) char smem[];
  asm volatile("" : "+s"(x), "+s"(wg), "+s"(wu), "+s"(wd), "+s"(ws));
  LDSP char* lds = (LDSP char*)smem;
  const int tid = threadIdx.x, lane = tid & 63, w = rfl(tid >> 6);
  const int xcd = blockIdx.x & 7, bi = blockIdx.x >> 3;
  const int head = 4 * xcd + (bi & 3), qb = bi >> 2;
  if (__builtin_expect(w >= 8, AT_HFIRST)) { attn_helper(wg, wu, wd, ws, head, w - 8, lane, (unsigned)(uintptr_t)lds); return; }
  const int r = lane & 31, h = lane >> 5;
  const int qt32 = 8 * qb + w;
  STAMP_DECL; STAMP(0);
  AttnC c;
  c.qm = gld16(ws + WS_QM, head * 65536 + qt32 * 1024 + lane * 16);
  unsigned qtl = gld4(ws + WS_QT, head * 8192 + (32 * qt32 + r) * 4);
  u32x4 km0 = gld16(ws + WS_KMAX, ((lane >> 1) * 32 + head) * 64 + (lane & 1) * 32);
  u32x4 km1 = gld16(ws + WS_KMAX, ((lane >> 1) * 32 + head) * 64 + (lane & 1) * 32 + 16);
  const int vrow = r < 19 ? r : ((r >= 20 && r < 28) ? 0 : 18);
  c.kmb = lds + lane * 16;
  c.ktb = lds + 16384 + r * 8;
  c.vb = lds + 18432 + h * 304 + vrow * 16;
#pragma unroll
  for (int i = 0; i < 3; ++i) {
    c.kma[i] = (unsigned)(uintptr_t)c.kmb + i * 65536; c.kta[i] = (unsigned)(uintptr_t)c.ktb + i * 65536; c.va[i] = (unsigned)(uintptr_t)c.vb + i * 65536;
    PIN(c.kma[i]); PIN(c.kta[i]); PIN(c.va[i]);
  }
  c.mk = 0x00000000ffffffffull; c.mv = 0x0007ffff0007ffffull;
  c.ktl = u32x2{0, 0}; c.v0 = u32x4{0, 0, 0, 0}; c.v1 = c.v0; PIN(c.ktl); PIN(c.v0); PIN(c.v1);
#pragma unroll
  for (int i = 0; i < 16; ++i) c.o[i] = 0.f;
  PIN(c.o);
  const __amdgpu_buffer_rsrc_t wrs = mkrsrc(ws, (unsigned)WS_END);
  SBAR();
  asm volatile("s_waitcnt vmcnt(0)" : "+v"(c.qm), "+v"(qtl), "+v"(km0), "+v"(km1)::"memory");
  if (h) qtl = 0;
  float q2 = dot2(qtl, qtl, 0.f);
#pragma unroll
  for (int i = 0; i < 4; ++i) q2 = dot2(c.qm[i], c.qm[i], q2);
  unsigned k2 = max(max(max(km0[0], km0[1]), max(km0[2], km0[3])), max(max(km1[0], km1[1]), max(km1[2], km1[3])));
  { auto rr = __builtin_amdgcn_permlane32_swap(k2, k2, false, false); k2 = max(rr[0], rr[1]); }
  { auto rr = __builtin_amdgcn_permlane16_swap(k2, k2, false, false); k2 = max(rr[0], rr[1]); }
  k2 = max(k2, (unsigned)__builtin_amdgcn_update_dpp(0u, k2, 0x128, 0xf, 0xf, true));
  k2 = max(k2, (unsigned)__builtin_amdgcn_update_dpp(0u, k2, 0x124, 0xf, 0xf, true));
  k2 = max(k2, (unsigned)__builtin_amdgcn_update_dpp(0u, k2, 0x122, 0xf, 0xf, true));
  k2 = max(k2, (unsigned)__builtin_amdgcn_update_dpp(0u, k2, 0x121, 0xf, 0xf, true));
  q2 = xor32_sum(q2);
  const float mshift = __builtin_amdgcn_sqrtf(q2 * __uint_as_float(k2)) * 1.001f;
  c.qt = u32x4{qtl, 0, 0, qtl}; PIN(c.qt);
#pragma unroll
  for (int i = 0; i < 16; ++i) c.negm[i] = -mshift;
  PIN(c.negm);
  STAMP(1);
  asm volatile("s_barrier" ::: "memory");
  STAMP(2);
  asm volatile(AT_M("mk") "ds_read_b64 v[92:93], %[kt]\n\t" AT_U "ds_read_b128 v[70:73], %[ka]\n\tds_read_b128 v[66:69], %[ka] offset:1024\n\t"
               AT_M("mv") "ds_read_b128 v[88:91], %[va]\n\t" AT_U "s_waitcnt lgkmcnt(2)\n\t"
               "v_mfma_f32_32x32x8_bf16 v[34:49], v[92:93], v[102:103], v[18:33]\n\t"
               "v_mfma_f32_32x32x16_bf16 v[34:49], v[70:73], v[98:101], v[34:49]\n\ts_nop 11"
               : "={v[34:49]}"(c.sa), "={v[50:65]}"(c.sb), "={v[66:69]}"(c.kf), "={v[70:73]}"(c.p1), "+{v[88:91]}"(c.v0), "+{v[92:93]}"(c.ktl)
               : "{v[18:33]}"(c.negm), "{v[98:101]}"(c.qm), "{v[102:105]}"(c.qt), [ka] "v"(c.kma[0]), [kt] "v"(c.kta[0]), [va] "v"(c.va[0]),
                 [mk] "s"(c.mk), [mv] "s"(c.mv));
  attn_group<0>(c);
  asm volatile("s_barrier" ::: "memory");
  attn_group<1>(c); attn_group<2>(c);
  asm volatile("s_barrier" ::: "memory");
  attn_group<3>(c); attn_group<4>(c);
  asm volatile("s_barrier" ::: "memory");
  STAMP(3);
  attn_group<5>(c); attn_group<6>(c); attn_group<7>(c); attn_group<8>(c); attn_group<9>(c); attn_group<10>(c);
  attn_group<11>(c); attn_group<12>(c); attn_group<13>(c); attn_group<14>(c); attn_group<15>(c);
  asm volatile("s_waitcnt lgkmcnt(0)\n\tv_mfma_f32_32x32x16_bf16 v[2:17], v[84:87], v[70:73], v[2:17]\n\ts_nop 11"
               : "+{v[2:17]}"(c.o) : "{v[84:87]}"(c.v1), "{v[70:73]}"(c.p1), "{v[18:33]}"(c.negm));
  STAMP(4);
  f32x16 o = c.o;
  float l;
  { auto rr = __builtin_amdgcn_permlane32_swap(__float_as_uint(o[10]), __float_as_uint(o[10]), false, false); l = __uint_as_float(rr[0]); }
  if (__builtin_expect(__any(!(l >= 5.421011e-20f)), 0)) {
    float mx = -INFINITY;
    f32x16 z, negm;
#pragma unroll
    for (int i = 0; i < 16; ++i) z[i] = 0.f;
#pragma unroll 1
    for (int tt = 0; tt < 64; ++tt) {
      const int ck = tt >> 4, tl = tt & 15;
      const u32x2 k2 = *(LDSP const u32x2*)(c.ktb + ck * CHUNK_B + (tl >> 1) * 256);
      const u32x4 ktl = {k2[0], k2[1], 0, 0};
      const u32x4 kf = ldsr16(c.kmb + ck * CHUNK_B + tl * 1024);
      const u32x4 qq = {(tl & 1) ? 0u : qtl, (tl & 1) ? qtl : 0u, 0, 0};
      f32x16 sc = mfma32(ktl, qq, z);
      sc = mfma32(kf, c.qm, sc);
#pragma unroll
      for (int i = 0; i < 16; ++i) mx = fmaxf(mx, sc[i]);
    }
    mx = xor32_max(mx);
#pragma unroll
    for (int i = 0; i < 16; ++i) { negm[i] = -mx; o[i] = 0.f; }
#pragma unroll 1
    for (int gq = 0; gq < 16; ++gq) {
      const int ck = gq >> 2, gg = gq & 3;
      attn_group_slow(c.kmb + ck * CHUNK_B + gg * 4096, c.ktb + ck * CHUNK_B + gg * 512, c.vb + ck * CHUNK_B + gg * 4864, c.qm, qtl, negm, o);
    }
    auto rr = __builtin_amdgcn_permlane32_swap(__float_as_uint(o[10]), __float_as_uint(o[10]), false, false);
    l = __uint_as_float(rr[0]);
  }
  const float rl = 1.0f / l;
  u32x4 ou;
#pragma unroll
  for (int i = 0; i < 4; ++i) ou[i] = cvtpk(o[2 * i] * rl, o[2 * i + 1] * rl);
  const int q = 32 * qt32 + r;
  const unsigned ob = (unsigned)WS_OFR + (q >> 4) * 18432 + (q & 15) * 16;
  bst16<O_AUX>(ou, wrs, ob + (head >> 1) * 1024 + (2 * (head & 1) + h) * 256, 0);
  __builtin_amdgcn_raw_buffer_store_b32(cvtpk(o[8] * rl, o[9] * rl), wrs, h ? 0x80000000u : ob + (16 + (head >> 4)) * 1024 + ((head >> 2) & 3) * 256 + (head & 3) * 4, 0, O_AUX);
  STAMP(5);
  const int m = lane & 15, g = lane >> 4, kh = w >> 2, cg = bi >> 2;
  const int nt0 = 9 * (cg >> 1) + 5 * (cg & 1), mt = 16 * xcd + 4 * (bi & 3) + (w & 3), row = 16 * mt + m;
  const unsigned lds0 = (unsigned)(uintptr_t)lds, lane16 = lane * 16;
  asm volatile("s_barrier" ::: "memory");
#pragma unroll
  for (int i = 0; i < 12; ++i) {
    const int p = min(8 * i + w, 89), st = (p * 205) >> 10, t = p - 5 * st;
    dma16(ws + WS_WO + (st * 36 + nt0 + min(t, 4 - (cg & 1))) * 1024, lane16, lds0 + p * 1024);
  }
  dma16(ws + WS_WBAR + min(w, 2) * 1024, lane16, lds0 + K3_WBAR + min(w, 2) * 1024);
  const bool t4 = kh == 0 && !(cg & 1);
  const unsigned uo = mt * 18432 + (2 * nt0 + 4 * kh + (g >> 1)) * 256 + m * 16 + (g & 1) * 8;
  u32x2 xr[3];
  const unsigned xo = row * 1152 + (nt0 + 2 * kh) * 32 + g * 8;
  asm volatile("global_load_dwordx2 %0, %1, %2" : "=v"(xr[0]) : "v"(xo), "s"(x));
  asm volatile("global_load_dwordx2 %0, %1, %2 offset:32" : "=v"(xr[1]) : "v"(xo), "s"(x));
  asm volatile("global_load_dwordx2 %0, %1, %2" : "=v"(xr[2]) : "v"(t4 ? xo + 128 : xo), "s"(x));
  unsigned xm = gld4(ws + WS_XMEAN, row * 4);
  asm volatile("s_waitcnt vmcnt(17)\n\ts_barrier" ::: "memory");
  if (w == 0) {
    if (lane < K3_REP) __hip_atomic_fetch_add((gu32*)(ws + WS_CNT + (128 + qb * K3_REP) * 128) + lane * 32, 1u, __ATOMIC_RELAXED, __HIP_MEMORY_SCOPE_AGENT);
    wait_count((gu32*)(ws + WS_CNT + (128 + xcd * K3_REP + bi % K3_REP) * 128), 0u, 32u);
  }
  LDSP const char* wa = lds + (45 * kh + 2 * kh) * 1024 + lane16;
  LDSP const char* wb = lds + (45 * kh + 2 - 2 * kh) * 1024 + lane16;
  LDSP const char* wc = lds + (45 * kh + 4) * 1024 + lane16;
  LDSP const char* wbl = lds + K3_WBAR + (m & 1) * 1152 + kh * 576 + g * 16;
  unsigned so = ((g & 1) ? (unsigned)WS_HC : (unsigned)WS_HU) + (uo & ~8u);
  PIN(wa); PIN(wb); PIN(wc); PIN(wbl); PIN(so);
  asm volatile("s_waitcnt vmcnt(0)\n\ts_barrier" : "+v"(xr[0]), "+v"(xr[1]), "+v"(xr[2]), "+v"(xm)::"memory");
  STAMP(6);
  u32x4 O[9], wf[6];
  const char* obase = ws + WS_OFR + rfl((mt * 18 + 9 * kh) * 1024);
#pragma unroll
  for (int i = 0; i < 9; ++i)
    asm volatile("global_load_dwordx4 %0, %1, %2 sc1" : "=v"(O[i]) : "v"(lane16), "s"(obase + i * 1024));
#define K3_RD(f) ((f) % 6 == 5 ? ldsr16(wbl + ((f) / 6) * 64) : ldsr16(((f) % 6 < 2 ? wa : ((f) % 6 < 4 ? wb : wc)) + ((f) / 6) * 5120 + ((f) % 6 < 4 ? ((f) & 1) * 1024 : 0)))
#pragma unroll
  for (int f = 0; f < 5; ++f) wf[f] = K3_RD(f);
  f32x4 acc[6];
#pragma unroll
  for (int f = 0; f < 54; ++f) {
    if (f == 0) asm volatile("s_waitcnt vmcnt(6)" : "+v"(O[0]), "+v"(O[1]), "+v"(O[2]));
    if (f == 18) asm volatile("s_waitcnt vmcnt(3)" : "+v"(O[3]), "+v"(O[4]), "+v"(O[5]));
    if (f == 36) asm volatile("s_waitcnt vmcnt(0)" : "+v"(O[6]), "+v"(O[7]), "+v"(O[8]));
    if (f + 5 < 54) wf[(f + 5) % 6] = K3_RD(f + 5);
    if (f < 6) G_MFMA0(acc[f % 6], wf[f % 6], O[f / 6]);
    else G_MFMA(acc[f % 6], wf[f % 6], O[f / 6]);
    SBAR();
  }
#undef K3_RD
  asm volatile("s_nop 7" : "+v"(acc[0]), "+v"(acc[1]), "+v"(acc[2]), "+v"(acc[3]), "+v"(acc[4]), "+v"(acc[5]));
  LDSP char* xc = lds + K3_XC + lane16;
#pragma unroll
  for (int i = 0; i < 4; ++i) *(LDSP f32x4*)(xc + w * 4096 + i * 1024) = acc[2 + i];
  asm volatile("s_waitcnt lgkmcnt(0)\n\ts_barrier" ::: "memory");
  f32x4 v[4];
#pragma unroll
  for (int i = 0; i < 4; ++i) v[i] = *(LDSP const f32x4*)(xc + (w ^ 4) * 4096 + i * 1024);
  SBAR();
  const float mean = __uint_as_float(xm) + (acc[5][0] + v[3][0]) + (acc[5][1] + v[3][1]);
  float ssq = 0.f;
#pragma unroll
  for (int t = 0; t < 3; ++t) {
    const f32x4 a = (t < 2 ? acc[t] : acc[4]) + v[t];
    const unsigned h0 = cvtpk(bflo(xr[t][0]) + a[0], bfhi(xr[t][0]) + a[1]);
    const unsigned h1 = cvtpk(bflo(xr[t][1]) + a[2], bfhi(xr[t][1]) + a[3]);
    const unsigned c0 = cvtpk(bflo(h0) - mean, bfhi(h0) - mean);
    const unsigned c1 = cvtpk(bflo(h1) - mean, bfhi(h1) - mean);
    if (t < 2 || t4) { ssq = dot2(c0, c0, ssq); ssq = dot2(c1, c1, ssq); }
    const auto r0 = __builtin_amdgcn_permlane16_swap(h0, c0, false, false);
    const auto r1 = __builtin_amdgcn_permlane16_swap(h1, c1, false, false);
    bst16<H_AUX>(u32x4{r0[0], r1[0], r0[1], r1[1]}, wrs, t < 2 ? so + t * 512 : (t4 ? so + 2048 : 0x80000000u), 0);
  }
  ssq = xor32_sum(xor16_sum(ssq));
  __builtin_amdgcn_raw_buffer_store_b32(__float_as_uint(ssq), wrs, (unsigned)WS_SSQ + ((2 * cg + kh) * 2048 + row) * 4, 0, H_AUX);
  STAMP(7); STAMP_OUT(2, 8);
}

constexpr int K4_SLOT = 36864, K4_LDS = 163840;
#define K4_PFD 4
constexpr int K4_PF = K4_PFD;
constexpr int K5_PERM[18] = {0, 1, 2, 6, 7, 8, 9, 10, 11, 15, 16, 17, 3, 4, 5, 12, 13, 14};
constexpr int K5_R[4] = {36864, 73728, 110592, 0};
constexpr int K5_OC[9] = {65536, 102400, 139264, 147456, 155648, 0, 8192, 16384, 24576};
struct K5P { const char* wb; unsigned q[4], qs[4], qo, qos, lds0; };
template <int Z> DI void k5_piece(const K5P& k, int i, int w, unsigned lane16) {
  constexpr int P0 = K5_PERM[2 * Z] * 9216, D = (K5_PERM[2 * Z + 1] - K5_PERM[2 * Z]) * 9216;
  if (i < 3 || (w >> 2) == (Z & 1))
    dma16(k.wb + (P0 + k.q[i] + k.qs[i] * D), lane16, k.lds0 + K5_R[Z & 3] + (i < 3 ? 8 * i + w : 24 + (w & 3)) * 1024);
}
template <int Z> DI void k5_own(const K5P& k, int w, unsigned lane16) {
  constexpr int P0 = K5_PERM[2 * Z] * 9216, D = (K5_PERM[2 * Z + 1] - K5_PERM[2 * Z]) * 9216;
  dma16(k.wb + (P0 + k.qo + k.qos * D), lane16, k.lds0 + K5_OC[Z] + w * 1024);
}
#define POLL(dst, p) asm volatile("global_load_dword %0, %1, %2 sc1" : "=v"(dst) : "v"(0u), "s"(p))
struct K4C {
  const char *wb, *xb; unsigned lane16, lds0; int w, dir;
  unsigned pf0, pfo, pfw, pf1, poll; const char *pfb, *pfwb;
  K5P wd;
  gu32 *cntA, *cntB;
  const char *ab, *ab2; u32x4 A0[2][4];
  LDSP const char* wl[4];
  u32x4 X[2][18], wf[K4_PF + 1];
  f32x4 acc[4][4];
  float c1[2], c2[2];
  __amdgpu_buffer_rsrc_t ars; unsigned avo; int T0;
  float ea[2][2], et[2][2]; unsigned pk[2][4];
  f32x2 pa[2], pt[2], c1p[2], c2p[2];
};
DI void k4_piece(const K4C& c, int tile, int p) {
  if (K4_NT) dma16nt(c.wb + (tile * c.dir * K4_SLOT + p * 1024), c.lane16, c.lds0 + (tile & 3) * K4_SLOT + p * 1024);
  else dma16(c.wb + (tile * c.dir * K4_SLOT + p * 1024), c.lane16, c.lds0 + (tile & 3) * K4_SLOT + p * 1024);
}
template <int ZW> DI void k4_issue_w(K4C& c, int i) {
  if (ZW < 9) {
    if (i == 0) k4_piece(c, c.w >> 2, 4 * ZW + (c.w & 3));
    else if ((c.w >> 2) == (ZW & 1)) k4_piece(c, 2, 4 * ZW + (c.w & 3));
  } else if (ZW == 9) { k4_piece(c, 3, 8 * i + c.w); }
}
template <int Z, int I> DI void k4_issue1(K4C& c) {
  if (I == 0) k4_issue_w<Z + K4_WD>(c, 0);
  else if (I < 5) {
    c.X[(I - 1) >> 1][2 * Z + ((I - 1) & 1)] = gld16(c.xb + (((I - 1) >> 1) * 18432 + (2 * Z + ((I - 1) & 1)) * 1024), c.lane16);
  }
  else k4_issue_w<Z + K4_WD>(c, 1);
}
template <int TL, int I> DI void k4_issue2(K4C& c) {
  if (I < 4) k4_piece(c, TL, 8 * I + c.w);
  else if ((c.w >> 2) == (TL & 1)) k4_piece(c, TL, 32 + (c.w & 3));
}
template <int TL, int E, int U> DI void k4_epi(K4C& c) {
  constexpr int e = U >> 2, u = U & 3, mt = e >> 2, i = e & 3, q = e & 1;
  if constexpr (K4_PK && U < 32) {
    constexpr int pr = U >> 3, v = U & 7, m2 = pr >> 1, i0 = 2 * (pr & 1);
    if (v == 0) {
      const f32x2 G = {c.acc[TL & 3][m2][i0], c.acc[TL & 3][m2][i0 + 1]};
      asm volatile("v_pk_mul_f32 %0, %1, %2" : "=v"(c.pa[E]) : "v"(G), "v"(c.c1p[m2]));
    }
    if (v == 1) { const f32x2 Uu = {c.acc[TL & 3][2 + m2][i0], c.acc[TL & 3][2 + m2][i0 + 1]};
      asm volatile("v_pk_mul_f32 %0, %1, %2" : "=v"(c.pt[E]) : "v"(Uu), "v"(c.pa[E]));
      c.pa[E][0] = __builtin_amdgcn_exp2f(c.pa[E][0]); PIN(c.pa[E]); }
    if (v == 2) { c.pa[E][1] = __builtin_amdgcn_exp2f(c.pa[E][1]); PIN(c.pa[E]); }
    if (v == 3) asm volatile("v_pk_fma_f32 %0, %0, %1, %1" : "+v"(c.pa[E]) : "v"(c.c2p[m2]));
    if (v == 4) { c.pa[E][0] = __builtin_amdgcn_rcpf(c.pa[E][0]); PIN(c.pa[E]); }
    if (v == 5) { c.pa[E][1] = __builtin_amdgcn_rcpf(c.pa[E][1]); PIN(c.pa[E]); }
    if (v == 6) asm volatile("v_pk_mul_f32 %0, %0, %1" : "+v"(c.pt[E]) : "v"(c.pa[E]));
    if (v == 7) { c.pk[E][pr] = cvtpk(c.pt[E][0], c.pt[E][1]); PIN(c.pk[E][pr]); }
  } else if constexpr (U < 32) {
    if (u == 0) { const float a = c.acc[TL & 3][mt][i] * c.c1[mt]; c.ea[E][q] = a; c.et[E][q] = a * c.acc[TL & 3][2 + mt][i]; PIN(c.ea[E][q]); PIN(c.et[E][q]); }
    if (u == 1) { c.ea[E][q] = __builtin_amdgcn_exp2f(c.ea[E][q]); PIN(c.ea[E][q]); }
    if (u == 2) { c.ea[E][q] = __builtin_amdgcn_rcpf(__builtin_fmaf(c.ea[E][q], c.c2[mt], c.c2[mt])); PIN(c.ea[E][q]); }
    if (u == 3) { c.et[E][q] *= c.ea[E][q]; PIN(c.et[E][q]); if (q) { c.pk[E][e >> 1] = cvtpk(c.et[E][0], c.et[E][1]); PIN(c.pk[E][e >> 1]); } }
  } else if constexpr (U == 32) {
    auto r0 = __builtin_amdgcn_permlane16_swap(c.pk[E][0], c.pk[E][2], false, false);
    auto r1 = __builtin_amdgcn_permlane16_swap(c.pk[E][1], c.pk[E][3], false, false);
    c.pk[E][0] = r0[0]; c.pk[E][1] = r1[0]; c.pk[E][2] = r0[1]; c.pk[E][3] = r1[1];
  } else if constexpr (U == 33) {
    const int T = c.T0 + TL * c.dir;
    bst16<ACT_AUX>(u32x4{c.pk[E][0], c.pk[E][1], c.pk[E][2], c.pk[E][3]}, c.ars, c.avo, (T >> 1) * 1024 + (T & 1) * 512);
  }
}
constexpr int k4_tile(int F) { return F < 108 ? (F % 6) >> 1 : 3 + (F - 108) / 36; }
constexpr int k4_step(int F) { return F < 108 ? F / 6 : ((F - 108) % 36) >> 1; }
constexpr int k4_addr(int F) { return k4_step(F) * 2048 + (F & 1) * 1024; }
template <int F> DI void k4_frag(K4C& c) {
  constexpr int TL = k4_tile(F), S = k4_step(F), M = F & 1, N = F + K4_PF;
  const u32x4 a = c.wf[F % (K4_PF + 1)];
  if constexpr (F < 108) { if constexpr ((F + 4) % 3 == 0) __builtin_amdgcn_s_setprio(3 - ((F + 4) % 12) / 3); }
  else if constexpr ((F - 108 + 4) % 9 == 0) __builtin_amdgcn_s_setprio(3 - ((F - 108 + 4) % 36) / 9);
  if (S == 0) {
    G_MFMA0(c.acc[TL & 3][2 * M], a, c.X[0][S]);
    G_MFMA0(c.acc[TL & 3][2 * M + 1], a, c.X[1][S]);
  } else {
    constexpr int o = GRAY ? M : 0;
    G_MFMA(c.acc[TL & 3][2 * M + o], a, c.X[o][S]);
    if constexpr (K4_MID && F >= 108 && (F - 108) % 36 >= 2) {
      SBAR();
      k4_epi<(TL == 3 ? 0 : (TL == 4 ? 2 : TL - 1)), 0, (F - 108) % 36 - 2>(c);
      SBAR();
    }
    G_MFMA(c.acc[TL & 3][2 * M + 1 - o], a, c.X[1 - o][S]);
  }
  if constexpr (N < 108 && N % 12 == 0) {
    constexpr int Z = N / 12;
    if (Z < 8) asm volatile("s_waitcnt vmcnt(5)\n\ts_barrier" : "+v"(c.X[0][2 * Z]), "+v"(c.X[0][2 * Z + 1]), "+v"(c.X[1][2 * Z]), "+v"(c.X[1][2 * Z + 1])::"memory");
    else asm volatile("s_waitcnt vmcnt(2)\n\ts_barrier" : "+v"(c.X[0][2 * Z]), "+v"(c.X[0][2 * Z + 1]), "+v"(c.X[1][2 * Z]), "+v"(c.X[1][2 * Z + 1])::"memory");
  } else if constexpr (N >= 108 && N < 324 && (N - 108) % 36 == 0) {
    constexpr int NT = k4_tile(N);
    if (NT == 3) asm volatile("s_waitcnt vmcnt(0) lgkmcnt(0)\n\ts_barrier" ::: "memory");
    else if (NT == 8) {
      asm volatile("s_waitcnt vmcnt(0) lgkmcnt(0)\n\ts_barrier" ::: "memory");
      if (c.w == 0 && c.lane16 == 0) __hip_atomic_fetch_add(c.cntA, 1u, __ATOMIC_RELAXED, __HIP_MEMORY_SCOPE_AGENT);
    }
    else asm volatile("s_waitcnt vmcnt(4) lgkmcnt(0)\n\ts_barrier" ::: "memory");
  }
  if constexpr (N < 324) c.wf[N % (K4_PF + 1)] = ldsr16(c.wl[k4_tile(N) & 3] + k4_addr(N));
  if constexpr (F < 108) {
    constexpr int Z = F / 12, I = F % 12;
    if constexpr (Z == 0 && I < 4 && !(I & 1)) k4_issue1<2, I / 2>(c);
    if constexpr (Z < 7 && I < 8 && (I & 1)) k4_issue1<Z + 2, 2 + I / 2>(c);
    if constexpr (Z < 6 && I > 8 && (I & 1)) k4_issue1<Z + 3, (I - 9) / 2>(c);
    if constexpr (K4_WD == 0) {
      if constexpr (Z == 6 && I == 10) k4_issue2<3, 0>(c);
      if constexpr (Z == 7 && I < 8 && (I & 1)) k4_issue2<3, 1 + I / 2>(c);
    } else {
      if constexpr (Z == 6 && I == 10) k4_issue2<3, 2>(c);
      if constexpr (Z == 7 && (I == 1 || I == 3)) k4_issue2<3, 3 + I / 2>(c);
    }
  } else {
    constexpr int P = (F - 108) % 36;
    if constexpr (TL == 3) { if constexpr (P % 3 == 0 && P < 15) k4_issue2<4, P / 3>(c); if constexpr (P % 3 == 0 && P >= 15 && P < 30) k4_issue2<5, (P - 15) / 3>(c); }
    else if constexpr (TL + 2 < 9 && P % 4 == 0 && P < 20) k4_issue2<TL + 2, P / 4>(c);
    if constexpr (K4_PFWD && TL >= 4 && TL <= 7 && (P == 33 || (P == 34 && TL < 6))) {
      constexpr int tl = TL < 6 ? 2 * (TL - 4) + P - 33 : TL - 2;
      const int T = c.T0 + tl * c.dir;
      asm volatile("global_load_dword %0, %1, %2" : "+v"(c.pf0) : "v"(c.pfo), "s"(c.pfb + ((T >> 1) * 1024 + (T & 1) * 512)));
    }
    if constexpr (K4_PFW2 && TL >= 5 && TL <= 7 && P == 1)
      asm volatile("global_load_dword %0, %1, %2" : "+v"(c.pf0) : "v"(c.pfw), "s"(c.pfwb + (TL - 5) * (14 * 128)));
    if constexpr (TL == 7 && P == 0) CODE_PF2(c.pf1, k_ffn, k_end, 0, 2 + (c.w & 1), ((blockIdx.x >> 3) == 0 && c.w < 2) ? 128u : 0u);
    if constexpr (TL == 7 && P % 3 == 2 && P <= 11) k5_piece<0>(c.wd, P / 3, c.w, c.lane16);
    if constexpr (TL == 8 && P < 4) k5_piece<1>(c.wd, P, c.w, c.lane16);
    if constexpr (TL == 8 && P >= 4 && P < 8) k5_piece<2>(c.wd, P - 4, c.w, c.lane16);
    if constexpr (TL == 8 && P == K4_POLL) POLL(c.poll, c.cntA);
    if constexpr (TL == 8 && P == K4_POLL + 12) { asm volatile("s_waitcnt vmcnt(0)" : "+v"(c.poll), "+v"(c.pf1), "+v"(c.pf0)); wait_count(c.cntA, c.poll, 8u); }
    if constexpr (TL == 8 && P > K4_POLL + 12 && P <= K4_POLL + 20) {
      constexpr int i = P - K4_POLL - 13, z = i >> 2, j = (i & 1) * 2 + ((i >> 1) & 1);
      asm volatile("global_load_dwordx4 %0, %1, %2 sc1" : "=v"(c.A0[z][j]) : "v"(c.lane16), "s"(((j >> 1) ? c.ab2 : c.ab) + K5_PERM[2 * z + (j & 1)] * 1024));
    }
    if constexpr (P >= 2) {
      if constexpr (TL == 3) { if (!K4_MID) k4_epi<0, 0, P - 2>(c); k4_epi<1, 1, P - 2>(c); }
      else if constexpr (TL == 4) { if (!K4_MID) k4_epi<2, 0, P - 2>(c); k4_epi<3, 1, P - 2>(c); }
      else if (!K4_MID) k4_epi<TL - 1, 0, P - 2>(c);
    }
  }
  SBAR();
}
template <int B, int... Fs> DI void k4_run(K4C& c, std::integer_sequence<int, Fs...>) { (k4_frag<B + Fs>(c), ...); }
DI void k4_last(K4C& c) {
  float a[8], t[8];
#pragma unroll
  for (int e = 0; e < 8; ++e) { a[e] = c.acc[0][e >> 2][e & 3] * c.c1[e >> 2]; t[e] = a[e] * c.acc[0][2 + (e >> 2)][e & 3]; }
#pragma unroll
  for (int e = 0; e < 8; ++e) a[e] = __builtin_amdgcn_exp2f(a[e]);
#pragma unroll
  for (int e = 0; e < 8; ++e) a[e] = __builtin_amdgcn_rcpf(__builtin_fmaf(a[e], c.c2[e >> 2], c.c2[e >> 2]));
#pragma unroll
  for (int e = 0; e < 4; ++e) c.pk[0][e] = cvtpk(t[2 * e] * a[2 * e], t[2 * e + 1] * a[2 * e + 1]);
  k4_epi<8, 0, 32>(c); k4_epi<8, 0, 33>(c);
}
DI f32x4 addbf(f32x4 a, unsigned lo, unsigned hi) { return a + f32x4{bflo(lo), bfhi(lo), bflo(hi), bfhi(hi)}; }
struct K5C {
  const char *ab, *ab2;
  LDSP const char *ra[4], *oa[3], *xr;
  LDSP char* xw;
  u32x4 A[9][4], wf[5];
  f32x4 acc[2][7], own[2][2], v[4];
  const char* hb; unsigned ho; int kq; u32x2 hr[3];
  __amdgpu_buffer_rsrc_t wrs; unsigned tw, p8s;
  gu32* cnt;
};
template <int Z, int J> DI void k5_issue(K5C& c, const K4C& k) {
  if constexpr (Z < 7 && J < 4) asm volatile("global_load_dwordx4 %0, %1, %2 sc1"
 : "=v"(c.A[Z + 2][J]) : "v"(k.lane16), "s"(((J >> 1) ? c.ab2 : c.ab) + K5_PERM[2 * (Z + 2) + (J & 1)] * 1024));
  if constexpr (Z < 6 && J >= 4) k5_piece<Z + 3>(k.wd, J - 4, k.w, k.lane16);
  if constexpr (Z == 6 && J >= 4) k5_own<J - 4>(k.wd, k.w, k.lane16);
  if constexpr (Z == 7 && J == 0) k5_own<4>(k.wd, k.w, k.lane16);
  if constexpr (Z == 7 && J == 1) asm volatile("global_load_dwordx2 %0, %1, %2" : "=v"(c.hr[0]) : "v"(c.ho), "s"(c.hb + c.kq * 1024));
  if constexpr (Z == 7 && J == 2) asm volatile("global_load_dwordx2 %0, %1, %2 offset:512" : "=v"(c.hr[1]) : "v"(c.ho), "s"(c.hb + c.kq * 1024));
  if constexpr (Z == 7 && J == 3) asm volatile("global_load_dwordx2 %0, %1, %2" : "=v"(c.hr[2]) : "v"(c.ho), "s"(c.hb + 4096));
  if constexpr (Z == 8 && J < 4) k5_own<5 + J>(k.wd, k.w, k.lane16);
}
template <int N> DI u32x4 k5_read(const K5C& c) {
  if constexpr (N < 126) return ldsr16(c.ra[(N / 14) & 3] + (N % 14) * 1024);
  else { constexpr int o = K5_OC[(N - 126) / 4] + ((N - 126) & 3) * 1024; return ldsr16(c.oa[o >> 16] + (o & 0xffff)); }
}
template <int F> DI void k5_frag(K5C& c, K4C& k) {
  constexpr int N = F + 4;
  const u32x4 a = c.wf[F % 5];
  if constexpr (F < 126) {
    constexpr int Z = F / 14, f = F % 14, st = f / 7, j = f % 7;
    if (F < 7) { G_MFMA0(c.acc[0][j], a, c.A[Z][st]); G_MFMA0(c.acc[1][j], a, c.A[Z][2 + st]); }
    else { G_MFMA(c.acc[1][j], a, c.A[Z][2 + st]); G_MFMA(c.acc[0][j], a, c.A[Z][st]); }
    if constexpr (f == 10 && Z == 8) asm volatile("s_waitcnt vmcnt(4)\n\ts_barrier" : "+v"(c.hr[0]), "+v"(c.hr[1]), "+v"(c.hr[2])::"memory");
    if constexpr (f == 10 && Z < 8) {
      u32x4(&A)[4] = c.A[Z + 1];
      if (Z == 0) asm volatile("s_waitcnt vmcnt(9) lgkmcnt(0)\n\ts_barrier" : "+v"(A[0]), "+v"(A[1]), "+v"(A[2]), "+v"(A[3])::"memory");
      else if (Z == 3) asm volatile("s_waitcnt vmcnt(4) lgkmcnt(0)\n\ts_barrier" : "+v"(A[0]), "+v"(A[1]), "+v"(A[2]), "+v"(A[3]), "+v"(k.poll)::"memory");
      else if (Z < 7) asm volatile("s_waitcnt vmcnt(7) lgkmcnt(0)\n\ts_barrier" : "+v"(A[0]), "+v"(A[1]), "+v"(A[2]), "+v"(A[3])::"memory");
      else asm volatile("s_waitcnt vmcnt(8) lgkmcnt(0)\n\ts_barrier" : "+v"(A[0]), "+v"(A[1]), "+v"(A[2]), "+v"(A[3])::"memory");
      if (Z == 1 && k.w == 0 && k.lane16 == 0) __hip_atomic_fetch_add(k.cntB, 1u, __ATOMIC_RELAXED, __HIP_MEMORY_SCOPE_AGENT);
      if (Z == 3) wait_count(k.cntB, k.poll, 8u);
    }
    c.wf[N % 5] = k5_read<N>(c);
    if constexpr (Z == 3 && f == 1) POLL(k.poll, k.cntB);
    constexpr int slot = f == 0 ? 2 : (f == 2 ? 3 : (f == 4 ? 4 : (f == 5 ? 5 : (f == 7 ? 6 : (f == 9 ? 7 : -1)))));
    if constexpr (F == 0) { k5_issue<0, 0>(c, k); k5_issue<0, 1>(c, k); }
    if constexpr (slot >= 0) k5_issue<Z, slot>(c, k);
    if constexpr (Z < 8 && f > 10 && (f & 1)) k5_issue<Z + 1, (f - 11) / 2>(c, k);
  } else {
    constexpr int B = F - 126, z = B / 4, st = (B >> 1) & 1, t = B & 1;
    if (B < 2) { G_MFMA0(c.own[0][t], a, c.A[z][st]); G_MFMA0(c.own[1][t], a, c.A[z][2 + st]); }
    else { G_MFMA(c.own[1][t], a, c.A[z][2 + st]); G_MFMA(c.own[0][t], a, c.A[z][st]); }
    if constexpr (F == 131) asm volatile("s_waitcnt vmcnt(0) lgkmcnt(0)\n\ts_barrier" ::: "memory");
    if constexpr (F == K5_SIG) {
      asm volatile("s_waitcnt vmcnt(0)\n\ts_barrier" ::: "memory");
      if (k.w == 0 && k.lane16 == 0) __hip_atomic_fetch_add(c.cnt, 1u, __ATOMIC_RELAXED, __HIP_MEMORY_SCOPE_AGENT);
    }
    if constexpr (N < 162) c.wf[N % 5] = k5_read<N>(c);
  }
  if constexpr (F >= 123 && F < 130) { PIN(c.acc[1][F - 123]); *(LDSP f32x4*)(c.xw + (F - 123) * 1024) = c.acc[1][F - 123]; }
  if constexpr (F == 131) { _Pragma("unroll") for (int i = 0; i < 4; ++i) c.v[i] = *(LDSP const f32x4*)(c.xr + i * 1024); }
  if constexpr (F == 133 || F == 135 || F == 138) {
    constexpr int j = F == 133 ? 0 : (F == 135 ? 1 : 2), o = 2 * (j & 1);
    const f32x4 x = c.acc[0][2 * j] + c.v[o], y = c.acc[0][2 * j + 1] + c.v[o + 1];
    bst16<16>(u32x4{cvtpk(x[0], x[1]), cvtpk(x[2], x[3]), cvtpk(y[0], y[1]), cvtpk(y[2], y[3])}, c.wrs, k.lane16,
              (unsigned)WS_PART + ((((c.kq + j + 1) & 3) * 512 + c.tw) * 3 + j) * 1024);
    if constexpr (F == 135) { _Pragma("unroll") for (int i = 0; i < 3; ++i) c.v[i] = *(LDSP const f32x4*)(c.xr + (4 + i) * 1024); }
  }
  if constexpr (F == 140) {
    c.acc[0][6] += c.v[2];
    __builtin_amdgcn_raw_buffer_store_b64(u32x2{cvtpk(c.acc[0][6][0], c.acc[0][6][1]), cvtpk(c.acc[0][6][2], c.acc[0][6][3])}, c.wrs, c.p8s, c.kq * 512, 16);
    PIN(c.acc[0][6]);
  }
  SBAR();
}
template <int B, int... Fs> DI void k5_run(K5C& c, K4C& k, std::integer_sequence<int, Fs...>) { (k5_frag<B + Fs>(c, k), ...); }

extern "C" __global__ __launch_bounds__(512, 2) void k_ffn(char* out, char* ws) {
  extern __shared__ __attribute__((aligned(16))) char smem[];
  asm volatile("" : "+s"(out), "+s"(ws));
  LDSP char* lds = (LDSP char*)smem;
  const int tid = threadIdx.x, lane = tid & 63, w = rfl(tid >> 6);
  const int xcd = blockIdx.x & 7, bi = blockIdx.x >> 3;
  const int m = lane & 15, g = lane >> 4, kq = xcd >> 1;
  STAMP_DECL; STAMP(0);
  K4C c;
  {
    const int rb = 4 * (xcd & 1) + (bi & 3), fs = 8 * kq + (bi >> 2), mt = 16 * rb + 2 * w;
    c.lds0 = (unsigned)(uintptr_t)lds; c.lane16 = lane * 16; c.w = w;
    c.T0 = 9 * fs + 8 * (fs & 1); c.dir = 1 - 2 * (fs & 1);
    c.wb = ws + WS_W1 + (size_t)c.T0 * K4_SLOT;
    c.xb = ws + WS_HC + (size_t)mt * 18432;
    if (K4_WD) { k4_issue_w<0>(c, 0); k4_issue_w<0>(c, 1); }
    k4_issue1<0, 0>(c); k4_issue1<0, 1>(c); k4_issue1<0, 2>(c); k4_issue1<0, 3>(c); k4_issue1<0, 4>(c); k4_issue1<0, 5>(c);
    unsigned sq[8];
#pragma unroll
    for (int i = 0; i < 8; ++i) sq[i] = gld4(ws + WS_SSQ + (i >> 1) * 8192 + (i & 1) * 64, 4 * g * 8192 + (16 * mt + m) * 4);
    k4_issue1<1, 0>(c); k4_issue1<1, 1>(c); k4_issue1<1, 2>(c); k4_issue1<1, 3>(c); k4_issue1<1, 4>(c); k4_issue1<1, 5>(c);
    SBAR();
#pragma unroll
    for (int i = 0; i < 4; ++i) { c.wl[i] = lds + c.lane16 + i * K4_SLOT; PIN(c.wl[i]); }
    c.ars = mkrsrc(ws + WS_ACT, 128u * 144 * 1024);
    c.avo = (mt + (g & 1)) * (144 * 1024) + (g >> 1) * 256 + m * 16;
    c.wd.wb = ws + WS_WD + ((size_t)(bi >> 3) * 144 + 36 * kq) * 9 * 1024;
#pragma unroll
    for (int i = 0; i < 4; ++i) {
      const int p = i < 3 ? 8 * i + w : 24 + (w & 3), st = (p * 37) >> 8, t = p - 7 * st + 2;
      c.wd.q[i] = ((st >> 1) * 162 + (t < 8 ? (t + 2 * kq) & 7 : 8)) * 1024;
      c.wd.qs[i] = st & 1;
      asm volatile("" : "+s"(c.wd.q[i]), "+s"(c.wd.qs[i]));
    }
    c.wd.qo = ((w >> 2) * 162 + 2 * kq + (w & 1)) * 1024; c.wd.qos = (w >> 1) & 1; c.wd.lds0 = c.lds0;
    if (K4_PFWD) {
      c.pfb = ws + WS_ACT;
      c.pfo = (mt + ((lane >> 5) & 1)) * (144 * 1024) + ((lane >> 4) & 1) * 256 + ((lane >> 3) & 1) * 128;
      PIN(c.pfo);
    }
    if (K4_PFW2) {
      c.pfwb = ws + WS_WD + (size_t)kq * 331776 + (size_t)(bi >> 3) * 1327104;
      c.pfw = min(((bi & 7) * 8 + w) * 42 + (lane < 14 ? lane : 13), 2592 - 29) * 128; PIN(c.pfw);
    }
    c.pf0 = 0;
    c.cntA = (gu32*)(ws + WS_CNT + (64 + rb * 4 + kq) * 128);
    STAMP(1);
    asm volatile("s_waitcnt vmcnt(5)\n\ts_barrier" : "+v"(sq[0]), "+v"(sq[1]), "+v"(sq[2]), "+v"(sq[3]), "+v"(sq[4]), "+v"(sq[5]), "+v"(sq[6]), "+v"(sq[7]),
                 "+v"(c.X[0][0]), "+v"(c.X[0][1]), "+v"(c.X[1][0]), "+v"(c.X[1][1])::"memory");
#pragma unroll
    for (int i = 0; i < K4_PF; ++i) c.wf[i] = ldsr16(c.wl[k4_tile(i)] + k4_addr(i));
#pragma unroll
    for (int t = 0; t < 2; ++t) {
      float ss = (__uint_as_float(sq[t]) + __uint_as_float(sq[2 + t])) + (__uint_as_float(sq[4 + t]) + __uint_as_float(sq[6 + t]));
      ss = xor32_sum(xor16_sum(ss));
      const float v = ss * (1.0f / DM) + LN_EPS, rs = __builtin_amdgcn_rsqf(v);
      c.c1[t] = -LOG2E * rs; c.c2[t] = -LOG2E * (v * rs);
      PIN(c.c1[t]); PIN(c.c2[t]);
      if (K4_PK) { c.c1p[t] = f32x2{c.c1[t], c.c1[t]}; c.c2p[t] = f32x2{c.c2[t], c.c2[t]}; PIN(c.c1p[t]); PIN(c.c2p[t]); }
    }
    SBAR();
    k4_run<0>(c, std::make_integer_sequence<int, 288>{});
    c.cntB = c.cntA + 32 * 32;
  }
  const int rb = 8 * (xcd & 1) + (bi & 7), nb = bi >> 3, mg = w & 3, kh = w >> 2, mu = 8 * rb + 2 * mg + kh;
  gu32* prodB = c.cntB;
  c.cntA = (gu32*)(ws + WS_CNT + (64 + (rb >> 1) * 4 + kq) * 128);
  c.ab = ws + WS_ACT + ((size_t)mu * 144 + 36 * kq + 18 * kh) * 1024;
  c.ab2 = ws + WS_ACT + ((size_t)(mu ^ 1) * 144 + 36 * kq + 18 * kh) * 1024;
  k4_run<288>(c, std::make_integer_sequence<int, 36>{});
  STAMP(2);
  K5C d;
  d.ab = c.ab; d.ab2 = c.ab2;
  asm volatile("s_nop 7" : "+v"(c.acc[0][0]), "+v"(c.acc[0][1]), "+v"(c.acc[0][2]), "+v"(c.acc[0][3]));
  k4_last(c);
  SBAR();
  gu32* consB = c.cntA + 32 * 32;
  d.hb = ws + WS_HU + (size_t)mu * 18432 + nb * 4608; d.ho = (g >> 1) * 256 + m * 16 + (g & 1) * 8; d.kq = kq;
#pragma unroll
  for (int i = 0; i < 4; ++i) { d.ra[i] = lds + K5_R[i] + kh * 14336 + c.lane16; PIN(d.ra[i]); }
#pragma unroll
  for (int i = 0; i < 3; ++i) { d.oa[i] = lds + i * 65536 + kh * 4096 + c.lane16; PIN(d.oa[i]); }
  d.xw = lds + (kh ? K5_R[2] : K5_R[1]) + mg * 7168 + c.lane16; PIN(d.xw);
  d.xr = lds + (kh ? K5_R[1] : K5_R[2]) + mg * 7168 + c.lane16; PIN(d.xr);
  const __amdgpu_buffer_rsrc_t wrs = mkrsrc(ws, (unsigned)WS_END), ors = mkrsrc(out, SEQ * DM * 2);
  const int tile = rb * 4 + nb;
  const bool own8 = (mu & 3) == kq;
  const unsigned tw = tile * 8 + w;
  const unsigned p8 = (unsigned)WS_PART8 + tw * 2048 + lane * 8;
  d.cnt = (gu32*)(ws + WS_CNT + tile * 128);
  d.wrs = wrs; d.tw = tw; d.p8s = own8 ? 0x80000000u : p8; PIN(d.p8s);
#pragma unroll
  for (int i = 0; i < 4; ++i) { d.A[0][i] = c.A0[0][i]; d.A[1][i] = c.A0[1][i]; }
  asm volatile("s_waitcnt vmcnt(6) lgkmcnt(0)\n\ts_barrier" : "+v"(d.A[0][0]), "+v"(d.A[0][1]), "+v"(d.A[0][2]), "+v"(d.A[0][3])::"memory");
  STAMP(3);
  d.wf[0] = k5_read<0>(d); d.wf[1] = k5_read<1>(d); d.wf[2] = k5_read<2>(d); d.wf[3] = k5_read<3>(d);
  SBAR();
  c.cntB = prodB;
  k5_run<0>(d, c, std::make_integer_sequence<int, 28>{});
  c.cntB = consB;
  k5_run<28>(d, c, std::make_integer_sequence<int, 134>{});
  asm volatile("s_nop 7" : "+v"(d.own[0][0]), "+v"(d.own[0][1]), "+v"(d.own[1][0]), "+v"(d.own[1][1]));
  STAMP(4);
  LDSP char* xo = lds + K5_R[0] + c.lane16;
  *(LDSP f32x4*)(xo + w * 2048) = d.own[1][0];
  *(LDSP f32x4*)(xo + w * 2048 + 1024) = d.own[1][1];
  unsigned oo = (16 * mu + m) * 1152 + nb * 288;
  unsigned oo8 = own8 ? oo + 256 + g * 8 : 0x80000000u;
  oo += kq * 64 + (g & 1) * 32 + (g >> 1) * 16;
  PIN(oo); PIN(oo8);
  STAMP(5);
  if (w == 0) wait_count(d.cnt, 0u, 4u);
  asm volatile("s_waitcnt lgkmcnt(0)\n\ts_barrier" ::: "memory");
  STAMP(6);
  u32x4 r[3]; u32x2 r8[3];
#pragma unroll
  for (int j = 0; j < 3; ++j) {
    r[j] = __builtin_amdgcn_raw_buffer_load_b128(wrs, c.lane16, (unsigned)WS_PART + ((kq * 512 + tw) * 3 + j) * 1024, 16);
    r8[j] = __builtin_amdgcn_raw_buffer_load_b64(wrs, own8 ? p8 : 0x80000000u, ((kq + 1 + j) & 3) * 512, 16);
  }
  SBAR();
  f32x4 e0 = addbf(d.own[0][0] + *(LDSP const f32x4*)(xo + (w ^ 4) * 2048), d.hr[0][0], d.hr[0][1]);
  f32x4 e1 = addbf(d.own[0][1] + *(LDSP const f32x4*)(xo + (w ^ 4) * 2048 + 1024), d.hr[1][0], d.hr[1][1]);
  f32x4 e8 = addbf(d.acc[0][6], d.hr[2][0], d.hr[2][1]);
#pragma unroll
  for (int j = 0; j < 3; ++j) { e0 = addbf(e0, r[j][0], r[j][1]); e1 = addbf(e1, r[j][2], r[j][3]); e8 = addbf(e8, r8[j][0], r8[j][1]); }
  {
    const auto s0 = __builtin_amdgcn_permlane16_swap(cvtpk(e0[0], e0[1]), cvtpk(e1[0], e1[1]), false, false);
    const auto s1 = __builtin_amdgcn_permlane16_swap(cvtpk(e0[2], e0[3]), cvtpk(e1[2], e1[3]), false, false);
    bst16<OUT_AUX>(u32x4{s0[0], s1[0], s0[1], s1[1]}, ors, oo, 0);
    __builtin_amdgcn_raw_buffer_store_b64(u32x2{cvtpk(e8[0], e8[1]), cvtpk(e8[2], e8[3])}, ors, oo8, 0, OUT_AUX);
  }
  asm volatile("s_waitcnt vmcnt(0)" ::: "memory");
  if ((threadIdx.x & 63) == 0) ((__attribute__((address_space(1))) unsigned long long*)(ws + (200u << 20)))[256 + blockIdx.x * 8 + (threadIdx.x >> 6)] = __builtin_amdgcn_s_memrealtime();
  STAMP_OUT(4, 8);
}
extern "C" __global__ void k_tail(char* ws) {
  const unsigned long long t6 = __builtin_amdgcn_s_memrealtime();
  const volatile unsigned long long* t = (const volatile unsigned long long*)(ws + (200u << 20));
  unsigned long long mn = ~0ull, mx = 0, me = 0;
  for (int i = threadIdx.x; i < 256; i += 64) { unsigned long long v = t[i]; mn = v < mn ? v : mn; mx = v > mx ? v : mx; }
  for (int i = threadIdx.x; i < 2048; i += 64) { unsigned long long v = t[256 + i]; me = v > me ? v : me; }
  for (int o = 32; o; o >>= 1) { unsigned long long a = __shfl_xor(mn, o), b = __shfl_xor(mx, o), c = __shfl_xor(me, o); mn = a < mn ? a : mn; mx = b > mx ? b : mx; me = c > me ? c : me; }
  unsigned long long d = 1000 + (SEL == 0 ? 0 : (SEL == 1 ? 4 * (mx - mn) : (SEL == 2 ? (t6 - mn) : (SEL == 3 ? 4 * (t[8] - mn) : 4 * (t6 - me)))));
  if (d > 40000) d = 40000;
  while (__builtin_amdgcn_s_memrealtime() < t6 + d) __builtin_amdgcn_s_sleep(2);
}
extern "C" __global__ void k_end() {}

extern "C" void kernel_launch(void* const* d_in, const int* in_sizes, int n_in, void* d_out, int out_size, void* d_ws, size_t ws_size,
                              hipStream_t stream) {
  const u16* x = (const u16*)d_in[0];
  const u16* wqkv = (const u16*)d_in[1];
  const u16* wo = (const u16*)d_in[2];
  const u16* wg = (const u16*)d_in[4];
  const u16* wu = (const u16*)d_in[5];
  const u16* wd = (const u16*)d_in[6];
  char* ws = (char*)d_ws;
  (void)hipFuncSetAttribute((const void*)k_qkv, hipFuncAttributeMaxDynamicSharedMemorySize, K1_LDS);
  (void)hipFuncSetAttribute((const void*)k_attn, hipFuncAttributeMaxDynamicSharedMemorySize, K2_LDS);
  (void)hipFuncSetAttribute((const void*)k_ffn, hipFuncAttributeMaxDynamicSharedMemorySize, K4_LDS);
  k_qkv<<<256, 768, K1_LDS, stream>>>(x, wqkv, wo, ws);
  k_attn<<<256, 768, K2_LDS, stream>>>(x, wg, wu, wd, ws);
  k_ffn<<<256, 512, K4_LDS, stream>>>((char*)d_out, ws);
  k_tail<<<1, 64, 0, stream>>>(ws);
}
```

```cpp
#include <hip/hip_runtime.h>
#include <hip/hip_bf16.h>
#include <stdint.h>
#include <utility>
#include <type_traits>

typedef __bf16 bf16x8 __attribute__((ext_vector_type(8)));
typedef __bf16 bf16x2 __attribute__((ext_vector_type(2)));
typedef short s16x4 __attribute__((ext_vector_type(4)));
typedef float f32x16 __attribute__((ext_vector_type(16)));
typedef float f32x4 __attribute__((ext_vector_type(4)));
typedef float f32x2 __attribute__((ext_vector_type(2)));
typedef unsigned u32x4 __attribute__((ext_vector_type(4)));
typedef unsigned u32x2 __attribute__((ext_vector_type(2)));
typedef unsigned short u16;
#define LDSP __attribute__((address_space(3)))
#define DI __device__ __forceinline__
#define SBAR() __builtin_amdgcn_sched_barrier(0)

constexpr int SEQ = 2048, DM = 576, NH = 32, DH = 18, FF = 4608;
constexpr float LOG2E = 1.4426950408889634f;
constexpr float QSCALE = 0.23570226039551584f * LOG2E;
constexpr float LN_EPS = 1e-5f;

constexpr size_t CHUNK_B = 37888;
constexpr size_t KV_HEAD_B = 4 * CHUNK_B;
constexpr size_t WS_QM = 0;
constexpr size_t WS_QT = WS_QM + (size_t)NH * 65536;
constexpr size_t WS_KV = WS_QT + (size_t)NH * 8192;
constexpr size_t WS_KMAX = WS_KV + (size_t)NH * KV_HEAD_B;
constexpr size_t WS_XMEAN = WS_KMAX + 65536;
constexpr size_t WS_OFR = WS_XMEAN + 8192;
constexpr size_t WS_WO = WS_OFR + (size_t)128 * 18 * 1024;
constexpr size_t WS_WBAR = WS_WO + (size_t)12 * 54 * 1024;
constexpr size_t WS_HC = WS_WBAR + 3072;
constexpr size_t WS_SSQ = WS_HC + (size_t)64 * 36 * 1024;
constexpr size_t WS_W1 = WS_SSQ + (size_t)16 * 8192;
constexpr size_t WS_WD = WS_W1 + (size_t)288 * 36 * 1024;
constexpr size_t WS_ACT = WS_WD + (size_t)4 * 144 * 9 * 1024;
constexpr size_t UNIT_B = (size_t)128 * 18432;
constexpr size_t WS_PART = WS_ACT + (size_t)128 * 144 * 1024;
constexpr size_t WS_PART8 = WS_PART + (size_t)4 * 64 * 8 * 3 * 1024;
constexpr size_t WS_CNT = WS_PART8 + (size_t)64 * 8 * 4 * 512;
constexpr size_t WS_HU = WS_CNT + 40960;
constexpr size_t WS_END = WS_HU + UNIT_B;

static __device__ unsigned long long g_ptr[8];
typedef __attribute__((address_space(4))) const unsigned long long* cptr_t;
DI unsigned cvtpk(float lo, float hi) {
  f32x2 v = {lo, hi};
  bf16x2 b = __builtin_convertvector(v, bf16x2);
  return __builtin_bit_cast(unsigned, b);
}
DI float bflo(unsigned u) { return __uint_as_float(u << 16); }
DI float bfhi(unsigned u) { return __uint_as_float(u & 0xffff0000u); }
DI float dot2(unsigned a, unsigned b, float c) {
  return __builtin_amdgcn_fdot2_f32_bf16(__builtin_bit_cast(bf16x2, a), __builtin_bit_cast(bf16x2, b), c, false);
}
DI f32x16 mfma32(u32x4 a, u32x4 b, f32x16 c) {
  return __builtin_amdgcn_mfma_f32_32x32x16_bf16(__builtin_bit_cast(bf16x8, a), __builtin_bit_cast(bf16x8, b), c, 0, 0, 0);
}
DI unsigned rfl(unsigned x) { return __builtin_amdgcn_readfirstlane(x); }
DI void dma16(const char* sbase, unsigned voff, unsigned lds) {
  asm volatile("s_mov_b32 m0, %2\n\ts_nop 0\n\tglobal_load_lds_dwordx4 %0, %1" ::"v"(voff), "s"(sbase), "s"(lds));
}
DI u32x4 gld16(const char* sbase, unsigned voff) {
  u32x4 r;
  asm volatile("global_load_dwordx4 %0, %1, %2" : "=v"(r) : "v"(voff), "s"(sbase));
  return r;
}
DI unsigned gld4(const char* sbase, unsigned voff) {
  unsigned r;
  asm volatile("global_load_dword %0, %1, %2" : "=v"(r) : "v"(voff), "s"(sbase));
  return r;
}
#define PIN(x) asm volatile("" : "+v"(x))
DI u32x4 ldsr16(LDSP const char* p) { return *(LDSP const u32x4*)p; }
DI float xor32_sum(float v) {
  auto r = __builtin_amdgcn_permlane32_swap(__float_as_uint(v), __float_as_uint(v), false, false);
  return __uint_as_float(r[0]) + __uint_as_float(r[1]);
}
DI float xor32_max(float v) {
  auto r = __builtin_amdgcn_permlane32_swap(__float_as_uint(v), __float_as_uint(v), false, false);
  return fmaxf(__uint_as_float(r[0]), __uint_as_float(r[1]));
}
DI float xor16_sum(float v) {
  auto r = __builtin_amdgcn_permlane16_swap(__float_as_uint(v), __float_as_uint(v), false, false);
  return __uint_as_float(r[0]) + __uint_as_float(r[1]);
}

#define STAMP_DECL
#define STAMP(i)
#define STAMP_OUT(kid, n)

DI int wo_row(int step, int g, int j) {
  if (step < 16) return 18 * (2 * step + (g >> 1)) + 4 * (g & 1) + (j & 3) + 8 * (j >> 2);
  return 18 * (4 * (4 * (step - 16) + g) + (j >> 1)) + 16 + (j & 1);
}

constexpr int SC1 = 16;
constexpr unsigned DROP = 0x80000000u;
#define CODE_PF(dst, first, last, extra, blk, stride)                                                                              \
  do {                                                                                                                     \
    uint64_t pc_; int b_, e_;                                                                                              \
    asm volatile("s_getpc_b64 %0\n.Lpf%=:\n\ts_mov_b32 %1, " #first "-.Lpf%=\n\ts_mov_b32 %2, " #last "-.Lpf%=" : "=s"(pc_), "=s"(b_), "=s"(e_)); \
    const unsigned o_ = (blk) * 8192u + (threadIdx.x & 63) * (stride), n_ = (unsigned)(e_ - b_) + (extra) - 4u;                \
    dst = gld4((const char*)(pc_ + b_), o_ < n_ ? o_ : n_);                                                                \
  } while (0)
#define G_MFMA(d, a, b) asm volatile("v_mfma_f32_16x16x32_bf16 %0, %1, %2, %0" : "+v"(d) : "v"(a), "v"(b))
#define G_MFMA0(d, a, b) asm volatile("v_mfma_f32_16x16x32_bf16 %0, %1, %2, 0" : "=&v"(d) : "v"(a), "v"(b))
template <int AUX> DI void bst16(u32x4 v, __amdgpu_buffer_rsrc_t rs, unsigned voff, unsigned soff) {
  __builtin_amdgcn_raw_buffer_store_b128(v, rs, voff, soff, AUX);
  asm volatile("s_nop 1" ::"v"(v) : "memory");
}
DI __amdgpu_buffer_rsrc_t mkrsrc(const void* p, unsigned bytes) {
  return __builtin_amdgcn_make_buffer_rsrc((void*)p, 0, bytes, 0x00020000);
}

typedef __attribute__((address_space(1))) unsigned gu32;
DI void wait_count(gu32* cnt, unsigned first, unsigned want) {
  if (rfl(first) >= want) return;
  for (unsigned spin = 0; spin < (1u << 16); ++spin) {
    if (rfl(__hip_atomic_load(cnt, __ATOMIC_RELAXED, __HIP_MEMORY_SCOPE_AGENT)) >= want) break;
    __builtin_amdgcn_s_sleep(1);
  }
}
constexpr int NJ_W1 = 72 * 36, NJ_WD = 9 * 144, NJ_WO = 9 * 18, NJ_ALL = NJ_W1 + NJ_WD + NJ_WO;
struct PrepJob { const char* src; unsigned rs, c1, c2, voff, dst[4]; };
DI void prep_setup(PrepJob& j, int J, int lane, const u16* wo, const u16* wg, const u16* wu, const u16* wd) {
  if (J >= NJ_ALL) J = NJ_ALL - 1;
  const int g = (lane >> 2) & 3;
  unsigned lrow = 8 * g;
  j.c1 = 0; j.c2 = 0;
  if (J < NJ_W1) {
    const int sm = J / 72, tg = J - sm * 72;
    j.src = (const char*)((sm & 1) ? wu : wg) + (size_t)(sm >> 1) * (32 * FF * 2) + tg * 128;
    j.rs = FF * 2;
#pragma unroll
    for (int t = 0; t < 4; ++t) j.dst[t] = (unsigned)WS_W1 + (4 * tg + t) * 36864 + sm * 1024;
  } else if (J < NJ_W1 + NJ_WD) {
    const int q = J - NJ_W1, cc = q / 9, tg = q - cc * 9;
    j.src = (const char*)wd + (size_t)cc * (32 * DM * 2) + tg * 128;
    j.rs = DM * 2;
#pragma unroll
    for (int t = 0; t < 4; ++t) { const int nt = 4 * tg + t, nb = nt / 9; j.dst[t] = (unsigned)WS_WD + ((nb * 144 + cc) * 9 + (nt - nb * 9)) * 1024; }
  } else {
    const int q = J - NJ_W1 - NJ_WD, step = q / 9, tg = q - step * 9;
    j.rs = DM * 2;
    if (step < 16) { j.src = (const char*)wo + (size_t)step * (36 * DM * 2) + tg * 128; lrow = 18 * (g >> 1) + 4 * (g & 1); j.c2 = 4; }
    else { j.src = (const char*)wo + (size_t)(288 * (step - 16) + 16) * (DM * 2) + tg * 128; lrow = 72 * g; j.c1 = 16; }
#pragma unroll
    for (int t = 0; t < 4; ++t) j.dst[t] = (unsigned)WS_WO + (step * 36 + 4 * tg + t) * 1024;
  }
#pragma unroll
  for (int t = 0; t < 4; ++t) j.dst[t] = rfl(j.dst[t]);
  j.voff = lrow * j.rs + (lane >> 4) * 32 + (lane & 3) * 8;
}
template <int JJ>
DI void prep_load(const PrepJob& j, u32x2 (&r)[8]) {
  const char* b = j.src + (JJ + j.c1 * (JJ >> 1) + j.c2 * (JJ >> 2)) * j.rs;
  asm volatile("global_load_dwordx2 %0, %1, %2" : "=v"(r[JJ]) : "v"(j.voff), "s"(b));
}
DI void prep_load_all(const PrepJob& j, u32x2 (&r)[8]) {
  prep_load<0>(j, r); prep_load<1>(j, r); prep_load<2>(j, r); prep_load<3>(j, r); prep_load<4>(j, r); prep_load<5>(j, r); prep_load<6>(j, r); prep_load<7>(j, r);
}
DI void prep_store(const PrepJob& j, const u32x2 (&r)[8], __amdgpu_buffer_rsrc_t rs, unsigned so) {
  u32x4 u[4];
#pragma unroll
  for (int i = 0; i < 4; ++i) {
    u[0][i] = __builtin_amdgcn_perm(r[2 * i + 1][0], r[2 * i][0], 0x05040100u);
    u[1][i] = __builtin_amdgcn_perm(r[2 * i + 1][0], r[2 * i][0], 0x07060302u);
    u[2][i] = __builtin_amdgcn_perm(r[2 * i + 1][1], r[2 * i][1], 0x05040100u);
    u[3][i] = __builtin_amdgcn_perm(r[2 * i + 1][1], r[2 * i][1], 0x07060302u);
  }
#pragma unroll
  for (int i = 0; i < 4; ++i) {
    const auto a0 = __builtin_amdgcn_permlane32_swap(u[0][i], u[2][i], false, false);
    const auto a1 = __builtin_amdgcn_permlane32_swap(u[1][i], u[3][i], false, false);
    const auto b0 = __builtin_amdgcn_permlane16_swap(a0[0], a1[0], false, false);
    const auto b1 = __builtin_amdgcn_permlane16_swap(a0[1], a1[1], false, false);
    u[0][i] = b0[0]; u[1][i] = b0[1]; u[2][i] = b1[0]; u[3][i] = b1[1];
  }
#pragma unroll
  for (int t = 0; t < 4; ++t) bst16<SC1>(u[t], rs, so, j.dst[t]);
}
#define PIN8(r) "+v"(r[0]), "+v"(r[1]), "+v"(r[2]), "+v"(r[3]), "+v"(r[4]), "+v"(r[5]), "+v"(r[6]), "+v"(r[7])

constexpr int K1_PF = 4, K1_DEPTH = 2;
constexpr int K1_PITCH = 224, K1_SLOT = 16384 + 64 * K1_PITCH  , K1_NS = K1_DEPTH + 1;
constexpr int K1_SP = 232;
constexpr int K1_STG = K1_NS * K1_SLOT, K1_CS = K1_STG + 128 * K1_SP + 64, K1_ST = K1_CS + 512 + 64, K1_LDS = K1_ST + 2048;
struct K1C {
  LDSP const char *xa[2], *wl, *wc0, *wc1;
  u32x4 X[3][2], wf[K1_PF + 1], ones;
  f32x4 acc[4][2];
  f32x4 sm[2]; float sq[2];
};
DI void k1_loader(const char* xb, const char* wb, const u16* wo_, char* ws, int j, int lane, unsigned lds0) {
  __builtin_amdgcn_s_setprio(3);
  unsigned xo[2], wo[4];
  const int g = lane >> 4;
  xo[0] = (lane >> 3) * 1152 + (((lane & 7) ^ g) * 16);
  xo[1] = (8 + (lane >> 3)) * 1152 + (((lane & 7) ^ (4 + g)) * 16);
  xb += j * (32 * 1152);
  const unsigned xd = lds0 + j * 4096, wd = lds0 + 16384 + j * 1024;
#define K1_X(z, so) do { dma16(xb + (z) * 128, xo[0], xd + (so)); dma16(xb + (z) * 128, xo[1], xd + (so) + 1024);                    \
    dma16(xb + (z) * 128 + 16 * 1152, xo[0], xd + (so) + 2048); dma16(xb + (z) * 128 + 16 * 1152, xo[1], xd + (so) + 3072); } while (0)
#define K1_W(z, so) do { _Pragma("unroll") for (int i = 0; i < 3; ++i) dma16(wb + (z) * (64 * 3456), wo[i], wd + (so) + i * 4096);     \
    if (j < 2) dma16(wb + (z) * (64 * 3456), wo[3], wd + (so) + 3 * 4096); } while (0)
  K1_X(0, 0);
  SBAR();
#pragma unroll
  for (int i = 0; i < 4; ++i) {
    const unsigned u = 64 * (j + 4 * i) + lane, r = u / 14;
    wo[i] = ((r & ~12u) | ((r & 4u) << 1) | ((r & 8u) >> 1)) * 3456 + (u - r * 14) * 16;
  }
  K1_W(0, 0);
#pragma unroll
  for (int z = 1; z < K1_DEPTH; ++z) { K1_X(z, z * K1_SLOT); K1_W(z, z * K1_SLOT); }
  unsigned so = K1_DEPTH * K1_SLOT;
#pragma unroll 1
  for (int z = K1_DEPTH; z < 9; ++z) {
    asm volatile("s_waitcnt vmcnt(%0)\n\ts_barrier" ::"i"(7 * (K1_DEPTH - 1)) : "memory");
    K1_X(z, so); K1_W(z, so);
    so = so == (K1_NS - 1) * K1_SLOT ? 0 : so + K1_SLOT;
  }
  asm volatile("s_waitcnt vmcnt(7)\n\ts_barrier" ::: "memory");
  const int b = blockIdx.x;
  const bool dojob = j == 1 && b < NJ_WO, dorow = j >= 2 || (j == 0 && b < 64);
  const int br = j >= 2 ? 2 * b + j - 2 : 512 + (b & 63);
  PrepJob jb; u32x2 r[8]; unsigned wr[5];
  if (dojob) prep_setup(jb, NJ_W1 + NJ_WD + b, lane, wo_, wo_, wo_, wo_);
  const char* rp = (const char*)wo_ + (size_t)wo_row(br >> 5, (br >> 3) & 3, br & 7) * (DM * 2);
  SBAR();
  asm volatile("s_waitcnt vmcnt(0)\n\ts_barrier" ::: "memory");
  if (dojob) prep_load_all(jb, r);
  if (dorow) {
#pragma unroll
    for (int i = 0; i < 5; ++i) wr[i] = gld4(rp, (i < 4 ? lane + 64 * i : 256 + (lane & 31)) * 4);
  }
  asm volatile("s_barrier");
  asm volatile("s_barrier");
  const __amdgpu_buffer_rsrc_t wrs = mkrsrc(ws, (unsigned)WS_END);
  asm volatile("s_waitcnt vmcnt(0)" : PIN8(r), "+v"(wr[0]), "+v"(wr[1]), "+v"(wr[2]), "+v"(wr[3]), "+v"(wr[4])::"memory");
  if (dojob) {
#pragma unroll
    for (int t = 0; t < 4; ++t) jb.dst[t] = rfl(jb.dst[t]);
    prep_store(jb, r, wrs, ((lane >> 2) & 3) * 256 + ((lane & 3) * 4 + (lane >> 4)) * 16);
  }
  if (dorow) {
    float a = 0.f;
#pragma unroll
    for (int i = 0; i < 4; ++i) a = dot2(wr[i], 0x3f803f80u, a);
    a = dot2(lane >= 32 ? 0u : wr[4], 0x3f803f80u, a);
    a = xor16_sum(xor32_sum(a));
    a += __uint_as_float(__builtin_amdgcn_update_dpp(0u, __float_as_uint(a), 0x128, 0xf, 0xf, false));
    a += __uint_as_float(__builtin_amdgcn_update_dpp(0u, __float_as_uint(a), 0x124, 0xf, 0xf, false));
    a += __uint_as_float(__builtin_amdgcn_update_dpp(0u, __float_as_uint(a), 0x122, 0xf, 0xf, false));
    a += __uint_as_float(__builtin_amdgcn_update_dpp(0u, __float_as_uint(a), 0x121, 0xf, 0xf, false));
    a *= (1.0f / DM);
    const unsigned hi = cvtpk(a, 0.f) & 0xffffu;
    const unsigned lo = cvtpk(a - bflo(hi), 0.f) & 0xffffu;
    __builtin_amdgcn_raw_buffer_store_b16((u16)(lane ? lo : hi), wrs, lane < 2 ? (unsigned)WS_WBAR + (lane * DM + br) * 2 : DROP, 0, 16);
  }
#undef K1_X
#undef K1_W
}
DI u32x4 tr_frag(LDSP const char* p, int d) {
  s16x4 lo = __builtin_amdgcn_ds_read_tr16_b64_v4i16((LDSP s16x4*)p);
  s16x4 hi = __builtin_amdgcn_ds_read_tr16_b64_v4i16((LDSP s16x4*)(p + d));
  return __builtin_bit_cast(u32x4, __builtin_shufflevector(lo, hi, 0, 1, 2, 3, 4, 5, 6, 7));
}
template <int NHG, int F> DI void k1_read(K1C& c) {
  constexpr int NF = 4 + NHG, S = F / NF, j = F % NF, Z = S >> 1, so = (Z % K1_NS) * K1_SLOT;
  if constexpr (j == 0) {
    c.X[S % 3][0] = ldsr16(c.xa[S & 1] + so);
    c.X[S % 3][1] = ldsr16(c.xa[S & 1] + so + 2048);
  }
  constexpr int wo = so + (S & 1) * 7168;
  if constexpr (NHG == 0 || j < 3) c.wf[F % (K1_PF + 1)] = tr_frag(c.wl + wo + j * 32, 1792);
  else c.wf[F % (K1_PF + 1)] = tr_frag((j == 3 ? c.wc0 : c.wc1) + wo, 1792);
}
template <int NHG, int F> DI void k1_frag(K1C& c) {
  constexpr int NF = 4 + NHG, S = F / NF, j = F % NF, N = F + K1_PF;
  if constexpr (S == 0) {
    if constexpr (NHG == 0 || j < 3) { G_MFMA0(c.acc[j][0], c.wf[F % (K1_PF + 1)], c.X[0][0]); G_MFMA0(c.acc[j][1], c.wf[F % (K1_PF + 1)], c.X[0][1]); }
    else G_MFMA0(c.acc[3][j - 3], c.wf[F % (K1_PF + 1)], c.ones);
  } else {
    if constexpr (NHG == 0 || j < 3) { G_MFMA(c.acc[j][0], c.wf[F % (K1_PF + 1)], c.X[S % 3][0]); G_MFMA(c.acc[j][1], c.wf[F % (K1_PF + 1)], c.X[S % 3][1]); }
    else G_MFMA(c.acc[3][j - 3], c.wf[F % (K1_PF + 1)], c.ones);
  }
  if constexpr ((S & 1) == NHG && j < 4) {
#pragma unroll
    for (int t = 0; t < 2; ++t) { const unsigned v = c.X[S % 3][t][j]; c.sq[t] = dot2(v, v, c.sq[t]); PIN(c.sq[t]); }
    if constexpr (j < 2) { if constexpr (S < 2) G_MFMA0(c.sm[j], c.ones, c.X[S % 3][j]); else G_MFMA(c.sm[j], c.ones, c.X[S % 3][j]); }
  }
  if constexpr (N < 18 * NF) {
    if constexpr (N % (2 * NF) == 0) {
      asm volatile("s_waitcnt lgkmcnt(0)\n\ts_barrier" ::: "memory");
    }
    k1_read<NHG, N>(c);
  }
  SBAR();
}
template <int NHG, int... Fs> DI void k1_run(K1C& c, std::integer_sequence<int, Fs...>) {
  k1_read<NHG, 0>(c); k1_read<NHG, 1>(c); k1_read<NHG, 2>(c); k1_read<NHG, 3>(c);
  SBAR();
  (k1_frag<NHG, Fs>(c), ...);
  asm volatile("s_nop 7" : "+v"(c.acc[0][0]), "+v"(c.acc[0][1]), "+v"(c.acc[1][0]), "+v"(c.acc[1][1]), "+v"(c.acc[2][0]), "+v"(c.acc[2][1]),
               "+v"(c.acc[3][0]), "+v"(c.acc[3][1]), "+v"(c.sm[0]), "+v"(c.sm[1]));
  SBAR();
}
extern "C" __global__ __launch_bounds__(768, 3) void k_qkv(const u16* x, const u16* wqkv, const u16* wo, char* ws, const u16* wg, const u16* wu, const u16* wd, char* out) {
  extern __shared__ __attribute__((aligned(16))) char smem[];
  const int tid = threadIdx.x, lane = tid & 63, w = rfl(tid >> 6);
  const int xcd = blockIdx.x & 7, bi = blockIdx.x >> 3;
  unsigned pf;
  CODE_PF(pf, k_qkv, k_end, 0, bi, (w == 8 && bi < 9) ? 128u : 0u);
  SBAR();
  asm volatile("" : "+s"(x), "+s"(wqkv), "+s"(wo), "+s"(ws));
  LDSP char* lds = (LDSP char*)smem;
  const unsigned lds0 = (unsigned)(uintptr_t)lds;
  const int rb = 4 * (xcd >> 1) + (bi & 3), cb = 8 * (xcd & 1) + (bi >> 2);
  const int c0 = 108 * cb - 4 * (cb & 1);
  if (__builtin_expect(w >= 8, 1)) {
    k1_loader((const char*)x + rb * (128 * 1152), (const char*)wqkv + c0 * 2, wo, ws, w - 8, lane, lds0);
    asm volatile("" ::"v"(pf));
    if (blockIdx.x == 0 && tid == 512) {
      g_ptr[0] = (unsigned long long)x; g_ptr[1] = (unsigned long long)wg; g_ptr[2] = (unsigned long long)wu; g_ptr[3] = (unsigned long long)wd;
      g_ptr[4] = (unsigned long long)ws; g_ptr[5] = (unsigned long long)out;
    }
    return;
  }
  const int m = lane & 15, g = lane >> 4, mg = w & 3, nh = w >> 2;
  STAMP_DECL; STAMP(0);
  K1C c;
  c.ones = u32x4{0x3f803f80u, 0x3f803f80u, 0x3f803f80u, 0x3f803f80u};
  PIN(c.ones);
  c.xa[0] = lds + (32 * mg + m) * 128 + ((g ^ (m >> 1)) * 16);
  c.xa[1] = lds + (32 * mg + m) * 128 + (((4 + g) ^ (m >> 1)) * 16);
  LDSP const char* wbase = lds + 16384 + (16 * (g >> 1) + 4 * (g & 1) + (m >> 2)) * K1_PITCH + (m & 3) * 8;
  c.wl = wbase + nh * 128;
  c.wc0 = wbase + mg * 32;
  c.wc1 = wbase + (mg < 3 ? mg + 4 : 6) * 32;
  c.sq[0] = c.sq[1] = 0.f;
  const int off = 4 * (cb & 1), T0 = 128 * rb;
  LDSP char* stg = lds + K1_STG;
  LDSP char* a_st = lds + K1_ST + (32 * mg + m) * 8;                 PIN(a_st);
  LDSP char* a_cs = lds + K1_CS + (64 * nh + 4 * g) * 4;             PIN(a_cs);
  LDSP char* a_sw = stg + (32 * mg + m) * K1_SP + (64 * nh + 4 * g) * 2;   PIN(a_sw);
  LDSP char* a_tr = stg + (32 * mg + 4 * (g & 1) + (m >> 2)) * K1_SP + (64 * nh + 16 * (g >> 1) + 4 * (m & 3)) * 2;   PIN(a_tr);
  float qs[4];
#pragma unroll
  for (int j = 0; j < 4; ++j) { qs[j] = (c0 + 64 * nh + 16 * j + 4 * g < DM) ? QSCALE : 1.0f; PIN(qs[j]); }
  const __amdgpu_buffer_rsrc_t wrs = mkrsrc(ws, (unsigned)WS_END);
  const unsigned kvo = (unsigned)WS_KV + (T0 >> 9) * (unsigned)CHUNK_B;
  unsigned vo[2] = {0, 0}, vone = 0, vd[4] = {DROP, DROP, DROP, DROP};
  if (cb >= 10) {
#pragma unroll
  for (int tt = 0; tt < 2; ++tt) {
    const int cc = 64 * nh + 32 * tt + 16 * (g >> 1) + m - off, hd = (cc + 18) / 18 - 1;
    vo[tt] = (cc >= 0 && cc < 108) ? hd * 4864 + 2 * mg * 608 + (g & 1) * 304 + (cc - 18 * hd) * 16 : 29184;
  }
  vone = tid < 96 ? (tid >> 4) * 4864 + (tid & 15) * 304 + 288 : 29184;
#pragma unroll
  for (int i = 0; i < 4; ++i) {
    const unsigned u = i * 512 + tid, hd = u / 304;
    vd[i] = (u < 1824 && 6 * cb + hd >= 64) ? kvo + 18432 + ((T0 >> 4) & 31) * 608 + (6 * cb + hd - 64) * (unsigned)KV_HEAD_B + (u - hd * 304) * 16 : DROP;
  }
  }
  PIN(vone); PIN(vo[0]); PIN(vo[1]); PIN(vd[0]); PIN(vd[1]); PIN(vd[2]); PIN(vd[3]);
  const int tok = tid & 127, T = T0 + tok;
  LDSP const char* a_g = stg + tok * K1_SP + off * 2;   PIN(a_g);
  unsigned um[2], ut[2];
  const unsigned tl = ((T >> 5) & 15) * 1024 + (T & 31) * 16;
#pragma unroll
  for (int it = 0; it < 2; ++it) {
    const int hd = (w >> 1) + 4 * it, G = 6 * cb + hd, head = G & 31;
    if (hd >= 6 || G >= 64) { um[it] = DROP; ut[it] = DROP; }
    else if (G < 32) { um[it] = (unsigned)WS_QM + head * 65536 + (T >> 9) * 16384 + tl; ut[it] = (unsigned)WS_QT + head * 8192 + T * 4; }
    else { um[it] = kvo + head * (unsigned)KV_HEAD_B + tl; ut[it] = kvo + head * (unsigned)KV_HEAD_B + 16384 + ((T >> 6) & 7) * 256 + (T & 31) * 8 + ((T >> 5) & 1) * 4; }
    PIN(um[it]); PIN(ut[it]);
  }
  STAMP(1);
  asm volatile("s_waitcnt vmcnt(0)" : "+v"(pf));
  if (blockIdx.x < 5 && w == 7) bst16<SC1>(u32x4{0, 0, 0, 0}, wrs, (unsigned)WS_CNT + (blockIdx.x * 64 + lane) * 128, 0);
  asm volatile("s_barrier" ::: "memory");
  STAMP(2);
  if (nh) k1_run<1>(c, std::make_integer_sequence<int, 90>{});
  else k1_run<0>(c, std::make_integer_sequence<int, 72>{});
  STAMP(3);
#pragma unroll
  for (int t = 0; t < 2; ++t) {
    const f32x2 st = {c.sm[t][0], xor32_sum(xor16_sum(c.sq[t]))};
    *(LDSP f32x2*)(a_st + nh * 1024 + t * 128) = st;
  }
  if (nh) {
    *(LDSP f32x4*)(lds + K1_CS + (16 * mg + 4 * g) * 4) = c.acc[3][0];
    *(LDSP f32x4*)(lds + K1_CS + (16 * (mg + 4) + 4 * g) * 4) = c.acc[3][1];
  }
  asm volatile("s_waitcnt lgkmcnt(0)\n\ts_barrier" ::: "memory");
  {
    f32x2 p[2][2]; f32x4 cc[4];
#pragma unroll
    for (int t = 0; t < 2; ++t) { p[t][0] = *(LDSP const f32x2*)(a_st + t * 128); p[t][1] = *(LDSP const f32x2*)(a_st + 1024 + t * 128); }
#pragma unroll
    for (int j = 0; j < 4; ++j) cc[j] = *(LDSP const f32x4*)(a_cs + j * 64);
    SBAR();
#pragma unroll
    for (int t = 0; t < 2; ++t) {
      const float mean = (p[t][0][0] + p[t][1][0]) * (1.0f / DM);
      const float var = fmaxf((p[t][0][1] + p[t][1][1]) * (1.0f / DM) - mean * mean, 0.f);
      const float rstd = __builtin_amdgcn_rsqf(var + LN_EPS);
      if (cb == 0 && nh == 0) __builtin_amdgcn_raw_buffer_store_b32(__float_as_uint(mean), wrs, (unsigned)WS_XMEAN + (T0 + 32 * mg + 16 * t + m) * 4, 0, SC1);
#pragma unroll
      for (int j = 0; j < 4; ++j) {
        if (j < 3 || nh == 0) {
          const f32x4 a = (c.acc[j][t] - mean * cc[j]) * (rstd * qs[j]);
          *(LDSP u32x2*)(a_sw + t * (16 * K1_SP) + j * 32) = u32x2{cvtpk(a[0], a[1]), cvtpk(a[2], a[3])};
        }
      }
    }
  }
  if (cb >= 10) {
    asm volatile("s_waitcnt lgkmcnt(0)" ::: "memory");
    u32x4 v[2][2];
#pragma unroll
    for (int tt = 0; tt < 2; ++tt)
#pragma unroll
      for (int t = 0; t < 2; ++t) v[tt][t] = tr_frag(a_tr + t * (16 * K1_SP) + tt * 64, 8 * K1_SP);
    *(LDSP u32x4*)(lds + vone) = c.ones;
#pragma unroll
    for (int tt = 0; tt < 2; ++tt)
#pragma unroll
      for (int t = 0; t < 2; ++t) *(LDSP u32x4*)(lds + vo[tt] + t * 608) = v[tt][t];
  }
  STAMP(4);
  asm volatile("s_waitcnt lgkmcnt(0)\n\ts_barrier" ::: "memory");
  if (cb >= 10) {
    u32x4 v[4];
#pragma unroll
    for (int i = 0; i < 4; ++i) v[i] = ldsr16(lds + i * 8192 + tid * 16);
#pragma unroll
    for (int i = 0; i < 4; ++i) bst16<SC1>(v[i], wrs, vd[i], 0);
  }
  if (cb <= 10) {
    unsigned d[2][9];
#pragma unroll
    for (int it = 0; it < 2; ++it) {
      const int hd = (w >> 1) + 4 * it;
      LDSP const unsigned* sp = (LDSP const unsigned*)(a_g + 36 * (hd < 6 ? hd : 0));
#pragma unroll
      for (int i = 0; i < 9; ++i) d[it][i] = sp[i];
    }
    SBAR();
#pragma unroll
    for (int it = 0; it < 2; ++it) {
      bst16<SC1>(u32x4{d[it][0], d[it][1], d[it][2], d[it][3]}, wrs, um[it], 0);
      bst16<SC1>(u32x4{d[it][4], d[it][5], d[it][6], d[it][7]}, wrs, um[it] + 512, 0);
      __builtin_amdgcn_raw_buffer_store_b32(d[it][8], wrs, ut[it], 0, SC1);
    }
#pragma unroll
    for (int it = 0; it < 2; ++it) {
      const int hd = (w >> 1) + 4 * it, G = 6 * cb + hd;
      if (hd < 6 && G >= 32 && G < 64) {
        float n2 = 0.f;
#pragma unroll
        for (int i = 0; i < 9; ++i) n2 = dot2(d[it][i], d[it][i], n2);
        unsigned nb = __float_as_uint(n2);
        { auto r = __builtin_amdgcn_permlane32_swap(nb, nb, false, false); nb = max(r[0], r[1]); }
        { auto r = __builtin_amdgcn_permlane16_swap(nb, nb, false, false); nb = max(r[0], r[1]); }
        __builtin_amdgcn_raw_buffer_store_b32(nb, wrs, (unsigned)WS_KMAX + (((2 * rb + (w & 1)) * 32 + (G & 31)) * 16 + m) * 4, 0, SC1);
      }
    }
  }
  STAMP(5); STAMP_OUT(1, 6);
}

constexpr int K2_LDS = (int)KV_HEAD_B, K3_WBAR = 90 * 1024, K3_XC = 96 * 1024, K3_REP = 16, K3_PRE = 4;
struct AttnC {
  LDSP const char *kmb, *ktb, *vb;
  unsigned kma[3], kta[3], va[3];
  u32x4 qm, qt, kf, v0, v1, p1, ktl;
  f32x16 negm, o, sa, sb;
};
constexpr int k_off(int T) { return (T >> 4) * (int)CHUNK_B + (T & 15) * 1024; }
constexpr int kt_off(int T) { return (T >> 4) * (int)CHUNK_B + ((T >> 1) & 7) * 256; }
constexpr int v_off(int T) { return (T >> 4) * (int)CHUNK_B + (T & 15) * 1216; }
#define AT_E(r) "v_exp_f32 v" #r ", v" #r "\n\t"
#define AT_C(d, a, b) "v_cvt_pk_bf16_f32 v" #d ", v" #a ", v" #b "\n\t"
#define AT_TAIL(sn, kt, qt) "v_mfma_f32_32x32x8_bf16 " sn ", " kt ", " qt ", v[18:33]\n\t"
#define AT_MAIN(sn) "v_mfma_f32_32x32x16_bf16 " sn ", v[66:69], v[98:101], " sn "\n\t"
#define AT_PV1 "v_mfma_f32_32x32x16_bf16 v[2:17], v[84:87], v[70:73], v[2:17]\n\t"
#define AT_PV0 "v_mfma_f32_32x32x16_bf16 v[2:17], v[88:91], v[74:77], v[2:17]\n\t"
#define AT_R1 "ds_read_b128 v[84:87], %[va] offset:%[vo1]\n\t"
#define AT_RK "ds_read_b128 v[66:69], %[ka] offset:%[ko]\n\t"
#define AT_R0 "ds_read_b128 v[88:91], %[vb] offset:%[vo0]\n\t"
#define AT_RT(kt) "ds_read_b64 " kt ", %[kt] offset:%[kto]\n\t"
#define AT_BODY(TL, P1, MN, RD, s0, s1, s2, s3, s4, s5, s6, s7, s8, s9, s10, s11, s12, s13, s14, s15)                                   \
  TL AT_E(s0) AT_E(s1) AT_E(s2) AT_E(s3) "s_waitcnt lgkmcnt(0)\n\t" P1 AT_C(74, s0, s1) AT_E(s4) AT_C(75, s2, s3) AT_E(s5) AT_E(s6) AT_E(s7) \
  MN AT_C(76, s4, s5) AT_E(s8) AT_C(77, s6, s7) AT_E(s9) AT_E(s10) AT_E(s11) AT_PV0 RD AT_C(70, s8, s9) AT_E(s12) AT_C(71, s10, s11)          \
  AT_E(s13) AT_E(s14) AT_E(s15) AT_C(72, s12, s13) AT_C(73, s14, s15)
#define AT_EVEN(TL, P1, MN, RD) AT_BODY(TL, P1, MN, RD, 34, 35, 36, 37, 38, 39, 40, 41, 42, 43, 44, 45, 46, 47, 48, 49)
#define AT_ODD(TL, P1, MN, RD) AT_BODY(TL, P1, MN, RD, 50, 51, 52, 53, 54, 55, 56, 57, 58, 59, 60, 61, 62, 63, 64, 65)
#define AT_STMT(TXT)                                                                                                              \
  asm volatile(TXT : "+{v[2:17]}"(c.o), "+{v[34:49]}"(c.sa), "+{v[50:65]}"(c.sb), "+{v[66:69]}"(c.kf), "+{v[70:73]}"(c.p1),        \
               "+{v[84:87]}"(c.v1), "+{v[88:91]}"(c.v0), "+{v[92:95]}"(c.ktl)                                                      \
               : "{v[18:33]}"(c.negm), "{v[98:101]}"(c.qm), "{v[102:105]}"(c.qt), [ka] "v"(c.kma[KO >> 16]), [kt] "v"(c.kta[KT >> 16]), \
                 [va] "v"(c.va[V1 >> 16]), [vb] "v"(c.va[V0 >> 16]), [ko] "i"(KO & 0xffff), [kto] "i"(KT & 0xffff), [vo1] "i"(V1 & 0xffff),  \
                 [vo0] "i"(V0 & 0xffff)                                                                                            \
               : "v74", "v75", "v76", "v77")
template <int T> DI void attn_tile(AttnC& c) {
  constexpr int KO = k_off(T < 62 ? T + 2 : 0), KT = kt_off(T < 61 ? T + 3 : 0), V1 = v_off(T) + 608, V0 = v_off(T < 63 ? T + 1 : 0);
  if constexpr (T == 0) AT_STMT(AT_EVEN(AT_TAIL("v[50:65]", "v[92:93]", "v[104:105]"), "", AT_MAIN("v[50:65]"), AT_R1 AT_RK AT_R0));
  else if constexpr (T == 61) AT_STMT(AT_ODD(AT_TAIL("v[34:49]", "v[94:95]", "v[102:103]"), AT_PV1, AT_MAIN("v[34:49]"), AT_R1 AT_RK AT_R0));
  else if constexpr (T == 62) AT_STMT(AT_EVEN(AT_TAIL("v[50:65]", "v[94:95]", "v[104:105]"), AT_PV1, AT_MAIN("v[50:65]"), AT_R1 AT_R0));
  else if constexpr (T == 63) AT_STMT(AT_ODD("", AT_PV1, "", AT_R1));
  else if constexpr ((T & 3) == 0) AT_STMT(AT_EVEN(AT_TAIL("v[50:65]", "v[92:93]", "v[104:105]"), AT_PV1, AT_MAIN("v[50:65]"), AT_R1 AT_RK AT_R0));
  else if constexpr ((T & 3) == 1) AT_STMT(AT_ODD(AT_TAIL("v[34:49]", "v[94:95]", "v[102:103]"), AT_PV1, AT_MAIN("v[34:49]"), AT_R1 AT_RK AT_R0 AT_RT("v[92:93]")));
  else if constexpr ((T & 3) == 2) AT_STMT(AT_EVEN(AT_TAIL("v[50:65]", "v[94:95]", "v[104:105]"), AT_PV1, AT_MAIN("v[50:65]"), AT_R1 AT_RK AT_R0));
  else AT_STMT(AT_ODD(AT_TAIL("v[34:49]", "v[92:93]", "v[102:103]"), AT_PV1, AT_MAIN("v[34:49]"), AT_R1 AT_RK AT_R0 AT_RT("v[94:95]")));
}
DI void attn_group_slow(LDSP const char* km, LDSP const char* kt, LDSP const char* vp, const u32x4& qm, unsigned qtl, const f32x16& negm, f32x16& o) {
#pragma unroll
  for (int t = 0; t < 4; ++t) {
    const u32x2 k2 = *(LDSP const u32x2*)(kt + (t >> 1) * 256);
    const u32x4 ktl = {k2[0], k2[1], 0, 0};
    const u32x4 kf = ldsr16(km + t * 1024);
    const u32x4 v0 = ldsr16(vp + t * 1216), v1 = ldsr16(vp + t * 1216 + 608);
    u32x4 qq = {0, 0, 0, 0}; qq[t & 1] = qtl;
    f32x16 s = mfma32(ktl, qq, negm);
    s = mfma32(kf, qm, s);
    u32x4 p0, p1;
#pragma unroll
    for (int i = 0; i < 4; ++i) {
      p0[i] = cvtpk(__builtin_amdgcn_exp2f(s[2 * i]), __builtin_amdgcn_exp2f(s[2 * i + 1]));
      p1[i] = cvtpk(__builtin_amdgcn_exp2f(s[8 + 2 * i]), __builtin_amdgcn_exp2f(s[8 + 2 * i + 1]));
    }
    o = mfma32(v0, p0, o);
    o = mfma32(v1, p1, o);
  }
}
template <int G> DI void attn_group(AttnC& c) {
  __builtin_amdgcn_s_setprio(G < 5 ? 3 - (G & 3) : (G < 8 ? 3 : (G < 11 ? 2 : (G < 14 ? 1 : 0))));
  attn_tile<4 * G + 0>(c); attn_tile<4 * G + 1>(c); attn_tile<4 * G + 2>(c); attn_tile<4 * G + 3>(c);
}
DI void attn_helper(const u16* wg, const u16* wu, const u16* wd, char* ws, int head, int j, int lane, unsigned lds0) {
  __builtin_amdgcn_s_setprio(3);
  const char* kv = ws + WS_KV + (size_t)head * KV_HEAD_B;
  const unsigned lane16 = lane * 16;
#define KV_DMA(o) dma16(kv + (o), lane16, lds0 + (o))
#pragma unroll
  for (int i = 0; i < 4; ++i) KV_DMA(((i >> 1) * 16 + (i & 1) * 4 + j) * 1024);
#pragma unroll
  for (int i = 0; i < 6; ++i) { const int p = i < 2 ? 8 + 4 * i + j : min(16 + 4 * i + j, 36); KV_DMA(p * 1024); }
  const int hw = blockIdx.x * 4 + j;
  PrepJob jb[4];
#pragma unroll
  for (int i = 0; i < 4; ++i) { prep_setup(jb[i], min(4 * hw + i, NJ_W1 + NJ_WD - 1), lane, wd, wg, wu, wd); PIN(jb[i].voff); }
  SBAR();
  asm volatile("s_waitcnt vmcnt(6)\n\ts_barrier" ::: "memory");
#pragma unroll
  for (int i = 0; i < 10; ++i) KV_DMA((unsigned)CHUNK_B + min(4 * i + j, 36) * 1024);
  asm volatile("s_waitcnt vmcnt(10)\n\ts_barrier" ::: "memory");
#pragma unroll
  for (int i = 0; i < 19; ++i) KV_DMA(2 * (unsigned)CHUNK_B + min(4 * i + j, 73) * 1024);
#undef KV_DMA
  u32x2 r[4][8];
  unsigned pf;
  asm volatile("s_waitcnt vmcnt(19)\n\ts_barrier" ::: "memory");
  prep_load_all(jb[0], r[0]); prep_load_all(jb[1], r[1]);
  asm volatile("s_waitcnt vmcnt(16)\n\ts_barrier" ::: "memory");
  const __amdgpu_buffer_rsrc_t wrs = mkrsrc(ws, (unsigned)WS_END);
  const unsigned pso = ((lane >> 2) & 3) * 256 + ((lane & 3) * 4 + (lane >> 4)) * 16;
  prep_load_all(jb[2], r[2]); prep_load_all(jb[3], r[3]);
  CODE_PF(pf, k_attn, k_end, 0, 2 * j + 1 + ((blockIdx.x >> 3) & 1), (blockIdx.x >> 3) < 2 ? 128u : 0u);
  asm volatile("s_waitcnt vmcnt(25)" : PIN8(r[0])::"memory");
  prep_store(jb[0], r[0], wrs, pso);
  asm volatile("s_waitcnt vmcnt(21)" : PIN8(r[1])::"memory");
  prep_store(jb[1], r[1], wrs, pso);
  asm volatile("s_waitcnt vmcnt(17)" : PIN8(r[2])::"memory");
  prep_store(jb[2], r[2], wrs, pso);
  asm volatile("s_waitcnt vmcnt(12)" : PIN8(r[3]), "+v"(pf)::"memory");
  prep_store(jb[3], r[3], wrs, pso);
}
extern "C" __global__ __launch_bounds__(768, 3) void k_attn() {
  extern __shared__ __attribute__((aligned(16))) char smem[];
  cptr_t gp = (cptr_t)(uintptr_t)g_ptr;
  const u16 *x = (const u16*)gp[0], *wg = (const u16*)gp[1], *wu = (const u16*)gp[2], *wd = (const u16*)gp[3]; char* ws = (char*)gp[4];
  asm volatile("" : "+s"(x), "+s"(wg), "+s"(wu), "+s"(wd), "+s"(ws));
  LDSP char* lds = (LDSP char*)smem;
  const int tid = threadIdx.x, lane = tid & 63, w = rfl(tid >> 6);
  const int xcd = blockIdx.x & 7, bi = blockIdx.x >> 3;
  const int head = 4 * xcd + (bi & 3), qb = bi >> 2;
  if (__builtin_expect(w >= 8, 0)) { attn_helper(wg, wu, wd, ws, head, w - 8, lane, (unsigned)(uintptr_t)lds); return; }
  const int r = lane & 31, h = lane >> 5;
  const int qt32 = 8 * qb + w;
  STAMP_DECL; STAMP(0);
  AttnC c;
  c.qm = gld16(ws + WS_QM, head * 65536 + qt32 * 1024 + lane * 16);
  unsigned qtl = gld4(ws + WS_QT, head * 8192 + (32 * qt32 + r) * 4);
  u32x4 km0 = gld16(ws + WS_KMAX, ((lane >> 1) * 32 + head) * 64 + (lane & 1) * 32);
  u32x4 km1 = gld16(ws + WS_KMAX, ((lane >> 1) * 32 + head) * 64 + (lane & 1) * 32 + 16);
  const int vrow = r < 19 ? r : ((r >= 20 && r < 28) ? 0 : 18);
  c.kmb = lds + lane * 16;
  c.ktb = lds + 16384 + r * 8;
  c.vb = lds + 18432 + h * 304 + vrow * 16;
#pragma unroll
  for (int i = 0; i < 3; ++i) {
    c.kma[i] = (unsigned)(uintptr_t)c.kmb + i * 65536; c.kta[i] = (unsigned)(uintptr_t)c.ktb + i * 65536; c.va[i] = (unsigned)(uintptr_t)c.vb + i * 65536;
    PIN(c.kma[i]); PIN(c.kta[i]); PIN(c.va[i]);
  }
#pragma unroll
  for (int i = 0; i < 16; ++i) c.o[i] = 0.f;
  PIN(c.o);
  const __amdgpu_buffer_rsrc_t wrs = mkrsrc(ws, (unsigned)WS_END);
  SBAR();
  asm volatile("s_waitcnt vmcnt(0)" : "+v"(c.qm), "+v"(qtl), "+v"(km0), "+v"(km1)::"memory");
  if (h) qtl = 0;
  float q2 = dot2(qtl, qtl, 0.f);
#pragma unroll
  for (int i = 0; i < 4; ++i) q2 = dot2(c.qm[i], c.qm[i], q2);
  unsigned k2 = max(max(max(km0[0], km0[1]), max(km0[2], km0[3])), max(max(km1[0], km1[1]), max(km1[2], km1[3])));
  { auto rr = __builtin_amdgcn_permlane32_swap(k2, k2, false, false); k2 = max(rr[0], rr[1]); }
  { auto rr = __builtin_amdgcn_permlane16_swap(k2, k2, false, false); k2 = max(rr[0], rr[1]); }
  k2 = max(k2, (unsigned)__builtin_amdgcn_update_dpp(0u, k2, 0x128, 0xf, 0xf, true));
  k2 = max(k2, (unsigned)__builtin_amdgcn_update_dpp(0u, k2, 0x124, 0xf, 0xf, true));
  k2 = max(k2, (unsigned)__builtin_amdgcn_update_dpp(0u, k2, 0x122, 0xf, 0xf, true));
  k2 = max(k2, (unsigned)__builtin_amdgcn_update_dpp(0u, k2, 0x121, 0xf, 0xf, true));
  q2 = xor32_sum(q2);
  const float mshift = __builtin_amdgcn_sqrtf(q2 * __uint_as_float(k2)) * 1.001f;
  c.qt = u32x4{qtl, 0, 0, qtl}; PIN(c.qt);
#pragma unroll
  for (int i = 0; i < 16; ++i) c.negm[i] = -mshift;
  PIN(c.negm);
  STAMP(1);
  asm volatile("s_barrier" ::: "memory");
  STAMP(2);
  asm volatile("ds_read_b64 v[92:93], %[kt]\n\tds_read_b128 v[70:73], %[ka]\n\tds_read_b128 v[66:69], %[ka] offset:1024\n\t"
               "ds_read_b128 v[88:91], %[va]\n\tds_read_b64 v[94:95], %[kt] offset:256\n\ts_waitcnt lgkmcnt(3)\n\t"
               "v_mfma_f32_32x32x8_bf16 v[34:49], v[92:93], v[102:103], v[18:33]\n\t"
               "v_mfma_f32_32x32x16_bf16 v[34:49], v[70:73], v[98:101], v[34:49]\n\ts_nop 11"
               : "={v[34:49]}"(c.sa), "={v[50:65]}"(c.sb), "={v[66:69]}"(c.kf), "={v[70:73]}"(c.p1), "={v[84:87]}"(c.v1), "={v[88:91]}"(c.v0), "={v[92:95]}"(c.ktl)
               : "{v[18:33]}"(c.negm), "{v[98:101]}"(c.qm), "{v[102:105]}"(c.qt), [ka] "v"(c.kma[0]), [kt] "v"(c.kta[0]), [va] "v"(c.va[0]));
  attn_group<0>(c);
  asm volatile("s_barrier" ::: "memory");
  attn_group<1>(c); attn_group<2>(c);
  asm volatile("s_barrier" ::: "memory");
  attn_group<3>(c); attn_group<4>(c);
  asm volatile("s_barrier" ::: "memory");
  STAMP(3);
  attn_group<5>(c); attn_group<6>(c); attn_group<7>(c); attn_group<8>(c); attn_group<9>(c); attn_group<10>(c);
  attn_group<11>(c); attn_group<12>(c); attn_group<13>(c); attn_group<14>(c); attn_group<15>(c);
  asm volatile("s_waitcnt lgkmcnt(0)\n\tv_mfma_f32_32x32x16_bf16 v[2:17], v[84:87], v[70:73], v[2:17]\n\ts_nop 11"
               : "+{v[2:17]}"(c.o) : "{v[84:87]}"(c.v1), "{v[70:73]}"(c.p1), "{v[18:33]}"(c.negm));
  STAMP(4);
  f32x16 o = c.o;
  float l;
  { auto rr = __builtin_amdgcn_permlane32_swap(__float_as_uint(o[10]), __float_as_uint(o[10]), false, false); l = __uint_as_float(rr[0]); }
  if (__builtin_expect(__any(!(l >= 5.421011e-20f)), 0)) {
    float mx = -INFINITY;
    f32x16 z, negm;
#pragma unroll
    for (int i = 0; i < 16; ++i) z[i] = 0.f;
#pragma unroll 1
    for (int tt = 0; tt < 64; ++tt) {
      const int ck = tt >> 4, tl = tt & 15;
      const u32x2 k2 = *(LDSP const u32x2*)(c.ktb + ck * CHUNK_B + (tl >> 1) * 256);
      const u32x4 ktl = {k2[0], k2[1], 0, 0};
      const u32x4 kf = ldsr16(c.kmb + ck * CHUNK_B + tl * 1024);
      const u32x4 qq = {(tl & 1) ? 0u : qtl, (tl & 1) ? qtl : 0u, 0, 0};
      f32x16 sc = mfma32(ktl, qq, z);
      sc = mfma32(kf, c.qm, sc);
#pragma unroll
      for (int i = 0; i < 16; ++i) mx = fmaxf(mx, sc[i]);
    }
    mx = xor32_max(mx);
#pragma unroll
    for (int i = 0; i < 16; ++i) { negm[i] = -mx; o[i] = 0.f; }
#pragma unroll 1
    for (int gq = 0; gq < 16; ++gq) {
      const int ck = gq >> 2, gg = gq & 3;
      attn_group_slow(c.kmb + ck * CHUNK_B + gg * 4096, c.ktb + ck * CHUNK_B + gg * 512, c.vb + ck * CHUNK_B + gg * 4864, c.qm, qtl, negm, o);
    }
    auto rr = __builtin_amdgcn_permlane32_swap(__float_as_uint(o[10]), __float_as_uint(o[10]), false, false);
    l = __uint_as_float(rr[0]);
  }
  const float rl = 1.0f / l;
  u32x4 ou;
#pragma unroll
  for (int i = 0; i < 4; ++i) ou[i] = cvtpk(o[2 * i] * rl, o[2 * i + 1] * rl);
  const int q = 32 * qt32 + r;
  const unsigned ob = (unsigned)WS_OFR + (q >> 4) * 18432 + (q & 15) * 16;
  bst16<SC1>(ou, wrs, ob + (head >> 1) * 1024 + (2 * (head & 1) + h) * 256, 0);
  __builtin_amdgcn_raw_buffer_store_b32(cvtpk(o[8] * rl, o[9] * rl), wrs, h ? DROP : ob + (16 + (head >> 4)) * 1024 + ((head >> 2) & 3) * 256 + (head & 3) * 4, 0, SC1);
  STAMP(5);
  const int m = lane & 15, g = lane >> 4, kh = w >> 2, cg = bi >> 2;
  const int nt0 = 9 * (cg >> 1) + 5 * (cg & 1), mt = 16 * xcd + 4 * (bi & 3) + (w & 3), row = 16 * mt + m;
  const unsigned lds0 = (unsigned)(uintptr_t)lds, lane16 = lane * 16;
  asm volatile("s_barrier" ::: "memory");
#define K3_W(i) do { const int p = min(8 * (i) + w, 89), st = (p * 205) >> 10, t = p - 5 * st;                                  \
    dma16(ws + WS_WO + (st * 36 + nt0 + min(t, 4 - (cg & 1))) * 1024, lane16, lds0 + p * 1024); } while (0)
#pragma unroll
  for (int i = 0; i < K3_PRE; ++i) K3_W(i);
  asm volatile("s_waitcnt vmcnt(%0)\n\ts_barrier" ::"i"(K3_PRE) : "memory");
  if (w == 0 && lane < K3_REP) __hip_atomic_fetch_add((gu32*)(ws + WS_CNT + (128 + qb * K3_REP) * 128) + lane * 32, 1u, __ATOMIC_RELAXED, __HIP_MEMORY_SCOPE_AGENT);
#pragma unroll
  for (int i = K3_PRE; i < 12; ++i) K3_W(i);
#undef K3_W
  dma16(ws + WS_WBAR + min(w, 2) * 1024, lane16, lds0 + K3_WBAR + min(w, 2) * 1024);
  const bool t4 = kh == 0 && !(cg & 1);
  const unsigned uo = mt * 18432 + (2 * nt0 + 4 * kh + (g >> 1)) * 256 + m * 16 + (g & 1) * 8;
  u32x2 xr[3];
  const unsigned xo = row * 1152 + (nt0 + 2 * kh) * 32 + g * 8;
  asm volatile("global_load_dwordx2 %0, %1, %2" : "=v"(xr[0]) : "v"(xo), "s"(x));
  asm volatile("global_load_dwordx2 %0, %1, %2 offset:32" : "=v"(xr[1]) : "v"(xo), "s"(x));
  asm volatile("global_load_dwordx2 %0, %1, %2" : "=v"(xr[2]) : "v"(t4 ? xo + 128 : xo), "s"(x));
  unsigned xm = gld4(ws + WS_XMEAN, row * 4);
  if (w == 0) wait_count((gu32*)(ws + WS_CNT + (128 + xcd * K3_REP + bi % K3_REP) * 128), 0u, 32u);
  LDSP const char* wa = lds + (45 * kh + 2 * kh) * 1024 + lane16;
  LDSP const char* wb = lds + (45 * kh + 2 - 2 * kh) * 1024 + lane16;
  LDSP const char* wc = lds + (45 * kh + 4) * 1024 + lane16;
  LDSP const char* wbl = lds + K3_WBAR + (m & 1) * 1152 + kh * 576 + g * 16;
  unsigned so = ((g & 1) ? (unsigned)WS_HC : (unsigned)WS_HU) + (uo & ~8u);
  PIN(wa); PIN(wb); PIN(wc); PIN(wbl); PIN(so);
  asm volatile("s_waitcnt vmcnt(0)\n\ts_barrier" : "+v"(xr[0]), "+v"(xr[1]), "+v"(xr[2]), "+v"(xm)::"memory");
  STAMP(6);
  u32x4 O[9], wf[6];
  const char* obase = ws + WS_OFR + rfl((mt * 18 + 9 * kh) * 1024);
#pragma unroll
  for (int i = 0; i < 9; ++i)
    asm volatile("global_load_dwordx4 %0, %1, %2 sc1" : "=v"(O[i]) : "v"(lane16), "s"(obase + i * 1024));
#define K3_A(f) (((f) + 2) % 6)
#define K3_RD(f) (K3_A(f) == 5 ? ldsr16(wbl + ((f) / 6) * 64) : ldsr16((K3_A(f) < 2 ? wa : (K3_A(f) < 4 ? wb : wc)) + ((f) / 6) * 5120 + (K3_A(f) < 4 ? ((f) & 1) * 1024 : 0)))
#pragma unroll
  for (int f = 0; f < 5; ++f) wf[f] = K3_RD(f);
  f32x4 acc[6];
#pragma unroll
  for (int f = 0; f < 54; ++f) {
    if (f % 6 == 0) asm volatile("s_waitcnt vmcnt(%1)" : "+v"(O[f / 6]) : "i"(8 - f / 6));
    if (f + 5 < 54) wf[(f + 5) % 6] = K3_RD(f + 5);
    if (f < 6) G_MFMA0(acc[K3_A(f)], wf[f % 6], O[f / 6]);
    else G_MFMA(acc[K3_A(f)], wf[f % 6], O[f / 6]);
    SBAR();
  }
#undef K3_RD
#undef K3_A
  asm volatile("s_nop 7" : "+v"(acc[0]), "+v"(acc[1]), "+v"(acc[2]), "+v"(acc[3]), "+v"(acc[4]), "+v"(acc[5]));
  LDSP char* xc = lds + K3_XC + lane16;
#pragma unroll
  for (int i = 0; i < 4; ++i) *(LDSP f32x4*)(xc + w * 4096 + i * 1024) = acc[2 + i];
  asm volatile("s_waitcnt lgkmcnt(0)\n\ts_barrier" ::: "memory");
  f32x4 v[4];
#pragma unroll
  for (int i = 0; i < 4; ++i) v[i] = *(LDSP const f32x4*)(xc + (w ^ 4) * 4096 + i * 1024);
  SBAR();
  const float mean = __uint_as_float(xm) + (acc[5][0] + v[3][0]) + (acc[5][1] + v[3][1]);
  float ssq = 0.f;
#pragma unroll
  for (int t = 0; t < 3; ++t) {
    const f32x4 a = (t < 2 ? acc[t] : acc[4]) + v[t];
    const unsigned h0 = cvtpk(bflo(xr[t][0]) + a[0], bfhi(xr[t][0]) + a[1]);
    const unsigned h1 = cvtpk(bflo(xr[t][1]) + a[2], bfhi(xr[t][1]) + a[3]);
    const unsigned c0 = cvtpk(bflo(h0) - mean, bfhi(h0) - mean);
    const unsigned c1 = cvtpk(bflo(h1) - mean, bfhi(h1) - mean);
    if (t < 2 || t4) { ssq = dot2(c0, c0, ssq); ssq = dot2(c1, c1, ssq); }
    const auto r0 = __builtin_amdgcn_permlane16_swap(h0, c0, false, false);
    const auto r1 = __builtin_amdgcn_permlane16_swap(h1, c1, false, false);
    bst16<SC1>(u32x4{r0[0], r1[0], r0[1], r1[1]}, wrs, t < 2 ? so + t * 512 : (t4 ? so + 2048 : DROP), 0);
  }
  ssq = xor32_sum(xor16_sum(ssq));
  __builtin_amdgcn_raw_buffer_store_b32(__float_as_uint(ssq), wrs, (unsigned)WS_SSQ + ((2 * cg + kh) * 2048 + row) * 4, 0, SC1);
  STAMP(7); STAMP_OUT(2, 8);
}

constexpr int K4_SLOT = 36864, K4_LDS = 163840;
constexpr int K4_PF = 4, K4_POLL = 10, K5_SIG = 154;
constexpr int K5_PERM[18] = {0, 1, 2, 6, 7, 8, 9, 10, 11, 15, 16, 17, 3, 4, 5, 12, 13, 14};
constexpr int K5_R[4] = {36864, 73728, 110592, 0};
constexpr int K5_OC[9] = {65536, 102400, 139264, 147456, 155648, 0, 8192, 16384, 24576};
struct K5P { const char* wb; unsigned q[4], qs[4], qo, qos, lds0; };
template <int Z> DI void k5_piece(const K5P& k, int i, int w, unsigned lane16) {
  constexpr int P0 = K5_PERM[2 * Z] * 9216, D = (K5_PERM[2 * Z + 1] - K5_PERM[2 * Z]) * 9216;
  if (i < 3 || (w >> 2) == (Z & 1))
    dma16(k.wb + (P0 + k.q[i] + k.qs[i] * D), lane16, k.lds0 + K5_R[Z & 3] + (i < 3 ? 8 * i + w : 24 + (w & 3)) * 1024);
}
template <int Z> DI void k5_own(const K5P& k, int w, unsigned lane16) {
  constexpr int P0 = K5_PERM[2 * Z] * 9216, D = (K5_PERM[2 * Z + 1] - K5_PERM[2 * Z]) * 9216;
  dma16(k.wb + (P0 + k.qo + k.qos * D), lane16, k.lds0 + K5_OC[Z] + w * 1024);
}
#define POLL(dst, p) asm volatile("global_load_dword %0, %1, %2 sc1" : "=v"(dst) : "v"(0u), "s"(p))
struct K4C {
  const char *wb, *xb; unsigned lane16, lds0; int w, dir;
  unsigned pf1, poll;
  K5P wd;
  gu32 *cntA, *cntB;
  const char *ab, *ab2; u32x4 A0[2][4];
  LDSP const char* wl[4];
  u32x4 X[2][18], wf[K4_PF + 1];
  f32x4 acc[4][4];
  float c1[2], c2[2];
  __amdgpu_buffer_rsrc_t ars; unsigned avo; int T0;
  float ea[2][2], et[2][2]; unsigned pk[2][4];
};
DI void k4_piece(const K4C& c, int tile, int p) {
  dma16(c.wb + (tile * c.dir * K4_SLOT + p * 1024), c.lane16, c.lds0 + (tile & 3) * K4_SLOT + p * 1024);
}
template <int ZW> DI void k4_issue_w(K4C& c, int i) {
  if (ZW < 9) {
    if (i == 0) k4_piece(c, c.w >> 2, 4 * ZW + (c.w & 3));
    else if ((c.w >> 2) == (ZW & 1)) k4_piece(c, 2, 4 * ZW + (c.w & 3));
  } else if (ZW == 9) { k4_piece(c, 3, 8 * i + c.w); }
}
template <int Z, int I> DI void k4_issue1(K4C& c) {
  if (I == 0) k4_issue_w<Z + 1>(c, 0);
  else if (I < 5) {
    c.X[(I - 1) >> 1][2 * Z + ((I - 1) & 1)] = gld16(c.xb + (((I - 1) >> 1) * 18432 + (2 * Z + ((I - 1) & 1)) * 1024), c.lane16);
  }
  else k4_issue_w<Z + 1>(c, 1);
}
template <int TL, int I> DI void k4_issue2(K4C& c) {
  if (I < 4) k4_piece(c, TL, 8 * I + c.w);
  else if ((c.w >> 2) == (TL & 1)) k4_piece(c, TL, 32 + (c.w & 3));
}
template <int TL, int E, int U> DI void k4_epi(K4C& c) {
  constexpr int e = U >> 2, u = U & 3, mt = e >> 2, i = e & 3, q = e & 1;
  if constexpr (U < 32) {
    if (u == 0) { const float a = c.acc[TL & 3][mt][i] * c.c1[mt]; c.ea[E][q] = a; c.et[E][q] = a * c.acc[TL & 3][2 + mt][i]; PIN(c.ea[E][q]); PIN(c.et[E][q]); }
    if (u == 1) { c.ea[E][q] = __builtin_amdgcn_exp2f(c.ea[E][q]); PIN(c.ea[E][q]); }
    if (u == 2) { c.ea[E][q] = __builtin_amdgcn_rcpf(__builtin_fmaf(c.ea[E][q], c.c2[mt], c.c2[mt])); PIN(c.ea[E][q]); }
    if (u == 3) { c.et[E][q] *= c.ea[E][q]; PIN(c.et[E][q]); if (q) { c.pk[E][e >> 1] = cvtpk(c.et[E][0], c.et[E][1]); PIN(c.pk[E][e >> 1]); } }
  } else if constexpr (U == 32) {
    auto r0 = __builtin_amdgcn_permlane16_swap(c.pk[E][0], c.pk[E][2], false, false);
    auto r1 = __builtin_amdgcn_permlane16_swap(c.pk[E][1], c.pk[E][3], false, false);
    c.pk[E][0] = r0[0]; c.pk[E][1] = r1[0]; c.pk[E][2] = r0[1]; c.pk[E][3] = r1[1];
  } else if constexpr (U == 33) {
    const int T = c.T0 + TL * c.dir;
    bst16<SC1>(u32x4{c.pk[E][0], c.pk[E][1], c.pk[E][2], c.pk[E][3]}, c.ars, c.avo, (T >> 1) * 1024 + (T & 1) * 512);
  }
}
constexpr int k4_tile(int F) { return F < 108 ? (F % 6) >> 1 : 3 + (F - 108) / 36; }
constexpr int k4_step(int F) { return F < 108 ? F / 6 : ((F - 108) % 36) >> 1; }
constexpr int k4_addr(int F) { return k4_step(F) * 2048 + (F & 1) * 1024; }
template <int F> DI void k4_frag(K4C& c) {
  constexpr int TL = k4_tile(F), S = k4_step(F), M = F & 1, N = F + K4_PF;
  const u32x4 a = c.wf[F % (K4_PF + 1)];
  if constexpr (F < 108) { if constexpr ((F + 4) % 3 == 0) __builtin_amdgcn_s_setprio(3 - ((F + 4) % 12) / 3); }
  else if constexpr ((F - 108 + 4) % 9 == 0) __builtin_amdgcn_s_setprio(3 - ((F - 108 + 4) % 36) / 9);
  if (S == 0) {
    G_MFMA0(c.acc[TL & 3][2 * M], a, c.X[0][S]);
    G_MFMA0(c.acc[TL & 3][2 * M + 1], a, c.X[1][S]);
  } else {
    G_MFMA(c.acc[TL & 3][3 * M], a, c.X[M][S]);
    G_MFMA(c.acc[TL & 3][M + 1], a, c.X[1 - M][S]);
  }
  if constexpr (N < 108 && N % 12 == 0) {
    constexpr int Z = N / 12;
    if (Z < 8) asm volatile("s_waitcnt vmcnt(5)\n\ts_barrier" : "+v"(c.X[0][2 * Z]), "+v"(c.X[0][2 * Z + 1]), "+v"(c.X[1][2 * Z]), "+v"(c.X[1][2 * Z + 1])::"memory");
    else asm volatile("s_waitcnt vmcnt(2)\n\ts_barrier" : "+v"(c.X[0][2 * Z]), "+v"(c.X[0][2 * Z + 1]), "+v"(c.X[1][2 * Z]), "+v"(c.X[1][2 * Z + 1])::"memory");
  } else if constexpr (N >= 108 && N < 324 && (N - 108) % 36 == 0) {
    constexpr int NT = k4_tile(N);
    if (NT == 3) asm volatile("s_waitcnt vmcnt(0) lgkmcnt(0)\n\ts_barrier" ::: "memory");
    else if (NT == 8) {
      asm volatile("s_waitcnt vmcnt(0) lgkmcnt(0)\n\ts_barrier" ::: "memory");
      if (c.w == 0 && c.lane16 == 0) __hip_atomic_fetch_add(c.cntA, 1u, __ATOMIC_RELAXED, __HIP_MEMORY_SCOPE_AGENT);
    }
    else asm volatile("s_waitcnt vmcnt(4) lgkmcnt(0)\n\ts_barrier" ::: "memory");
  }
  if constexpr (N < 324) c.wf[N % (K4_PF + 1)] = ldsr16(c.wl[k4_tile(N) & 3] + k4_addr(N));
  if constexpr (F < 108) {
    constexpr int Z = F / 12, I = F % 12;
    if constexpr (Z == 0 && I < 4 && !(I & 1)) k4_issue1<2, I / 2>(c);
    if constexpr (Z < 7 && I < 8 && (I & 1)) k4_issue1<Z + 2, 2 + I / 2>(c);
    if constexpr (Z < 6 && I > 8 && (I & 1)) k4_issue1<Z + 3, (I - 9) / 2>(c);
    if constexpr (Z == 6 && I == 10) k4_issue2<3, 2>(c);
    if constexpr (Z == 7 && (I == 1 || I == 3)) k4_issue2<3, 3 + I / 2>(c);
  } else {
    constexpr int P = (F - 108) % 36;
    if constexpr (TL == 3) { if constexpr (P % 3 == 0 && P < 15) k4_issue2<4, P / 3>(c); if constexpr (P % 3 == 0 && P >= 15 && P < 30) k4_issue2<5, (P - 15) / 3>(c); }
    else if constexpr (TL + 2 < 9 && P % 4 == 0 && P < 20) k4_issue2<TL + 2, P / 4>(c);
    if constexpr (TL == 7 && P == 0) CODE_PF(c.pf1, k_ffn, k_end, 0, 2 + (c.w & 1), ((blockIdx.x >> 3) == 0 && c.w < 2) ? 128u : 0u);
    if constexpr (TL == 7 && P % 3 == 2 && P <= 11) k5_piece<0>(c.wd, P / 3, c.w, c.lane16);
    if constexpr (TL == 8 && P < 4) k5_piece<1>(c.wd, P, c.w, c.lane16);
    if constexpr (TL == 8 && P >= 4 && P < 8) k5_piece<2>(c.wd, P - 4, c.w, c.lane16);
    if constexpr (TL == 8 && P == K4_POLL) POLL(c.poll, c.cntA);
    if constexpr (TL == 8 && P == K4_POLL + 12) { asm volatile("s_waitcnt vmcnt(0)" : "+v"(c.poll), "+v"(c.pf1)); wait_count(c.cntA, c.poll, 8u); }
    if constexpr (TL == 8 && P > K4_POLL + 12 && P <= K4_POLL + 20) {
      constexpr int i = P - K4_POLL - 13, z = i >> 2, j = (i & 1) * 2 + ((i >> 1) & 1);
      asm volatile("global_load_dwordx4 %0, %1, %2 sc1" : "=v"(c.A0[z][j]) : "v"(c.lane16), "s"(((j >> 1) ? c.ab2 : c.ab) + K5_PERM[2 * z + (j & 1)] * 1024));
    }
    if constexpr (P >= 2) {
      if constexpr (TL == 3) { k4_epi<0, 0, P - 2>(c); k4_epi<1, 1, P - 2>(c); }
      else if constexpr (TL == 4) { k4_epi<2, 0, P - 2>(c); k4_epi<3, 1, P - 2>(c); }
      else k4_epi<TL - 1, 0, P - 2>(c);
    }
  }
  SBAR();
}
template <int B, int... Fs> DI void k4_run(K4C& c, std::integer_sequence<int, Fs...>) { (k4_frag<B + Fs>(c), ...); }
DI void k4_last(K4C& c) {
  float a[8], t[8];
#pragma unroll
  for (int e = 0; e < 8; ++e) { a[e] = c.acc[0][e >> 2][e & 3] * c.c1[e >> 2]; t[e] = a[e] * c.acc[0][2 + (e >> 2)][e & 3]; }
#pragma unroll
  for (int e = 0; e < 8; ++e) a[e] = __builtin_amdgcn_exp2f(a[e]);
#pragma unroll
  for (int e = 0; e < 8; ++e) a[e] = __builtin_amdgcn_rcpf(__builtin_fmaf(a[e], c.c2[e >> 2], c.c2[e >> 2]));
#pragma unroll
  for (int e = 0; e < 4; ++e) c.pk[0][e] = cvtpk(t[2 * e] * a[2 * e], t[2 * e + 1] * a[2 * e + 1]);
  k4_epi<8, 0, 32>(c); k4_epi<8, 0, 33>(c);
}
DI f32x4 addbf(f32x4 a, unsigned lo, unsigned hi) { return a + f32x4{bflo(lo), bfhi(lo), bflo(hi), bfhi(hi)}; }
struct K5C {
  const char *ab, *ab2;
  LDSP const char *ra[4], *oa[3], *xr;
  LDSP char* xw;
  u32x4 A[9][4], wf[5];
  f32x4 acc[2][7], own[2][2], v[4];
  const char* hb; unsigned ho; int kq; u32x2 hr[3];
  __amdgpu_buffer_rsrc_t wrs; unsigned tw, p8s;
  gu32* cnt;
};
template <int Z, int J> DI void k5_issue(K5C& c, const K4C& k) {
  if constexpr (Z < 7 && J < 4) asm volatile("global_load_dwordx4 %0, %1, %2 sc1" : "=v"(c.A[Z + 2][J]) : "v"(k.lane16), "s"(((J >> 1) ? c.ab2 : c.ab) + K5_PERM[2 * (Z + 2) + (J & 1)] * 1024));
  if constexpr (Z < 6 && J >= 4) k5_piece<Z + 3>(k.wd, J - 4, k.w, k.lane16);
  if constexpr (Z == 6 && J >= 4) k5_own<J - 4>(k.wd, k.w, k.lane16);
  if constexpr (Z == 7 && J == 0) k5_own<4>(k.wd, k.w, k.lane16);
  if constexpr (Z == 7 && J == 1) asm volatile("global_load_dwordx2 %0, %1, %2" : "=v"(c.hr[0]) : "v"(c.ho), "s"(c.hb + c.kq * 1024));
  if constexpr (Z == 7 && J == 2) asm volatile("global_load_dwordx2 %0, %1, %2 offset:512" : "=v"(c.hr[1]) : "v"(c.ho), "s"(c.hb + c.kq * 1024));
  if constexpr (Z == 7 && J == 3) asm volatile("global_load_dwordx2 %0, %1, %2" : "=v"(c.hr[2]) : "v"(c.ho), "s"(c.hb + 4096));
  if constexpr (Z == 8 && J < 4) k5_own<5 + J>(k.wd, k.w, k.lane16);
}
template <int N> DI u32x4 k5_read(const K5C& c) {
  if constexpr (N < 126) return ldsr16(c.ra[(N / 14) & 3] + (N % 14) * 1024);
  else { constexpr int o = K5_OC[(N - 126) / 4] + ((N - 126) & 3) * 1024; return ldsr16(c.oa[o >> 16] + (o & 0xffff)); }
}
template <int F> DI void k5_frag(K5C& c, K4C& k) {
  constexpr int N = F + 4;
  const u32x4 a = c.wf[F % 5];
  if constexpr (F < 126) {
    constexpr int Z = F / 14, f = F % 14, st = f / 7, j = f % 7;
    if (F < 7) { G_MFMA0(c.acc[0][j], a, c.A[Z][st]); G_MFMA0(c.acc[1][j], a, c.A[Z][2 + st]); }
    else { G_MFMA(c.acc[1][j], a, c.A[Z][2 + st]); G_MFMA(c.acc[0][j], a, c.A[Z][st]); }
    if constexpr (f == 10 && Z == 8) asm volatile("s_waitcnt vmcnt(4)\n\ts_barrier" : "+v"(c.hr[0]), "+v"(c.hr[1]), "+v"(c.hr[2])::"memory");
    if constexpr (f == 10 && Z < 8) {
      u32x4(&A)[4] = c.A[Z + 1];
      if (Z == 0) asm volatile("s_waitcnt vmcnt(9) lgkmcnt(0)\n\ts_barrier" : "+v"(A[0]), "+v"(A[1]), "+v"(A[2]), "+v"(A[3])::"memory");
      else if (Z == 3) asm volatile("s_waitcnt vmcnt(4) lgkmcnt(0)\n\ts_barrier" : "+v"(A[0]), "+v"(A[1]), "+v"(A[2]), "+v"(A[3]), "+v"(k.poll)::"memory");
      else if (Z < 7) asm volatile("s_waitcnt vmcnt(7) lgkmcnt(0)\n\ts_barrier" : "+v"(A[0]), "+v"(A[1]), "+v"(A[2]), "+v"(A[3])::"memory");
      else asm volatile("s_waitcnt vmcnt(8) lgkmcnt(0)\n\ts_barrier" : "+v"(A[0]), "+v"(A[1]), "+v"(A[2]), "+v"(A[3])::"memory");
      if (Z == 1 && k.w == 0 && k.lane16 == 0) __hip_atomic_fetch_add(k.cntB, 1u, __ATOMIC_RELAXED, __HIP_MEMORY_SCOPE_AGENT);
      if (Z == 3) wait_count(k.cntB, k.poll, 8u);
    }
    c.wf[N % 5] = k5_read<N>(c);
    if constexpr (Z == 3 && f == 1) POLL(k.poll, k.cntB);
    constexpr int slot = f == 0 ? 2 : (f == 2 ? 3 : (f == 4 ? 4 : (f == 5 ? 5 : (f == 7 ? 6 : (f == 9 ? 7 : -1)))));
    if constexpr (F == 0) { k5_issue<0, 0>(c, k); k5_issue<0, 1>(c, k); }
    if constexpr (slot >= 0) k5_issue<Z, slot>(c, k);
    if constexpr (Z < 8 && f > 10 && (f & 1)) k5_issue<Z + 1, (f - 11) / 2>(c, k);
  } else {
    constexpr int B = F - 126, z = B / 4, st = (B >> 1) & 1, t = B & 1;
    if (B < 2) { G_MFMA0(c.own[0][t], a, c.A[z][st]); G_MFMA0(c.own[1][t], a, c.A[z][2 + st]); }
    else { G_MFMA(c.own[1][t], a, c.A[z][2 + st]); G_MFMA(c.own[0][t], a, c.A[z][st]); }
    if constexpr (F == 131) asm volatile("s_waitcnt vmcnt(0) lgkmcnt(0)\n\ts_barrier" ::: "memory");
    if constexpr (F == K5_SIG) {
      asm volatile("s_waitcnt vmcnt(0)\n\ts_barrier" ::: "memory");
      if (k.w == 0 && k.lane16 == 0) __hip_atomic_fetch_add(c.cnt, 1u, __ATOMIC_RELAXED, __HIP_MEMORY_SCOPE_AGENT);
    }
    if constexpr (N < 162) c.wf[N % 5] = k5_read<N>(c);
  }
  if constexpr (F >= 123 && F < 130) { PIN(c.acc[1][F - 123]); *(LDSP f32x4*)(c.xw + (F - 123) * 1024) = c.acc[1][F - 123]; }
  if constexpr (F == 131) { _Pragma("unroll") for (int i = 0; i < 4; ++i) c.v[i] = *(LDSP const f32x4*)(c.xr + i * 1024); }
  if constexpr (F == 133 || F == 135 || F == 138) {
    constexpr int j = F == 133 ? 0 : (F == 135 ? 1 : 2), o = 2 * (j & 1);
    const f32x4 x = c.acc[0][2 * j] + c.v[o], y = c.acc[0][2 * j + 1] + c.v[o + 1];
    bst16<SC1>(u32x4{cvtpk(x[0], x[1]), cvtpk(x[2], x[3]), cvtpk(y[0], y[1]), cvtpk(y[2], y[3])}, c.wrs, k.lane16,
              (unsigned)WS_PART + ((((c.kq + j + 1) & 3) * 512 + c.tw) * 3 + j) * 1024);
    if constexpr (F == 135) { _Pragma("unroll") for (int i = 0; i < 3; ++i) c.v[i] = *(LDSP const f32x4*)(c.xr + (4 + i) * 1024); }
  }
  if constexpr (F == 140) {
    c.acc[0][6] += c.v[2];
    __builtin_amdgcn_raw_buffer_store_b64(u32x2{cvtpk(c.acc[0][6][0], c.acc[0][6][1]), cvtpk(c.acc[0][6][2], c.acc[0][6][3])}, c.wrs, c.p8s, c.kq * 512, 16);
    PIN(c.acc[0][6]);
  }
  SBAR();
}
template <int B, int... Fs> DI void k5_run(K5C& c, K4C& k, std::integer_sequence<int, Fs...>) { (k5_frag<B + Fs>(c, k), ...); }

extern "C" __global__ __launch_bounds__(512, 2) void k_ffn() {
  extern __shared__ __attribute__((aligned(16))) char smem[];
  char* ws = (char*)((cptr_t)(uintptr_t)g_ptr)[4]; char* out = (char*)((cptr_t)(uintptr_t)g_ptr)[5];
  asm volatile("" : "+s"(out), "+s"(ws));
  LDSP char* lds = (LDSP char*)smem;
  const int tid = threadIdx.x, lane = tid & 63, w = rfl(tid >> 6);
  const int xcd = blockIdx.x & 7, bi = blockIdx.x >> 3;
  const int m = lane & 15, g = lane >> 4, kq = xcd >> 1;
  STAMP_DECL; STAMP(0);
  K4C c;
  {
    const int rb = 4 * (xcd & 1) + (bi & 3), fs = 8 * kq + (bi >> 2), mt = 16 * rb + 2 * w;
    c.lds0 = (unsigned)(uintptr_t)lds; c.lane16 = lane * 16; c.w = w;
    c.T0 = 9 * fs + 8 * (fs & 1); c.dir = 1 - 2 * (fs & 1);
    c.wb = ws + WS_W1 + (size_t)c.T0 * K4_SLOT;
    c.xb = ws + WS_HC + (size_t)mt * 18432;
    k4_issue_w<0>(c, 0); k4_issue1<0, 1>(c); k4_issue1<0, 3>(c); k4_issue_w<0>(c, 1); k4_issue1<0, 2>(c); k4_issue1<0, 4>(c);
    k4_issue1<0, 0>(c); k4_issue1<0, 5>(c);
    unsigned sq[8];
#pragma unroll
    for (int i = 0; i < 8; ++i) sq[i] = gld4(ws + WS_SSQ + (i >> 1) * 8192 + (i & 1) * 64, 4 * g * 8192 + (16 * mt + m) * 4);
    k4_issue1<1, 0>(c); k4_issue1<1, 1>(c); k4_issue1<1, 2>(c); k4_issue1<1, 3>(c); k4_issue1<1, 4>(c); k4_issue1<1, 5>(c);
    SBAR();
#pragma unroll
    for (int i = 0; i < 4; ++i) { c.wl[i] = lds + c.lane16 + i * K4_SLOT; PIN(c.wl[i]); }
    c.ars = mkrsrc(ws + WS_ACT, 128u * 144 * 1024);
    c.avo = (mt + (g & 1)) * (144 * 1024) + (g >> 1) * 256 + m * 16;
    c.wd.wb = ws + WS_WD + ((size_t)(bi >> 3) * 144 + 36 * kq) * 9 * 1024;
#pragma unroll
    for (int i = 0; i < 4; ++i) {
      const int p = i < 3 ? 8 * i + w : 24 + (w & 3), st = (p * 37) >> 8, t = p - 7 * st + 2;
      c.wd.q[i] = ((st >> 1) * 162 + (t < 8 ? (t + 2 * kq) & 7 : 8)) * 1024;
      c.wd.qs[i] = st & 1;
      asm volatile("" : "+s"(c.wd.q[i]), "+s"(c.wd.qs[i]));
    }
    c.wd.qo = ((w >> 2) * 162 + 2 * kq + (w & 1)) * 1024; c.wd.qos = (w >> 1) & 1; c.wd.lds0 = c.lds0;
    c.cntA = (gu32*)(ws + WS_CNT + (64 + rb * 4 + kq) * 128);
    STAMP(1);
    asm volatile("s_waitcnt vmcnt(15)\n\ts_barrier" : "+v"(c.X[0][0]), "+v"(c.X[0][1]), "+v"(c.X[1][0]), "+v"(c.X[1][1])::"memory");
#pragma unroll
    for (int i = 0; i < K4_PF; ++i) c.wf[i] = ldsr16(c.wl[k4_tile(i)] + k4_addr(i));
    SBAR();
    k4_run<0>(c, std::make_integer_sequence<int, 12>{});
    asm volatile("" : "+v"(sq[0]), "+v"(sq[1]), "+v"(sq[2]), "+v"(sq[3]), "+v"(sq[4]), "+v"(sq[5]), "+v"(sq[6]), "+v"(sq[7]));
#pragma unroll
    for (int t = 0; t < 2; ++t) {
      float ss = (__uint_as_float(sq[t]) + __uint_as_float(sq[2 + t])) + (__uint_as_float(sq[4 + t]) + __uint_as_float(sq[6 + t]));
      ss = xor32_sum(xor16_sum(ss));
      const float v = ss * (1.0f / DM) + LN_EPS, rs = __builtin_amdgcn_rsqf(v);
      c.c1[t] = -LOG2E * rs; c.c2[t] = -LOG2E * (v * rs);
      PIN(c.c1[t]); PIN(c.c2[t]);
    }
    SBAR();
    k4_run<12>(c, std::make_integer_sequence<int, 276>{});
    c.cntB = c.cntA + 32 * 32;
  }
  const int rb = 8 * (xcd & 1) + (bi & 7), nb = bi >> 3, mg = w & 3, kh = w >> 2, mu = 8 * rb + 2 * mg + kh;
  gu32* prodB = c.cntB;
  c.cntA = (gu32*)(ws + WS_CNT + (64 + (rb >> 1) * 4 + kq) * 128);
  c.ab = ws + WS_ACT + ((size_t)mu * 144 + 36 * kq + 18 * kh) * 1024;
  c.ab2 = ws + WS_ACT + ((size_t)(mu ^ 1) * 144 + 36 * kq + 18 * kh) * 1024;
  k4_run<288>(c, std::make_integer_sequence<int, 36>{});
  STAMP(2);
  K5C d;
  d.ab = c.ab; d.ab2 = c.ab2;
  asm volatile("s_nop 7" : "+v"(c.acc[0][0]), "+v"(c.acc[0][1]), "+v"(c.acc[0][2]), "+v"(c.acc[0][3]));
  k4_last(c);
  SBAR();
  gu32* consB = c.cntA + 32 * 32;
  d.hb = ws + WS_HU + (size_t)mu * 18432 + nb * 4608; d.ho = (g >> 1) * 256 + m * 16 + (g & 1) * 8; d.kq = kq;
#pragma unroll
  for (int i = 0; i < 4; ++i) { d.ra[i] = lds + K5_R[i] + kh * 14336 + c.lane16; PIN(d.ra[i]); }
#pragma unroll
  for (int i = 0; i < 3; ++i) { d.oa[i] = lds + i * 65536 + kh * 4096 + c.lane16; PIN(d.oa[i]); }
  d.xw = lds + (kh ? K5_R[2] : K5_R[1]) + mg * 7168 + c.lane16; PIN(d.xw);
  d.xr = lds + (kh ? K5_R[1] : K5_R[2]) + mg * 7168 + c.lane16; PIN(d.xr);
  const __amdgpu_buffer_rsrc_t wrs = mkrsrc(ws, (unsigned)WS_END), ors = mkrsrc(out, SEQ * DM * 2);
  const int tile = rb * 4 + nb;
  const bool own8 = (mu & 3) == kq;
  const unsigned tw = tile * 8 + w;
  const unsigned p8 = (unsigned)WS_PART8 + tw * 2048 + lane * 8;
  d.cnt = (gu32*)(ws + WS_CNT + tile * 128);
  d.wrs = wrs; d.tw = tw; d.p8s = own8 ? DROP : p8; PIN(d.p8s);
#pragma unroll
  for (int i = 0; i < 4; ++i) { d.A[0][i] = c.A0[0][i]; d.A[1][i] = c.A0[1][i]; }
  asm volatile("s_waitcnt vmcnt(6) lgkmcnt(0)\n\ts_barrier" : "+v"(d.A[0][0]), "+v"(d.A[0][1]), "+v"(d.A[0][2]), "+v"(d.A[0][3])::"memory");
  STAMP(3);
  d.wf[0] = k5_read<0>(d); d.wf[1] = k5_read<1>(d); d.wf[2] = k5_read<2>(d); d.wf[3] = k5_read<3>(d);
  SBAR();
  c.cntB = prodB;
  k5_run<0>(d, c, std::make_integer_sequence<int, 28>{});
  c.cntB = consB;
  k5_run<28>(d, c, std::make_integer_sequence<int, 134>{});
  asm volatile("s_nop 7" : "+v"(d.own[0][0]), "+v"(d.own[0][1]), "+v"(d.own[1][0]), "+v"(d.own[1][1]));
  STAMP(4);
  LDSP char* xo = lds + K5_R[0] + c.lane16;
  *(LDSP f32x4*)(xo + w * 2048) = d.own[1][0];
  *(LDSP f32x4*)(xo + w * 2048 + 1024) = d.own[1][1];
  unsigned oo = (16 * mu + m) * 1152 + nb * 288;
  unsigned oo8 = own8 ? oo + 256 + g * 8 : DROP;
  oo += kq * 64 + (g & 1) * 32 + (g >> 1) * 16;
  PIN(oo); PIN(oo8);
  STAMP(5);
  if (w == 0) wait_count(d.cnt, 0u, 4u);
  asm volatile("s_waitcnt lgkmcnt(0)\n\ts_barrier" ::: "memory");
  STAMP(6);
  u32x4 r[3]; u32x2 r8[3];
#pragma unroll
  for (int j = 0; j < 3; ++j) {
    r[j] = __builtin_amdgcn_raw_buffer_load_b128(wrs, c.lane16, (unsigned)WS_PART + ((kq * 512 + tw) * 3 + j) * 1024, 16);
    r8[j] = __builtin_amdgcn_raw_buffer_load_b64(wrs, own8 ? p8 : DROP, ((kq + 1 + j) & 3) * 512, 16);
  }
  SBAR();
  f32x4 e0 = addbf(d.own[0][0] + *(LDSP const f32x4*)(xo + (w ^ 4) * 2048), d.hr[0][0], d.hr[0][1]);
  f32x4 e1 = addbf(d.own[0][1] + *(LDSP const f32x4*)(xo + (w ^ 4) * 2048 + 1024), d.hr[1][0], d.hr[1][1]);
  f32x4 e8 = addbf(d.acc[0][6], d.hr[2][0], d.hr[2][1]);
#pragma unroll
  for (int j = 0; j < 3; ++j) { e0 = addbf(e0, r[j][0], r[j][1]); e1 = addbf(e1, r[j][2], r[j][3]); e8 = addbf(e8, r8[j][0], r8[j][1]); }
  {
    const auto s0 = __builtin_amdgcn_permlane16_swap(cvtpk(e0[0], e0[1]), cvtpk(e1[0], e1[1]), false, false);
    const auto s1 = __builtin_amdgcn_permlane16_swap(cvtpk(e0[2], e0[3]), cvtpk(e1[2], e1[3]), false, false);
    bst16<SC1>(u32x4{s0[0], s1[0], s0[1], s1[1]}, ors, oo, 0);
    __builtin_amdgcn_raw_buffer_store_b64(u32x2{cvtpk(e8[0], e8[1]), cvtpk(e8[2], e8[3])}, ors, oo8, 0, SC1);
  }
  STAMP(7);
  STAMP_OUT(4, 8);
}
extern "C" __global__ void k_end() {}

extern "C" void kernel_launch(void* const* d_in, const int* in_sizes, int n_in, void* d_out, int out_size, void* d_ws, size_t ws_size,
                              hipStream_t stream) {
  const u16* x = (const u16*)d_in[0];
  const u16* wqkv = (const u16*)d_in[1];
  const u16* wo = (const u16*)d_in[2];
  const u16* wg = (const u16*)d_in[4];
  const u16* wu = (const u16*)d_in[5];
  const u16* wd = (const u16*)d_in[6];
  char* ws = (char*)d_ws;
  (void)hipFuncSetAttribute((const void*)k_qkv, hipFuncAttributeMaxDynamicSharedMemorySize, K1_LDS);
  (void)hipFuncSetAttribute((const void*)k_attn, hipFuncAttributeMaxDynamicSharedMemorySize, K2_LDS);
  (void)hipFuncSetAttribute((const void*)k_ffn, hipFuncAttributeMaxDynamicSharedMemorySize, K4_LDS);
  k_qkv<<<256, 768, K1_LDS, stream>>>(x, wqkv, wo, ws, wg, wu, wd, (char*)d_out);
  k_attn<<<256, 768, K2_LDS, stream>>>();
  k_ffn<<<256, 512, K4_LDS, stream>>>();
}
```

```cpp
#include <hip/hip_runtime.h>
#include <hip/hip_bf16.h>
#include <stdint.h>
#include <utility>
#include <type_traits>

typedef __bf16 bf16x8 __attribute__((ext_vector_type(8)));
typedef __bf16 bf16x2 __attribute__((ext_vector_type(2)));
typedef short s16x4 __attribute__((ext_vector_type(4)));
typedef float f32x16 __attribute__((ext_vector_type(16)));
typedef float f32x4 __attribute__((ext_vector_type(4)));
typedef float f32x2 __attribute__((ext_vector_type(2)));
typedef unsigned u32x4 __attribute__((ext_vector_type(4)));
typedef unsigned u32x2 __attribute__((ext_vector_type(2)));
typedef unsigned short u16;
#define LDSP __attribute__((address_space(3)))
#define DI __device__ __forceinline__
#define SBAR() __builtin_amdgcn_sched_barrier(0)

constexpr int SEQ = 2048, DM = 576, NH = 32, DH = 18, FF = 4608;
constexpr float LOG2E = 1.4426950408889634f;
constexpr float QSCALE = 0.23570226039551584f * LOG2E;
constexpr float LN_EPS = 1e-5f;

constexpr size_t CHUNK_B = 37888;
constexpr size_t KV_HEAD_B = 4 * CHUNK_B;
constexpr size_t WS_QM = 0;
constexpr size_t WS_QT = WS_QM + (size_t)NH * 65536;
constexpr size_t WS_KV = WS_QT + (size_t)NH * 8192;
constexpr size_t WS_KMAX = WS_KV + (size_t)NH * KV_HEAD_B;
constexpr size_t WS_XMEAN = WS_KMAX + 65536;
constexpr size_t WS_OFR = WS_XMEAN + 8192;
constexpr size_t WS_WO = WS_OFR + (size_t)128 * 18 * 1024;
constexpr size_t WS_WBAR = WS_WO + (size_t)12 * 54 * 1024;
constexpr size_t WS_HC = WS_WBAR + 3072;
constexpr size_t WS_SSQ = WS_HC + (size_t)64 * 36 * 1024;
constexpr size_t WS_W1 = WS_SSQ + (size_t)16 * 8192;
constexpr size_t WS_WD = WS_W1 + (size_t)288 * 36 * 1024;
constexpr size_t WS_ACT = WS_WD + (size_t)4 * 144 * 9 * 1024;
constexpr size_t UNIT_B = (size_t)128 * 18432;
constexpr size_t WS_PART = WS_ACT + (size_t)128 * 144 * 1024;
constexpr size_t WS_PART8 = WS_PART + (size_t)4 * 64 * 8 * 3 * 1024;
constexpr size_t WS_CNT = WS_PART8 + (size_t)64 * 8 * 4 * 512;
constexpr size_t WS_HU = WS_CNT + 40960;
constexpr size_t WS_END = WS_HU + UNIT_B;

static __device__ unsigned long long g_ptr[8];
typedef __attribute__((address_space(4))) const unsigned long long* cptr_t;
DI unsigned cvtpk(float lo, float hi) {
  f32x2 v = {lo, hi};
  bf16x2 b = __builtin_convertvector(v, bf16x2);
  return __builtin_bit_cast(unsigned, b);
}
DI float bflo(unsigned u) { return __uint_as_float(u << 16); }
DI float bfhi(unsigned u) { return __uint_as_float(u & 0xffff0000u); }
DI float dot2(unsigned a, unsigned b, float c) {
  return __builtin_amdgcn_fdot2_f32_bf16(__builtin_bit_cast(bf16x2, a), __builtin_bit_cast(bf16x2, b), c, false);
}
DI f32x16 mfma32(u32x4 a, u32x4 b, f32x16 c) {
  return __builtin_amdgcn_mfma_f32_32x32x16_bf16(__builtin_bit_cast(bf16x8, a), __builtin_bit_cast(bf16x8, b), c, 0, 0, 0);
}
DI unsigned rfl(unsigned x) { return __builtin_amdgcn_readfirstlane(x); }
DI void dma16(const char* sbase, unsigned voff, unsigned lds) {
  asm volatile("s_mov_b32 m0, %2\n\ts_nop 0\n\tglobal_load_lds_dwordx4 %0, %1" ::"v"(voff), "s"(sbase), "s"(lds));
}
DI u32x4 gld16(const char* sbase, unsigned voff) {
  u32x4 r;
  asm volatile("global_load_dwordx4 %0, %1, %2" : "=v"(r) : "v"(voff), "s"(sbase));
  return r;
}
DI unsigned gld4(const char* sbase, unsigned voff) {
  unsigned r;
  asm volatile("global_load_dword %0, %1, %2" : "=v"(r) : "v"(voff), "s"(sbase));
  return r;
}
#define PIN(x) asm volatile("" : "+v"(x))
DI u32x4 ldsr16(LDSP const char* p) { return *(LDSP const u32x4*)p; }
DI float xor32_sum(float v) {
  auto r = __builtin_amdgcn_permlane32_swap(__float_as_uint(v), __float_as_uint(v), false, false);
  return __uint_as_float(r[0]) + __uint_as_float(r[1]);
}
DI float xor32_max(float v) {
  auto r = __builtin_amdgcn_permlane32_swap(__float_as_uint(v), __float_as_uint(v), false, false);
  return fmaxf(__uint_as_float(r[0]), __uint_as_float(r[1]));
}
DI float xor16_sum(float v) {
  auto r = __builtin_amdgcn_permlane16_swap(__float_as_uint(v), __float_as_uint(v), false, false);
  return __uint_as_float(r[0]) + __uint_as_float(r[1]);
}

#define STAMP_DECL
#define STAMP(i)
#define STAMP_OUT(kid, n)

DI int wo_row(int step, int g, int j) {
  if (step < 16) return 18 * (2 * step + (g >> 1)) + 4 * (g & 1) + (j & 3) + 8 * (j >> 2);
  return 18 * (4 * (4 * (step - 16) + g) + (j >> 1)) + 16 + (j & 1);
}

constexpr int SC1 = 16;
constexpr unsigned DROP = 0x80000000u;
#define CODE_PF(dst, first, last, extra, blk, stride)                                                                              \
  do {                                                                                                                     \
    uint64_t pc_; int b_, e_;                                                                                              \
    asm volatile("s_getpc_b64 %0\n.Lpf%=:\n\ts_mov_b32 %1, " #first "-.Lpf%=\n\ts_mov_b32 %2, " #last "-.Lpf%=" : "=s"(pc_), "=s"(b_), "=s"(e_)); \
    const unsigned o_ = (blk) * 8192u + (threadIdx.x & 63) * (stride), n_ = (unsigned)(e_ - b_) + (extra) - 4u;                \
    dst = gld4((const char*)(pc_ + b_), o_ < n_ ? o_ : n_);                                                                \
  } while (0)
#define G_MFMA(d, a, b) asm volatile("v_mfma_f32_16x16x32_bf16 %0, %1, %2, %0" : "+v"(d) : "v"(a), "v"(b))
#define G_MFMA0(d, a, b) asm volatile("v_mfma_f32_16x16x32_bf16 %0, %1, %2, 0" : "=&v"(d) : "v"(a), "v"(b))
template <int AUX> DI void bst16(u32x4 v, __amdgpu_buffer_rsrc_t rs, unsigned voff, unsigned soff) {
  __builtin_amdgcn_raw_buffer_store_b128(v, rs, voff, soff, AUX);
  asm volatile("s_nop 1" ::"v"(v) : "memory");
}
DI __amdgpu_buffer_rsrc_t mkrsrc(const void* p, unsigned bytes) {
  return __builtin_amdgcn_make_buffer_rsrc((void*)p, 0, bytes, 0x00020000);
}

typedef __attribute__((address_space(1))) unsigned gu32;
DI void wait_count(gu32* cnt, unsigned first, unsigned want) {
  if (rfl(first) >= want) return;
  for (unsigned spin = 0; spin < (1u << 16); ++spin) {
    if (rfl(__hip_atomic_load(cnt, __ATOMIC_RELAXED, __HIP_MEMORY_SCOPE_AGENT)) >= want) break;
    __builtin_amdgcn_s_sleep(1);
  }
}
constexpr int NJ_W1 = 72 * 36, NJ_WD = 9 * 144, NJ_WO = 9 * 18, NJ_ALL = NJ_W1 + NJ_WD + NJ_WO;
struct PrepJob { const char* src; unsigned rs, c1, c2, voff, dst[4]; };
DI void prep_setup(PrepJob& j, int J, int lane, const u16* wo, const u16* wg, const u16* wu, const u16* wd) {
  if (J >= NJ_ALL) J = NJ_ALL - 1;
  const int g = (lane >> 2) & 3;
  unsigned lrow = 8 * g;
  j.c1 = 0; j.c2 = 0;
  if (J < NJ_W1) {
    const int sm = J / 72, tg = J - sm * 72;
    j.src = (const char*)((sm & 1) ? wu : wg) + (size_t)(sm >> 1) * (32 * FF * 2) + tg * 128;
    j.rs = FF * 2;
#pragma unroll
    for (int t = 0; t < 4; ++t) j.dst[t] = (unsigned)WS_W1 + (4 * tg + t) * 36864 + sm * 1024;
  } else if (J < NJ_W1 + NJ_WD) {
    const int q = J - NJ_W1, cc = q / 9, tg = q - cc * 9;
    j.src = (const char*)wd + (size_t)cc * (32 * DM * 2) + tg * 128;
    j.rs = DM * 2;
#pragma unroll
    for (int t = 0; t < 4; ++t) { const int nt = 4 * tg + t, nb = nt / 9; j.dst[t] = (unsigned)WS_WD + ((nb * 144 + cc) * 9 + (nt - nb * 9)) * 1024; }
  } else {
    const int q = J - NJ_W1 - NJ_WD, step = q / 9, tg = q - step * 9;
    j.rs = DM * 2;
    if (step < 16) { j.src = (const char*)wo + (size_t)step * (36 * DM * 2) + tg * 128; lrow = 18 * (g >> 1) + 4 * (g & 1); j.c2 = 4; }
    else { j.src = (const char*)wo + (size_t)(288 * (step - 16) + 16) * (DM * 2) + tg * 128; lrow = 72 * g; j.c1 = 16; }
#pragma unroll
    for (int t = 0; t < 4; ++t) j.dst[t] = (unsigned)WS_WO + (step * 36 + 4 * tg + t) * 1024;
  }
#pragma unroll
  for (int t = 0; t < 4; ++t) j.dst[t] = rfl(j.dst[t]);
  j.voff = lrow * j.rs + (lane >> 4) * 32 + (lane & 3) * 8;
}
template <int JJ>
DI void prep_load(const PrepJob& j, u32x2 (&r)[8]) {
  const char* b = j.src + (JJ + j.c1 * (JJ >> 1) + j.c2 * (JJ >> 2)) * j.rs;
  asm volatile("global_load_dwordx2 %0, %1, %2" : "=v"(r[JJ]) : "v"(j.voff), "s"(b));
}
DI void prep_load_all(const PrepJob& j, u32x2 (&r)[8]) {
  prep_load<0>(j, r); prep_load<1>(j, r); prep_load<2>(j, r); prep_load<3>(j, r); prep_load<4>(j, r); prep_load<5>(j, r); prep_load<6>(j, r); prep_load<7>(j, r);
}
DI void prep_store(const PrepJob& j, const u32x2 (&r)[8], __amdgpu_buffer_rsrc_t rs, unsigned so) {
  u32x4 u[4];
#pragma unroll
  for (int i = 0; i < 4; ++i) {
    u[0][i] = __builtin_amdgcn_perm(r[2 * i + 1][0], r[2 * i][0], 0x05040100u);
    u[1][i] = __builtin_amdgcn_perm(r[2 * i + 1][0], r[2 * i][0], 0x07060302u);
    u[2][i] = __builtin_amdgcn_perm(r[2 * i + 1][1], r[2 * i][1], 0x05040100u);
    u[3][i] = __builtin_amdgcn_perm(r[2 * i + 1][1], r[2 * i][1], 0x07060302u);
  }
#pragma unroll
  for (int i = 0; i < 4; ++i) {
    const auto a0 = __builtin_amdgcn_permlane32_swap(u[0][i], u[2][i], false, false);
    const auto a1 = __builtin_amdgcn_permlane32_swap(u[1][i], u[3][i], false, false);
    const auto b0 = __builtin_amdgcn_permlane16_swap(a0[0], a1[0], false, false);
    const auto b1 = __builtin_amdgcn_permlane16_swap(a0[1], a1[1], false, false);
    u[0][i] = b0[0]; u[1][i] = b0[1]; u[2][i] = b1[0]; u[3][i] = b1[1];
  }
#pragma unroll
  for (int t = 0; t < 4; ++t) bst16<SC1>(u[t], rs, so, j.dst[t]);
}
#define PIN8(r) "+v"(r[0]), "+v"(r[1]), "+v"(r[2]), "+v"(r[3]), "+v"(r[4]), "+v"(r[5]), "+v"(r[6]), "+v"(r[7])

constexpr int K1_PF = 4;
constexpr int K1_PITCH = 224, K1_SLOT = 16384 + 64 * K1_PITCH  , K1_NS = 4;
constexpr int K1_SP = 232;
constexpr int K1_STG = K1_NS * K1_SLOT, K1_CS = K1_STG + 128 * K1_SP + 64, K1_ST = K1_CS + 512 + 64, K1_LDS = K1_ST + 2048 + 256;
struct K1C {
  LDSP const char *xa[2], *wl, *wc0, *wc1;
  u32x4 X[3][2], wf[K1_PF + 1], ones;
  f32x4 acc[4][2];
  f32x4 sm[2], sq[2];
};
DI void k1_loader(const char* xb, const char* wb, const u16* wo_, char* ws, int j, int lane, unsigned lds0) {
  __builtin_amdgcn_s_setprio(3);
  unsigned xo[2], wo[4];
  const int g = lane >> 4;
  xo[0] = (lane >> 3) * 1152 + (((lane & 7) ^ g) * 16);
  xo[1] = (8 + (lane >> 3)) * 1152 + (((lane & 7) ^ (4 + g)) * 16);
  xb += j * (32 * 1152);
  const unsigned xd = lds0 + j * 4096, wd = lds0 + 16384 + j * 1024;
#define K1_X(z, so) do { dma16(xb + (z) * 128, xo[0], xd + (so)); dma16(xb + (z) * 128, xo[1], xd + (so) + 1024);                    \
    dma16(xb + (z) * 128 + 16 * 1152, xo[0], xd + (so) + 2048); dma16(xb + (z) * 128 + 16 * 1152, xo[1], xd + (so) + 3072); } while (0)
#define K1_W(z, so) do { _Pragma("unroll") for (int i = 0; i < 3; ++i) dma16(wb + (z) * (64 * 3456), wo[i], wd + (so) + i * 4096);     \
    if (j < 2) dma16(wb + (z) * (64 * 3456), wo[3], wd + (so) + 3 * 4096); } while (0)
  K1_X(0, 0);
  SBAR();
#pragma unroll
  for (int i = 0; i < 4; ++i) {
    const unsigned u = 64 * (j + 4 * i) + lane, r = u / 14;
    wo[i] = ((r & ~12u) | ((r & 4u) << 1) | ((r & 8u) >> 1)) * 3456 + (u - r * 14) * 16;
  }
  K1_W(0, 0);
  K1_X(1, K1_SLOT); K1_W(1, K1_SLOT);
  unsigned so = 2 * K1_SLOT;
#pragma unroll 1
  for (int z = 2; z < 9; ++z) {
    asm volatile("s_waitcnt vmcnt(7)\n\ts_barrier" ::: "memory");
    K1_X(z, so); K1_W(z, so);
    so = so == 3 * K1_SLOT ? 0 : so + K1_SLOT;
  }
  asm volatile("s_waitcnt vmcnt(7)\n\ts_barrier" ::: "memory");
  const int b = blockIdx.x;
  const bool dojob = j == 1 && b < NJ_WO, dorow = j >= 2 || (j == 0 && b < 64);
  const int br = j >= 2 ? 2 * b + j - 2 : 512 + (b & 63);
  PrepJob jb; u32x2 r[8]; unsigned wr[5];
  if (dojob) prep_setup(jb, NJ_W1 + NJ_WD + b, lane, wo_, wo_, wo_, wo_);
  const char* rp = (const char*)wo_ + (size_t)wo_row(br >> 5, (br >> 3) & 3, br & 7) * (DM * 2);
  SBAR();
  asm volatile("s_waitcnt vmcnt(0)\n\ts_barrier" ::: "memory");
  if (dojob) prep_load_all(jb, r);
  if (dorow) {
#pragma unroll
    for (int i = 0; i < 5; ++i) wr[i] = gld4(rp, (i < 4 ? lane + 64 * i : 256 + (lane & 31)) * 4);
  }
  asm volatile("s_barrier");
  asm volatile("s_barrier");
  const __amdgpu_buffer_rsrc_t wrs = mkrsrc(ws, (unsigned)WS_END);
  asm volatile("s_waitcnt vmcnt(0)" : PIN8(r), "+v"(wr[0]), "+v"(wr[1]), "+v"(wr[2]), "+v"(wr[3]), "+v"(wr[4])::"memory");
  if (dojob) {
#pragma unroll
    for (int t = 0; t < 4; ++t) jb.dst[t] = rfl(jb.dst[t]);
    prep_store(jb, r, wrs, ((lane >> 2) & 3) * 256 + ((lane & 3) * 4 + (lane >> 4)) * 16);
  }
  if (dorow) {
    float a = 0.f;
#pragma unroll
    for (int i = 0; i < 4; ++i) a = dot2(wr[i], 0x3f803f80u, a);
    a = dot2(lane >= 32 ? 0u : wr[4], 0x3f803f80u, a);
    a = xor16_sum(xor32_sum(a));
    a += __uint_as_float(__builtin_amdgcn_update_dpp(0u, __float_as_uint(a), 0x128, 0xf, 0xf, false));
    a += __uint_as_float(__builtin_amdgcn_update_dpp(0u, __float_as_uint(a), 0x124, 0xf, 0xf, false));
    a += __uint_as_float(__builtin_amdgcn_update_dpp(0u, __float_as_uint(a), 0x122, 0xf, 0xf, false));
    a += __uint_as_float(__builtin_amdgcn_update_dpp(0u, __float_as_uint(a), 0x121, 0xf, 0xf, false));
    a *= (1.0f / DM);
    const unsigned hi = cvtpk(a, 0.f) & 0xffffu;
    const unsigned lo = cvtpk(a - bflo(hi), 0.f) & 0xffffu;
    __builtin_amdgcn_raw_buffer_store_b16((u16)(lane ? lo : hi), wrs, lane < 2 ? (unsigned)WS_WBAR + (lane * DM + br) * 2 : DROP, 0, 16);
  }
#undef K1_X
#undef K1_W
}
DI u32x4 tr_frag(LDSP const char* p, int d) {
  s16x4 lo = __builtin_amdgcn_ds_read_tr16_b64_v4i16((LDSP s16x4*)p);
  s16x4 hi = __builtin_amdgcn_ds_read_tr16_b64_v4i16((LDSP s16x4*)(p + d));
  return __builtin_bit_cast(u32x4, __builtin_shufflevector(lo, hi, 0, 1, 2, 3, 4, 5, 6, 7));
}
template <int NHG, int F> DI void k1_read(K1C& c) {
  constexpr int NF = 4 + NHG, S = F / NF, j = F % NF, Z = S >> 1, so = (Z % K1_NS) * K1_SLOT;
  if constexpr (j == 0) {
    c.X[S % 3][0] = ldsr16(c.xa[S & 1] + so);
    c.X[S % 3][1] = ldsr16(c.xa[S & 1] + so + 2048);
  }
  constexpr int wo = so + (S & 1) * 7168;
  if constexpr (NHG == 0 || j < 3) c.wf[F % (K1_PF + 1)] = tr_frag(c.wl + wo + j * 32, 1792);
  else c.wf[F % (K1_PF + 1)] = tr_frag((j == 3 ? c.wc0 : c.wc1) + wo, 1792);
}
template <int NHG, int F> DI void k1_frag(K1C& c) {
  constexpr int NF = 4 + NHG, S = F / NF, j = F % NF, N = F + K1_PF;
  if constexpr (S == 0) {
    if constexpr (NHG == 0 || j < 3) { G_MFMA0(c.acc[j][0], c.wf[F % (K1_PF + 1)], c.X[0][0]); G_MFMA0(c.acc[j][1], c.wf[F % (K1_PF + 1)], c.X[0][1]); }
    else G_MFMA0(c.acc[3][j - 3], c.wf[F % (K1_PF + 1)], c.ones);
  } else {
    if constexpr (NHG == 0 || j < 3) { G_MFMA(c.acc[j][0], c.wf[F % (K1_PF + 1)], c.X[S % 3][0]); G_MFMA(c.acc[j][1], c.wf[F % (K1_PF + 1)], c.X[S % 3][1]); }
    else G_MFMA(c.acc[3][j - 3], c.wf[F % (K1_PF + 1)], c.ones);
  }
  if constexpr ((S & 1) == NHG && j < 4) {
    constexpr int t = j >> 1;
    if constexpr (j & 1) { if constexpr (S < 2) G_MFMA0(c.sq[t], c.X[S % 3][t], c.X[S % 3][t]); else G_MFMA(c.sq[t], c.X[S % 3][t], c.X[S % 3][t]); }
    else { if constexpr (S < 2) G_MFMA0(c.sm[t], c.ones, c.X[S % 3][t]); else G_MFMA(c.sm[t], c.ones, c.X[S % 3][t]); }
  }
  if constexpr (N < 18 * NF) {
    if constexpr (N % (2 * NF) == 0) asm volatile("s_barrier" ::: "memory");
    k1_read<NHG, N>(c);
  }
  SBAR();
}
template <int NHG, int... Fs> DI void k1_run(K1C& c, std::integer_sequence<int, Fs...>) {
  k1_read<NHG, 0>(c); k1_read<NHG, 1>(c); k1_read<NHG, 2>(c); k1_read<NHG, 3>(c);
  SBAR();
  (k1_frag<NHG, Fs>(c), ...);
  asm volatile("s_nop 7" : "+v"(c.acc[0][0]), "+v"(c.acc[0][1]), "+v"(c.acc[1][0]), "+v"(c.acc[1][1]), "+v"(c.acc[2][0]), "+v"(c.acc[2][1]),
               "+v"(c.acc[3][0]), "+v"(c.acc[3][1]), "+v"(c.sm[0]), "+v"(c.sm[1]), "+v"(c.sq[0]), "+v"(c.sq[1]));
  SBAR();
}
extern "C" __global__ __launch_bounds__(768, 3) void k_qkv(const u16* x, const u16* wqkv, const u16* wo, char* ws, const u16* wg, const u16* wu, const u16* wd, char* out) {
  extern __shared__ __attribute__((aligned(16))) char smem[];
  const int tid = threadIdx.x, lane = tid & 63, w = rfl(tid >> 6);
  const int xcd = blockIdx.x & 7, bi = blockIdx.x >> 3;
  unsigned pf;
  CODE_PF(pf, k_qkv, k_end, 0, bi, (w == 8 && bi < 9) ? 128u : 0u);
  SBAR();
  asm volatile("" : "+s"(x), "+s"(wqkv), "+s"(wo), "+s"(ws));
  LDSP char* lds = (LDSP char*)smem;
  const unsigned lds0 = (unsigned)(uintptr_t)lds;
  const int rb = 4 * (xcd >> 1) + (bi & 3), cb = 8 * (xcd & 1) + (bi >> 2);
  const int c0 = 108 * cb - 4 * (cb & 1);
  if (__builtin_expect(w >= 8, 1)) {
    k1_loader((const char*)x + rb * (128 * 1152), (const char*)wqkv + c0 * 2, wo, ws, w - 8, lane, lds0);
    asm volatile("" ::"v"(pf));
    if (blockIdx.x == 0 && tid == 512) {
      g_ptr[0] = (unsigned long long)x; g_ptr[1] = (unsigned long long)wg; g_ptr[2] = (unsigned long long)wu; g_ptr[3] = (unsigned long long)wd;
      g_ptr[4] = (unsigned long long)ws; g_ptr[5] = (unsigned long long)out;
    }
    return;
  }
  const int m = lane & 15, g = lane >> 4, mg = w & 3, nh = w >> 2;
  STAMP_DECL; STAMP(0);
  K1C c;
  c.ones = u32x4{0x3f803f80u, 0x3f803f80u, 0x3f803f80u, 0x3f803f80u};
  PIN(c.ones);
  c.xa[0] = lds + (32 * mg + m) * 128 + ((g ^ (m >> 1)) * 16);
  c.xa[1] = lds + (32 * mg + m) * 128 + (((4 + g) ^ (m >> 1)) * 16);
  LDSP const char* wbase = lds + 16384 + (16 * (g >> 1) + 4 * (g & 1) + (m >> 2)) * K1_PITCH + (m & 3) * 8;
  c.wl = wbase + nh * 128;
  c.wc0 = wbase + mg * 32;
  c.wc1 = wbase + (mg < 3 ? mg + 4 : 6) * 32;
  const int off = 4 * (cb & 1), T0 = 128 * rb;
  LDSP char* stg = lds + K1_STG;
  LDSP char* a_st = lds + K1_ST + (32 * mg + m) * 8;                 PIN(a_st);
  LDSP char* a_sd = g == (m >> 2) ? a_st + nh * 1024 : lds + K1_ST + 2048;   PIN(a_sd);
  LDSP char* a_cs = lds + K1_CS + (64 * nh + 4 * g) * 4;             PIN(a_cs);
  LDSP char* a_sw = stg + (32 * mg + m) * K1_SP + (64 * nh + 4 * g) * 2;   PIN(a_sw);
  LDSP char* a_tr = stg + (32 * mg + 4 * (g & 1) + (m >> 2)) * K1_SP + (64 * nh + 16 * (g >> 1) + 4 * (m & 3)) * 2;   PIN(a_tr);
  float qs[4];
#pragma unroll
  for (int j = 0; j < 4; ++j) { qs[j] = (c0 + 64 * nh + 16 * j + 4 * g < DM) ? QSCALE : 1.0f; PIN(qs[j]); }
  const __amdgpu_buffer_rsrc_t wrs = mkrsrc(ws, (unsigned)WS_END);
  const unsigned kvo = (unsigned)WS_KV + (T0 >> 9) * (unsigned)CHUNK_B;
  unsigned vo[2] = {0, 0}, vone = 0, vd[4] = {DROP, DROP, DROP, DROP};
  if (cb >= 10) {
#pragma unroll
  for (int tt = 0; tt < 2; ++tt) {
    const int cc = 64 * nh + 32 * tt + 16 * (g >> 1) + m - off, hd = (cc + 18) / 18 - 1;
    vo[tt] = (cc >= 0 && cc < 108) ? hd * 4864 + 2 * mg * 608 + (g & 1) * 304 + (cc - 18 * hd) * 16 : 29184;
  }
  vone = tid < 96 ? (tid >> 4) * 4864 + (tid & 15) * 304 + 288 : 29184;
#pragma unroll
  for (int i = 0; i < 4; ++i) {
    const unsigned u = i * 512 + tid, hd = u / 304;
    vd[i] = (u < 1824 && 6 * cb + hd >= 64) ? kvo + 18432 + ((T0 >> 4) & 31) * 608 + (6 * cb + hd - 64) * (unsigned)KV_HEAD_B + (u - hd * 304) * 16 : DROP;
  }
  }
  PIN(vone); PIN(vo[0]); PIN(vo[1]); PIN(vd[0]); PIN(vd[1]); PIN(vd[2]); PIN(vd[3]);
  const int tok = tid & 127, T = T0 + tok;
  LDSP const char* a_g = stg + tok * K1_SP + off * 2;   PIN(a_g);
  unsigned um[2], ut[2];
  const unsigned tl = ((T >> 5) & 15) * 1024 + (T & 31) * 16;
#pragma unroll
  for (int it = 0; it < 2; ++it) {
    const int hd = (w >> 1) + 4 * it, G = 6 * cb + hd, head = G & 31;
    if (hd >= 6 || G >= 64) { um[it] = DROP; ut[it] = DROP; }
    else if (G < 32) { um[it] = (unsigned)WS_QM + head * 65536 + (T >> 9) * 16384 + tl; ut[it] = (unsigned)WS_QT + head * 8192 + T * 4; }
    else { um[it] = kvo + head * (unsigned)KV_HEAD_B + tl; ut[it] = kvo + head * (unsigned)KV_HEAD_B + 16384 + ((T >> 6) & 7) * 256 + (T & 31) * 8 + ((T >> 5) & 1) * 4; }
    PIN(um[it]); PIN(ut[it]);
  }
  STAMP(1);
  asm volatile("s_waitcnt vmcnt(0)" : "+v"(pf));
  if (blockIdx.x < 5 && w == 7) bst16<SC1>(u32x4{0, 0, 0, 0}, wrs, (unsigned)WS_CNT + (blockIdx.x * 64 + lane) * 128, 0);
  asm volatile("s_barrier" ::: "memory");
  STAMP(2);
  if (nh) k1_run<1>(c, std::make_integer_sequence<int, 90>{});
  else k1_run<0>(c, std::make_integer_sequence<int, 72>{});
  STAMP(3);
#pragma unroll
  for (int t = 0; t < 2; ++t) {
    const float lo = (m & 1) ? c.sq[t][1] : c.sq[t][0], hi = (m & 1) ? c.sq[t][3] : c.sq[t][2];
    *(LDSP f32x2*)(a_sd + t * 128) = f32x2{c.sm[t][0], (m & 2) ? hi : lo};
  }
  if (nh) {
    *(LDSP f32x4*)(lds + K1_CS + (16 * mg + 4 * g) * 4) = c.acc[3][0];
    *(LDSP f32x4*)(lds + K1_CS + (16 * (mg + 4) + 4 * g) * 4) = c.acc[3][1];
  }
  asm volatile("s_waitcnt lgkmcnt(0)\n\ts_barrier" ::: "memory");
  {
    f32x2 p[2][2]; f32x4 cc[4];
#pragma unroll
    for (int t = 0; t < 2; ++t) { p[t][0] = *(LDSP const f32x2*)(a_st + t * 128); p[t][1] = *(LDSP const f32x2*)(a_st + 1024 + t * 128); }
#pragma unroll
    for (int j = 0; j < 4; ++j) cc[j] = *(LDSP const f32x4*)(a_cs + j * 64);
    SBAR();
#pragma unroll
    for (int t = 0; t < 2; ++t) {
      const float mean = (p[t][0][0] + p[t][1][0]) * (1.0f / DM);
      const float var = fmaxf((p[t][0][1] + p[t][1][1]) * (1.0f / DM) - mean * mean, 0.f);
      const float rstd = __builtin_amdgcn_rsqf(var + LN_EPS);
      if (cb == 0 && nh == 0) __builtin_amdgcn_raw_buffer_store_b32(__float_as_uint(mean), wrs, (unsigned)WS_XMEAN + (T0 + 32 * mg + 16 * t + m) * 4, 0, SC1);
#pragma unroll
      for (int j = 0; j < 4; ++j) {
        if (j < 3 || nh == 0) {
          const f32x4 a = (c.acc[j][t] - mean * cc[j]) * (rstd * qs[j]);
          *(LDSP u32x2*)(a_sw + t * (16 * K1_SP) + j * 32) = u32x2{cvtpk(a[0], a[1]), cvtpk(a[2], a[3])};
        }
      }
    }
  }
  if (cb >= 10) {
    asm volatile("s_waitcnt lgkmcnt(0)" ::: "memory");
    u32x4 v[2][2];
#pragma unroll
    for (int tt = 0; tt < 2; ++tt)
#pragma unroll
      for (int t = 0; t < 2; ++t) v[tt][t] = tr_frag(a_tr + t * (16 * K1_SP) + tt * 64, 8 * K1_SP);
    *(LDSP u32x4*)(lds + vone) = c.ones;
#pragma unroll
    for (int tt = 0; tt < 2; ++tt)
#pragma unroll
      for (int t = 0; t < 2; ++t) *(LDSP u32x4*)(lds + vo[tt] + t * 608) = v[tt][t];
  }
  STAMP(4);
  asm volatile("s_waitcnt lgkmcnt(0)\n\ts_barrier" ::: "memory");
  if (cb >= 10) {
    u32x4 v[4];
#pragma unroll
    for (int i = 0; i < 4; ++i) v[i] = ldsr16(lds + i * 8192 + tid * 16);
#pragma unroll
    for (int i = 0; i < 4; ++i) if (__builtin_amdgcn_ballot_w64(vd[i] != DROP)) bst16<SC1>(v[i], wrs, vd[i], 0);
  }
  if (cb <= 10) {
    unsigned d[2][9];
#pragma unroll
    for (int it = 0; it < 2; ++it) {
      const int hd = (w >> 1) + 4 * it;
      LDSP const unsigned* sp = (LDSP const unsigned*)(a_g + 36 * (hd < 6 ? hd : 0));
#pragma unroll
      for (int i = 0; i < 9; ++i) d[it][i] = sp[i];
    }
    SBAR();
#pragma unroll
    for (int it = 0; it < 2; ++it) {
      if ((w >> 1) + 4 * it >= 6 || 6 * cb + (w >> 1) + 4 * it >= 64) continue;
      bst16<SC1>(u32x4{d[it][0], d[it][1], d[it][2], d[it][3]}, wrs, um[it], 0);
      bst16<SC1>(u32x4{d[it][4], d[it][5], d[it][6], d[it][7]}, wrs, um[it] + 512, 0);
      __builtin_amdgcn_raw_buffer_store_b32(d[it][8], wrs, ut[it], 0, SC1);
    }
#pragma unroll
    for (int it = 0; it < 2; ++it) {
      const int hd = (w >> 1) + 4 * it, G = 6 * cb + hd;
      if (hd < 6 && G >= 32 && G < 64) {
        float n2 = 0.f;
#pragma unroll
        for (int i = 0; i < 9; ++i) n2 = dot2(d[it][i], d[it][i], n2);
        unsigned nb = __float_as_uint(n2);
        { auto r = __builtin_amdgcn_permlane32_swap(nb, nb, false, false); nb = max(r[0], r[1]); }
        { auto r = __builtin_amdgcn_permlane16_swap(nb, nb, false, false); nb = max(r[0], r[1]); }
        __builtin_amdgcn_raw_buffer_store_b32(nb, wrs, (unsigned)WS_KMAX + (((2 * rb + (w & 1)) * 32 + (G & 31)) * 16 + m) * 4, 0, SC1);
      }
    }
  }
  STAMP(5); STAMP_OUT(1, 6);
}

constexpr int K2_LDS = (int)KV_HEAD_B, K3_WBAR = 90 * 1024, K3_XC = 96 * 1024, K3_REP = 16, K3_PRE = 4;
struct AttnC {
  LDSP const char *kmb, *ktb, *vb;
  unsigned kma[3], kta[3], va[3];
  u32x4 qm, qt, kf, v0, v1, p1, ktl;
  f32x16 negm, o, sa, sb;
};
constexpr int k_off(int T) { return (T >> 4) * (int)CHUNK_B + (T & 15) * 1024; }
constexpr int kt_off(int T) { return (T >> 4) * (int)CHUNK_B + ((T >> 1) & 7) * 256; }
constexpr int v_off(int T) { return (T >> 4) * (int)CHUNK_B + (T & 15) * 1216; }
#define AT_E(r) "v_exp_f32 v" #r ", v" #r "\n\t"
#define AT_C(d, a, b) "v_cvt_pk_bf16_f32 v" #d ", v" #a ", v" #b "\n\t"
#define AT_TAIL(sn, kt, qt) "v_mfma_f32_32x32x8_bf16 " sn ", " kt ", " qt ", v[18:33]\n\t"
#define AT_MAIN(sn) "v_mfma_f32_32x32x16_bf16 " sn ", v[66:69], v[98:101], " sn "\n\t"
#define AT_PV1 "v_mfma_f32_32x32x16_bf16 v[2:17], v[84:87], v[70:73], v[2:17]\n\t"
#define AT_PV0 "v_mfma_f32_32x32x16_bf16 v[2:17], v[88:91], v[74:77], v[2:17]\n\t"
#define AT_R1 "ds_read_b128 v[84:87], %[va] offset:%[vo1]\n\t"
#define AT_RK "ds_read_b128 v[66:69], %[ka] offset:%[ko]\n\t"
#define AT_R0 "ds_read_b128 v[88:91], %[vb] offset:%[vo0]\n\t"
#define AT_RT(kt) "ds_read_b64 " kt ", %[kt] offset:%[kto]\n\t"
#define AT_BODY(TL, P1, MN, RD, s0, s1, s2, s3, s4, s5, s6, s7, s8, s9, s10, s11, s12, s13, s14, s15)                                   \
  TL AT_E(s0) AT_E(s1) AT_E(s2) AT_E(s3) "s_waitcnt lgkmcnt(0)\n\t" P1 AT_C(74, s0, s1) AT_E(s4) AT_C(75, s2, s3) AT_E(s5) AT_E(s6) AT_E(s7) \
  MN AT_C(76, s4, s5) AT_E(s8) AT_C(77, s6, s7) AT_E(s9) AT_E(s10) AT_E(s11) AT_PV0 RD AT_C(70, s8, s9) AT_E(s12) AT_C(71, s10, s11)          \
  AT_E(s13) AT_E(s14) AT_E(s15) AT_C(72, s12, s13) AT_C(73, s14, s15)
#define AT_EVEN(TL, P1, MN, RD) AT_BODY(TL, P1, MN, RD, 34, 35, 36, 37, 38, 39, 40, 41, 42, 43, 44, 45, 46, 47, 48, 49)
#define AT_ODD(TL, P1, MN, RD) AT_BODY(TL, P1, MN, RD, 50, 51, 52, 53, 54, 55, 56, 57, 58, 59, 60, 61, 62, 63, 64, 65)
#define AT_STMT(TXT)                                                                                                              \
  asm volatile(TXT : "+{v[2:17]}"(c.o), "+{v[34:49]}"(c.sa), "+{v[50:65]}"(c.sb), "+{v[66:69]}"(c.kf), "+{v[70:73]}"(c.p1),        \
               "+{v[84:87]}"(c.v1), "+{v[88:91]}"(c.v0), "+{v[92:95]}"(c.ktl)                                                      \
               : "{v[18:33]}"(c.negm), "{v[98:101]}"(c.qm), "{v[102:105]}"(c.qt), [ka] "v"(c.kma[KO >> 16]), [kt] "v"(c.kta[KT >> 16]), \
                 [va] "v"(c.va[V1 >> 16]), [vb] "v"(c.va[V0 >> 16]), [ko] "i"(KO & 0xffff), [kto] "i"(KT & 0xffff), [vo1] "i"(V1 & 0xffff),  \
                 [vo0] "i"(V0 & 0xffff)                                                                                            \
               : "v74", "v75", "v76", "v77")
template <int T> DI void attn_tile(AttnC& c) {
  constexpr int KO = k_off(T < 62 ? T + 2 : 0), KT = kt_off(T < 61 ? T + 3 : 0), V1 = v_off(T) + 608, V0 = v_off(T < 63 ? T + 1 : 0);
  if constexpr (T == 0) AT_STMT(AT_EVEN(AT_TAIL("v[50:65]", "v[92:93]", "v[104:105]"), "", AT_MAIN("v[50:65]"), AT_R1 AT_RK AT_R0));
  else if constexpr (T == 61) AT_STMT(AT_ODD(AT_TAIL("v[34:49]", "v[94:95]", "v[102:103]"), AT_PV1, AT_MAIN("v[34:49]"), AT_R1 AT_RK AT_R0));
  else if constexpr (T == 62) AT_STMT(AT_EVEN(AT_TAIL("v[50:65]", "v[94:95]", "v[104:105]"), AT_PV1, AT_MAIN("v[50:65]"), AT_R1 AT_R0));
  else if constexpr (T == 63) AT_STMT(AT_ODD("", AT_PV1, "", AT_R1));
  else if constexpr ((T & 3) == 0) AT_STMT(AT_EVEN(AT_TAIL("v[50:65]", "v[92:93]", "v[104:105]"), AT_PV1, AT_MAIN("v[50:65]"), AT_R1 AT_RK AT_R0));
  else if constexpr ((T & 3) == 1) AT_STMT(AT_ODD(AT_TAIL("v[34:49]", "v[94:95]", "v[102:103]"), AT_PV1, AT_MAIN("v[34:49]"), AT_R1 AT_RK AT_R0 AT_RT("v[92:93]")));
  else if constexpr ((T & 3) == 2) AT_STMT(AT_EVEN(AT_TAIL("v[50:65]", "v[94:95]", "v[104:105]"), AT_PV1, AT_MAIN("v[50:65]"), AT_R1 AT_RK AT_R0));
  else AT_STMT(AT_ODD(AT_TAIL("v[34:49]", "v[92:93]", "v[102:103]"), AT_PV1, AT_MAIN("v[34:49]"), AT_R1 AT_RK AT_R0 AT_RT("v[94:95]")));
}
DI void attn_group_slow(LDSP const char* km, LDSP const char* kt, LDSP const char* vp, const u32x4& qm, unsigned qtl, const f32x16& negm, f32x16& o) {
#pragma unroll
  for (int t = 0; t < 4; ++t) {
    const u32x2 k2 = *(LDSP const u32x2*)(kt + (t >> 1) * 256);
    const u32x4 ktl = {k2[0], k2[1], 0, 0};
    const u32x4 kf = ldsr16(km + t * 1024);
    const u32x4 v0 = ldsr16(vp + t * 1216), v1 = ldsr16(vp + t * 1216 + 608);
    u32x4 qq = {0, 0, 0, 0}; qq[t & 1] = qtl;
    f32x16 s = mfma32(ktl, qq, negm);
    s = mfma32(kf, qm, s);
    u32x4 p0, p1;
#pragma unroll
    for (int i = 0; i < 4; ++i) {
      p0[i] = cvtpk(__builtin_amdgcn_exp2f(s[2 * i]), __builtin_amdgcn_exp2f(s[2 * i + 1]));
      p1[i] = cvtpk(__builtin_amdgcn_exp2f(s[8 + 2 * i]), __builtin_amdgcn_exp2f(s[8 + 2 * i + 1]));
    }
    o = mfma32(v0, p0, o);
    o = mfma32(v1, p1, o);
  }
}
template <int G> DI void attn_group(AttnC& c) {
  __builtin_amdgcn_s_setprio(G < 5 ? 3 - (G & 3) : (G < 8 ? 3 : (G < 11 ? 2 : (G < 14 ? 1 : 0))));
  attn_tile<4 * G + 0>(c); attn_tile<4 * G + 1>(c); attn_tile<4 * G + 2>(c); attn_tile<4 * G + 3>(c);
}
DI void attn_helper(const u16* wg, const u16* wu, const u16* wd, char* ws, int head, int j, int lane, unsigned lds0) {
  __builtin_amdgcn_s_setprio(3);
  const char* kv = ws + WS_KV + (size_t)head * KV_HEAD_B;
  const unsigned lane16 = lane * 16;
#define KV_DMA(o) dma16(kv + (o), lane16, lds0 + (o))
#pragma unroll
  for (int i = 0; i < 4; ++i) KV_DMA(((i >> 1) * 16 + (i & 1) * 4 + j) * 1024);
#pragma unroll
  for (int i = 0; i < 6; ++i) { const int p = i < 2 ? 8 + 4 * i + j : min(16 + 4 * i + j, 36); KV_DMA(p * 1024); }
  const int hw = blockIdx.x * 4 + j;
  PrepJob jb[4];
#pragma unroll
  for (int i = 0; i < 4; ++i) { prep_setup(jb[i], min(4 * hw + i, NJ_W1 + NJ_WD - 1), lane, wd, wg, wu, wd); PIN(jb[i].voff); }
  SBAR();
  asm volatile("s_waitcnt vmcnt(6)\n\ts_barrier" ::: "memory");
#pragma unroll
  for (int i = 0; i < 10; ++i) KV_DMA((unsigned)CHUNK_B + min(4 * i + j, 36) * 1024);
  asm volatile("s_waitcnt vmcnt(10)\n\ts_barrier" ::: "memory");
#pragma unroll
  for (int i = 0; i < 19; ++i) KV_DMA(2 * (unsigned)CHUNK_B + min(4 * i + j, 73) * 1024);
#undef KV_DMA
  u32x2 r[4][8];
  unsigned pf;
  prep_load_all(jb[0], r[0]);
  asm volatile("s_waitcnt vmcnt(27)\n\ts_barrier" ::: "memory");
  prep_load_all(jb[1], r[1]); prep_load_all(jb[2], r[2]);
  asm volatile("s_waitcnt vmcnt(24)\n\ts_barrier" ::: "memory");
  const __amdgpu_buffer_rsrc_t wrs = mkrsrc(ws, (unsigned)WS_END);
  const unsigned pso = ((lane >> 2) & 3) * 256 + ((lane & 3) * 4 + (lane >> 4)) * 16;
  prep_load_all(jb[3], r[3]);
  CODE_PF(pf, k_attn, k_end, 0, 2 * j + 1 + ((blockIdx.x >> 3) & 1), (blockIdx.x >> 3) < 2 ? 128u : 0u);
  asm volatile("s_waitcnt vmcnt(25)" : PIN8(r[0])::"memory");
  prep_store(jb[0], r[0], wrs, pso);
  asm volatile("s_waitcnt vmcnt(21)" : PIN8(r[1])::"memory");
  prep_store(jb[1], r[1], wrs, pso);
  asm volatile("s_waitcnt vmcnt(17)" : PIN8(r[2])::"memory");
  prep_store(jb[2], r[2], wrs, pso);
  asm volatile("s_waitcnt vmcnt(12)" : PIN8(r[3]), "+v"(pf)::"memory");
  prep_store(jb[3], r[3], wrs, pso);
}
extern "C" __global__ __launch_bounds__(768, 3) void k_attn() {
  extern __shared__ __attribute__((aligned(16))) char smem[];
  cptr_t gp = (cptr_t)(uintptr_t)g_ptr;
  const u16 *x = (const u16*)gp[0], *wg = (const u16*)gp[1], *wu = (const u16*)gp[2], *wd = (const u16*)gp[3]; char* ws = (char*)gp[4];
  asm volatile("" : "+s"(x), "+s"(wg), "+s"(wu), "+s"(wd), "+s"(ws));
  LDSP char* lds = (LDSP char*)smem;
  const int tid = threadIdx.x, lane = tid & 63, w = rfl(tid >> 6);
  const int xcd = blockIdx.x & 7, bi = blockIdx.x >> 3;
  const int head = 4 * xcd + (bi & 3), qb = bi >> 2;
  if (__builtin_expect(w >= 8, 0)) { attn_helper(wg, wu, wd, ws, head, w - 8, lane, (unsigned)(uintptr_t)lds); return; }
  const int r = lane & 31, h = lane >> 5;
  const int qt32 = 8 * qb + w;
  STAMP_DECL; STAMP(0);
  AttnC c;
  c.qm = gld16(ws + WS_QM, head * 65536 + qt32 * 1024 + lane * 16);
  unsigned qtl = gld4(ws + WS_QT, head * 8192 + (32 * qt32 + r) * 4);
  u32x4 km0 = gld16(ws + WS_KMAX, ((lane >> 1) * 32 + head) * 64 + (lane & 1) * 32);
  u32x4 km1 = gld16(ws + WS_KMAX, ((lane >> 1) * 32 + head) * 64 + (lane & 1) * 32 + 16);
  const int vrow = r < 19 ? r : ((r >= 20 && r < 28) ? 0 : 18);
  c.kmb = lds + lane * 16;
  c.ktb = lds + 16384 + r * 8;
  c.vb = lds + 18432 + h * 304 + vrow * 16;
#pragma unroll
  for (int i = 0; i < 3; ++i) {
    c.kma[i] = (unsigned)(uintptr_t)c.kmb + i * 65536; c.kta[i] = (unsigned)(uintptr_t)c.ktb + i * 65536; c.va[i] = (unsigned)(uintptr_t)c.vb + i * 65536;
    PIN(c.kma[i]); PIN(c.kta[i]); PIN(c.va[i]);
  }
#pragma unroll
  for (int i = 0; i < 16; ++i) c.o[i] = 0.f;
  PIN(c.o);
  const __amdgpu_buffer_rsrc_t wrs = mkrsrc(ws, (unsigned)WS_END);
  SBAR();
  asm volatile("s_waitcnt vmcnt(0)" : "+v"(c.qm), "+v"(qtl), "+v"(km0), "+v"(km1)::"memory");
  if (h) qtl = 0;
  float q2 = dot2(qtl, qtl, 0.f);
#pragma unroll
  for (int i = 0; i < 4; ++i) q2 = dot2(c.qm[i], c.qm[i], q2);
  unsigned k2 = max(max(max(km0[0], km0[1]), max(km0[2], km0[3])), max(max(km1[0], km1[1]), max(km1[2], km1[3])));
  { auto rr = __builtin_amdgcn_permlane32_swap(k2, k2, false, false); k2 = max(rr[0], rr[1]); }
  { auto rr = __builtin_amdgcn_permlane16_swap(k2, k2, false, false); k2 = max(rr[0], rr[1]); }
  k2 = max(k2, (unsigned)__builtin_amdgcn_update_dpp(0u, k2, 0x128, 0xf, 0xf, true));
  k2 = max(k2, (unsigned)__builtin_amdgcn_update_dpp(0u, k2, 0x124, 0xf, 0xf, true));
  k2 = max(k2, (unsigned)__builtin_amdgcn_update_dpp(0u, k2, 0x122, 0xf, 0xf, true));
  k2 = max(k2, (unsigned)__builtin_amdgcn_update_dpp(0u, k2, 0x121, 0xf, 0xf, true));
  q2 = xor32_sum(q2);
  const float mshift = __builtin_amdgcn_sqrtf(q2 * __uint_as_float(k2)) * 1.001f;
  c.qt = u32x4{qtl, 0, 0, qtl}; PIN(c.qt);
#pragma unroll
  for (int i = 0; i < 16; ++i) c.negm[i] = -mshift;
  PIN(c.negm);
  STAMP(1);
  asm volatile("s_barrier" ::: "memory");
  STAMP(2);
  asm volatile("ds_read_b64 v[92:93], %[kt]\n\tds_read_b128 v[70:73], %[ka]\n\tds_read_b128 v[66:69], %[ka] offset:1024\n\t"
               "ds_read_b128 v[88:91], %[va]\n\tds_read_b64 v[94:95], %[kt] offset:256\n\ts_waitcnt lgkmcnt(3)\n\t"
               "v_mfma_f32_32x32x8_bf16 v[34:49], v[92:93], v[102:103], v[18:33]\n\t"
               "v_mfma_f32_32x32x16_bf16 v[34:49], v[70:73], v[98:101], v[34:49]\n\ts_nop 11"
               : "={v[34:49]}"(c.sa), "={v[50:65]}"(c.sb), "={v[66:69]}"(c.kf), "={v[70:73]}"(c.p1), "={v[84:87]}"(c.v1), "={v[88:91]}"(c.v0), "={v[92:95]}"(c.ktl)
               : "{v[18:33]}"(c.negm), "{v[98:101]}"(c.qm), "{v[102:105]}"(c.qt), [ka] "v"(c.kma[0]), [kt] "v"(c.kta[0]), [va] "v"(c.va[0]));
  attn_group<0>(c);
  asm volatile("s_barrier" ::: "memory");
  attn_group<1>(c); attn_group<2>(c);
  asm volatile("s_barrier" ::: "memory");
  attn_group<3>(c); attn_group<4>(c);
  asm volatile("s_barrier" ::: "memory");
  STAMP(3);
  attn_group<5>(c); attn_group<6>(c); attn_group<7>(c); attn_group<8>(c); attn_group<9>(c); attn_group<10>(c);
  attn_group<11>(c); attn_group<12>(c); attn_group<13>(c); attn_group<14>(c); attn_group<15>(c);
  asm volatile("s_waitcnt lgkmcnt(0)\n\tv_mfma_f32_32x32x16_bf16 v[2:17], v[84:87], v[70:73], v[2:17]\n\ts_nop 11"
               : "+{v[2:17]}"(c.o) : "{v[84:87]}"(c.v1), "{v[70:73]}"(c.p1), "{v[18:33]}"(c.negm));
  STAMP(4);
  f32x16 o = c.o;
  float l;
  { auto rr = __builtin_amdgcn_permlane32_swap(__float_as_uint(o[10]), __float_as_uint(o[10]), false, false); l = __uint_as_float(rr[0]); }
  if (__builtin_expect(__any(!(l >= 5.421011e-20f)), 0)) {
    float mx = -INFINITY;
    f32x16 z, negm;
#pragma unroll
    for (int i = 0; i < 16; ++i) z[i] = 0.f;
#pragma unroll 1
    for (int tt = 0; tt < 64; ++tt) {
      const int ck = tt >> 4, tl = tt & 15;
      const u32x2 k2 = *(LDSP const u32x2*)(c.ktb + ck * CHUNK_B + (tl >> 1) * 256);
      const u32x4 ktl = {k2[0], k2[1], 0, 0};
      const u32x4 kf = ldsr16(c.kmb + ck * CHUNK_B + tl * 1024);
      const u32x4 qq = {(tl & 1) ? 0u : qtl, (tl & 1) ? qtl : 0u, 0, 0};
      f32x16 sc = mfma32(ktl, qq, z);
      sc = mfma32(kf, c.qm, sc);
#pragma unroll
      for (int i = 0; i < 16; ++i) mx = fmaxf(mx, sc[i]);
    }
    mx = xor32_max(mx);
#pragma unroll
    for (int i = 0; i < 16; ++i) { negm[i] = -mx; o[i] = 0.f; }
#pragma unroll 1
    for (int gq = 0; gq < 16; ++gq) {
      const int ck = gq >> 2, gg = gq & 3;
      attn_group_slow(c.kmb + ck * CHUNK_B + gg * 4096, c.ktb + ck * CHUNK_B + gg * 512, c.vb + ck * CHUNK_B + gg * 4864, c.qm, qtl, negm, o);
    }
    auto rr = __builtin_amdgcn_permlane32_swap(__float_as_uint(o[10]), __float_as_uint(o[10]), false, false);
    l = __uint_as_float(rr[0]);
  }
  const float rl = 1.0f / l;
  u32x4 ou;
#pragma unroll
  for (int i = 0; i < 4; ++i) ou[i] = cvtpk(o[2 * i] * rl, o[2 * i + 1] * rl);
  const int q = 32 * qt32 + r;
  const unsigned ob = (unsigned)WS_OFR + (q >> 4) * 18432 + (q & 15) * 16;
  bst16<SC1>(ou, wrs, ob + (head >> 1) * 1024 + (2 * (head & 1) + h) * 256, 0);
  __builtin_amdgcn_raw_buffer_store_b32(cvtpk(o[8] * rl, o[9] * rl), wrs, h ? DROP : ob + (16 + (head >> 4)) * 1024 + ((head >> 2) & 3) * 256 + (head & 3) * 4, 0, SC1);
  STAMP(5);
  const int m = lane & 15, g = lane >> 4, kh = w >> 2, cg = bi >> 2;
  const int nt0 = 9 * (cg >> 1) + 5 * (cg & 1), mt = 16 * xcd + 4 * (bi & 3) + (w & 3), row = 16 * mt + m;
  const unsigned lds0 = (unsigned)(uintptr_t)lds, lane16 = lane * 16;
  asm volatile("s_barrier" ::: "memory");
#define K3_W(i) do { const int p = min(8 * (i) + w, 89), st = (p * 205) >> 10, t = p - 5 * st;                                  \
    dma16(ws + WS_WO + (st * 36 + nt0 + min(t, 4 - (cg & 1))) * 1024, lane16, lds0 + p * 1024); } while (0)
#pragma unroll
  for (int i = 0; i < K3_PRE; ++i) K3_W(i);
  asm volatile("s_waitcnt vmcnt(%0)\n\ts_barrier" ::"i"(K3_PRE) : "memory");
  if (w == 0 && lane < K3_REP) __hip_atomic_fetch_add((gu32*)(ws + WS_CNT + (128 + qb * K3_REP) * 128) + lane * 32, 1u, __ATOMIC_RELAXED, __HIP_MEMORY_SCOPE_AGENT);
#pragma unroll
  for (int i = K3_PRE; i < 12; ++i) K3_W(i);
#undef K3_W
  dma16(ws + WS_WBAR + min(w, 2) * 1024, lane16, lds0 + K3_WBAR + min(w, 2) * 1024);
  const bool t4 = kh == 0 && !(cg & 1);
  const unsigned uo = mt * 18432 + (2 * nt0 + 4 * kh + (g >> 1)) * 256 + m * 16 + (g & 1) * 8;
  u32x2 xr[3];
  const unsigned xo = row * 1152 + (nt0 + 2 * kh) * 32 + g * 8;
  asm volatile("global_load_dwordx2 %0, %1, %2" : "=v"(xr[0]) : "v"(xo), "s"(x));
  asm volatile("global_load_dwordx2 %0, %1, %2 offset:32" : "=v"(xr[1]) : "v"(xo), "s"(x));
  asm volatile("global_load_dwordx2 %0, %1, %2" : "=v"(xr[2]) : "v"(t4 ? xo + 128 : xo), "s"(x));
  unsigned xm = gld4(ws + WS_XMEAN, row * 4);
  if (w == 0) wait_count((gu32*)(ws + WS_CNT + (128 + xcd * K3_REP + bi % K3_REP) * 128), 0u, 32u);
  LDSP const char* wa = lds + (45 * kh + 2 * kh) * 1024 + lane16;
  LDSP const char* wb = lds + (45 * kh + 2 - 2 * kh) * 1024 + lane16;
  LDSP const char* wc = lds + (45 * kh + 4) * 1024 + lane16;
  LDSP const char* wbl = lds + K3_WBAR + (m & 1) * 1152 + kh * 576 + g * 16;
  unsigned so = ((g & 1) ? (unsigned)WS_HC : (unsigned)WS_HU) + (uo & ~8u);
  PIN(wa); PIN(wb); PIN(wc); PIN(wbl); PIN(so);
  asm volatile("s_waitcnt vmcnt(0)\n\ts_barrier" : "+v"(xr[0]), "+v"(xr[1]), "+v"(xr[2]), "+v"(xm)::"memory");
  STAMP(6);
  u32x4 O[9], wf[6];
  const char* obase = ws + WS_OFR + rfl((mt * 18 + 9 * kh) * 1024);
#pragma unroll
  for (int i = 0; i < 9; ++i)
    asm volatile("global_load_dwordx4 %0, %1, %2 sc1" : "=v"(O[i]) : "v"(lane16), "s"(obase + i * 1024));
#define K3_A(f) (((f) + 2) % 6)
#define K3_RD(f) (K3_A(f) == 5 ? ldsr16(wbl + ((f) / 6) * 64) : ldsr16((K3_A(f) < 2 ? wa : (K3_A(f) < 4 ? wb : wc)) + ((f) / 6) * 5120 + (K3_A(f) < 4 ? ((f) & 1) * 1024 : 0)))
#pragma unroll
  for (int f = 0; f < 5; ++f) wf[f] = K3_RD(f);
  f32x4 acc[6];
#pragma unroll
  for (int f = 0; f < 54; ++f) {
    if (f % 6 == 0) asm volatile("s_waitcnt vmcnt(%1)" : "+v"(O[f / 6]) : "i"(8 - f / 6));
    if (f + 5 < 54) wf[(f + 5) % 6] = K3_RD(f + 5);
    if (f < 6) G_MFMA0(acc[K3_A(f)], wf[f % 6], O[f / 6]);
    else G_MFMA(acc[K3_A(f)], wf[f % 6], O[f / 6]);
    SBAR();
  }
#undef K3_RD
#undef K3_A
  asm volatile("s_nop 7" : "+v"(acc[0]), "+v"(acc[1]), "+v"(acc[2]), "+v"(acc[3]), "+v"(acc[4]), "+v"(acc[5]));
  LDSP char* xc = lds + K3_XC + lane16;
#pragma unroll
  for (int i = 0; i < 4; ++i) *(LDSP f32x4*)(xc + w * 4096 + i * 1024) = acc[2 + i];
  asm volatile("s_waitcnt lgkmcnt(0)\n\ts_barrier" ::: "memory");
  f32x4 v[4];
#pragma unroll
  for (int i = 0; i < 4; ++i) v[i] = *(LDSP const f32x4*)(xc + (w ^ 4) * 4096 + i * 1024);
  SBAR();
  const float mean = __uint_as_float(xm) + (acc[5][0] + v[3][0]) + (acc[5][1] + v[3][1]);
  float ssq = 0.f;
#pragma unroll
  for (int t = 0; t < 3; ++t) {
    const f32x4 a = (t < 2 ? acc[t] : acc[4]) + v[t];
    const unsigned h0 = cvtpk(bflo(xr[t][0]) + a[0], bfhi(xr[t][0]) + a[1]);
    const unsigned h1 = cvtpk(bflo(xr[t][1]) + a[2], bfhi(xr[t][1]) + a[3]);
    const unsigned c0 = cvtpk(bflo(h0) - mean, bfhi(h0) - mean);
    const unsigned c1 = cvtpk(bflo(h1) - mean, bfhi(h1) - mean);
    if (t < 2 || t4) { ssq = dot2(c0, c0, ssq); ssq = dot2(c1, c1, ssq); }
    const auto r0 = __builtin_amdgcn_permlane16_swap(h0, c0, false, false);
    const auto r1 = __builtin_amdgcn_permlane16_swap(h1, c1, false, false);
    bst16<SC1>(u32x4{r0[0], r1[0], r0[1], r1[1]}, wrs, t < 2 ? so + t * 512 : (t4 ? so + 2048 : DROP), 0);
  }
  ssq = xor32_sum(xor16_sum(ssq));
  __builtin_amdgcn_raw_buffer_store_b32(__float_as_uint(ssq), wrs, (unsigned)WS_SSQ + ((2 * cg + kh) * 2048 + row) * 4, 0, SC1);
  STAMP(7); STAMP_OUT(2, 8);
}

constexpr int K4_SLOT = 36864, K4_LDS = 163840;
constexpr int K4_PF = 4, K4_POLL = 10, K5_SIG = 154;
constexpr int K5_PERM[18] = {0, 1, 2, 6, 7, 8, 9, 10, 11, 15, 16, 17, 3, 4, 5, 12, 13, 14};
constexpr int K5_R[4] = {36864, 73728, 110592, 0};
constexpr int K5_OC[9] = {65536, 102400, 139264, 147456, 155648, 0, 8192, 16384, 24576};
struct K5P { const char* wb; unsigned q[4], qs[4], qo, qos, lds0; };
template <int Z> DI void k5_piece(const K5P& k, int i, int w, unsigned lane16) {
  constexpr int P0 = K5_PERM[2 * Z] * 9216, D = (K5_PERM[2 * Z + 1] - K5_PERM[2 * Z]) * 9216;
  if (i < 3 || (w >> 2) == (Z & 1))
    dma16(k.wb + (P0 + k.q[i] + k.qs[i] * D), lane16, k.lds0 + K5_R[Z & 3] + (i < 3 ? 8 * i + w : 24 + (w & 3)) * 1024);
}
template <int Z> DI void k5_own(const K5P& k, int w, unsigned lane16) {
  constexpr int P0 = K5_PERM[2 * Z] * 9216, D = (K5_PERM[2 * Z + 1] - K5_PERM[2 * Z]) * 9216;
  dma16(k.wb + (P0 + k.qo + k.qos * D), lane16, k.lds0 + K5_OC[Z] + w * 1024);
}
#define POLL(dst, p) asm volatile("global_load_dword %0, %1, %2 sc1" : "=v"(dst) : "v"(0u), "s"(p))
struct K4C {
  const char *wb, *xb; unsigned lane16, lds0; int w, dir;
  unsigned pf1, poll;
  K5P wd;
  gu32 *cntA, *cntB;
  const char *ab, *ab2; u32x4 A0[2][4];
  LDSP const char* wl[4];
  u32x4 X[2][18], wf[K4_PF + 1];
  f32x4 acc[4][4];
  float c1[2], c2[2];
  __amdgpu_buffer_rsrc_t ars; unsigned avo; int T0;
  float ea[2][2], et[2][2]; unsigned pk[2][4];
};
DI void k4_piece(const K4C& c, int tile, int p) {
  dma16(c.wb + (tile * c.dir * K4_SLOT + p * 1024), c.lane16, c.lds0 + (tile & 3) * K4_SLOT + p * 1024);
}
template <int ZW> DI void k4_issue_w(K4C& c, int i) {
  if (ZW < 9) {
    if (i == 0) k4_piece(c, c.w >> 2, 4 * ZW + (c.w & 3));
    else if ((c.w >> 2) == (ZW & 1)) k4_piece(c, 2, 4 * ZW + (c.w & 3));
  } else if (ZW == 9) { k4_piece(c, 3, 8 * i + c.w); }
}
template <int Z, int I> DI void k4_issue1(K4C& c) {
  if (I == 0) k4_issue_w<Z + 1>(c, 0);
  else if (I < 5) {
    c.X[(I - 1) >> 1][2 * Z + ((I - 1) & 1)] = gld16(c.xb + (((I - 1) >> 1) * 18432 + (2 * Z + ((I - 1) & 1)) * 1024), c.lane16);
  }
  else k4_issue_w<Z + 1>(c, 1);
}
template <int TL, int I> DI void k4_issue2(K4C& c) {
  if (I < 4) k4_piece(c, TL, 8 * I + c.w);
  else if ((c.w >> 2) == (TL & 1)) k4_piece(c, TL, 32 + (c.w & 3));
}
template <int TL, int E, int U> DI void k4_epi(K4C& c) {
  constexpr int e = U >> 2, u = U & 3, mt = e >> 2, i = e & 3, q = e & 1;
  if constexpr (U < 32) {
    if (u == 0) { const float a = c.acc[TL & 3][mt][i] * c.c1[mt]; c.ea[E][q] = a; c.et[E][q] = a * c.acc[TL & 3][2 + mt][i]; PIN(c.ea[E][q]); PIN(c.et[E][q]); }
    if (u == 1) { c.ea[E][q] = __builtin_amdgcn_exp2f(c.ea[E][q]); PIN(c.ea[E][q]); }
    if (u == 2) { c.ea[E][q] = __builtin_amdgcn_rcpf(__builtin_fmaf(c.ea[E][q], c.c2[mt], c.c2[mt])); PIN(c.ea[E][q]); }
    if (u == 3) { c.et[E][q] *= c.ea[E][q]; PIN(c.et[E][q]); if (q) { c.pk[E][e >> 1] = cvtpk(c.et[E][0], c.et[E][1]); PIN(c.pk[E][e >> 1]); } }
  } else if constexpr (U == 32) {
    auto r0 = __builtin_amdgcn_permlane16_swap(c.pk[E][0], c.pk[E][2], false, false);
    auto r1 = __builtin_amdgcn_permlane16_swap(c.pk[E][1], c.pk[E][3], false, false);
    c.pk[E][0] = r0[0]; c.pk[E][1] = r1[0]; c.pk[E][2] = r0[1]; c.pk[E][3] = r1[1];
  } else if constexpr (U == 33) {
    const int T = c.T0 + TL * c.dir;
    bst16<SC1>(u32x4{c.pk[E][0], c.pk[E][1], c.pk[E][2], c.pk[E][3]}, c.ars, c.avo, (T >> 1) * 1024 + (T & 1) * 512);
  }
}
constexpr int k4_tile(int F) { return F < 108 ? (F % 6) >> 1 : 3 + (F - 108) / 36; }
constexpr int k4_step(int F) { return F < 108 ? F / 6 : ((F - 108) % 36) >> 1; }
constexpr int k4_addr(int F) { return k4_step(F) * 2048 + (F & 1) * 1024; }
template <int F> DI void k4_frag(K4C& c) {
  constexpr int TL = k4_tile(F), S = k4_step(F), M = F & 1, N = F + K4_PF;
  const u32x4 a = c.wf[F % (K4_PF + 1)];
  if constexpr (F < 108) { if constexpr ((F + 4) % 3 == 0) __builtin_amdgcn_s_setprio(3 - ((F + 4) % 12) / 3); }
  else if constexpr ((F - 108 + 4) % 9 == 0) __builtin_amdgcn_s_setprio(3 - ((F - 108 + 4) % 36) / 9);
  if (S == 0) {
    G_MFMA0(c.acc[TL & 3][2 * M], a, c.X[0][S]);
    G_MFMA0(c.acc[TL & 3][2 * M + 1], a, c.X[1][S]);
  } else {
    G_MFMA(c.acc[TL & 3][3 * M], a, c.X[M][S]);
    G_MFMA(c.acc[TL & 3][M + 1], a, c.X[1 - M][S]);
  }
  if constexpr (N < 108 && N % 12 == 0) {
    constexpr int Z = N / 12;
    if (Z < 8) asm volatile("s_waitcnt vmcnt(5)\n\ts_barrier" : "+v"(c.X[0][2 * Z]), "+v"(c.X[0][2 * Z + 1]), "+v"(c.X[1][2 * Z]), "+v"(c.X[1][2 * Z + 1])::"memory");
    else asm volatile("s_waitcnt vmcnt(2)\n\ts_barrier" : "+v"(c.X[0][2 * Z]), "+v"(c.X[0][2 * Z + 1]), "+v"(c.X[1][2 * Z]), "+v"(c.X[1][2 * Z + 1])::"memory");
  } else if constexpr (N >= 108 && N < 324 && (N - 108) % 36 == 0) {
    constexpr int NT = k4_tile(N);
    if (NT == 3) asm volatile("s_waitcnt vmcnt(0) lgkmcnt(0)\n\ts_barrier" ::: "memory");
    else if (NT == 8) {
      asm volatile("s_waitcnt vmcnt(0) lgkmcnt(0)\n\ts_barrier" ::: "memory");
      if (c.w == 0 && c.lane16 == 0) __hip_atomic_fetch_add(c.cntA, 1u, __ATOMIC_RELAXED, __HIP_MEMORY_SCOPE_AGENT);
    }
    else asm volatile("s_waitcnt vmcnt(4) lgkmcnt(0)\n\ts_barrier" ::: "memory");
  }
  if constexpr (N < 324) c.wf[N % (K4_PF + 1)] = ldsr16(c.wl[k4_tile(N) & 3] + k4_addr(N));
  if constexpr (F < 108) {
    constexpr int Z = F / 12, I = F % 12;
    if constexpr (Z == 0 && I < 4 && !(I & 1)) k4_issue1<2, I / 2>(c);
    if constexpr (Z < 7 && I < 8 && (I & 1)) k4_issue1<Z + 2, 2 + I / 2>(c);
    if constexpr (Z < 6 && I > 8 && (I & 1)) k4_issue1<Z + 3, (I - 9) / 2>(c);
    if constexpr (Z == 6 && I == 10) k4_issue2<3, 2>(c);
    if constexpr (Z == 7 && (I == 1 || I == 3)) k4_issue2<3, 3 + I / 2>(c);
  } else {
    constexpr int P = (F - 108) % 36;
    if constexpr (TL == 3) { if constexpr (P % 3 == 0 && P < 15) k4_issue2<4, P / 3>(c); if constexpr (P % 3 == 0 && P >= 15 && P < 30) k4_issue2<5, (P - 15) / 3>(c); }
    else if constexpr (TL + 2 < 9 && P % 4 == 0 && P < 20) k4_issue2<TL + 2, P / 4>(c);
    if constexpr (TL == 7 && P == 0) CODE_PF(c.pf1, k_ffn, k_end, 0, 2 + (c.w & 1), ((blockIdx.x >> 3) == 0 && c.w < 2) ? 128u : 0u);
    if constexpr (TL == 7 && P % 3 == 2 && P <= 11) k5_piece<0>(c.wd, P / 3, c.w, c.lane16);
    if constexpr (TL == 8 && P < 4) k5_piece<1>(c.wd, P, c.w, c.lane16);
    if constexpr (TL == 8 && P >= 4 && P < 8) k5_piece<2>(c.wd, P - 4, c.w, c.lane16);
    if constexpr (TL == 8 && P == K4_POLL) POLL(c.poll, c.cntA);
    if constexpr (TL == 8 && P == K4_POLL + 12) { asm volatile("s_waitcnt vmcnt(0)" : "+v"(c.poll), "+v"(c.pf1)); wait_count(c.cntA, c.poll, 8u); }
    if constexpr (TL == 8 && P > K4_POLL + 12 && P <= K4_POLL + 20) {
      constexpr int i = P - K4_POLL - 13, z = i >> 2, j = (i & 1) * 2 + ((i >> 1) & 1);
      asm volatile("global_load_dwordx4 %0, %1, %2 sc1" : "=v"(c.A0[z][j]) : "v"(c.lane16), "s"(((j >> 1) ? c.ab2 : c.ab) + K5_PERM[2 * z + (j & 1)] * 1024));
    }
    if constexpr (P >= 2) {
      if constexpr (TL == 3) { k4_epi<0, 0, P - 2>(c); k4_epi<1, 1, P - 2>(c); }
      else if constexpr (TL == 4) { k4_epi<2, 0, P - 2>(c); k4_epi<3, 1, P - 2>(c); }
      else k4_epi<TL - 1, 0, P - 2>(c);
    }
  }
  SBAR();
}
template <int B, int... Fs> DI void k4_run(K4C& c, std::integer_sequence<int, Fs...>) { (k4_frag<B + Fs>(c), ...); }
DI void k4_last(K4C& c) {
  float a[8], t[8];
#pragma unroll
  for (int e = 0; e < 8; ++e) { a[e] = c.acc[0][e >> 2][e & 3] * c.c1[e >> 2]; t[e] = a[e] * c.acc[0][2 + (e >> 2)][e & 3]; }
#pragma unroll
  for (int e = 0; e < 8; ++e) a[e] = __builtin_amdgcn_exp2f(a[e]);
#pragma unroll
  for (int e = 0; e < 8; ++e) a[e] = __builtin_amdgcn_rcpf(__builtin_fmaf(a[e], c.c2[e >> 2], c.c2[e >> 2]));
#pragma unroll
  for (int e = 0; e < 4; ++e) c.pk[0][e] = cvtpk(t[2 * e] * a[2 * e], t[2 * e + 1] * a[2 * e + 1]);
  k4_epi<8, 0, 32>(c); k4_epi<8, 0, 33>(c);
}
DI f32x4 addbf(f32x4 a, unsigned lo, unsigned hi) { return a + f32x4{bflo(lo), bfhi(lo), bflo(hi), bfhi(hi)}; }
struct K5C {
  const char *ab, *ab2;
  LDSP const char *ra[4], *oa[3], *xr;
  LDSP char* xw;
  u32x4 A[9][4], wf[5];
  f32x4 acc[2][7], own[2][2], v[4];
  const char* hb; unsigned ho; int kq; u32x2 hr[3];
  __amdgpu_buffer_rsrc_t wrs; unsigned tw, p8s;
  gu32* cnt;
};
template <int Z, int J> DI void k5_issue(K5C& c, const K4C& k) {
  if constexpr (Z < 7 && J < 4) asm volatile("global_load_dwordx4 %0, %1, %2 sc1" : "=v"(c.A[Z + 2][J]) : "v"(k.lane16), "s"(((J >> 1) ? c.ab2 : c.ab) + K5_PERM[2 * (Z + 2) + (J & 1)] * 1024));
  if constexpr (Z < 6 && J >= 4) k5_piece<Z + 3>(k.wd, J - 4, k.w, k.lane16);
  if constexpr (Z == 6 && J >= 4) k5_own<J - 4>(k.wd, k.w, k.lane16);
  if constexpr (Z == 7 && J == 0) k5_own<4>(k.wd, k.w, k.lane16);
  if constexpr (Z == 7 && J == 1) asm volatile("global_load_dwordx2 %0, %1, %2" : "=v"(c.hr[0]) : "v"(c.ho), "s"(c.hb + c.kq * 1024));
  if constexpr (Z == 7 && J == 2) asm volatile("global_load_dwordx2 %0, %1, %2 offset:512" : "=v"(c.hr[1]) : "v"(c.ho), "s"(c.hb + c.kq * 1024));
  if constexpr (Z == 7 && J == 3) asm volatile("global_load_dwordx2 %0, %1, %2" : "=v"(c.hr[2]) : "v"(c.ho), "s"(c.hb + 4096));
  if constexpr (Z == 8 && J < 4) k5_own<5 + J>(k.wd, k.w, k.lane16);
}
template <int N> DI u32x4 k5_read(const K5C& c) {
  if constexpr (N < 126) return ldsr16(c.ra[(N / 14) & 3] + (N % 14) * 1024);
  else { constexpr int o = K5_OC[(N - 126) / 4] + ((N - 126) & 3) * 1024; return ldsr16(c.oa[o >> 16] + (o & 0xffff)); }
}
template <int F> DI void k5_frag(K5C& c, K4C& k) {
  constexpr int N = F + 4;
  const u32x4 a = c.wf[F % 5];
  if constexpr (F < 126) {
    constexpr int Z = F / 14, f = F % 14, st = f / 7, j = f % 7;
    if (F < 7) { G_MFMA0(c.acc[0][j], a, c.A[Z][st]); G_MFMA0(c.acc[1][j], a, c.A[Z][2 + st]); }
    else { G_MFMA(c.acc[1][j], a, c.A[Z][2 + st]); G_MFMA(c.acc[0][j], a, c.A[Z][st]); }
    if constexpr (f == 10 && Z == 8) asm volatile("s_waitcnt vmcnt(4)\n\ts_barrier" : "+v"(c.hr[0]), "+v"(c.hr[1]), "+v"(c.hr[2])::"memory");
    if constexpr (f == 10 && Z < 8) {
      u32x4(&A)[4] = c.A[Z + 1];
      if (Z == 0) asm volatile("s_waitcnt vmcnt(9) lgkmcnt(0)\n\ts_barrier" : "+v"(A[0]), "+v"(A[1]), "+v"(A[2]), "+v"(A[3])::"memory");
      else if (Z == 3) asm volatile("s_waitcnt vmcnt(4) lgkmcnt(0)\n\ts_barrier" : "+v"(A[0]), "+v"(A[1]), "+v"(A[2]), "+v"(A[3]), "+v"(k.poll)::"memory");
      else if (Z < 7) asm volatile("s_waitcnt vmcnt(7) lgkmcnt(0)\n\ts_barrier" : "+v"(A[0]), "+v"(A[1]), "+v"(A[2]), "+v"(A[3])::"memory");
      else asm volatile("s_waitcnt vmcnt(8) lgkmcnt(0)\n\ts_barrier" : "+v"(A[0]), "+v"(A[1]), "+v"(A[2]), "+v"(A[3])::"memory");
      if (Z == 1 && k.w == 0 && k.lane16 == 0) __hip_atomic_fetch_add(k.cntB, 1u, __ATOMIC_RELAXED, __HIP_MEMORY_SCOPE_AGENT);
      if (Z == 3) wait_count(k.cntB, k.poll, 8u);
    }
    c.wf[N % 5] = k5_read<N>(c);
    if constexpr (Z == 3 && f == 1) POLL(k.poll, k.cntB);
    constexpr int slot = f == 0 ? 2 : (f == 2 ? 3 : (f == 4 ? 4 : (f == 5 ? 5 : (f == 7 ? 6 : (f == 9 ? 7 : -1)))));
    if constexpr (F == 0) { k5_issue<0, 0>(c, k); k5_issue<0, 1>(c, k); }
    if constexpr (slot >= 0) k5_issue<Z, slot>(c, k);
    if constexpr (Z < 8 && f > 10 && (f & 1)) k5_issue<Z + 1, (f - 11) / 2>(c, k);
  } else {
    constexpr int B = F - 126, z = B / 4, st = (B >> 1) & 1, t = B & 1;
    if (B < 2) { G_MFMA0(c.own[0][t], a, c.A[z][st]); G_MFMA0(c.own[1][t], a, c.A[z][2 + st]); }
    else { G_MFMA(c.own[1][t], a, c.A[z][2 + st]); G_MFMA(c.own[0][t], a, c.A[z][st]); }
    if constexpr (F == 131) asm volatile("s_waitcnt vmcnt(0) lgkmcnt(0)\n\ts_barrier" ::: "memory");
    if constexpr (F == K5_SIG) {
      asm volatile("s_waitcnt vmcnt(0)\n\ts_barrier" ::: "memory");
      if (k.w == 0 && k.lane16 == 0) __hip_atomic_fetch_add(c.cnt, 1u, __ATOMIC_RELAXED, __HIP_MEMORY_SCOPE_AGENT);
    }
    if constexpr (N < 162) c.wf[N % 5] = k5_read<N>(c);
  }
  if constexpr (F >= 123 && F < 130) { PIN(c.acc[1][F - 123]); *(LDSP f32x4*)(c.xw + (F - 123) * 1024) = c.acc[1][F - 123]; }
  if constexpr (F == 131) { _Pragma("unroll") for (int i = 0; i < 4; ++i) c.v[i] = *(LDSP const f32x4*)(c.xr + i * 1024); }
  if constexpr (F == 133 || F == 135 || F == 138) {
    constexpr int j = F == 133 ? 0 : (F == 135 ? 1 : 2), o = 2 * (j & 1);
    const f32x4 x = c.acc[0][2 * j] + c.v[o], y = c.acc[0][2 * j + 1] + c.v[o + 1];
    bst16<SC1>(u32x4{cvtpk(x[0], x[1]), cvtpk(x[2], x[3]), cvtpk(y[0], y[1]), cvtpk(y[2], y[3])}, c.wrs, k.lane16,
              (unsigned)WS_PART + ((((c.kq + j + 1) & 3) * 512 + c.tw) * 3 + j) * 1024);
    if constexpr (F == 135) { _Pragma("unroll") for (int i = 0; i < 3; ++i) c.v[i] = *(LDSP const f32x4*)(c.xr + (4 + i) * 1024); }
  }
  if constexpr (F == 140) {
    c.acc[0][6] += c.v[2];
    __builtin_amdgcn_raw_buffer_store_b64(u32x2{cvtpk(c.acc[0][6][0], c.acc[0][6][1]), cvtpk(c.acc[0][6][2], c.acc[0][6][3])}, c.wrs, c.p8s, c.kq * 512, 16);
    PIN(c.acc[0][6]);
  }
  SBAR();
}
template <int B, int... Fs> DI void k5_run(K5C& c, K4C& k, std::integer_sequence<int, Fs...>) { (k5_frag<B + Fs>(c, k), ...); }

extern "C" __global__ __launch_bounds__(512, 2) void k_ffn() {
  extern __shared__ __attribute__((aligned(16))) char smem[];
  char* ws = (char*)((cptr_t)(uintptr_t)g_ptr)[4]; char* out = (char*)((cptr_t)(uintptr_t)g_ptr)[5];
  asm volatile("" : "+s"(out), "+s"(ws));
  LDSP char* lds = (LDSP char*)smem;
  const int tid = threadIdx.x, lane = tid & 63, w = rfl(tid >> 6);
  const int xcd = blockIdx.x & 7, bi = blockIdx.x >> 3;
  const int m = lane & 15, g = lane >> 4, kq = xcd >> 1;
  STAMP_DECL; STAMP(0);
  K4C c;
  {
    const int rb = 4 * (xcd & 1) + (bi & 3), fs = 8 * kq + (bi >> 2), mt = 16 * rb + 2 * w;
    c.lds0 = (unsigned)(uintptr_t)lds; c.lane16 = lane * 16; c.w = w;
    c.T0 = 9 * fs + 8 * (fs & 1); c.dir = 1 - 2 * (fs & 1);
    c.wb = ws + WS_W1 + (size_t)c.T0 * K4_SLOT;
    c.xb = ws + WS_HC + (size_t)mt * 18432;
    k4_issue_w<0>(c, 0); k4_issue1<0, 1>(c); k4_issue1<0, 3>(c); k4_issue_w<0>(c, 1); k4_issue1<0, 2>(c); k4_issue1<0, 4>(c);
    k4_issue1<0, 0>(c); k4_issue1<0, 5>(c);
    unsigned sq[8];
#pragma unroll
    for (int i = 0; i < 8; ++i) sq[i] = gld4(ws + WS_SSQ + (i >> 1) * 8192 + (i & 1) * 64, 4 * g * 8192 + (16 * mt + m) * 4);
    k4_issue1<1, 0>(c); k4_issue1<1, 1>(c); k4_issue1<1, 2>(c); k4_issue1<1, 3>(c); k4_issue1<1, 4>(c); k4_issue1<1, 5>(c);
    SBAR();
#pragma unroll
    for (int i = 0; i < 4; ++i) { c.wl[i] = lds + c.lane16 + i * K4_SLOT; PIN(c.wl[i]); }
    c.ars = mkrsrc(ws + WS_ACT, 128u * 144 * 1024);
    c.avo = (mt + (g & 1)) * (144 * 1024) + (g >> 1) * 256 + m * 16;
    c.wd.wb = ws + WS_WD + ((size_t)(bi >> 3) * 144 + 36 * kq) * 9 * 1024;
#pragma unroll
    for (int i = 0; i < 4; ++i) {
      const int p = i < 3 ? 8 * i + w : 24 + (w & 3), st = (p * 37) >> 8, t = p - 7 * st + 2;
      c.wd.q[i] = ((st >> 1) * 162 + (t < 8 ? (t + 2 * kq) & 7 : 8)) * 1024;
      c.wd.qs[i] = st & 1;
      asm volatile("" : "+s"(c.wd.q[i]), "+s"(c.wd.qs[i]));
    }
    c.wd.qo = ((w >> 2) * 162 + 2 * kq + (w & 1)) * 1024; c.wd.qos = (w >> 1) & 1; c.wd.lds0 = c.lds0;
    c.cntA = (gu32*)(ws + WS_CNT + (64 + rb * 4 + kq) * 128);
    STAMP(1);
    asm volatile("s_waitcnt vmcnt(15)\n\ts_barrier" : "+v"(c.X[0][0]), "+v"(c.X[0][1]), "+v"(c.X[1][0]), "+v"(c.X[1][1])::"memory");
#pragma unroll
    for (int i = 0; i < K4_PF; ++i) c.wf[i] = ldsr16(c.wl[k4_tile(i)] + k4_addr(i));
    SBAR();
    k4_run<0>(c, std::make_integer_sequence<int, 12>{});
    asm volatile("" : "+v"(sq[0]), "+v"(sq[1]), "+v"(sq[2]), "+v"(sq[3]), "+v"(sq[4]), "+v"(sq[5]), "+v"(sq[6]), "+v"(sq[7]));
#pragma unroll
    for (int t = 0; t < 2; ++t) {
      float ss = (__uint_as_float(sq[t]) + __uint_as_float(sq[2 + t])) + (__uint_as_float(sq[4 + t]) + __uint_as_float(sq[6 + t]));
      ss = xor32_sum(xor16_sum(ss));
      const float v = ss * (1.0f / DM) + LN_EPS, rs = __builtin_amdgcn_rsqf(v);
      c.c1[t] = -LOG2E * rs; c.c2[t] = -LOG2E * (v * rs);
      PIN(c.c1[t]); PIN(c.c2[t]);
    }
    SBAR();
    k4_run<12>(c, std::make_integer_sequence<int, 276>{});
    c.cntB = c.cntA + 32 * 32;
  }
  const int rb = 8 * (xcd & 1) + (bi & 7), nb = bi >> 3, mg = w & 3, kh = w >> 2, mu = 8 * rb + 2 * mg + kh;
  gu32* prodB = c.cntB;
  c.cntA = (gu32*)(ws + WS_CNT + (64 + (rb >> 1) * 4 + kq) * 128);
  c.ab = ws + WS_ACT + ((size_t)mu * 144 + 36 * kq + 18 * kh) * 1024;
  c.ab2 = ws + WS_ACT + ((size_t)(mu ^ 1) * 144 + 36 * kq + 18 * kh) * 1024;
  k4_run<288>(c, std::make_integer_sequence<int, 36>{});
  STAMP(2);
  K5C d;
  d.ab = c.ab; d.ab2 = c.ab2;
  asm volatile("s_nop 7" : "+v"(c.acc[0][0]), "+v"(c.acc[0][1]), "+v"(c.acc[0][2]), "+v"(c.acc[0][3]));
  k4_last(c);
  SBAR();
  gu32* consB = c.cntA + 32 * 32;
  d.hb = ws + WS_HU + (size_t)mu * 18432 + nb * 4608; d.ho = (g >> 1) * 256 + m * 16 + (g & 1) * 8; d.kq = kq;
#pragma unroll
  for (int i = 0; i < 4; ++i) { d.ra[i] = lds + K5_R[i] + kh * 14336 + c.lane16; PIN(d.ra[i]); }
#pragma unroll
  for (int i = 0; i < 3; ++i) { d.oa[i] = lds + i * 65536 + kh * 4096 + c.lane16; PIN(d.oa[i]); }
  d.xw = lds + (kh ? K5_R[2] : K5_R[1]) + mg * 7168 + c.lane16; PIN(d.xw);
  d.xr = lds + (kh ? K5_R[1] : K5_R[2]) + mg * 7168 + c.lane16; PIN(d.xr);
  const __amdgpu_buffer_rsrc_t wrs = mkrsrc(ws, (unsigned)WS_END), ors = mkrsrc(out, SEQ * DM * 2);
  const int tile = rb * 4 + nb;
  const bool own8 = (mu & 3) == kq;
  const unsigned tw = tile * 8 + w;
  const unsigned p8 = (unsigned)WS_PART8 + tw * 2048 + lane * 8;
  d.cnt = (gu32*)(ws + WS_CNT + tile * 128);
  d.wrs = wrs; d.tw = tw; d.p8s = own8 ? DROP : p8; PIN(d.p8s);
#pragma unroll
  for (int i = 0; i < 4; ++i) { d.A[0][i] = c.A0[0][i]; d.A[1][i] = c.A0[1][i]; }
  asm volatile("s_waitcnt vmcnt(6) lgkmcnt(0)\n\ts_barrier" : "+v"(d.A[0][0]), "+v"(d.A[0][1]), "+v"(d.A[0][2]), "+v"(d.A[0][3])::"memory");
  STAMP(3);
  d.wf[0] = k5_read<0>(d); d.wf[1] = k5_read<1>(d); d.wf[2] = k5_read<2>(d); d.wf[3] = k5_read<3>(d);
  SBAR();
  c.cntB = prodB;
  k5_run<0>(d, c, std::make_integer_sequence<int, 28>{});
  c.cntB = consB;
  k5_run<28>(d, c, std::make_integer_sequence<int, 134>{});
  asm volatile("s_nop 7" : "+v"(d.own[0][0]), "+v"(d.own[0][1]), "+v"(d.own[1][0]), "+v"(d.own[1][1]));
  STAMP(4);
  LDSP char* xo = lds + K5_R[0] + c.lane16;
  *(LDSP f32x4*)(xo + w * 2048) = d.own[1][0];
  *(LDSP f32x4*)(xo + w * 2048 + 1024) = d.own[1][1];
  unsigned oo = (16 * mu + m) * 1152 + nb * 288;
  unsigned oo8 = own8 ? oo + 256 + g * 8 : DROP;
  oo += kq * 64 + (g & 1) * 32 + (g >> 1) * 16;
  PIN(oo); PIN(oo8);
  STAMP(5);
  if (w == 0) wait_count(d.cnt, 0u, 4u);
  asm volatile("s_waitcnt lgkmcnt(0)\n\ts_barrier" ::: "memory");
  STAMP(6);
  u32x4 r[3]; u32x2 r8[3];
#pragma unroll
  for (int j = 0; j < 3; ++j) {
    r[j] = __builtin_amdgcn_raw_buffer_load_b128(wrs, c.lane16, (unsigned)WS_PART + ((kq * 512 + tw) * 3 + j) * 1024, 16);
    r8[j] = __builtin_amdgcn_raw_buffer_load_b64(wrs, own8 ? p8 : DROP, ((kq + 1 + j) & 3) * 512, 16);
  }
  SBAR();
  f32x4 e0 = addbf(d.own[0][0] + *(LDSP const f32x4*)(xo + (w ^ 4) * 2048), d.hr[0][0], d.hr[0][1]);
  f32x4 e1 = addbf(d.own[0][1] + *(LDSP const f32x4*)(xo + (w ^ 4) * 2048 + 1024), d.hr[1][0], d.hr[1][1]);
  f32x4 e8 = addbf(d.acc[0][6], d.hr[2][0], d.hr[2][1]);
#pragma unroll
  for (int j = 0; j < 3; ++j) { e0 = addbf(e0, r[j][0], r[j][1]); e1 = addbf(e1, r[j][2], r[j][3]); e8 = addbf(e8, r8[j][0], r8[j][1]); }
  {
    const auto s0 = __builtin_amdgcn_permlane16_swap(cvtpk(e0[0], e0[1]), cvtpk(e1[0], e1[1]), false, false);
    const auto s1 = __builtin_amdgcn_permlane16_swap(cvtpk(e0[2], e0[3]), cvtpk(e1[2], e1[3]), false, false);
    bst16<SC1>(u32x4{s0[0], s1[0], s0[1], s1[1]}, ors, oo, 0);
    __builtin_amdgcn_raw_buffer_store_b64(u32x2{cvtpk(e8[0], e8[1]), cvtpk(e8[2], e8[3])}, ors, oo8, 0, SC1);
  }
  STAMP(7);
  STAMP_OUT(4, 8);
}
extern "C" __global__ void k_end() {}

extern "C" void kernel_launch(void* const* d_in, const int* in_sizes, int n_in, void* d_out, int out_size, void* d_ws, size_t ws_size,
                              hipStream_t stream) {
  const u16* x = (const u16*)d_in[0];
  const u16* wqkv = (const u16*)d_in[1];
  const u16* wo = (const u16*)d_in[2];
  const u16* wg = (const u16*)d_in[4];
  const u16* wu = (const u16*)d_in[5];
  const u16* wd = (const u16*)d_in[6];
  char* ws = (char*)d_ws;
  (void)hipFuncSetAttribute((const void*)k_qkv, hipFuncAttributeMaxDynamicSharedMemorySize, K1_LDS);
  (void)hipFuncSetAttribute((const void*)k_attn, hipFuncAttributeMaxDynamicSharedMemorySize, K2_LDS);
  (void)hipFuncSetAttribute((const void*)k_ffn, hipFuncAttributeMaxDynamicSharedMemorySize, K4_LDS);
  k_qkv<<<256, 768, K1_LDS, stream>>>(x, wqkv, wo, ws, wg, wu, wd, (char*)d_out);
  k_attn<<<256, 768, K2_LDS, stream>>>();
  k_ffn<<<256, 512, K4_LDS, stream>>>();
}
```

```cpp
#define SEL 0
#include <hip/hip_runtime.h>
#include <hip/hip_bf16.h>
#include <stdint.h>
#include <utility>
#include <type_traits>

typedef __bf16 bf16x8 __attribute__((ext_vector_type(8)));
typedef __bf16 bf16x2 __attribute__((ext_vector_type(2)));
typedef short s16x4 __attribute__((ext_vector_type(4)));
typedef float f32x16 __attribute__((ext_vector_type(16)));
typedef float f32x4 __attribute__((ext_vector_type(4)));
typedef float f32x2 __attribute__((ext_vector_type(2)));
typedef unsigned u32x4 __attribute__((ext_vector_type(4)));
typedef unsigned u32x2 __attribute__((ext_vector_type(2)));
typedef unsigned short u16;
#define LDSP __attribute__((address_space(3)))
#define DI __device__ __forceinline__
#define SBAR() __builtin_amdgcn_sched_barrier(0)

constexpr int SEQ = 2048, DM = 576, NH = 32, DH = 18, FF = 4608;
constexpr float LOG2E = 1.4426950408889634f;
constexpr float QSCALE = 0.23570226039551584f * LOG2E;
constexpr float LN_EPS = 1e-5f;

constexpr size_t CHUNK_B = 37888;
constexpr size_t KV_HEAD_B = 4 * CHUNK_B;
constexpr size_t WS_QM = 0;
constexpr size_t WS_QT = WS_QM + (size_t)NH * 65536;
constexpr size_t WS_KV = WS_QT + (size_t)NH * 8192;
constexpr size_t WS_KMAX = WS_KV + (size_t)NH * KV_HEAD_B;
constexpr size_t WS_XMEAN = WS_KMAX + 65536;
constexpr size_t WS_OFR = WS_XMEAN + 8192;
constexpr size_t WS_WO = WS_OFR + (size_t)128 * 18 * 1024;
constexpr size_t WS_WBAR = WS_WO + (size_t)12 * 54 * 1024;
constexpr size_t WS_HC = WS_WBAR + 3072;
constexpr size_t WS_SSQ = WS_HC + (size_t)64 * 36 * 1024;
constexpr size_t WS_W1 = WS_SSQ + (size_t)16 * 8192;
constexpr size_t WS_WD = WS_W1 + (size_t)288 * 36 * 1024;
constexpr size_t WS_ACT = WS_WD + (size_t)4 * 144 * 9 * 1024;
constexpr size_t UNIT_B = (size_t)128 * 18432;
constexpr size_t WS_PART = WS_ACT + (size_t)128 * 144 * 1024;
constexpr size_t WS_PART8 = WS_PART + (size_t)4 * 64 * 8 * 3 * 1024;
constexpr size_t WS_CNT = WS_PART8 + (size_t)64 * 8 * 4 * 512;
constexpr size_t WS_HU = WS_CNT + 40960;
constexpr size_t WS_END = WS_HU + UNIT_B;

static __device__ unsigned long long g_ptr[8];
typedef __attribute__((address_space(4))) const unsigned long long* cptr_t;
DI unsigned cvtpk(float lo, float hi) {
  f32x2 v = {lo, hi};
  bf16x2 b = __builtin_convertvector(v, bf16x2);
  return __builtin_bit_cast(unsigned, b);
}
DI float bflo(unsigned u) { return __uint_as_float(u << 16); }
DI float bfhi(unsigned u) { return __uint_as_float(u & 0xffff0000u); }
DI float dot2(unsigned a, unsigned b, float c) {
  return __builtin_amdgcn_fdot2_f32_bf16(__builtin_bit_cast(bf16x2, a), __builtin_bit_cast(bf16x2, b), c, false);
}
DI f32x16 mfma32(u32x4 a, u32x4 b, f32x16 c) {
  return __builtin_amdgcn_mfma_f32_32x32x16_bf16(__builtin_bit_cast(bf16x8, a), __builtin_bit_cast(bf16x8, b), c, 0, 0, 0);
}
DI unsigned rfl(unsigned x) { return __builtin_amdgcn_readfirstlane(x); }
DI void dma16(const char* sbase, unsigned voff, unsigned lds) {
  asm volatile("s_mov_b32 m0, %2\n\ts_nop 0\n\tglobal_load_lds_dwordx4 %0, %1" ::"v"(voff), "s"(sbase), "s"(lds));
}
DI u32x4 gld16(const char* sbase, unsigned voff) {
  u32x4 r;
  asm volatile("global_load_dwordx4 %0, %1, %2" : "=v"(r) : "v"(voff), "s"(sbase));
  return r;
}
DI unsigned gld4(const char* sbase, unsigned voff) {
  unsigned r;
  asm volatile("global_load_dword %0, %1, %2" : "=v"(r) : "v"(voff), "s"(sbase));
  return r;
}
#define PIN(x) asm volatile("" : "+v"(x))
DI u32x4 ldsr16(LDSP const char* p) { return *(LDSP const u32x4*)p; }
DI float xor32_sum(float v) {
  auto r = __builtin_amdgcn_permlane32_swap(__float_as_uint(v), __float_as_uint(v), false, false);
  return __uint_as_float(r[0]) + __uint_as_float(r[1]);
}
DI float xor32_max(float v) {
  auto r = __builtin_amdgcn_permlane32_swap(__float_as_uint(v), __float_as_uint(v), false, false);
  return fmaxf(__uint_as_float(r[0]), __uint_as_float(r[1]));
}
DI float xor16_sum(float v) {
  auto r = __builtin_amdgcn_permlane16_swap(__float_as_uint(v), __float_as_uint(v), false, false);
  return __uint_as_float(r[0]) + __uint_as_float(r[1]);
}

#define STAMP_DECL
#define STAMP(i)
#define STAMP_OUT(kid, n)

DI int wo_row(int step, int g, int j) {
  if (step < 16) return 18 * (2 * step + (g >> 1)) + 4 * (g & 1) + (j & 3) + 8 * (j >> 2);
  return 18 * (4 * (4 * (step - 16) + g) + (j >> 1)) + 16 + (j & 1);
}

constexpr int SC1 = 16;
constexpr unsigned DROP = 0x80000000u;
#define CODE_PF(dst, first, last, extra, blk, stride)                                                                              \
  do {                                                                                                                     \
    uint64_t pc_; int b_, e_;                                                                                              \
    asm volatile("s_getpc_b64 %0\n.Lpf%=:\n\ts_mov_b32 %1, " #first "-.Lpf%=\n\ts_mov_b32 %2, " #last "-.Lpf%=" : "=s"(pc_), "=s"(b_), "=s"(e_)); \
    const unsigned o_ = (blk) * 8192u + (threadIdx.x & 63) * (stride), n_ = (unsigned)(e_ - b_) + (extra) - 4u;                \
    dst = gld4((const char*)(pc_ + b_), o_ < n_ ? o_ : n_);                                                                \
  } while (0)
#define G_MFMA(d, a, b) asm volatile("v_mfma_f32_16x16x32_bf16 %0, %1, %2, %0" : "+v"(d) : "v"(a), "v"(b))
#define G_MFMA0(d, a, b) asm volatile("v_mfma_f32_16x16x32_bf16 %0, %1, %2, 0" : "=&v"(d) : "v"(a), "v"(b))
template <int AUX> DI void bst16(u32x4 v, __amdgpu_buffer_rsrc_t rs, unsigned voff, unsigned soff) {
  __builtin_amdgcn_raw_buffer_store_b128(v, rs, voff, soff, AUX);
  asm volatile("s_nop 1" ::"v"(v) : "memory");
}
DI __amdgpu_buffer_rsrc_t mkrsrc(const void* p, unsigned bytes) {
  return __builtin_amdgcn_make_buffer_rsrc((void*)p, 0, bytes, 0x00020000);
}

typedef __attribute__((address_space(1))) unsigned gu32;
DI void wait_count(gu32* cnt, unsigned first, unsigned want) {
  if (rfl(first) >= want) return;
  for (unsigned spin = 0; spin < (1u << 16); ++spin) {
    if (rfl(__hip_atomic_load(cnt, __ATOMIC_RELAXED, __HIP_MEMORY_SCOPE_AGENT)) >= want) break;
    __builtin_amdgcn_s_sleep(1);
  }
}
constexpr int NJ_W1 = 72 * 36, NJ_WD = 9 * 144, NJ_WO = 9 * 18, NJ_ALL = NJ_W1 + NJ_WD + NJ_WO;
struct PrepJob { const char* src; unsigned rs, c1, c2, voff, dst[4]; };
DI void prep_setup(PrepJob& j, int J, int lane, const u16* wo, const u16* wg, const u16* wu, const u16* wd) {
  if (J >= NJ_ALL) J = NJ_ALL - 1;
  const int g = (lane >> 2) & 3;
  unsigned lrow = 8 * g;
  j.c1 = 0; j.c2 = 0;
  if (J < NJ_W1) {
    const int sm = J / 72, tg = J - sm * 72;
    j.src = (const char*)((sm & 1) ? wu : wg) + (size_t)(sm >> 1) * (32 * FF * 2) + tg * 128;
    j.rs = FF * 2;
#pragma unroll
    for (int t = 0; t < 4; ++t) j.dst[t] = (unsigned)WS_W1 + (4 * tg + t) * 36864 + sm * 1024;
  } else if (J < NJ_W1 + NJ_WD) {
    const int q = J - NJ_W1, cc = q / 9, tg = q - cc * 9;
    j.src = (const char*)wd + (size_t)cc * (32 * DM * 2) + tg * 128;
    j.rs = DM * 2;
#pragma unroll
    for (int t = 0; t < 4; ++t) { const int nt = 4 * tg + t, nb = nt / 9; j.dst[t] = (unsigned)WS_WD + ((nb * 144 + cc) * 9 + (nt - nb * 9)) * 1024; }
  } else {
    const int q = J - NJ_W1 - NJ_WD, step = q / 9, tg = q - step * 9;
    j.rs = DM * 2;
    if (step < 16) { j.src = (const char*)wo + (size_t)step * (36 * DM * 2) + tg * 128; lrow = 18 * (g >> 1) + 4 * (g & 1); j.c2 = 4; }
    else { j.src = (const char*)wo + (size_t)(288 * (step - 16) + 16) * (DM * 2) + tg * 128; lrow = 72 * g; j.c1 = 16; }
#pragma unroll
    for (int t = 0; t < 4; ++t) j.dst[t] = (unsigned)WS_WO + (step * 36 + 4 * tg + t) * 1024;
  }
#pragma unroll
  for (int t = 0; t < 4; ++t) j.dst[t] = rfl(j.dst[t]);
  j.voff = lrow * j.rs + (lane >> 4) * 32 + (lane & 3) * 8;
}
template <int JJ>
DI void prep_load(const PrepJob& j, u32x2 (&r)[8]) {
  const char* b = j.src + (JJ + j.c1 * (JJ >> 1) + j.c2 * (JJ >> 2)) * j.rs;
  asm volatile("global_load_dwordx2 %0, %1, %2" : "=v"(r[JJ]) : "v"(j.voff), "s"(b));
}
DI void prep_load_all(const PrepJob& j, u32x2 (&r)[8]) {
  prep_load<0>(j, r); prep_load<1>(j, r); prep_load<2>(j, r); prep_load<3>(j, r); prep_load<4>(j, r); prep_load<5>(j, r); prep_load<6>(j, r); prep_load<7>(j, r);
}
DI void prep_store(const PrepJob& j, const u32x2 (&r)[8], __amdgpu_buffer_rsrc_t rs, unsigned so) {
  u32x4 u[4];
#pragma unroll
  for (int i = 0; i < 4; ++i) {
    u[0][i] = __builtin_amdgcn_perm(r[2 * i + 1][0], r[2 * i][0], 0x05040100u);
    u[1][i] = __builtin_amdgcn_perm(r[2 * i + 1][0], r[2 * i][0], 0x07060302u);
    u[2][i] = __builtin_amdgcn_perm(r[2 * i + 1][1], r[2 * i][1], 0x05040100u);
    u[3][i] = __builtin_amdgcn_perm(r[2 * i + 1][1], r[2 * i][1], 0x07060302u);
  }
#pragma unroll
  for (int i = 0; i < 4; ++i) {
    const auto a0 = __builtin_amdgcn_permlane32_swap(u[0][i], u[2][i], false, false);
    const auto a1 = __builtin_amdgcn_permlane32_swap(u[1][i], u[3][i], false, false);
    const auto b0 = __builtin_amdgcn_permlane16_swap(a0[0], a1[0], false, false);
    const auto b1 = __builtin_amdgcn_permlane16_swap(a0[1], a1[1], false, false);
    u[0][i] = b0[0]; u[1][i] = b0[1]; u[2][i] = b1[0]; u[3][i] = b1[1];
  }
#pragma unroll
  for (int t = 0; t < 4; ++t) bst16<SC1>(u[t], rs, so, j.dst[t]);
}
#define PIN8(r) "+v"(r[0]), "+v"(r[1]), "+v"(r[2]), "+v"(r[3]), "+v"(r[4]), "+v"(r[5]), "+v"(r[6]), "+v"(r[7])

constexpr int K1_PF = 4;
constexpr int K1_PITCH = 224, K1_SLOT = 16384 + 64 * K1_PITCH  , K1_NS = 4;
constexpr int K1_SP = 232;
constexpr int K1_STG = K1_NS * K1_SLOT, K1_CS = K1_STG + 128 * K1_SP + 64, K1_ST = K1_CS + 512 + 64, K1_LDS = K1_ST + 2048 + 256;
struct K1C {
  LDSP const char *xa[2], *wl, *wc0, *wc1;
  u32x4 X[3][2], wf[K1_PF + 1], ones;
  f32x4 acc[4][2];
  f32x4 sm[2], sq[2];
};
DI void k1_loader(const char* xb, const char* wb, const u16* wo_, char* ws, int j, int lane, unsigned lds0) {
  __builtin_amdgcn_s_setprio(3);
  unsigned xo[2], wo[4];
  const int g = lane >> 4;
  xo[0] = (lane >> 3) * 1152 + (((lane & 7) ^ g) * 16);
  xo[1] = (8 + (lane >> 3)) * 1152 + (((lane & 7) ^ (4 + g)) * 16);
  xb += j * (32 * 1152);
  const unsigned xd = lds0 + j * 4096, wd = lds0 + 16384 + j * 1024;
#define K1_X(z, so) do { dma16(xb + (z) * 128, xo[0], xd + (so)); dma16(xb + (z) * 128, xo[1], xd + (so) + 1024);                    \
    dma16(xb + (z) * 128 + 16 * 1152, xo[0], xd + (so) + 2048); dma16(xb + (z) * 128 + 16 * 1152, xo[1], xd + (so) + 3072); } while (0)
#define K1_W(z, so) do { _Pragma("unroll") for (int i = 0; i < 3; ++i) dma16(wb + (z) * (64 * 3456), wo[i], wd + (so) + i * 4096);     \
    if (j < 2) dma16(wb + (z) * (64 * 3456), wo[3], wd + (so) + 3 * 4096); } while (0)
  K1_X(0, 0);
  SBAR();
#pragma unroll
  for (int i = 0; i < 4; ++i) {
    const unsigned u = 64 * (j + 4 * i) + lane, r = u / 14;
    wo[i] = ((r & ~12u) | ((r & 4u) << 1) | ((r & 8u) >> 1)) * 3456 + (u - r * 14) * 16;
  }
  K1_W(0, 0);
  K1_X(1, K1_SLOT); K1_W(1, K1_SLOT);
  unsigned so = 2 * K1_SLOT;
#pragma unroll 1
  for (int z = 2; z < 9; ++z) {
    asm volatile("s_waitcnt vmcnt(7)\n\ts_barrier" ::: "memory");
    K1_X(z, so); K1_W(z, so);
    so = so == 3 * K1_SLOT ? 0 : so + K1_SLOT;
  }
  asm volatile("s_waitcnt vmcnt(7)\n\ts_barrier" ::: "memory");
  const int b = blockIdx.x;
  const bool dojob = j == 1 && b < NJ_WO, dorow = j >= 2 || (j == 0 && b < 64);
  const int br = j >= 2 ? 2 * b + j - 2 : 512 + (b & 63);
  PrepJob jb; u32x2 r[8]; unsigned wr[5];
  if (dojob) prep_setup(jb, NJ_W1 + NJ_WD + b, lane, wo_, wo_, wo_, wo_);
  const char* rp = (const char*)wo_ + (size_t)wo_row(br >> 5, (br >> 3) & 3, br & 7) * (DM * 2);
  SBAR();
  asm volatile("s_waitcnt vmcnt(0)\n\ts_barrier" ::: "memory");
  if (dojob) prep_load_all(jb, r);
  if (dorow) {
#pragma unroll
    for (int i = 0; i < 5; ++i) wr[i] = gld4(rp, (i < 4 ? lane + 64 * i : 256 + (lane & 31)) * 4);
  }
  asm volatile("s_barrier");
  asm volatile("s_barrier");
  const __amdgpu_buffer_rsrc_t wrs = mkrsrc(ws, (unsigned)WS_END);
  asm volatile("s_waitcnt vmcnt(0)" : PIN8(r), "+v"(wr[0]), "+v"(wr[1]), "+v"(wr[2]), "+v"(wr[3]), "+v"(wr[4])::"memory");
  if (dojob) {
#pragma unroll
    for (int t = 0; t < 4; ++t) jb.dst[t] = rfl(jb.dst[t]);
    prep_store(jb, r, wrs, ((lane >> 2) & 3) * 256 + ((lane & 3) * 4 + (lane >> 4)) * 16);
  }
  if (dorow) {
    float a = 0.f;
#pragma unroll
    for (int i = 0; i < 4; ++i) a = dot2(wr[i], 0x3f803f80u, a);
    a = dot2(lane >= 32 ? 0u : wr[4], 0x3f803f80u, a);
    a = xor16_sum(xor32_sum(a));
    a += __uint_as_float(__builtin_amdgcn_update_dpp(0u, __float_as_uint(a), 0x128, 0xf, 0xf, false));
    a += __uint_as_float(__builtin_amdgcn_update_dpp(0u, __float_as_uint(a), 0x124, 0xf, 0xf, false));
    a += __uint_as_float(__builtin_amdgcn_update_dpp(0u, __float_as_uint(a), 0x122, 0xf, 0xf, false));
    a += __uint_as_float(__builtin_amdgcn_update_dpp(0u, __float_as_uint(a), 0x121, 0xf, 0xf, false));
    a *= (1.0f / DM);
    const unsigned hi = cvtpk(a, 0.f) & 0xffffu;
    const unsigned lo = cvtpk(a - bflo(hi), 0.f) & 0xffffu;
    __builtin_amdgcn_raw_buffer_store_b16((u16)(lane ? lo : hi), wrs, lane < 2 ? (unsigned)WS_WBAR + (lane * DM + br) * 2 : DROP, 0, 16);
  }
#undef K1_X
#undef K1_W
}
DI u32x4 tr_frag(LDSP const char* p, int d) {
  s16x4 lo = __builtin_amdgcn_ds_read_tr16_b64_v4i16((LDSP s16x4*)p);
  s16x4 hi = __builtin_amdgcn_ds_read_tr16_b64_v4i16((LDSP s16x4*)(p + d));
  return __builtin_bit_cast(u32x4, __builtin_shufflevector(lo, hi, 0, 1, 2, 3, 4, 5, 6, 7));
}
template <int NHG, int F> DI void k1_read(K1C& c) {
  constexpr int NF = 4 + NHG, S = F / NF, j = F % NF, Z = S >> 1, so = (Z % K1_NS) * K1_SLOT;
  if constexpr (j == 0) {
    c.X[S % 3][0] = ldsr16(c.xa[S & 1] + so);
    c.X[S % 3][1] = ldsr16(c.xa[S & 1] + so + 2048);
  }
  constexpr int wo = so + (S & 1) * 7168;
  if constexpr (NHG == 0 || j < 3) c.wf[F % (K1_PF + 1)] = tr_frag(c.wl + wo + j * 32, 1792);
  else c.wf[F % (K1_PF + 1)] = tr_frag((j == 3 ? c.wc0 : c.wc1) + wo, 1792);
}
template <int NHG, int F> DI void k1_frag(K1C& c) {
  constexpr int NF = 4 + NHG, S = F / NF, j = F % NF, N = F + K1_PF;
  if constexpr (S == 0) {
    if constexpr (NHG == 0 || j < 3) { G_MFMA0(c.acc[j][0], c.wf[F % (K1_PF + 1)], c.X[0][0]); G_MFMA0(c.acc[j][1], c.wf[F % (K1_PF + 1)], c.X[0][1]); }
    else G_MFMA0(c.acc[3][j - 3], c.wf[F % (K1_PF + 1)], c.ones);
  } else {
    if constexpr (NHG == 0 || j < 3) { G_MFMA(c.acc[j][0], c.wf[F % (K1_PF + 1)], c.X[S % 3][0]); G_MFMA(c.acc[j][1], c.wf[F % (K1_PF + 1)], c.X[S % 3][1]); }
    else G_MFMA(c.acc[3][j - 3], c.wf[F % (K1_PF + 1)], c.ones);
  }
  if constexpr ((S & 1) == NHG && j < 4) {
    constexpr int t = j >> 1;
    if constexpr (j & 1) { if constexpr (S < 2) G_MFMA0(c.sq[t], c.X[S % 3][t], c.X[S % 3][t]); else G_MFMA(c.sq[t], c.X[S % 3][t], c.X[S % 3][t]); }
    else { if constexpr (S < 2) G_MFMA0(c.sm[t], c.ones, c.X[S % 3][t]); else G_MFMA(c.sm[t], c.ones, c.X[S % 3][t]); }
  }
  if constexpr (N < 18 * NF) {
    if constexpr (N % (2 * NF) == 0) asm volatile("s_barrier" ::: "memory");
    k1_read<NHG, N>(c);
  }
  SBAR();
}
template <int NHG, int... Fs> DI void k1_run(K1C& c, std::integer_sequence<int, Fs...>) {
  k1_read<NHG, 0>(c); k1_read<NHG, 1>(c); k1_read<NHG, 2>(c); k1_read<NHG, 3>(c);
  SBAR();
  (k1_frag<NHG, Fs>(c), ...);
  asm volatile("s_nop 7" : "+v"(c.acc[0][0]), "+v"(c.acc[0][1]), "+v"(c.acc[1][0]), "+v"(c.acc[1][1]), "+v"(c.acc[2][0]), "+v"(c.acc[2][1]),
               "+v"(c.acc[3][0]), "+v"(c.acc[3][1]), "+v"(c.sm[0]), "+v"(c.sm[1]), "+v"(c.sq[0]), "+v"(c.sq[1]));
  SBAR();
}
extern "C" __global__ __launch_bounds__(768, 3) void k_qkv(const u16* x, const u16* wqkv, const u16* wo, char* ws, const u16* wg, const u16* wu, const u16* wd, char* out) {
  extern __shared__ __attribute__((aligned(16))) char smem[];
  const int tid = threadIdx.x, lane = tid & 63, w = rfl(tid >> 6);
  const int xcd = blockIdx.x & 7, bi = blockIdx.x >> 3;
  unsigned pf;
  CODE_PF(pf, k_qkv, k_end, 0, bi, (w == 8 && bi < 9) ? 128u : 0u);
  SBAR();
  asm volatile("" : "+s"(x), "+s"(wqkv), "+s"(wo), "+s"(ws));
  LDSP char* lds = (LDSP char*)smem;
  const unsigned lds0 = (unsigned)(uintptr_t)lds;
  const int rb = 4 * (xcd >> 1) + (bi & 3), cb = 8 * (xcd & 1) + (bi >> 2);
  const int c0 = 108 * cb - 4 * (cb & 1);
  if (__builtin_expect(w >= 8, 1)) {
    k1_loader((const char*)x + rb * (128 * 1152), (const char*)wqkv + c0 * 2, wo, ws, w - 8, lane, lds0);
    asm volatile("" ::"v"(pf));
    if (blockIdx.x == 0 && tid == 512) {
      g_ptr[0] = (unsigned long long)x; g_ptr[1] = (unsigned long long)wg; g_ptr[2] = (unsigned long long)wu; g_ptr[3] = (unsigned long long)wd;
      g_ptr[4] = (unsigned long long)ws; g_ptr[5] = (unsigned long long)out;
    }
    return;
  }
  const int m = lane & 15, g = lane >> 4, mg = w & 3, nh = w >> 2;
  STAMP_DECL; STAMP(0);
  K1C c;
  c.ones = u32x4{0x3f803f80u, 0x3f803f80u, 0x3f803f80u, 0x3f803f80u};
  PIN(c.ones);
  c.xa[0] = lds + (32 * mg + m) * 128 + ((g ^ (m >> 1)) * 16);
  c.xa[1] = lds + (32 * mg + m) * 128 + (((4 + g) ^ (m >> 1)) * 16);
  LDSP const char* wbase = lds + 16384 + (16 * (g >> 1) + 4 * (g & 1) + (m >> 2)) * K1_PITCH + (m & 3) * 8;
  c.wl = wbase + nh * 128;
  c.wc0 = wbase + mg * 32;
  c.wc1 = wbase + (mg < 3 ? mg + 4 : 6) * 32;
  const int off = 4 * (cb & 1), T0 = 128 * rb;
  LDSP char* stg = lds + K1_STG;
  LDSP char* a_st = lds + K1_ST + (32 * mg + m) * 8;                 PIN(a_st);
  LDSP char* a_sd = g == (m >> 2) ? a_st + nh * 1024 : lds + K1_ST + 2048;   PIN(a_sd);
  LDSP char* a_cs = lds + K1_CS + (64 * nh + 4 * g) * 4;             PIN(a_cs);
  LDSP char* a_sw = stg + (32 * mg + m) * K1_SP + (64 * nh + 4 * g) * 2;   PIN(a_sw);
  LDSP char* a_tr = stg + (32 * mg + 4 * (g & 1) + (m >> 2)) * K1_SP + (64 * nh + 16 * (g >> 1) + 4 * (m & 3)) * 2;   PIN(a_tr);
  float qs[4];
#pragma unroll
  for (int j = 0; j < 4; ++j) { qs[j] = (c0 + 64 * nh + 16 * j + 4 * g < DM) ? QSCALE : 1.0f; PIN(qs[j]); }
  const __amdgpu_buffer_rsrc_t wrs = mkrsrc(ws, (unsigned)WS_END);
  const unsigned kvo = (unsigned)WS_KV + (T0 >> 9) * (unsigned)CHUNK_B;
  unsigned vo[2] = {0, 0}, vone = 0, vd[4] = {DROP, DROP, DROP, DROP};
  if (cb >= 10) {
#pragma unroll
  for (int tt = 0; tt < 2; ++tt) {
    const int cc = 64 * nh + 32 * tt + 16 * (g >> 1) + m - off, hd = (cc + 18) / 18 - 1;
    vo[tt] = (cc >= 0 && cc < 108) ? hd * 4864 + 2 * mg * 608 + (g & 1) * 304 + (cc - 18 * hd) * 16 : 29184;
  }
  vone = tid < 96 ? (tid >> 4) * 4864 + (tid & 15) * 304 + 288 : 29184;
#pragma unroll
  for (int i = 0; i < 4; ++i) {
    const unsigned u = i * 512 + tid, hd = u / 304;
    vd[i] = (u < 1824 && 6 * cb + hd >= 64) ? kvo + 18432 + ((T0 >> 4) & 31) * 608 + (6 * cb + hd - 64) * (unsigned)KV_HEAD_B + (u - hd * 304) * 16 : DROP;
  }
  }
  PIN(vone); PIN(vo[0]); PIN(vo[1]); PIN(vd[0]); PIN(vd[1]); PIN(vd[2]); PIN(vd[3]);
  const int tok = tid & 127, T = T0 + tok;
  LDSP const char* a_g = stg + tok * K1_SP + off * 2;   PIN(a_g);
  unsigned um[2], ut[2];
  const unsigned tl = ((T >> 5) & 15) * 1024 + (T & 31) * 16;
#pragma unroll
  for (int it = 0; it < 2; ++it) {
    const int hd = (w >> 1) + 4 * it, G = 6 * cb + hd, head = G & 31;
    if (hd >= 6 || G >= 64) { um[it] = DROP; ut[it] = DROP; }
    else if (G < 32) { um[it] = (unsigned)WS_QM + head * 65536 + (T >> 9) * 16384 + tl; ut[it] = (unsigned)WS_QT + head * 8192 + T * 4; }
    else { um[it] = kvo + head * (unsigned)KV_HEAD_B + tl; ut[it] = kvo + head * (unsigned)KV_HEAD_B + 16384 + ((T >> 6) & 7) * 256 + (T & 31) * 8 + ((T >> 5) & 1) * 4; }
    PIN(um[it]); PIN(ut[it]);
  }
  STAMP(1);
  asm volatile("s_waitcnt vmcnt(0)" : "+v"(pf));
  if (blockIdx.x < 5 && w == 7) bst16<SC1>(u32x4{0, 0, 0, 0}, wrs, (unsigned)WS_CNT + (blockIdx.x * 64 + lane) * 128, 0);
  asm volatile("s_barrier" ::: "memory");
  STAMP(2);
  if (nh) k1_run<1>(c, std::make_integer_sequence<int, 90>{});
  else k1_run<0>(c, std::make_integer_sequence<int, 72>{});
  STAMP(3);
#pragma unroll
  for (int t = 0; t < 2; ++t) {
    const float lo = (m & 1) ? c.sq[t][1] : c.sq[t][0], hi = (m & 1) ? c.sq[t][3] : c.sq[t][2];
    *(LDSP f32x2*)(a_sd + t * 128) = f32x2{c.sm[t][0], (m & 2) ? hi : lo};
  }
  if (nh) {
    *(LDSP f32x4*)(lds + K1_CS + (16 * mg + 4 * g) * 4) = c.acc[3][0];
    *(LDSP f32x4*)(lds + K1_CS + (16 * (mg + 4) + 4 * g) * 4) = c.acc[3][1];
  }
  asm volatile("s_waitcnt lgkmcnt(0)\n\ts_barrier" ::: "memory");
  {
    f32x2 p[2][2]; f32x4 cc[4];
#pragma unroll
    for (int t = 0; t < 2; ++t) { p[t][0] = *(LDSP const f32x2*)(a_st + t * 128); p[t][1] = *(LDSP const f32x2*)(a_st + 1024 + t * 128); }
#pragma unroll
    for (int j = 0; j < 4; ++j) cc[j] = *(LDSP const f32x4*)(a_cs + j * 64);
    SBAR();
#pragma unroll
    for (int t = 0; t < 2; ++t) {
      const float mean = (p[t][0][0] + p[t][1][0]) * (1.0f / DM);
      const float var = fmaxf((p[t][0][1] + p[t][1][1]) * (1.0f / DM) - mean * mean, 0.f);
      const float rstd = __builtin_amdgcn_rsqf(var + LN_EPS);
      if (cb == 0 && nh == 0) __builtin_amdgcn_raw_buffer_store_b32(__float_as_uint(mean), wrs, (unsigned)WS_XMEAN + (T0 + 32 * mg + 16 * t + m) * 4, 0, SC1);
#pragma unroll
      for (int j = 0; j < 4; ++j) {
        if (j < 3 || nh == 0) {
          const f32x4 a = (c.acc[j][t] - mean * cc[j]) * (rstd * qs[j]);
          *(LDSP u32x2*)(a_sw + t * (16 * K1_SP) + j * 32) = u32x2{cvtpk(a[0], a[1]), cvtpk(a[2], a[3])};
        }
      }
    }
  }
  if (cb >= 10) {
    asm volatile("s_waitcnt lgkmcnt(0)" ::: "memory");
    u32x4 v[2][2];
#pragma unroll
    for (int tt = 0; tt < 2; ++tt)
#pragma unroll
      for (int t = 0; t < 2; ++t) v[tt][t] = tr_frag(a_tr + t * (16 * K1_SP) + tt * 64, 8 * K1_SP);
    *(LDSP u32x4*)(lds + vone) = c.ones;
#pragma unroll
    for (int tt = 0; tt < 2; ++tt)
#pragma unroll
      for (int t = 0; t < 2; ++t) *(LDSP u32x4*)(lds + vo[tt] + t * 608) = v[tt][t];
  }
  STAMP(4);
  asm volatile("s_waitcnt lgkmcnt(0)\n\ts_barrier" ::: "memory");
  if (cb >= 10) {
    u32x4 v[4];
#pragma unroll
    for (int i = 0; i < 4; ++i) v[i] = ldsr16(lds + i * 8192 + tid * 16);
#pragma unroll
    for (int i = 0; i < 4; ++i) if (__builtin_amdgcn_ballot_w64(vd[i] != DROP)) bst16<SC1>(v[i], wrs, vd[i], 0);
  }
  if (cb <= 10) {
    unsigned d[2][9];
#pragma unroll
    for (int it = 0; it < 2; ++it) {
      const int hd = (w >> 1) + 4 * it;
      LDSP const unsigned* sp = (LDSP const unsigned*)(a_g + 36 * (hd < 6 ? hd : 0));
#pragma unroll
      for (int i = 0; i < 9; ++i) d[it][i] = sp[i];
    }
    SBAR();
#pragma unroll
    for (int it = 0; it < 2; ++it) {
      if ((w >> 1) + 4 * it >= 6 || 6 * cb + (w >> 1) + 4 * it >= 64) continue;
      bst16<SC1>(u32x4{d[it][0], d[it][1], d[it][2], d[it][3]}, wrs, um[it], 0);
      bst16<SC1>(u32x4{d[it][4], d[it][5], d[it][6], d[it][7]}, wrs, um[it] + 512, 0);
      __builtin_amdgcn_raw_buffer_store_b32(d[it][8], wrs, ut[it], 0, SC1);
    }
#pragma unroll
    for (int it = 0; it < 2; ++it) {
      const int hd = (w >> 1) + 4 * it, G = 6 * cb + hd;
      if (hd < 6 && G >= 32 && G < 64) {
        float n2 = 0.f;
#pragma unroll
        for (int i = 0; i < 9; ++i) n2 = dot2(d[it][i], d[it][i], n2);
        unsigned nb = __float_as_uint(n2);
        { auto r = __builtin_amdgcn_permlane32_swap(nb, nb, false, false); nb = max(r[0], r[1]); }
        { auto r = __builtin_amdgcn_permlane16_swap(nb, nb, false, false); nb = max(r[0], r[1]); }
        __builtin_amdgcn_raw_buffer_store_b32(nb, wrs, (unsigned)WS_KMAX + (((2 * rb + (w & 1)) * 32 + (G & 31)) * 16 + m) * 4, 0, SC1);
      }
    }
  }
  STAMP(5); STAMP_OUT(1, 6);
}

constexpr int K2_LDS = (int)KV_HEAD_B, K3_WBAR = 90 * 1024, K3_XC = 96 * 1024, K3_REP = 16, K3_PRE = 4;
struct AttnC {
  LDSP const char *kmb, *ktb, *vb;
  unsigned kma[3], kta[3], va[3];
  u32x4 qm, qt, kf, v0, v1, p1, ktl;
  f32x16 negm, o, sa, sb;
};
constexpr int k_off(int T) { return (T >> 4) * (int)CHUNK_B + (T & 15) * 1024; }
constexpr int kt_off(int T) { return (T >> 4) * (int)CHUNK_B + ((T >> 1) & 7) * 256; }
constexpr int v_off(int T) { return (T >> 4) * (int)CHUNK_B + (T & 15) * 1216; }
#define AT_E(r) "v_exp_f32 v" #r ", v" #r "\n\t"
#define AT_C(d, a, b) "v_cvt_pk_bf16_f32 v" #d ", v" #a ", v" #b "\n\t"
#define AT_TAIL(sn, kt, qt) "v_mfma_f32_32x32x8_bf16 " sn ", " kt ", " qt ", v[18:33]\n\t"
#define AT_MAIN(sn) "v_mfma_f32_32x32x16_bf16 " sn ", v[66:69], v[98:101], " sn "\n\t"
#define AT_PV1 "v_mfma_f32_32x32x16_bf16 v[2:17], v[84:87], v[70:73], v[2:17]\n\t"
#define AT_PV0 "v_mfma_f32_32x32x16_bf16 v[2:17], v[88:91], v[74:77], v[2:17]\n\t"
#define AT_R1 "ds_read_b128 v[84:87], %[va] offset:%[vo1]\n\t"
#define AT_RK "ds_read_b128 v[66:69], %[ka] offset:%[ko]\n\t"
#define AT_R0 "ds_read_b128 v[88:91], %[vb] offset:%[vo0]\n\t"
#define AT_RT(kt) "ds_read_b64 " kt ", %[kt] offset:%[kto]\n\t"
#define AT_BODY(TL, P1, MN, RD, s0, s1, s2, s3, s4, s5, s6, s7, s8, s9, s10, s11, s12, s13, s14, s15)                                   \
  TL AT_E(s0) AT_E(s1) AT_E(s2) AT_E(s3) "s_waitcnt lgkmcnt(0)\n\t" P1 AT_C(74, s0, s1) AT_E(s4) AT_C(75, s2, s3) AT_E(s5) AT_E(s6) AT_E(s7) \
  MN AT_C(76, s4, s5) AT_E(s8) AT_C(77, s6, s7) AT_E(s9) AT_E(s10) AT_E(s11) AT_PV0 RD AT_C(70, s8, s9) AT_E(s12) AT_C(71, s10, s11)          \
  AT_E(s13) AT_E(s14) AT_E(s15) AT_C(72, s12, s13) AT_C(73, s14, s15)
#define AT_EVEN(TL, P1, MN, RD) AT_BODY(TL, P1, MN, RD, 34, 35, 36, 37, 38, 39, 40, 41, 42, 43, 44, 45, 46, 47, 48, 49)
#define AT_ODD(TL, P1, MN, RD) AT_BODY(TL, P1, MN, RD, 50, 51, 52, 53, 54, 55, 56, 57, 58, 59, 60, 61, 62, 63, 64, 65)
#define AT_STMT(TXT)                                                                                                              \
  asm volatile(TXT : "+{v[2:17]}"(c.o), "+{v[34:49]}"(c.sa), "+{v[50:65]}"(c.sb), "+{v[66:69]}"(c.kf), "+{v[70:73]}"(c.p1),        \
               "+{v[84:87]}"(c.v1), "+{v[88:91]}"(c.v0), "+{v[92:95]}"(c.ktl)                                                      \
               : "{v[18:33]}"(c.negm), "{v[98:101]}"(c.qm), "{v[102:105]}"(c.qt), [ka] "v"(c.kma[KO >> 16]), [kt] "v"(c.kta[KT >> 16]), \
                 [va] "v"(c.va[V1 >> 16]), [vb] "v"(c.va[V0 >> 16]), [ko] "i"(KO & 0xffff), [kto] "i"(KT & 0xffff), [vo1] "i"(V1 & 0xffff),  \
                 [vo0] "i"(V0 & 0xffff)                                                                                            \
               : "v74", "v75", "v76", "v77")
template <int T> DI void attn_tile(AttnC& c) {
  constexpr int KO = k_off(T < 62 ? T + 2 : 0), KT = kt_off(T < 61 ? T + 3 : 0), V1 = v_off(T) + 608, V0 = v_off(T < 63 ? T + 1 : 0);
  if constexpr (T == 0) AT_STMT(AT_EVEN(AT_TAIL("v[50:65]", "v[92:93]", "v[104:105]"), "", AT_MAIN("v[50:65]"), AT_R1 AT_RK AT_R0));
  else if constexpr (T == 61) AT_STMT(AT_ODD(AT_TAIL("v[34:49]", "v[94:95]", "v[102:103]"), AT_PV1, AT_MAIN("v[34:49]"), AT_R1 AT_RK AT_R0));
  else if constexpr (T == 62) AT_STMT(AT_EVEN(AT_TAIL("v[50:65]", "v[94:95]", "v[104:105]"), AT_PV1, AT_MAIN("v[50:65]"), AT_R1 AT_R0));
  else if constexpr (T == 63) AT_STMT(AT_ODD("", AT_PV1, "", AT_R1));
  else if constexpr ((T & 3) == 0) AT_STMT(AT_EVEN(AT_TAIL("v[50:65]", "v[92:93]", "v[104:105]"), AT_PV1, AT_MAIN("v[50:65]"), AT_R1 AT_RK AT_R0));
  else if constexpr ((T & 3) == 1) AT_STMT(AT_ODD(AT_TAIL("v[34:49]", "v[94:95]", "v[102:103]"), AT_PV1, AT_MAIN("v[34:49]"), AT_R1 AT_RK AT_R0 AT_RT("v[92:93]")));
  else if constexpr ((T & 3) == 2) AT_STMT(AT_EVEN(AT_TAIL("v[50:65]", "v[94:95]", "v[104:105]"), AT_PV1, AT_MAIN("v[50:65]"), AT_R1 AT_RK AT_R0));
  else AT_STMT(AT_ODD(AT_TAIL("v[34:49]", "v[92:93]", "v[102:103]"), AT_PV1, AT_MAIN("v[34:49]"), AT_R1 AT_RK AT_R0 AT_RT("v[94:95]")));
}
DI void attn_group_slow(LDSP const char* km, LDSP const char* kt, LDSP const char* vp, const u32x4& qm, unsigned qtl, const f32x16& negm, f32x16& o) {
#pragma unroll
  for (int t = 0; t < 4; ++t) {
    const u32x2 k2 = *(LDSP const u32x2*)(kt + (t >> 1) * 256);
    const u32x4 ktl = {k2[0], k2[1], 0, 0};
    const u32x4 kf = ldsr16(km + t * 1024);
    const u32x4 v0 = ldsr16(vp + t * 1216), v1 = ldsr16(vp + t * 1216 + 608);
    u32x4 qq = {0, 0, 0, 0}; qq[t & 1] = qtl;
    f32x16 s = mfma32(ktl, qq, negm);
    s = mfma32(kf, qm, s);
    u32x4 p0, p1;
#pragma unroll
    for (int i = 0; i < 4; ++i) {
      p0[i] = cvtpk(__builtin_amdgcn_exp2f(s[2 * i]), __builtin_amdgcn_exp2f(s[2 * i + 1]));
      p1[i] = cvtpk(__builtin_amdgcn_exp2f(s[8 + 2 * i]), __builtin_amdgcn_exp2f(s[8 + 2 * i + 1]));
    }
    o = mfma32(v0, p0, o);
    o = mfma32(v1, p1, o);
  }
}
template <int G> DI void attn_group(AttnC& c) {
  __builtin_amdgcn_s_setprio(G < 5 ? 3 - (G & 3) : (G < 8 ? 3 : (G < 11 ? 2 : (G < 14 ? 1 : 0))));
  attn_tile<4 * G + 0>(c); attn_tile<4 * G + 1>(c); attn_tile<4 * G + 2>(c); attn_tile<4 * G + 3>(c);
}
DI void attn_helper(const u16* wg, const u16* wu, const u16* wd, char* ws, int head, int j, int lane, unsigned lds0) {
  __builtin_amdgcn_s_setprio(3);
  const char* kv = ws + WS_KV + (size_t)head * KV_HEAD_B;
  const unsigned lane16 = lane * 16;
#define KV_DMA(o) dma16(kv + (o), lane16, lds0 + (o))
#pragma unroll
  for (int i = 0; i < 4; ++i) KV_DMA(((i >> 1) * 16 + (i & 1) * 4 + j) * 1024);
#pragma unroll
  for (int i = 0; i < 6; ++i) { const int p = i < 2 ? 8 + 4 * i + j : min(16 + 4 * i + j, 36); KV_DMA(p * 1024); }
  const int hw = blockIdx.x * 4 + j;
  PrepJob jb[4];
#pragma unroll
  for (int i = 0; i < 4; ++i) { prep_setup(jb[i], min(4 * hw + i, NJ_W1 + NJ_WD - 1), lane, wd, wg, wu, wd); PIN(jb[i].voff); }
  SBAR();
  asm volatile("s_waitcnt vmcnt(6)\n\ts_barrier" ::: "memory");
#pragma unroll
  for (int i = 0; i < 10; ++i) KV_DMA((unsigned)CHUNK_B + min(4 * i + j, 36) * 1024);
  asm volatile("s_waitcnt vmcnt(10)\n\ts_barrier" ::: "memory");
#pragma unroll
  for (int i = 0; i < 19; ++i) KV_DMA(2 * (unsigned)CHUNK_B + min(4 * i + j, 73) * 1024);
#undef KV_DMA
  u32x2 r[4][8];
  unsigned pf;
  prep_load_all(jb[0], r[0]);
  asm volatile("s_waitcnt vmcnt(27)\n\ts_barrier" ::: "memory");
  prep_load_all(jb[1], r[1]); prep_load_all(jb[2], r[2]);
  asm volatile("s_waitcnt vmcnt(24)\n\ts_barrier" ::: "memory");
  const __amdgpu_buffer_rsrc_t wrs = mkrsrc(ws, (unsigned)WS_END);
  const unsigned pso = ((lane >> 2) & 3) * 256 + ((lane & 3) * 4 + (lane >> 4)) * 16;
  prep_load_all(jb[3], r[3]);
  CODE_PF(pf, k_attn, k_end, 0, 2 * j + 1 + ((blockIdx.x >> 3) & 1), (blockIdx.x >> 3) < 2 ? 128u : 0u);
  asm volatile("s_waitcnt vmcnt(25)" : PIN8(r[0])::"memory");
  prep_store(jb[0], r[0], wrs, pso);
  asm volatile("s_waitcnt vmcnt(21)" : PIN8(r[1])::"memory");
  prep_store(jb[1], r[1], wrs, pso);
  asm volatile("s_waitcnt vmcnt(17)" : PIN8(r[2])::"memory");
  prep_store(jb[2], r[2], wrs, pso);
  asm volatile("s_waitcnt vmcnt(12)" : PIN8(r[3]), "+v"(pf)::"memory");
  prep_store(jb[3], r[3], wrs, pso);
}
extern "C" __global__ __launch_bounds__(768, 3) void k_attn() {
  extern __shared__ __attribute__((aligned(16))) char smem[];
  cptr_t gp = (cptr_t)(uintptr_t)g_ptr;
  const u16 *x = (const u16*)gp[0], *wg = (const u16*)gp[1], *wu = (const u16*)gp[2], *wd = (const u16*)gp[3]; char* ws = (char*)gp[4];
  asm volatile("" : "+s"(x), "+s"(wg), "+s"(wu), "+s"(wd), "+s"(ws));
  LDSP char* lds = (LDSP char*)smem;
  const int tid = threadIdx.x, lane = tid & 63, w = rfl(tid >> 6);
  const int xcd = blockIdx.x & 7, bi = blockIdx.x >> 3;
  const int head = 4 * xcd + (bi & 3), qb = bi >> 2;
  if (__builtin_expect(w >= 8, 0)) { attn_helper(wg, wu, wd, ws, head, w - 8, lane, (unsigned)(uintptr_t)lds); return; }
  const int r = lane & 31, h = lane >> 5;
  const int qt32 = 8 * qb + w;
  STAMP_DECL; STAMP(0);
  unsigned long long C0, R0, C1, R1; asm volatile("s_memtime %0\n\ts_memrealtime %1\n\ts_waitcnt lgkmcnt(0)" : "=s"(C0), "=s"(R0)::"memory");
  AttnC c;
  c.qm = gld16(ws + WS_QM, head * 65536 + qt32 * 1024 + lane * 16);
  unsigned qtl = gld4(ws + WS_QT, head * 8192 + (32 * qt32 + r) * 4);
  u32x4 km0 = gld16(ws + WS_KMAX, ((lane >> 1) * 32 + head) * 64 + (lane & 1) * 32);
  u32x4 km1 = gld16(ws + WS_KMAX, ((lane >> 1) * 32 + head) * 64 + (lane & 1) * 32 + 16);
  const int vrow = r < 19 ? r : ((r >= 20 && r < 28) ? 0 : 18);
  c.kmb = lds + lane * 16;
  c.ktb = lds + 16384 + r * 8;
  c.vb = lds + 18432 + h * 304 + vrow * 16;
#pragma unroll
  for (int i = 0; i < 3; ++i) {
    c.kma[i] = (unsigned)(uintptr_t)c.kmb + i * 65536; c.kta[i] = (unsigned)(uintptr_t)c.ktb + i * 65536; c.va[i] = (unsigned)(uintptr_t)c.vb + i * 65536;
    PIN(c.kma[i]); PIN(c.kta[i]); PIN(c.va[i]);
  }
#pragma unroll
  for (int i = 0; i < 16; ++i) c.o[i] = 0.f;
  PIN(c.o);
  const __amdgpu_buffer_rsrc_t wrs = mkrsrc(ws, (unsigned)WS_END);
  SBAR();
  asm volatile("s_waitcnt vmcnt(0)" : "+v"(c.qm), "+v"(qtl), "+v"(km0), "+v"(km1)::"memory");
  if (h) qtl = 0;
  float q2 = dot2(qtl, qtl, 0.f);
#pragma unroll
  for (int i = 0; i < 4; ++i) q2 = dot2(c.qm[i], c.qm[i], q2);
  unsigned k2 = max(max(max(km0[0], km0[1]), max(km0[2], km0[3])), max(max(km1[0], km1[1]), max(km1[2], km1[3])));
  { auto rr = __builtin_amdgcn_permlane32_swap(k2, k2, false, false); k2 = max(rr[0], rr[1]); }
  { auto rr = __builtin_amdgcn_permlane16_swap(k2, k2, false, false); k2 = max(rr[0], rr[1]); }
  k2 = max(k2, (unsigned)__builtin_amdgcn_update_dpp(0u, k2, 0x128, 0xf, 0xf, true));
  k2 = max(k2, (unsigned)__builtin_amdgcn_update_dpp(0u, k2, 0x124, 0xf, 0xf, true));
  k2 = max(k2, (unsigned)__builtin_amdgcn_update_dpp(0u, k2, 0x122, 0xf, 0xf, true));
  k2 = max(k2, (unsigned)__builtin_amdgcn_update_dpp(0u, k2, 0x121, 0xf, 0xf, true));
  q2 = xor32_sum(q2);
  const float mshift = __builtin_amdgcn_sqrtf(q2 * __uint_as_float(k2)) * 1.001f;
  c.qt = u32x4{qtl, 0, 0, qtl}; PIN(c.qt);
#pragma unroll
  for (int i = 0; i < 16; ++i) c.negm[i] = -mshift;
  PIN(c.negm);
  STAMP(1);
  asm volatile("s_barrier" ::: "memory");
  STAMP(2);
  asm volatile("ds_read_b64 v[92:93], %[kt]\n\tds_read_b128 v[70:73], %[ka]\n\tds_read_b128 v[66:69], %[ka] offset:1024\n\t"
               "ds_read_b128 v[88:91], %[va]\n\tds_read_b64 v[94:95], %[kt] offset:256\n\ts_waitcnt lgkmcnt(3)\n\t"
               "v_mfma_f32_32x32x8_bf16 v[34:49], v[92:93], v[102:103], v[18:33]\n\t"
               "v_mfma_f32_32x32x16_bf16 v[34:49], v[70:73], v[98:101], v[34:49]\n\ts_nop 11"
               : "={v[34:49]}"(c.sa), "={v[50:65]}"(c.sb), "={v[66:69]}"(c.kf), "={v[70:73]}"(c.p1), "={v[84:87]}"(c.v1), "={v[88:91]}"(c.v0), "={v[92:95]}"(c.ktl)
               : "{v[18:33]}"(c.negm), "{v[98:101]}"(c.qm), "{v[102:105]}"(c.qt), [ka] "v"(c.kma[0]), [kt] "v"(c.kta[0]), [va] "v"(c.va[0]));
  attn_group<0>(c);
  asm volatile("s_barrier" ::: "memory");
  attn_group<1>(c); attn_group<2>(c);
  asm volatile("s_barrier" ::: "memory");
  attn_group<3>(c); attn_group<4>(c);
  asm volatile("s_barrier" ::: "memory");
  STAMP(3);
  attn_group<5>(c); attn_group<6>(c); attn_group<7>(c); attn_group<8>(c); attn_group<9>(c); attn_group<10>(c);
  attn_group<11>(c); attn_group<12>(c); attn_group<13>(c); attn_group<14>(c); attn_group<15>(c);
  asm volatile("s_waitcnt lgkmcnt(0)\n\tv_mfma_f32_32x32x16_bf16 v[2:17], v[84:87], v[70:73], v[2:17]\n\ts_nop 11"
               : "+{v[2:17]}"(c.o) : "{v[84:87]}"(c.v1), "{v[70:73]}"(c.p1), "{v[18:33]}"(c.negm));
  STAMP(4);
  f32x16 o = c.o;
  float l;
  { auto rr = __builtin_amdgcn_permlane32_swap(__float_as_uint(o[10]), __float_as_uint(o[10]), false, false); l = __uint_as_float(rr[0]); }
  if (__builtin_expect(__any(!(l >= 5.421011e-20f)), 0)) {
    float mx = -INFINITY;
    f32x16 z, negm;
#pragma unroll
    for (int i = 0; i < 16; ++i) z[i] = 0.f;
#pragma unroll 1
    for (int tt = 0; tt < 64; ++tt) {
      const int ck = tt >> 4, tl = tt & 15;
      const u32x2 k2 = *(LDSP const u32x2*)(c.ktb + ck * CHUNK_B + (tl >> 1) * 256);
      const u32x4 ktl = {k2[0], k2[1], 0, 0};
      const u32x4 kf = ldsr16(c.kmb + ck * CHUNK_B + tl * 1024);
      const u32x4 qq = {(tl & 1) ? 0u : qtl, (tl & 1) ? qtl : 0u, 0, 0};
      f32x16 sc = mfma32(ktl, qq, z);
      sc = mfma32(kf, c.qm, sc);
#pragma unroll
      for (int i = 0; i < 16; ++i) mx = fmaxf(mx, sc[i]);
    }
    mx = xor32_max(mx);
#pragma unroll
    for (int i = 0; i < 16; ++i) { negm[i] = -mx; o[i] = 0.f; }
#pragma unroll 1
    for (int gq = 0; gq < 16; ++gq) {
      const int ck = gq >> 2, gg = gq & 3;
      attn_group_slow(c.kmb + ck * CHUNK_B + gg * 4096, c.ktb + ck * CHUNK_B + gg * 512, c.vb + ck * CHUNK_B + gg * 4864, c.qm, qtl, negm, o);
    }
    auto rr = __builtin_amdgcn_permlane32_swap(__float_as_uint(o[10]), __float_as_uint(o[10]), false, false);
    l = __uint_as_float(rr[0]);
  }
  const float rl = 1.0f / l;
  u32x4 ou;
#pragma unroll
  for (int i = 0; i < 4; ++i) ou[i] = cvtpk(o[2 * i] * rl, o[2 * i + 1] * rl);
  const int q = 32 * qt32 + r;
  const unsigned ob = (unsigned)WS_OFR + (q >> 4) * 18432 + (q & 15) * 16;
  bst16<SC1>(ou, wrs, ob + (head >> 1) * 1024 + (2 * (head & 1) + h) * 256, 0);
  __builtin_amdgcn_raw_buffer_store_b32(cvtpk(o[8] * rl, o[9] * rl), wrs, h ? DROP : ob + (16 + (head >> 4)) * 1024 + ((head >> 2) & 3) * 256 + (head & 3) * 4, 0, SC1);
  asm volatile("s_memtime %0\n\ts_memrealtime %1\n\ts_waitcnt lgkmcnt(0)" : "=s"(C1), "=s"(R1)::"memory");
  if (blockIdx.x == 8 && threadIdx.x == 0) { ((__attribute__((address_space(1))) unsigned long long*)(ws + (200u << 20)))[0] = C1 - C0; ((__attribute__((address_space(1))) unsigned long long*)(ws + (200u << 20)))[1] = R1 - R0; }
  const int m = lane & 15, g = lane >> 4, kh = w >> 2, cg = bi >> 2;
  const int nt0 = 9 * (cg >> 1) + 5 * (cg & 1), mt = 16 * xcd + 4 * (bi & 3) + (w & 3), row = 16 * mt + m;
  const unsigned lds0 = (unsigned)(uintptr_t)lds, lane16 = lane * 16;
  asm volatile("s_barrier" ::: "memory");
#define K3_W(i) do { const int p = min(8 * (i) + w, 89), st = (p * 205) >> 10, t = p - 5 * st;                                  \
    dma16(ws + WS_WO + (st * 36 + nt0 + min(t, 4 - (cg & 1))) * 1024, lane16, lds0 + p * 1024); } while (0)
#pragma unroll
  for (int i = 0; i < K3_PRE; ++i) K3_W(i);
  asm volatile("s_waitcnt vmcnt(%0)\n\ts_barrier" ::"i"(K3_PRE) : "memory");
  if (w == 0 && lane < K3_REP) __hip_atomic_fetch_add((gu32*)(ws + WS_CNT + (128 + qb * K3_REP) * 128) + lane * 32, 1u, __ATOMIC_RELAXED, __HIP_MEMORY_SCOPE_AGENT);
#pragma unroll
  for (int i = K3_PRE; i < 12; ++i) K3_W(i);
#undef K3_W
  dma16(ws + WS_WBAR + min(w, 2) * 1024, lane16, lds0 + K3_WBAR + min(w, 2) * 1024);
  const bool t4 = kh == 0 && !(cg & 1);
  const unsigned uo = mt * 18432 + (2 * nt0 + 4 * kh + (g >> 1)) * 256 + m * 16 + (g & 1) * 8;
  u32x2 xr[3];
  const unsigned xo = row * 1152 + (nt0 + 2 * kh) * 32 + g * 8;
  asm volatile("global_load_dwordx2 %0, %1, %2" : "=v"(xr[0]) : "v"(xo), "s"(x));
  asm volatile("global_load_dwordx2 %0, %1, %2 offset:32" : "=v"(xr[1]) : "v"(xo), "s"(x));
  asm volatile("global_load_dwordx2 %0, %1, %2" : "=v"(xr[2]) : "v"(t4 ? xo + 128 : xo), "s"(x));
  unsigned xm = gld4(ws + WS_XMEAN, row * 4);
  if (w == 0) wait_count((gu32*)(ws + WS_CNT + (128 + xcd * K3_REP + bi % K3_REP) * 128), 0u, 32u);
  LDSP const char* wa = lds + (45 * kh + 2 * kh) * 1024 + lane16;
  LDSP const char* wb = lds + (45 * kh + 2 - 2 * kh) * 1024 + lane16;
  LDSP const char* wc = lds + (45 * kh + 4) * 1024 + lane16;
  LDSP const char* wbl = lds + K3_WBAR + (m & 1) * 1152 + kh * 576 + g * 16;
  unsigned so = ((g & 1) ? (unsigned)WS_HC : (unsigned)WS_HU) + (uo & ~8u);
  PIN(wa); PIN(wb); PIN(wc); PIN(wbl); PIN(so);
  asm volatile("s_waitcnt vmcnt(0)\n\ts_barrier" : "+v"(xr[0]), "+v"(xr[1]), "+v"(xr[2]), "+v"(xm)::"memory");
  STAMP(6);
  u32x4 O[9], wf[6];
  const char* obase = ws + WS_OFR + rfl((mt * 18 + 9 * kh) * 1024);
#pragma unroll
  for (int i = 0; i < 9; ++i)
    asm volatile("global_load_dwordx4 %0, %1, %2 sc1" : "=v"(O[i]) : "v"(lane16), "s"(obase + i * 1024));
#define K3_A(f) (((f) + 2) % 6)
#define K3_RD(f) (K3_A(f) == 5 ? ldsr16(wbl + ((f) / 6) * 64) : ldsr16((K3_A(f) < 2 ? wa : (K3_A(f) < 4 ? wb : wc)) + ((f) / 6) * 5120 + (K3_A(f) < 4 ? ((f) & 1) * 1024 : 0)))
#pragma unroll
  for (int f = 0; f < 5; ++f) wf[f] = K3_RD(f);
  f32x4 acc[6];
#pragma unroll
  for (int f = 0; f < 54; ++f) {
    if (f % 6 == 0) asm volatile("s_waitcnt vmcnt(%1)" : "+v"(O[f / 6]) : "i"(8 - f / 6));
    if (f + 5 < 54) wf[(f + 5) % 6] = K3_RD(f + 5);
    if (f < 6) G_MFMA0(acc[K3_A(f)], wf[f % 6], O[f / 6]);
    else G_MFMA(acc[K3_A(f)], wf[f % 6], O[f / 6]);
    SBAR();
  }
#undef K3_RD
#undef K3_A
  asm volatile("s_nop 7" : "+v"(acc[0]), "+v"(acc[1]), "+v"(acc[2]), "+v"(acc[3]), "+v"(acc[4]), "+v"(acc[5]));
  LDSP char* xc = lds + K3_XC + lane16;
#pragma unroll
  for (int i = 0; i < 4; ++i) *(LDSP f32x4*)(xc + w * 4096 + i * 1024) = acc[2 + i];
  asm volatile("s_waitcnt lgkmcnt(0)\n\ts_barrier" ::: "memory");
  f32x4 v[4];
#pragma unroll
  for (int i = 0; i < 4; ++i) v[i] = *(LDSP const f32x4*)(xc + (w ^ 4) * 4096 + i * 1024);
  SBAR();
  const float mean = __uint_as_float(xm) + (acc[5][0] + v[3][0]) + (acc[5][1] + v[3][1]);
  float ssq = 0.f;
#pragma unroll
  for (int t = 0; t < 3; ++t) {
    const f32x4 a = (t < 2 ? acc[t] : acc[4]) + v[t];
    const unsigned h0 = cvtpk(bflo(xr[t][0]) + a[0], bfhi(xr[t][0]) + a[1]);
    const unsigned h1 = cvtpk(bflo(xr[t][1]) + a[2], bfhi(xr[t][1]) + a[3]);
    const unsigned c0 = cvtpk(bflo(h0) - mean, bfhi(h0) - mean);
    const unsigned c1 = cvtpk(bflo(h1) - mean, bfhi(h1) - mean);
    if (t < 2 || t4) { ssq = dot2(c0, c0, ssq); ssq = dot2(c1, c1, ssq); }
    const auto r0 = __builtin_amdgcn_permlane16_swap(h0, c0, false, false);
    const auto r1 = __builtin_amdgcn_permlane16_swap(h1, c1, false, false);
    bst16<SC1>(u32x4{r0[0], r1[0], r0[1], r1[1]}, wrs, t < 2 ? so + t * 512 : (t4 ? so + 2048 : DROP), 0);
  }
  ssq = xor32_sum(xor16_sum(ssq));
  __builtin_amdgcn_raw_buffer_store_b32(__float_as_uint(ssq), wrs, (unsigned)WS_SSQ + ((2 * cg + kh) * 2048 + row) * 4, 0, SC1);
  STAMP(7); STAMP_OUT(2, 8);
}

constexpr int K4_SLOT = 36864, K4_LDS = 163840;
constexpr int K4_PF = 4, K4_POLL = 10, K5_SIG = 154;
constexpr int K5_PERM[18] = {0, 1, 2, 6, 7, 8, 9, 10, 11, 15, 16, 17, 3, 4, 5, 12, 13, 14};
constexpr int K5_R[4] = {36864, 73728, 110592, 0};
constexpr int K5_OC[9] = {65536, 102400, 139264, 147456, 155648, 0, 8192, 16384, 24576};
struct K5P { const char* wb; unsigned q[4], qs[4], qo, qos, lds0; };
template <int Z> DI void k5_piece(const K5P& k, int i, int w, unsigned lane16) {
  constexpr int P0 = K5_PERM[2 * Z] * 9216, D = (K5_PERM[2 * Z + 1] - K5_PERM[2 * Z]) * 9216;
  if (i < 3 || (w >> 2) == (Z & 1))
    dma16(k.wb + (P0 + k.q[i] + k.qs[i] * D), lane16, k.lds0 + K5_R[Z & 3] + (i < 3 ? 8 * i + w : 24 + (w & 3)) * 1024);
}
template <int Z> DI void k5_own(const K5P& k, int w, unsigned lane16) {
  constexpr int P0 = K5_PERM[2 * Z] * 9216, D = (K5_PERM[2 * Z + 1] - K5_PERM[2 * Z]) * 9216;
  dma16(k.wb + (P0 + k.qo + k.qos * D), lane16, k.lds0 + K5_OC[Z] + w * 1024);
}
#define POLL(dst, p) asm volatile("global_load_dword %0, %1, %2 sc1" : "=v"(dst) : "v"(0u), "s"(p))
struct K4C {
  const char *wb, *xb; unsigned lane16, lds0; int w, dir;
  unsigned pf1, poll;
  K5P wd;
  gu32 *cntA, *cntB;
  const char *ab, *ab2; u32x4 A0[2][4];
  LDSP const char* wl[4];
  u32x4 X[2][18], wf[K4_PF + 1];
  f32x4 acc[4][4];
  float c1[2], c2[2];
  __amdgpu_buffer_rsrc_t ars; unsigned avo; int T0;
  float ea[2][2], et[2][2]; unsigned pk[2][4];
};
DI void k4_piece(const K4C& c, int tile, int p) {
  dma16(c.wb + (tile * c.dir * K4_SLOT + p * 1024), c.lane16, c.lds0 + (tile & 3) * K4_SLOT + p * 1024);
}
template <int ZW> DI void k4_issue_w(K4C& c, int i) {
  if (ZW < 9) {
    if (i == 0) k4_piece(c, c.w >> 2, 4 * ZW + (c.w & 3));
    else if ((c.w >> 2) == (ZW & 1)) k4_piece(c, 2, 4 * ZW + (c.w & 3));
  } else if (ZW == 9) { k4_piece(c, 3, 8 * i + c.w); }
}
template <int Z, int I> DI void k4_issue1(K4C& c) {
  if (I == 0) k4_issue_w<Z + 1>(c, 0);
  else if (I < 5) {
    c.X[(I - 1) >> 1][2 * Z + ((I - 1) & 1)] = gld16(c.xb + (((I - 1) >> 1) * 18432 + (2 * Z + ((I - 1) & 1)) * 1024), c.lane16);
  }
  else k4_issue_w<Z + 1>(c, 1);
}
template <int TL, int I> DI void k4_issue2(K4C& c) {
  if (I < 4) k4_piece(c, TL, 8 * I + c.w);
  else if ((c.w >> 2) == (TL & 1)) k4_piece(c, TL, 32 + (c.w & 3));
}
template <int TL, int E, int U> DI void k4_epi(K4C& c) {
  constexpr int e = U >> 2, u = U & 3, mt = e >> 2, i = e & 3, q = e & 1;
  if constexpr (U < 32) {
    if (u == 0) { const float a = c.acc[TL & 3][mt][i] * c.c1[mt]; c.ea[E][q] = a; c.et[E][q] = a * c.acc[TL & 3][2 + mt][i]; PIN(c.ea[E][q]); PIN(c.et[E][q]); }
    if (u == 1) { c.ea[E][q] = __builtin_amdgcn_exp2f(c.ea[E][q]); PIN(c.ea[E][q]); }
    if (u == 2) { c.ea[E][q] = __builtin_amdgcn_rcpf(__builtin_fmaf(c.ea[E][q], c.c2[mt], c.c2[mt])); PIN(c.ea[E][q]); }
    if (u == 3) { c.et[E][q] *= c.ea[E][q]; PIN(c.et[E][q]); if (q) { c.pk[E][e >> 1] = cvtpk(c.et[E][0], c.et[E][1]); PIN(c.pk[E][e >> 1]); } }
  } else if constexpr (U == 32) {
    auto r0 = __builtin_amdgcn_permlane16_swap(c.pk[E][0], c.pk[E][2], false, false);
    auto r1 = __builtin_amdgcn_permlane16_swap(c.pk[E][1], c.pk[E][3], false, false);
    c.pk[E][0] = r0[0]; c.pk[E][1] = r1[0]; c.pk[E][2] = r0[1]; c.pk[E][3] = r1[1];
  } else if constexpr (U == 33) {
    const int T = c.T0 + TL * c.dir;
    bst16<SC1>(u32x4{c.pk[E][0], c.pk[E][1], c.pk[E][2], c.pk[E][3]}, c.ars, c.avo, (T >> 1) * 1024 + (T & 1) * 512);
  }
}
constexpr int k4_tile(int F) { return F < 108 ? (F % 6) >> 1 : 3 + (F - 108) / 36; }
constexpr int k4_step(int F) { return F < 108 ? F / 6 : ((F - 108) % 36) >> 1; }
constexpr int k4_addr(int F) { return k4_step(F) * 2048 + (F & 1) * 1024; }
template <int F> DI void k4_frag(K4C& c) {
  constexpr int TL = k4_tile(F), S = k4_step(F), M = F & 1, N = F + K4_PF;
  const u32x4 a = c.wf[F % (K4_PF + 1)];
  if constexpr (F < 108) { if constexpr ((F + 4) % 3 == 0) __builtin_amdgcn_s_setprio(3 - ((F + 4) % 12) / 3); }
  else if constexpr ((F - 108 + 4) % 9 == 0) __builtin_amdgcn_s_setprio(3 - ((F - 108 + 4) % 36) / 9);
  if (S == 0) {
    G_MFMA0(c.acc[TL & 3][2 * M], a, c.X[0][S]);
    G_MFMA0(c.acc[TL & 3][2 * M + 1], a, c.X[1][S]);
  } else {
    G_MFMA(c.acc[TL & 3][3 * M], a, c.X[M][S]);
    G_MFMA(c.acc[TL & 3][M + 1], a, c.X[1 - M][S]);
  }
  if constexpr (N < 108 && N % 12 == 0) {
    constexpr int Z = N / 12;
    if (Z < 8) asm volatile("s_waitcnt vmcnt(5)\n\ts_barrier" : "+v"(c.X[0][2 * Z]), "+v"(c.X[0][2 * Z + 1]), "+v"(c.X[1][2 * Z]), "+v"(c.X[1][2 * Z + 1])::"memory");
    else asm volatile("s_waitcnt vmcnt(2)\n\ts_barrier" : "+v"(c.X[0][2 * Z]), "+v"(c.X[0][2 * Z + 1]), "+v"(c.X[1][2 * Z]), "+v"(c.X[1][2 * Z + 1])::"memory");
  } else if constexpr (N >= 108 && N < 324 && (N - 108) % 36 == 0) {
    constexpr int NT = k4_tile(N);
    if (NT == 3) asm volatile("s_waitcnt vmcnt(0) lgkmcnt(0)\n\ts_barrier" ::: "memory");
    else if (NT == 8) {
      asm volatile("s_waitcnt vmcnt(0) lgkmcnt(0)\n\ts_barrier" ::: "memory");
      if (c.w == 0 && c.lane16 == 0) __hip_atomic_fetch_add(c.cntA, 1u, __ATOMIC_RELAXED, __HIP_MEMORY_SCOPE_AGENT);
    }
    else asm volatile("s_waitcnt vmcnt(4) lgkmcnt(0)\n\ts_barrier" ::: "memory");
  }
  if constexpr (N < 324) c.wf[N % (K4_PF + 1)] = ldsr16(c.wl[k4_tile(N) & 3] + k4_addr(N));
  if constexpr (F < 108) {
    constexpr int Z = F / 12, I = F % 12;
    if constexpr (Z == 0 && I < 4 && !(I & 1)) k4_issue1<2, I / 2>(c);
    if constexpr (Z < 7 && I < 8 && (I & 1)) k4_issue1<Z + 2, 2 + I / 2>(c);
    if constexpr (Z < 6 && I > 8 && (I & 1)) k4_issue1<Z + 3, (I - 9) / 2>(c);
    if constexpr (Z == 6 && I == 10) k4_issue2<3, 2>(c);
    if constexpr (Z == 7 && (I == 1 || I == 3)) k4_issue2<3, 3 + I / 2>(c);
  } else {
    constexpr int P = (F - 108) % 36;
    if constexpr (TL == 3) { if constexpr (P % 3 == 0 && P < 15) k4_issue2<4, P / 3>(c); if constexpr (P % 3 == 0 && P >= 15 && P < 30) k4_issue2<5, (P - 15) / 3>(c); }
    else if constexpr (TL + 2 < 9 && P % 4 == 0 && P < 20) k4_issue2<TL + 2, P / 4>(c);
    if constexpr (TL == 7 && P == 0) CODE_PF(c.pf1, k_ffn, k_end, 0, 2 + (c.w & 1), ((blockIdx.x >> 3) == 0 && c.w < 2) ? 128u : 0u);
    if constexpr (TL == 7 && P % 3 == 2 && P <= 11) k5_piece<0>(c.wd, P / 3, c.w, c.lane16);
    if constexpr (TL == 8 && P < 4) k5_piece<1>(c.wd, P, c.w, c.lane16);
    if constexpr (TL == 8 && P >= 4 && P < 8) k5_piece<2>(c.wd, P - 4, c.w, c.lane16);
    if constexpr (TL == 8 && P == K4_POLL) POLL(c.poll, c.cntA);
    if constexpr (TL == 8 && P == K4_POLL + 12) { asm volatile("s_waitcnt vmcnt(0)" : "+v"(c.poll), "+v"(c.pf1)); wait_count(c.cntA, c.poll, 8u); }
    if constexpr (TL == 8 && P > K4_POLL + 12 && P <= K4_POLL + 20) {
      constexpr int i = P - K4_POLL - 13, z = i >> 2, j = (i & 1) * 2 + ((i >> 1) & 1);
      asm volatile("global_load_dwordx4 %0, %1, %2 sc1" : "=v"(c.A0[z][j]) : "v"(c.lane16), "s"(((j >> 1) ? c.ab2 : c.ab) + K5_PERM[2 * z + (j & 1)] * 1024));
    }
    if constexpr (P >= 2) {
      if constexpr (TL == 3) { k4_epi<0, 0, P - 2>(c); k4_epi<1, 1, P - 2>(c); }
      else if constexpr (TL == 4) { k4_epi<2, 0, P - 2>(c); k4_epi<3, 1, P - 2>(c); }
      else k4_epi<TL - 1, 0, P - 2>(c);
    }
  }
  SBAR();
}
template <int B, int... Fs> DI void k4_run(K4C& c, std::integer_sequence<int, Fs...>) { (k4_frag<B + Fs>(c), ...); }
DI void k4_last(K4C& c) {
  float a[8], t[8];
#pragma unroll
  for (int e = 0; e < 8; ++e) { a[e] = c.acc[0][e >> 2][e & 3] * c.c1[e >> 2]; t[e] = a[e] * c.acc[0][2 + (e >> 2)][e & 3]; }
#pragma unroll
  for (int e = 0; e < 8; ++e) a[e] = __builtin_amdgcn_exp2f(a[e]);
#pragma unroll
  for (int e = 0; e < 8; ++e) a[e] = __builtin_amdgcn_rcpf(__builtin_fmaf(a[e], c.c2[e >> 2], c.c2[e >> 2]));
#pragma unroll
  for (int e = 0; e < 4; ++e) c.pk[0][e] = cvtpk(t[2 * e] * a[2 * e], t[2 * e + 1] * a[2 * e + 1]);
  k4_epi<8, 0, 32>(c); k4_epi<8, 0, 33>(c);
}
DI f32x4 addbf(f32x4 a, unsigned lo, unsigned hi) { return a + f32x4{bflo(lo), bfhi(lo), bflo(hi), bfhi(hi)}; }
struct K5C {
  const char *ab, *ab2;
  LDSP const char *ra[4], *oa[3], *xr;
  LDSP char* xw;
  u32x4 A[9][4], wf[5];
  f32x4 acc[2][7], own[2][2], v[4];
  const char* hb; unsigned ho; int kq; u32x2 hr[3];
  __amdgpu_buffer_rsrc_t wrs; unsigned tw, p8s;
  gu32* cnt;
};
template <int Z, int J> DI void k5_issue(K5C& c, const K4C& k) {
  if constexpr (Z < 7 && J < 4) asm volatile("global_load_dwordx4 %0, %1, %2 sc1" : "=v"(c.A[Z + 2][J]) : "v"(k.lane16), "s"(((J >> 1) ? c.ab2 : c.ab) + K5_PERM[2 * (Z + 2) + (J & 1)] * 1024));
  if constexpr (Z < 6 && J >= 4) k5_piece<Z + 3>(k.wd, J - 4, k.w, k.lane16);
  if constexpr (Z == 6 && J >= 4) k5_own<J - 4>(k.wd, k.w, k.lane16);
  if constexpr (Z == 7 && J == 0) k5_own<4>(k.wd, k.w, k.lane16);
  if constexpr (Z == 7 && J == 1) asm volatile("global_load_dwordx2 %0, %1, %2" : "=v"(c.hr[0]) : "v"(c.ho), "s"(c.hb + c.kq * 1024));
  if constexpr (Z == 7 && J == 2) asm volatile("global_load_dwordx2 %0, %1, %2 offset:512" : "=v"(c.hr[1]) : "v"(c.ho), "s"(c.hb + c.kq * 1024));
  if constexpr (Z == 7 && J == 3) asm volatile("global_load_dwordx2 %0, %1, %2" : "=v"(c.hr[2]) : "v"(c.ho), "s"(c.hb + 4096));
  if constexpr (Z == 8 && J < 4) k5_own<5 + J>(k.wd, k.w, k.lane16);
}
template <int N> DI u32x4 k5_read(const K5C& c) {
  if constexpr (N < 126) return ldsr16(c.ra[(N / 14) & 3] + (N % 14) * 1024);
  else { constexpr int o = K5_OC[(N - 126) / 4] + ((N - 126) & 3) * 1024; return ldsr16(c.oa[o >> 16] + (o & 0xffff)); }
}
template <int F> DI void k5_frag(K5C& c, K4C& k) {
  constexpr int N = F + 4;
  const u32x4 a = c.wf[F % 5];
  if constexpr (F < 126) {
    constexpr int Z = F / 14, f = F % 14, st = f / 7, j = f % 7;
    if (F < 7) { G_MFMA0(c.acc[0][j], a, c.A[Z][st]); G_MFMA0(c.acc[1][j], a, c.A[Z][2 + st]); }
    else { G_MFMA(c.acc[1][j], a, c.A[Z][2 + st]); G_MFMA(c.acc[0][j], a, c.A[Z][st]); }
    if constexpr (f == 10 && Z == 8) asm volatile("s_waitcnt vmcnt(4)\n\ts_barrier" : "+v"(c.hr[0]), "+v"(c.hr[1]), "+v"(c.hr[2])::"memory");
    if constexpr (f == 10 && Z < 8) {
      u32x4(&A)[4] = c.A[Z + 1];
      if (Z == 0) asm volatile("s_waitcnt vmcnt(9) lgkmcnt(0)\n\ts_barrier" : "+v"(A[0]), "+v"(A[1]), "+v"(A[2]), "+v"(A[3])::"memory");
      else if (Z == 3) asm volatile("s_waitcnt vmcnt(4) lgkmcnt(0)\n\ts_barrier" : "+v"(A[0]), "+v"(A[1]), "+v"(A[2]), "+v"(A[3]), "+v"(k.poll)::"memory");
      else if (Z < 7) asm volatile("s_waitcnt vmcnt(7) lgkmcnt(0)\n\ts_barrier" : "+v"(A[0]), "+v"(A[1]), "+v"(A[2]), "+v"(A[3])::"memory");
      else asm volatile("s_waitcnt vmcnt(8) lgkmcnt(0)\n\ts_barrier" : "+v"(A[0]), "+v"(A[1]), "+v"(A[2]), "+v"(A[3])::"memory");
      if (Z == 1 && k.w == 0 && k.lane16 == 0) __hip_atomic_fetch_add(k.cntB, 1u, __ATOMIC_RELAXED, __HIP_MEMORY_SCOPE_AGENT);
      if (Z == 3) wait_count(k.cntB, k.poll, 8u);
    }
    c.wf[N % 5] = k5_read<N>(c);
    if constexpr (Z == 3 && f == 1) POLL(k.poll, k.cntB);
    constexpr int slot = f == 0 ? 2 : (f == 2 ? 3 : (f == 4 ? 4 : (f == 5 ? 5 : (f == 7 ? 6 : (f == 9 ? 7 : -1)))));
    if constexpr (F == 0) { k5_issue<0, 0>(c, k); k5_issue<0, 1>(c, k); }
    if constexpr (slot >= 0) k5_issue<Z, slot>(c, k);
    if constexpr (Z < 8 && f > 10 && (f & 1)) k5_issue<Z + 1, (f - 11) / 2>(c, k);
  } else {
    constexpr int B = F - 126, z = B / 4, st = (B >> 1) & 1, t = B & 1;
    if (B < 2) { G_MFMA0(c.own[0][t], a, c.A[z][st]); G_MFMA0(c.own[1][t], a, c.A[z][2 + st]); }
    else { G_MFMA(c.own[1][t], a, c.A[z][2 + st]); G_MFMA(c.own[0][t], a, c.A[z][st]); }
    if constexpr (F == 131) asm volatile("s_waitcnt vmcnt(0) lgkmcnt(0)\n\ts_barrier" ::: "memory");
    if constexpr (F == K5_SIG) {
      asm volatile("s_waitcnt vmcnt(0)\n\ts_barrier" ::: "memory");
      if (k.w == 0 && k.lane16 == 0) __hip_atomic_fetch_add(c.cnt, 1u, __ATOMIC_RELAXED, __HIP_MEMORY_SCOPE_AGENT);
    }
    if constexpr (N < 162) c.wf[N % 5] = k5_read<N>(c);
  }
  if constexpr (F >= 123 && F < 130) { PIN(c.acc[1][F - 123]); *(LDSP f32x4*)(c.xw + (F - 123) * 1024) = c.acc[1][F - 123]; }
  if constexpr (F == 131) { _Pragma("unroll") for (int i = 0; i < 4; ++i) c.v[i] = *(LDSP const f32x4*)(c.xr + i * 1024); }
  if constexpr (F == 133 || F == 135 || F == 138) {
    constexpr int j = F == 133 ? 0 : (F == 135 ? 1 : 2), o = 2 * (j & 1);
    const f32x4 x = c.acc[0][2 * j] + c.v[o], y = c.acc[0][2 * j + 1] + c.v[o + 1];
    bst16<SC1>(u32x4{cvtpk(x[0], x[1]), cvtpk(x[2], x[3]), cvtpk(y[0], y[1]), cvtpk(y[2], y[3])}, c.wrs, k.lane16,
              (unsigned)WS_PART + ((((c.kq + j + 1) & 3) * 512 + c.tw) * 3 + j) * 1024);
    if constexpr (F == 135) { _Pragma("unroll") for (int i = 0; i < 3; ++i) c.v[i] = *(LDSP const f32x4*)(c.xr + (4 + i) * 1024); }
  }
  if constexpr (F == 140) {
    c.acc[0][6] += c.v[2];
    __builtin_amdgcn_raw_buffer_store_b64(u32x2{cvtpk(c.acc[0][6][0], c.acc[0][6][1]), cvtpk(c.acc[0][6][2], c.acc[0][6][3])}, c.wrs, c.p8s, c.kq * 512, 16);
    PIN(c.acc[0][6]);
  }
  SBAR();
}
template <int B, int... Fs> DI void k5_run(K5C& c, K4C& k, std::integer_sequence<int, Fs...>) { (k5_frag<B + Fs>(c, k), ...); }

extern "C" __global__ __launch_bounds__(512, 2) void k_ffn() {
  extern __shared__ __attribute__((aligned(16))) char smem[];
  char* ws = (char*)((cptr_t)(uintptr_t)g_ptr)[4]; char* out = (char*)((cptr_t)(uintptr_t)g_ptr)[5];
  asm volatile("" : "+s"(out), "+s"(ws));
  LDSP char* lds = (LDSP char*)smem;
  const int tid = threadIdx.x, lane = tid & 63, w = rfl(tid >> 6);
  const int xcd = blockIdx.x & 7, bi = blockIdx.x >> 3;
  const int m = lane & 15, g = lane >> 4, kq = xcd >> 1;
  STAMP_DECL; STAMP(0);
  unsigned long long C0, R0, C1, R1; asm volatile("s_memtime %0\n\ts_memrealtime %1\n\ts_waitcnt lgkmcnt(0)" : "=s"(C0), "=s"(R0)::"memory");
  K4C c;
  {
    const int rb = 4 * (xcd & 1) + (bi & 3), fs = 8 * kq + (bi >> 2), mt = 16 * rb + 2 * w;
    c.lds0 = (unsigned)(uintptr_t)lds; c.lane16 = lane * 16; c.w = w;
    c.T0 = 9 * fs + 8 * (fs & 1); c.dir = 1 - 2 * (fs & 1);
    c.wb = ws + WS_W1 + (size_t)c.T0 * K4_SLOT;
    c.xb = ws + WS_HC + (size_t)mt * 18432;
    k4_issue_w<0>(c, 0); k4_issue1<0, 1>(c); k4_issue1<0, 3>(c); k4_issue_w<0>(c, 1); k4_issue1<0, 2>(c); k4_issue1<0, 4>(c);
    k4_issue1<0, 0>(c); k4_issue1<0, 5>(c);
    unsigned sq[8];
#pragma unroll
    for (int i = 0; i < 8; ++i) sq[i] = gld4(ws + WS_SSQ + (i >> 1) * 8192 + (i & 1) * 64, 4 * g * 8192 + (16 * mt + m) * 4);
    k4_issue1<1, 0>(c); k4_issue1<1, 1>(c); k4_issue1<1, 2>(c); k4_issue1<1, 3>(c); k4_issue1<1, 4>(c); k4_issue1<1, 5>(c);
    SBAR();
#pragma unroll
    for (int i = 0; i < 4; ++i) { c.wl[i] = lds + c.lane16 + i * K4_SLOT; PIN(c.wl[i]); }
    c.ars = mkrsrc(ws + WS_ACT, 128u * 144 * 1024);
    c.avo = (mt + (g & 1)) * (144 * 1024) + (g >> 1) * 256 + m * 16;
    c.wd.wb = ws + WS_WD + ((size_t)(bi >> 3) * 144 + 36 * kq) * 9 * 1024;
#pragma unroll
    for (int i = 0; i < 4; ++i) {
      const int p = i < 3 ? 8 * i + w : 24 + (w & 3), st = (p * 37) >> 8, t = p - 7 * st + 2;
      c.wd.q[i] = ((st >> 1) * 162 + (t < 8 ? (t + 2 * kq) & 7 : 8)) * 1024;
      c.wd.qs[i] = st & 1;
      asm volatile("" : "+s"(c.wd.q[i]), "+s"(c.wd.qs[i]));
    }
    c.wd.qo = ((w >> 2) * 162 + 2 * kq + (w & 1)) * 1024; c.wd.qos = (w >> 1) & 1; c.wd.lds0 = c.lds0;
    c.cntA = (gu32*)(ws + WS_CNT + (64 + rb * 4 + kq) * 128);
    STAMP(1);
    asm volatile("s_waitcnt vmcnt(15)\n\ts_barrier" : "+v"(c.X[0][0]), "+v"(c.X[0][1]), "+v"(c.X[1][0]), "+v"(c.X[1][1])::"memory");
#pragma unroll
    for (int i = 0; i < K4_PF; ++i) c.wf[i] = ldsr16(c.wl[k4_tile(i)] + k4_addr(i));
    SBAR();
    k4_run<0>(c, std::make_integer_sequence<int, 12>{});
    asm volatile("" : "+v"(sq[0]), "+v"(sq[1]), "+v"(sq[2]), "+v"(sq[3]), "+v"(sq[4]), "+v"(sq[5]), "+v"(sq[6]), "+v"(sq[7]));
#pragma unroll
    for (int t = 0; t < 2; ++t) {
      float ss = (__uint_as_float(sq[t]) + __uint_as_float(sq[2 + t])) + (__uint_as_float(sq[4 + t]) + __uint_as_float(sq[6 + t]));
      ss = xor32_sum(xor16_sum(ss));
      const float v = ss * (1.0f / DM) + LN_EPS, rs = __builtin_amdgcn_rsqf(v);
      c.c1[t] = -LOG2E * rs; c.c2[t] = -LOG2E * (v * rs);
      PIN(c.c1[t]); PIN(c.c2[t]);
    }
    SBAR();
    k4_run<12>(c, std::make_integer_sequence<int, 276>{});
    c.cntB = c.cntA + 32 * 32;
  }
  const int rb = 8 * (xcd & 1) + (bi & 7), nb = bi >> 3, mg = w & 3, kh = w >> 2, mu = 8 * rb + 2 * mg + kh;
  gu32* prodB = c.cntB;
  c.cntA = (gu32*)(ws + WS_CNT + (64 + (rb >> 1) * 4 + kq) * 128);
  c.ab = ws + WS_ACT + ((size_t)mu * 144 + 36 * kq + 18 * kh) * 1024;
  c.ab2 = ws + WS_ACT + ((size_t)(mu ^ 1) * 144 + 36 * kq + 18 * kh) * 1024;
  k4_run<288>(c, std::make_integer_sequence<int, 36>{});
  asm volatile("s_memtime %0\n\ts_memrealtime %1\n\ts_waitcnt lgkmcnt(0)" : "=s"(C1), "=s"(R1)::"memory");
  K5C d;
  d.ab = c.ab; d.ab2 = c.ab2;
  asm volatile("s_nop 7" : "+v"(c.acc[0][0]), "+v"(c.acc[0][1]), "+v"(c.acc[0][2]), "+v"(c.acc[0][3]));
  k4_last(c);
  SBAR();
  gu32* consB = c.cntA + 32 * 32;
  d.hb = ws + WS_HU + (size_t)mu * 18432 + nb * 4608; d.ho = (g >> 1) * 256 + m * 16 + (g & 1) * 8; d.kq = kq;
#pragma unroll
  for (int i = 0; i < 4; ++i) { d.ra[i] = lds + K5_R[i] + kh * 14336 + c.lane16; PIN(d.ra[i]); }
#pragma unroll
  for (int i = 0; i < 3; ++i) { d.oa[i] = lds + i * 65536 + kh * 4096 + c.lane16; PIN(d.oa[i]); }
  d.xw = lds + (kh ? K5_R[2] : K5_R[1]) + mg * 7168 + c.lane16; PIN(d.xw);
  d.xr = lds + (kh ? K5_R[1] : K5_R[2]) + mg * 7168 + c.lane16; PIN(d.xr);
  const __amdgpu_buffer_rsrc_t wrs = mkrsrc(ws, (unsigned)WS_END), ors = mkrsrc(out, SEQ * DM * 2);
  const int tile = rb * 4 + nb;
  const bool own8 = (mu & 3) == kq;
  const unsigned tw = tile * 8 + w;
  const unsigned p8 = (unsigned)WS_PART8 + tw * 2048 + lane * 8;
  d.cnt = (gu32*)(ws + WS_CNT + tile * 128);
  d.wrs = wrs; d.tw = tw; d.p8s = own8 ? DROP : p8; PIN(d.p8s);
#pragma unroll
  for (int i = 0; i < 4; ++i) { d.A[0][i] = c.A0[0][i]; d.A[1][i] = c.A0[1][i]; }
  asm volatile("s_waitcnt vmcnt(6) lgkmcnt(0)\n\ts_barrier" : "+v"(d.A[0][0]), "+v"(d.A[0][1]), "+v"(d.A[0][2]), "+v"(d.A[0][3])::"memory");
  STAMP(3);
  d.wf[0] = k5_read<0>(d); d.wf[1] = k5_read<1>(d); d.wf[2] = k5_read<2>(d); d.wf[3] = k5_read<3>(d);
  SBAR();
  c.cntB = prodB;
  k5_run<0>(d, c, std::make_integer_sequence<int, 28>{});
  c.cntB = consB;
  k5_run<28>(d, c, std::make_integer_sequence<int, 134>{});
  asm volatile("s_nop 7" : "+v"(d.own[0][0]), "+v"(d.own[0][1]), "+v"(d.own[1][0]), "+v"(d.own[1][1]));
  STAMP(4);
  LDSP char* xo = lds + K5_R[0] + c.lane16;
  *(LDSP f32x4*)(xo + w * 2048) = d.own[1][0];
  *(LDSP f32x4*)(xo + w * 2048 + 1024) = d.own[1][1];
  unsigned oo = (16 * mu + m) * 1152 + nb * 288;
  unsigned oo8 = own8 ? oo + 256 + g * 8 : DROP;
  oo += kq * 64 + (g & 1) * 32 + (g >> 1) * 16;
  PIN(oo); PIN(oo8);
  STAMP(5);
  if (w == 0) wait_count(d.cnt, 0u, 4u);
  asm volatile("s_waitcnt lgkmcnt(0)\n\ts_barrier" ::: "memory");
  STAMP(6);
  u32x4 r[3]; u32x2 r8[3];
#pragma unroll
  for (int j = 0; j < 3; ++j) {
    r[j] = __builtin_amdgcn_raw_buffer_load_b128(wrs, c.lane16, (unsigned)WS_PART + ((kq * 512 + tw) * 3 + j) * 1024, 16);
    r8[j] = __builtin_amdgcn_raw_buffer_load_b64(wrs, own8 ? p8 : DROP, ((kq + 1 + j) & 3) * 512, 16);
  }
  SBAR();
  f32x4 e0 = addbf(d.own[0][0] + *(LDSP const f32x4*)(xo + (w ^ 4) * 2048), d.hr[0][0], d.hr[0][1]);
  f32x4 e1 = addbf(d.own[0][1] + *(LDSP const f32x4*)(xo + (w ^ 4) * 2048 + 1024), d.hr[1][0], d.hr[1][1]);
  f32x4 e8 = addbf(d.acc[0][6], d.hr[2][0], d.hr[2][1]);
#pragma unroll
  for (int j = 0; j < 3; ++j) { e0 = addbf(e0, r[j][0], r[j][1]); e1 = addbf(e1, r[j][2], r[j][3]); e8 = addbf(e8, r8[j][0], r8[j][1]); }
  {
    const auto s0 = __builtin_amdgcn_permlane16_swap(cvtpk(e0[0], e0[1]), cvtpk(e1[0], e1[1]), false, false);
    const auto s1 = __builtin_amdgcn_permlane16_swap(cvtpk(e0[2], e0[3]), cvtpk(e1[2], e1[3]), false, false);
    bst16<SC1>(u32x4{s0[0], s1[0], s0[1], s1[1]}, ors, oo, 0);
    __builtin_amdgcn_raw_buffer_store_b64(u32x2{cvtpk(e8[0], e8[1]), cvtpk(e8[2], e8[3])}, ors, oo8, 0, SC1);
  }
  if (blockIdx.x == 8 && threadIdx.x == 0) { ((__attribute__((address_space(1))) unsigned long long*)(ws + (200u << 20)))[2] = C1 - C0; ((__attribute__((address_space(1))) unsigned long long*)(ws + (200u << 20)))[3] = R1 - R0; }

}
extern "C" __global__ void k_tail(char* ws) {
  const unsigned long long t6 = __builtin_amdgcn_s_memrealtime();
  const volatile unsigned long long* t = (const volatile unsigned long long*)(ws + (200u << 20));
  unsigned long long d = 300 + (SEL == 0 ? 0 : (SEL == 1 ? t[0] / 10 : (SEL == 2 ? 2 * t[1] : (SEL == 3 ? t[2] / 10 : 2 * t[3]))));
  if (d > 40000) d = 40000;
  while (__builtin_amdgcn_s_memrealtime() < t6 + d) __builtin_amdgcn_s_sleep(2);
}
extern "C" __global__ void k_end() {}

extern "C" void kernel_launch(void* const* d_in, const int* in_sizes, int n_in, void* d_out, int out_size, void* d_ws, size_t ws_size,
                              hipStream_t stream) {
  const u16* x = (const u16*)d_in[0];
  const u16* wqkv = (const u16*)d_in[1];
  const u16* wo = (const u16*)d_in[2];
  const u16* wg = (const u16*)d_in[4];
  const u16* wu = (const u16*)d_in[5];
  const u16* wd = (const u16*)d_in[6];
  char* ws = (char*)d_ws;
  (void)hipFuncSetAttribute((const void*)k_qkv, hipFuncAttributeMaxDynamicSharedMemorySize, K1_LDS);
  (void)hipFuncSetAttribute((const void*)k_attn, hipFuncAttributeMaxDynamicSharedMemorySize, K2_LDS);
  (void)hipFuncSetAttribute((const void*)k_ffn, hipFuncAttributeMaxDynamicSharedMemorySize, K4_LDS);
  k_qkv<<<256, 768, K1_LDS, stream>>>(x, wqkv, wo, ws, wg, wu, wd, (char*)d_out);
  k_attn<<<256, 768, K2_LDS, stream>>>();
  k_ffn<<<256, 512, K4_LDS, stream>>>();
  k_tail<<<1, 64, 0, stream>>>(ws);
}
```

```cpp
#include <hip/hip_runtime.h>
#include <hip/hip_bf16.h>
#include <stdint.h>
#include <utility>
#include <type_traits>

typedef __bf16 bf16x8 __attribute__((ext_vector_type(8)));
typedef __bf16 bf16x2 __attribute__((ext_vector_type(2)));
typedef short s16x4 __attribute__((ext_vector_type(4)));
typedef float f32x16 __attribute__((ext_vector_type(16)));
typedef float f32x4 __attribute__((ext_vector_type(4)));
typedef float f32x2 __attribute__((ext_vector_type(2)));
typedef unsigned u32x4 __attribute__((ext_vector_type(4)));
typedef unsigned u32x2 __attribute__((ext_vector_type(2)));
typedef unsigned short u16;
#define LDSP __attribute__((address_space(3)))
#define DI __device__ __forceinline__
#define SBAR() __builtin_amdgcn_sched_barrier(0)

constexpr int SEQ = 2048, DM = 576, NH = 32, DH = 18, FF = 4608;
constexpr float LOG2E = 1.4426950408889634f;
constexpr float QSCALE = 0.23570226039551584f * LOG2E;
constexpr float LN_EPS = 1e-5f;

constexpr size_t CHUNK_B = 37888;
constexpr size_t KV_HEAD_B = 4 * CHUNK_B;
constexpr size_t WS_QM = 0;
constexpr size_t WS_QT = WS_QM + (size_t)NH * 65536;
constexpr size_t WS_KV = WS_QT + (size_t)NH * 8192;
constexpr size_t WS_KMAX = WS_KV + (size_t)NH * KV_HEAD_B;
constexpr size_t WS_XMEAN = WS_KMAX + 65536;
constexpr size_t WS_OFR = WS_XMEAN + 8192;
constexpr size_t WS_WO = WS_OFR + (size_t)128 * 18 * 1024;
constexpr size_t WS_WBAR = WS_WO + (size_t)12 * 54 * 1024;
constexpr size_t WS_HC = WS_WBAR + 3072;
constexpr size_t WS_SSQ = WS_HC + (size_t)64 * 36 * 1024;
constexpr size_t WS_W1 = WS_SSQ + (size_t)16 * 8192;
constexpr size_t WS_WD = WS_W1 + (size_t)288 * 36 * 1024;
constexpr size_t WS_ACT = WS_WD + (size_t)4 * 144 * 9 * 1024;
constexpr size_t UNIT_B = (size_t)128 * 18432;
constexpr size_t WS_PART = WS_ACT + (size_t)128 * 144 * 1024;
constexpr size_t WS_PART8 = WS_PART + (size_t)4 * 64 * 8 * 3 * 1024;
constexpr size_t WS_CNT = WS_PART8 + (size_t)64 * 8 * 4 * 512;
constexpr size_t WS_HU = WS_CNT + 40960;
constexpr size_t WS_END = WS_HU + UNIT_B;

static __device__ unsigned long long g_ptr[8];
typedef __attribute__((address_space(4))) const unsigned long long* cptr_t;
DI unsigned cvtpk(float lo, float hi) {
  f32x2 v = {lo, hi};
  bf16x2 b = __builtin_convertvector(v, bf16x2);
  return __builtin_bit_cast(unsigned, b);
}
DI float bflo(unsigned u) { return __uint_as_float(u << 16); }
DI float bfhi(unsigned u) { return __uint_as_float(u & 0xffff0000u); }
DI float dot2(unsigned a, unsigned b, float c) {
  return __builtin_amdgcn_fdot2_f32_bf16(__builtin_bit_cast(bf16x2, a), __builtin_bit_cast(bf16x2, b), c, false);
}
DI f32x16 mfma32(u32x4 a, u32x4 b, f32x16 c) {
  return __builtin_amdgcn_mfma_f32_32x32x16_bf16(__builtin_bit_cast(bf16x8, a), __builtin_bit_cast(bf16x8, b), c, 0, 0, 0);
}
DI unsigned rfl(unsigned x) { return __builtin_amdgcn_readfirstlane(x); }
DI void dma16(const char* sbase, unsigned voff, unsigned lds) {
  asm volatile("s_mov_b32 m0, %2\n\ts_nop 0\n\tglobal_load_lds_dwordx4 %0, %1" ::"v"(voff), "s"(sbase), "s"(lds));
}
DI u32x4 gld16(const char* sbase, unsigned voff) {
  u32x4 r;
  asm volatile("global_load_dwordx4 %0, %1, %2" : "=v"(r) : "v"(voff), "s"(sbase));
  return r;
}
DI unsigned gld4(const char* sbase, unsigned voff) {
  unsigned r;
  asm volatile("global_load_dword %0, %1, %2" : "=v"(r) : "v"(voff), "s"(sbase));
  return r;
}
#define PIN(x) asm volatile("" : "+v"(x))
DI u32x4 ldsr16(LDSP const char* p) { return *(LDSP const u32x4*)p; }
DI float xor32_sum(float v) {
  auto r = __builtin_amdgcn_permlane32_swap(__float_as_uint(v), __float_as_uint(v), false, false);
  return __uint_as_float(r[0]) + __uint_as_float(r[1]);
}
DI float xor32_max(float v) {
  auto r = __builtin_amdgcn_permlane32_swap(__float_as_uint(v), __float_as_uint(v), false, false);
  return fmaxf(__uint_as_float(r[0]), __uint_as_float(r[1]));
}
DI float xor16_sum(float v) {
  auto r = __builtin_amdgcn_permlane16_swap(__float_as_uint(v), __float_as_uint(v), false, false);
  return __uint_as_float(r[0]) + __uint_as_float(r[1]);
}

#define STAMP_DECL
#define STAMP(i)
#define STAMP_OUT(kid, n)

DI int wo_row(int step, int g, int j) {
  if (step < 16) return 18 * (2 * step + (g >> 1)) + 4 * (g & 1) + (j & 3) + 8 * (j >> 2);
  return 18 * (4 * (4 * (step - 16) + g) + (j >> 1)) + 16 + (j & 1);
}

constexpr int SC1 = 16;
constexpr unsigned DROP = 0x80000000u;
#define CODE_PF(dst, first, last, extra, blk, stride)                                                                              \
  do {                                                                                                                     \
    uint64_t pc_; int b_, e_;                                                                                              \
    asm volatile("s_getpc_b64 %0\n.Lpf%=:\n\ts_mov_b32 %1, " #first "-.Lpf%=\n\ts_mov_b32 %2, " #last "-.Lpf%=" : "=s"(pc_), "=s"(b_), "=s"(e_)); \
    const unsigned o_ = (blk) * 8192u + (threadIdx.x & 63) * (stride), n_ = (unsigned)(e_ - b_) + (extra) - 4u;                \
    dst = gld4((const char*)(pc_ + b_), o_ < n_ ? o_ : n_);                                                                \
  } while (0)
#define G_MFMA(d, a, b) asm volatile("v_mfma_f32_16x16x32_bf16 %0, %1, %2, %0" : "+v"(d) : "v"(a), "v"(b))
#define G_MFMA0(d, a, b) asm volatile("v_mfma_f32_16x16x32_bf16 %0, %1, %2, 0" : "=&v"(d) : "v"(a), "v"(b))
template <int AUX> DI void bst16(u32x4 v, __amdgpu_buffer_rsrc_t rs, unsigned voff, unsigned soff) {
  __builtin_amdgcn_raw_buffer_store_b128(v, rs, voff, soff, AUX);
  asm volatile("s_nop 1" ::"v"(v) : "memory");
}
DI __amdgpu_buffer_rsrc_t mkrsrc(const void* p, unsigned bytes) {
  return __builtin_amdgcn_make_buffer_rsrc((void*)p, 0, bytes, 0x00020000);
}

typedef __attribute__((address_space(1))) unsigned gu32;
DI void wait_count(gu32* cnt, unsigned first, unsigned want) {
  if (rfl(first) >= want) return;
  for (unsigned spin = 0; spin < (1u << 16); ++spin) {
    if (rfl(__hip_atomic_load(cnt, __ATOMIC_RELAXED, __HIP_MEMORY_SCOPE_AGENT)) >= want) break;
    __builtin_amdgcn_s_sleep(1);
  }
}
constexpr int NJ_W1 = 72 * 36, NJ_WD = 9 * 144, NJ_WO = 9 * 18, NJ_ALL = NJ_W1 + NJ_WD + NJ_WO;
struct PrepJob { const char* src; unsigned rs, c1, c2, voff, dst[4]; };
DI void prep_setup(PrepJob& j, int J, int lane, const u16* wo, const u16* wg, const u16* wu, const u16* wd) {
  if (J >= NJ_ALL) J = NJ_ALL - 1;
  const int g = (lane >> 2) & 3;
  unsigned lrow = 8 * g;
  j.c1 = 0; j.c2 = 0;
  if (J < NJ_W1) {
    const int sm = J / 72, tg = J - sm * 72;
    j.src = (const char*)((sm & 1) ? wu : wg) + (size_t)(sm >> 1) * (32 * FF * 2) + tg * 128;
    j.rs = FF * 2;
#pragma unroll
    for (int t = 0; t < 4; ++t) j.dst[t] = (unsigned)WS_W1 + (4 * tg + t) * 36864 + sm * 1024;
  } else if (J < NJ_W1 + NJ_WD) {
    const int q = J - NJ_W1, cc = q / 9, tg = q - cc * 9;
    j.src = (const char*)wd + (size_t)cc * (32 * DM * 2) + tg * 128;
    j.rs = DM * 2;
#pragma unroll
    for (int t = 0; t < 4; ++t) { const int nt = 4 * tg + t, nb = nt / 9; j.dst[t] = (unsigned)WS_WD + ((nb * 144 + cc) * 9 + (nt - nb * 9)) * 1024; }
  } else {
    const int q = J - NJ_W1 - NJ_WD, step = q / 9, tg = q - step * 9;
    j.rs = DM * 2;
    if (step < 16) { j.src = (const char*)wo + (size_t)step * (36 * DM * 2) + tg * 128; lrow = 18 * (g >> 1) + 4 * (g & 1); j.c2 = 4; }
    else { j.src = (const char*)wo + (size_t)(288 * (step - 16) + 16) * (DM * 2) + tg * 128; lrow = 72 * g; j.c1 = 16; }
#pragma unroll
    for (int t = 0; t < 4; ++t) j.dst[t] = (unsigned)WS_WO + (step * 36 + 4 * tg + t) * 1024;
  }
#pragma unroll
  for (int t = 0; t < 4; ++t) j.dst[t] = rfl(j.dst[t]);
  j.voff = lrow * j.rs + (lane >> 4) * 32 + (lane & 3) * 8;
}
template <int JJ>
DI void prep_load(const PrepJob& j, u32x2 (&r)[8]) {
  const char* b = j.src + (JJ + j.c1 * (JJ >> 1) + j.c2 * (JJ >> 2)) * j.rs;
  asm volatile("global_load_dwordx2 %0, %1, %2" : "=v"(r[JJ]) : "v"(j.voff), "s"(b));
}
DI void prep_load_all(const PrepJob& j, u32x2 (&r)[8]) {
  prep_load<0>(j, r); prep_load<1>(j, r); prep_load<2>(j, r); prep_load<3>(j, r); prep_load<4>(j, r); prep_load<5>(j, r); prep_load<6>(j, r); prep_load<7>(j, r);
}
DI void prep_store(const PrepJob& j, const u32x2 (&r)[8], __amdgpu_buffer_rsrc_t rs, unsigned so) {
  u32x4 u[4];
#pragma unroll
  for (int i = 0; i < 4; ++i) {
    u[0][i] = __builtin_amdgcn_perm(r[2 * i + 1][0], r[2 * i][0], 0x05040100u);
    u[1][i] = __builtin_amdgcn_perm(r[2 * i + 1][0], r[2 * i][0], 0x07060302u);
    u[2][i] = __builtin_amdgcn_perm(r[2 * i + 1][1], r[2 * i][1], 0x05040100u);
    u[3][i] = __builtin_amdgcn_perm(r[2 * i + 1][1], r[2 * i][1], 0x07060302u);
  }
#pragma unroll
  for (int i = 0; i < 4; ++i) {
    const auto a0 = __builtin_amdgcn_permlane32_swap(u[0][i], u[2][i], false, false);
    const auto a1 = __builtin_amdgcn_permlane32_swap(u[1][i], u[3][i], false, false);
    const auto b0 = __builtin_amdgcn_permlane16_swap(a0[0], a1[0], false, false);
    const auto b1 = __builtin_amdgcn_permlane16_swap(a0[1], a1[1], false, false);
    u[0][i] = b0[0]; u[1][i] = b0[1]; u[2][i] = b1[0]; u[3][i] = b1[1];
  }
#pragma unroll
  for (int t = 0; t < 4; ++t) bst16<SC1>(u[t], rs, so, j.dst[t]);
}
#define PIN8(r) "+v"(r[0]), "+v"(r[1]), "+v"(r[2]), "+v"(r[3]), "+v"(r[4]), "+v"(r[5]), "+v"(r[6]), "+v"(r[7])

constexpr int K1_PF = 4;
constexpr int K1_PITCH = 224, K1_SLOT = 16384 + 64 * K1_PITCH  , K1_NS = 4;
constexpr int K1_SP = 232;
constexpr int K1_STG = K1_NS * K1_SLOT, K1_CS = K1_STG + 128 * K1_SP + 64, K1_ST = K1_CS + 512 + 64, K1_LDS = K1_ST + 2048 + 256;
struct K1C {
  LDSP const char *xa[2], *wl, *wc0, *wc1;
  u32x4 X[3][2], wf[K1_PF + 1], ones;
  f32x4 acc[4][2];
  f32x4 sm[2], sq[2];
};
DI void k1_loader(const char* xb, const char* wb, const u16* wo_, char* ws, int j, int lane, unsigned lds0) {
  __builtin_amdgcn_s_setprio(3);
  unsigned xo[2], wo[4];
  const int g = lane >> 4;
  xo[0] = (lane >> 3) * 1152 + (((lane & 7) ^ g) * 16);
  xo[1] = (8 + (lane >> 3)) * 1152 + (((lane & 7) ^ (4 + g)) * 16);
  xb += j * (32 * 1152);
  const unsigned xd = lds0 + j * 4096, wd = lds0 + 16384 + j * 1024;
#define K1_X(z, so) do { dma16(xb + (z) * 128, xo[0], xd + (so)); dma16(xb + (z) * 128, xo[1], xd + (so) + 1024);                    \
    dma16(xb + (z) * 128 + 16 * 1152, xo[0], xd + (so) + 2048); dma16(xb + (z) * 128 + 16 * 1152, xo[1], xd + (so) + 3072); } while (0)
#define K1_W(z, so) do { _Pragma("unroll") for (int i = 0; i < 3; ++i) dma16(wb + (z) * (64 * 3456), wo[i], wd + (so) + i * 4096);     \
    if (j < 2) dma16(wb + (z) * (64 * 3456), wo[3], wd + (so) + 3 * 4096); } while (0)
  K1_X(0, 0);
  SBAR();
#pragma unroll
  for (int i = 0; i < 4; ++i) {
    const unsigned u = 64 * (j + 4 * i) + lane, r = u / 14;
    wo[i] = ((r & ~12u) | ((r & 4u) << 1) | ((r & 8u) >> 1)) * 3456 + (u - r * 14) * 16;
  }
  K1_W(0, 0);
  K1_X(1, K1_SLOT); K1_W(1, K1_SLOT);
  unsigned so = 2 * K1_SLOT;
#pragma unroll 1
  for (int z = 2; z < 9; ++z) {
    asm volatile("s_waitcnt vmcnt(7)\n\ts_barrier" ::: "memory");
    K1_X(z, so); K1_W(z, so);
    so = so == 3 * K1_SLOT ? 0 : so + K1_SLOT;
  }
  asm volatile("s_waitcnt vmcnt(7)\n\ts_barrier" ::: "memory");
  const int b = blockIdx.x;
  const bool dojob = j == 1 && b < NJ_WO, dorow = j >= 2 || (j == 0 && b < 64);
  const int br = j >= 2 ? 2 * b + j - 2 : 512 + (b & 63);
  PrepJob jb; u32x2 r[8]; unsigned wr[5];
  if (dojob) prep_setup(jb, NJ_W1 + NJ_WD + b, lane, wo_, wo_, wo_, wo_);
  const char* rp = (const char*)wo_ + (size_t)wo_row(br >> 5, (br >> 3) & 3, br & 7) * (DM * 2);
  SBAR();
  asm volatile("s_waitcnt vmcnt(0)\n\ts_barrier" ::: "memory");
  if (dojob) prep_load_all(jb, r);
  if (dorow) {
#pragma unroll
    for (int i = 0; i < 5; ++i) wr[i] = gld4(rp, (i < 4 ? lane + 64 * i : 256 + (lane & 31)) * 4);
  }
  asm volatile("s_barrier");
  asm volatile("s_barrier");
  const __amdgpu_buffer_rsrc_t wrs = mkrsrc(ws, (unsigned)WS_END);
  asm volatile("s_waitcnt vmcnt(0)" : PIN8(r), "+v"(wr[0]), "+v"(wr[1]), "+v"(wr[2]), "+v"(wr[3]), "+v"(wr[4])::"memory");
  if (dojob) {
#pragma unroll
    for (int t = 0; t < 4; ++t) jb.dst[t] = rfl(jb.dst[t]);
    prep_store(jb, r, wrs, ((lane >> 2) & 3) * 256 + ((lane & 3) * 4 + (lane >> 4)) * 16);
  }
  if (dorow) {
    float a = 0.f;
#pragma unroll
    for (int i = 0; i < 4; ++i) a = dot2(wr[i], 0x3f803f80u, a);
    a = dot2(lane >= 32 ? 0u : wr[4], 0x3f803f80u, a);
    a = xor16_sum(xor32_sum(a));
    a += __uint_as_float(__builtin_amdgcn_update_dpp(0u, __float_as_uint(a), 0x128, 0xf, 0xf, false));
    a += __uint_as_float(__builtin_amdgcn_update_dpp(0u, __float_as_uint(a), 0x124, 0xf, 0xf, false));
    a += __uint_as_float(__builtin_amdgcn_update_dpp(0u, __float_as_uint(a), 0x122, 0xf, 0xf, false));
    a += __uint_as_float(__builtin_amdgcn_update_dpp(0u, __float_as_uint(a), 0x121, 0xf, 0xf, false));
    a *= (1.0f / DM);
    const unsigned hi = cvtpk(a, 0.f) & 0xffffu;
    const unsigned lo = cvtpk(a - bflo(hi), 0.f) & 0xffffu;
    __builtin_amdgcn_raw_buffer_store_b16((u16)(lane ? lo : hi), wrs, lane < 2 ? (unsigned)WS_WBAR + (lane * DM + br) * 2 : DROP, 0, SC1);
  }
#undef K1_X
#undef K1_W
}
DI u32x4 tr_frag(LDSP const char* p, int d) {
  s16x4 lo = __builtin_amdgcn_ds_read_tr16_b64_v4i16((LDSP s16x4*)p);
  s16x4 hi = __builtin_amdgcn_ds_read_tr16_b64_v4i16((LDSP s16x4*)(p + d));
  return __builtin_bit_cast(u32x4, __builtin_shufflevector(lo, hi, 0, 1, 2, 3, 4, 5, 6, 7));
}
template <int NHG, int F> DI void k1_read(K1C& c) {
  constexpr int NF = 4 + NHG, S = F / NF, j = F % NF, Z = S >> 1, so = (Z % K1_NS) * K1_SLOT;
  if constexpr (j == 0) {
    c.X[S % 3][0] = ldsr16(c.xa[S & 1] + so);
    c.X[S % 3][1] = ldsr16(c.xa[S & 1] + so + 2048);
  }
  constexpr int wo = so + (S & 1) * 7168;
  if constexpr (NHG == 0 || j < 3) c.wf[F % (K1_PF + 1)] = tr_frag(c.wl + wo + j * 32, 1792);
  else c.wf[F % (K1_PF + 1)] = tr_frag((j == 3 ? c.wc0 : c.wc1) + wo, 1792);
}
template <int NHG, int F> DI void k1_frag(K1C& c) {
  constexpr int NF = 4 + NHG, S = F / NF, j = F % NF, N = F + K1_PF;
  if constexpr (S == 0) {
    if constexpr (NHG == 0 || j < 3) { G_MFMA0(c.acc[j][0], c.wf[F % (K1_PF + 1)], c.X[0][0]); G_MFMA0(c.acc[j][1], c.wf[F % (K1_PF + 1)], c.X[0][1]); }
    else G_MFMA0(c.acc[3][j - 3], c.wf[F % (K1_PF + 1)], c.ones);
  } else {
    if constexpr (NHG == 0 || j < 3) { G_MFMA(c.acc[j][0], c.wf[F % (K1_PF + 1)], c.X[S % 3][0]); G_MFMA(c.acc[j][1], c.wf[F % (K1_PF + 1)], c.X[S % 3][1]); }
    else G_MFMA(c.acc[3][j - 3], c.wf[F % (K1_PF + 1)], c.ones);
  }
  if constexpr ((S & 1) == NHG && j < 4) {
    constexpr int t = j >> 1;
    if constexpr (j & 1) { if constexpr (S < 2) G_MFMA0(c.sq[t], c.X[S % 3][t], c.X[S % 3][t]); else G_MFMA(c.sq[t], c.X[S % 3][t], c.X[S % 3][t]); }
    else { if constexpr (S < 2) G_MFMA0(c.sm[t], c.ones, c.X[S % 3][t]); else G_MFMA(c.sm[t], c.ones, c.X[S % 3][t]); }
  }
  if constexpr (N < 18 * NF) {
    if constexpr (N % (2 * NF) == 0) asm volatile("s_barrier" ::: "memory");
    k1_read<NHG, N>(c);
  }
  SBAR();
}
template <int NHG, int... Fs> DI void k1_run(K1C& c, std::integer_sequence<int, Fs...>) {
  k1_read<NHG, 0>(c); k1_read<NHG, 1>(c); k1_read<NHG, 2>(c); k1_read<NHG, 3>(c);
  SBAR();
  (k1_frag<NHG, Fs>(c), ...);
  asm volatile("s_nop 7" : "+v"(c.acc[0][0]), "+v"(c.acc[0][1]), "+v"(c.acc[1][0]), "+v"(c.acc[1][1]), "+v"(c.acc[2][0]), "+v"(c.acc[2][1]),
               "+v"(c.acc[3][0]), "+v"(c.acc[3][1]), "+v"(c.sm[0]), "+v"(c.sm[1]), "+v"(c.sq[0]), "+v"(c.sq[1]));
  SBAR();
}
extern "C" __global__ __launch_bounds__(768, 3) void k_qkv(const u16* x, const u16* wqkv, const u16* wo, char* ws, const u16* wg, const u16* wu, const u16* wd, char* out) {
  extern __shared__ __attribute__((aligned(16))) char smem[];
  const int tid = threadIdx.x, lane = tid & 63, w = rfl(tid >> 6);
  const int xcd = blockIdx.x & 7, bi = blockIdx.x >> 3;
  unsigned pf;
  CODE_PF(pf, k_qkv, k_end, 0, bi, (w == 8 && bi < 9) ? 128u : 0u);
  SBAR();
  asm volatile("" : "+s"(x), "+s"(wqkv), "+s"(wo), "+s"(ws));
  LDSP char* lds = (LDSP char*)smem;
  const unsigned lds0 = (unsigned)(uintptr_t)lds;
  const int rb = 4 * (xcd >> 1) + (bi & 3), cb = 8 * (xcd & 1) + (bi >> 2);
  const int c0 = 108 * cb - 4 * (cb & 1);
  if (__builtin_expect(w >= 8, 1)) {
    k1_loader((const char*)x + rb * (128 * 1152), (const char*)wqkv + c0 * 2, wo, ws, w - 8, lane, lds0);
    asm volatile("" ::"v"(pf));
    if (blockIdx.x == 0 && tid == 512) {
      g_ptr[0] = (unsigned long long)x; g_ptr[1] = (unsigned long long)wg; g_ptr[2] = (unsigned long long)wu; g_ptr[3] = (unsigned long long)wd;
      g_ptr[4] = (unsigned long long)ws; g_ptr[5] = (unsigned long long)out;
    }
    return;
  }
  const int m = lane & 15, g = lane >> 4, mg = w & 3, nh = w >> 2;
  STAMP_DECL; STAMP(0);
  K1C c;
  c.ones = u32x4{0x3f803f80u, 0x3f803f80u, 0x3f803f80u, 0x3f803f80u};
  PIN(c.ones);
  c.xa[0] = lds + (32 * mg + m) * 128 + ((g ^ (m >> 1)) * 16);
  c.xa[1] = lds + (32 * mg + m) * 128 + (((4 + g) ^ (m >> 1)) * 16);
  LDSP const char* wbase = lds + 16384 + (16 * (g >> 1) + 4 * (g & 1) + (m >> 2)) * K1_PITCH + (m & 3) * 8;
  c.wl = wbase + nh * 128;
  c.wc0 = wbase + mg * 32;
  c.wc1 = wbase + (mg < 3 ? mg + 4 : 6) * 32;
  const int off = 4 * (cb & 1), T0 = 128 * rb;
  LDSP char* stg = lds + K1_STG;
  LDSP char* a_st = lds + K1_ST + (32 * mg + m) * 8;                 PIN(a_st);
  LDSP char* a_sd = g == (m >> 2) ? a_st + nh * 1024 : lds + K1_ST + 2048;   PIN(a_sd);
  LDSP char* a_cs = lds + K1_CS + (64 * nh + 4 * g) * 4;             PIN(a_cs);
  LDSP char* a_sw = stg + (32 * mg + m) * K1_SP + (64 * nh + 4 * g) * 2;   PIN(a_sw);
  LDSP char* a_tr = stg + (32 * mg + 4 * (g & 1) + (m >> 2)) * K1_SP + (64 * nh + 16 * (g >> 1) + 4 * (m & 3)) * 2;   PIN(a_tr);
  float qs[4];
#pragma unroll
  for (int j = 0; j < 4; ++j) { qs[j] = (c0 + 64 * nh + 16 * j + 4 * g < DM) ? QSCALE : 1.0f; PIN(qs[j]); }
  const __amdgpu_buffer_rsrc_t wrs = mkrsrc(ws, (unsigned)WS_END);
  const unsigned kvo = (unsigned)WS_KV + (T0 >> 9) * (unsigned)CHUNK_B;
  unsigned vo[2] = {0, 0}, vone = 0, vd[4] = {DROP, DROP, DROP, DROP};
  if (cb >= 10) {
#pragma unroll
  for (int tt = 0; tt < 2; ++tt) {
    const int cc = 64 * nh + 32 * tt + 16 * (g >> 1) + m - off, hd = (cc + 18) / 18 - 1;
    vo[tt] = (cc >= 0 && cc < 108) ? hd * 4864 + 2 * mg * 608 + (g & 1) * 304 + (cc - 18 * hd) * 16 : 29184;
  }
  vone = tid < 96 ? (tid >> 4) * 4864 + (tid & 15) * 304 + 288 : 29184;
#pragma unroll
  for (int i = 0; i < 4; ++i) {
    const unsigned u = i * 512 + tid, hd = u / 304;
    vd[i] = (u < 1824 && 6 * cb + hd >= 64) ? kvo + 18432 + ((T0 >> 4) & 31) * 608 + (6 * cb + hd - 64) * (unsigned)KV_HEAD_B + (u - hd * 304) * 16 : DROP;
  }
  }
  PIN(vone); PIN(vo[0]); PIN(vo[1]); PIN(vd[0]); PIN(vd[1]); PIN(vd[2]); PIN(vd[3]);
  const int tok = tid & 127, T = T0 + tok;
  LDSP const char* a_g = stg + tok * K1_SP + off * 2;   PIN(a_g);
  unsigned um[2], ut[2];
  const unsigned tl = ((T >> 5) & 15) * 1024 + (T & 31) * 16;
#pragma unroll
  for (int it = 0; it < 2; ++it) {
    const int hd = (w >> 1) + 4 * it, G = 6 * cb + hd, head = G & 31;
    if (hd >= 6 || G >= 64) { um[it] = DROP; ut[it] = DROP; }
    else if (G < 32) { um[it] = (unsigned)WS_QM + head * 65536 + (T >> 9) * 16384 + tl; ut[it] = (unsigned)WS_QT + head * 8192 + T * 4; }
    else { um[it] = kvo + head * (unsigned)KV_HEAD_B + tl; ut[it] = kvo + head * (unsigned)KV_HEAD_B + 16384 + ((T >> 6) & 7) * 256 + (T & 31) * 8 + ((T >> 5) & 1) * 4; }
    PIN(um[it]); PIN(ut[it]);
  }
  STAMP(1);
  asm volatile("s_waitcnt vmcnt(0)" : "+v"(pf));
  if (blockIdx.x < 5 && w == 7) bst16<SC1>(u32x4{0, 0, 0, 0}, wrs, (unsigned)WS_CNT + (blockIdx.x * 64 + lane) * 128, 0);
  asm volatile("s_barrier" ::: "memory");
  STAMP(2);
  if (nh) k1_run<1>(c, std::make_integer_sequence<int, 90>{});
  else k1_run<0>(c, std::make_integer_sequence<int, 72>{});
  STAMP(3);
#pragma unroll
  for (int t = 0; t < 2; ++t) {
    const float lo = (m & 1) ? c.sq[t][1] : c.sq[t][0], hi = (m & 1) ? c.sq[t][3] : c.sq[t][2];
    *(LDSP f32x2*)(a_sd + t * 128) = f32x2{c.sm[t][0], (m & 2) ? hi : lo};
  }
  if (nh) {
    *(LDSP f32x4*)(lds + K1_CS + (16 * mg + 4 * g) * 4) = c.acc[3][0];
    *(LDSP f32x4*)(lds + K1_CS + (16 * (mg + 4) + 4 * g) * 4) = c.acc[3][1];
  }
  asm volatile("s_waitcnt lgkmcnt(0)\n\ts_barrier" ::: "memory");
  {
    f32x2 p[2][2]; f32x4 cc[4];
#pragma unroll
    for (int t = 0; t < 2; ++t) { p[t][0] = *(LDSP const f32x2*)(a_st + t * 128); p[t][1] = *(LDSP const f32x2*)(a_st + 1024 + t * 128); }
#pragma unroll
    for (int j = 0; j < 4; ++j) cc[j] = *(LDSP const f32x4*)(a_cs + j * 64);
    SBAR();
#pragma unroll
    for (int t = 0; t < 2; ++t) {
      const float mean = (p[t][0][0] + p[t][1][0]) * (1.0f / DM);
      const float var = fmaxf((p[t][0][1] + p[t][1][1]) * (1.0f / DM) - mean * mean, 0.f);
      const float rstd = __builtin_amdgcn_rsqf(var + LN_EPS);
      if (cb == 0 && nh == 0) __builtin_amdgcn_raw_buffer_store_b32(__float_as_uint(mean), wrs, (unsigned)WS_XMEAN + (T0 + 32 * mg + 16 * t + m) * 4, 0, SC1);
#pragma unroll
      for (int j = 0; j < 4; ++j) {
        if (j < 3 || nh == 0) {
          const f32x4 a = (c.acc[j][t] - mean * cc[j]) * (rstd * qs[j]);
          *(LDSP u32x2*)(a_sw + t * (16 * K1_SP) + j * 32) = u32x2{cvtpk(a[0], a[1]), cvtpk(a[2], a[3])};
        }
      }
    }
  }
  if (cb >= 10) {
    asm volatile("s_waitcnt lgkmcnt(0)" ::: "memory");
    u32x4 v[2][2];
#pragma unroll
    for (int tt = 0; tt < 2; ++tt)
#pragma unroll
      for (int t = 0; t < 2; ++t) v[tt][t] = tr_frag(a_tr + t * (16 * K1_SP) + tt * 64, 8 * K1_SP);
    *(LDSP u32x4*)(lds + vone) = c.ones;
#pragma unroll
    for (int tt = 0; tt < 2; ++tt)
#pragma unroll
      for (int t = 0; t < 2; ++t) *(LDSP u32x4*)(lds + vo[tt] + t * 608) = v[tt][t];
  }
  STAMP(4);
  asm volatile("s_waitcnt lgkmcnt(0)\n\ts_barrier" ::: "memory");
  if (cb >= 10) {
    u32x4 v[4];
#pragma unroll
    for (int i = 0; i < 4; ++i) v[i] = ldsr16(lds + i * 8192 + tid * 16);
#pragma unroll
    for (int i = 0; i < 4; ++i) if (__builtin_amdgcn_ballot_w64(vd[i] != DROP)) bst16<SC1>(v[i], wrs, vd[i], 0);
  }
  if (cb <= 10) {
    unsigned d[2][9];
#pragma unroll
    for (int it = 0; it < 2; ++it) {
      const int hd = (w >> 1) + 4 * it;
      LDSP const unsigned* sp = (LDSP const unsigned*)(a_g + 36 * (hd < 6 ? hd : 0));
#pragma unroll
      for (int i = 0; i < 9; ++i) d[it][i] = sp[i];
    }
    SBAR();
#pragma unroll
    for (int it = 0; it < 2; ++it) {
      if ((w >> 1) + 4 * it >= 6 || 6 * cb + (w >> 1) + 4 * it >= 64) continue;
      bst16<SC1>(u32x4{d[it][0], d[it][1], d[it][2], d[it][3]}, wrs, um[it], 0);
      bst16<SC1>(u32x4{d[it][4], d[it][5], d[it][6], d[it][7]}, wrs, um[it] + 512, 0);
      __builtin_amdgcn_raw_buffer_store_b32(d[it][8], wrs, ut[it], 0, SC1);
    }
#pragma unroll
    for (int it = 0; it < 2; ++it) {
      const int hd = (w >> 1) + 4 * it, G = 6 * cb + hd;
      if (hd < 6 && G >= 32 && G < 64) {
        float n2 = 0.f;
#pragma unroll
        for (int i = 0; i < 9; ++i) n2 = dot2(d[it][i], d[it][i], n2);
        unsigned nb = __float_as_uint(n2);
        { auto r = __builtin_amdgcn_permlane32_swap(nb, nb, false, false); nb = max(r[0], r[1]); }
        { auto r = __builtin_amdgcn_permlane16_swap(nb, nb, false, false); nb = max(r[0], r[1]); }
        __builtin_amdgcn_raw_buffer_store_b32(nb, wrs, (unsigned)WS_KMAX + (((2 * rb + (w & 1)) * 32 + (G & 31)) * 16 + m) * 4, 0, SC1);
      }
    }
  }
  STAMP(5); STAMP_OUT(1, 6);
}

constexpr int K2_VOTE = (int)KV_HEAD_B, K2_LDS = K2_VOTE + 64, K3_WBAR = 90 * 1024, K3_XC = 96 * 1024, K3_REP = 16;
struct AttnC {
  LDSP const char *kmb, *ktb, *vb;
  unsigned kma[3], kta[3], va[3];
  u32x4 qm, qt, kf, v0, v1, p1, ktl;
  f32x16 negm, o, sa, sb;
};
constexpr int k_off(int T) { return (T >> 4) * (int)CHUNK_B + (T & 15) * 1024; }
constexpr int kt_off(int T) { return (T >> 4) * (int)CHUNK_B + ((T >> 1) & 7) * 256; }
constexpr int v_off(int T) { return (T >> 4) * (int)CHUNK_B + (T & 15) * 1216; }
#define AT_E(r) "v_exp_f32 v" #r ", v" #r "\n\t"
#define AT_C(d, a, b) "v_cvt_pk_bf16_f32 v" #d ", v" #a ", v" #b "\n\t"
#define AT_TAIL(sn, kt, qt) "v_mfma_f32_32x32x8_bf16 " sn ", " kt ", " qt ", v[18:33]\n\t"
#define AT_MAIN(sn) "v_mfma_f32_32x32x16_bf16 " sn ", v[66:69], v[98:101], " sn "\n\t"
#define AT_PV1 "v_mfma_f32_32x32x16_bf16 v[2:17], v[84:87], v[70:73], v[2:17]\n\t"
#define AT_PV0 "v_mfma_f32_32x32x16_bf16 v[2:17], v[88:91], v[74:77], v[2:17]\n\t"
#define AT_R1 "ds_read_b128 v[84:87], %[va] offset:%[vo1]\n\t"
#define AT_RK "ds_read_b128 v[66:69], %[ka] offset:%[ko]\n\t"
#define AT_R0 "ds_read_b128 v[88:91], %[vb] offset:%[vo0]\n\t"
#define AT_RT(kt) "ds_read_b64 " kt ", %[kt] offset:%[kto]\n\t"
#define AT_BODY(TL, P1, MN, RD, s0, s1, s2, s3, s4, s5, s6, s7, s8, s9, s10, s11, s12, s13, s14, s15)                                   \
  TL AT_E(s0) AT_E(s1) AT_E(s2) AT_E(s3) "s_waitcnt lgkmcnt(0)\n\t" P1 AT_C(74, s0, s1) AT_E(s4) AT_C(75, s2, s3) AT_E(s5) AT_E(s6) AT_E(s7) \
  MN AT_C(76, s4, s5) AT_E(s8) AT_C(77, s6, s7) AT_E(s9) AT_E(s10) AT_E(s11) AT_PV0 RD AT_C(70, s8, s9) AT_E(s12) AT_C(71, s10, s11)          \
  AT_E(s13) AT_E(s14) AT_E(s15) AT_C(72, s12, s13) AT_C(73, s14, s15)
#define AT_EVEN(TL, P1, MN, RD) AT_BODY(TL, P1, MN, RD, 34, 35, 36, 37, 38, 39, 40, 41, 42, 43, 44, 45, 46, 47, 48, 49)
#define AT_ODD(TL, P1, MN, RD) AT_BODY(TL, P1, MN, RD, 50, 51, 52, 53, 54, 55, 56, 57, 58, 59, 60, 61, 62, 63, 64, 65)
#define AT_STMT(TXT)                                                                                                              \
  asm volatile(TXT : "+{v[2:17]}"(c.o), "+{v[34:49]}"(c.sa), "+{v[50:65]}"(c.sb), "+{v[66:69]}"(c.kf), "+{v[70:73]}"(c.p1),        \
               "+{v[84:87]}"(c.v1), "+{v[88:91]}"(c.v0), "+{v[92:95]}"(c.ktl)                                                      \
               : "{v[18:33]}"(c.negm), "{v[98:101]}"(c.qm), "{v[102:105]}"(c.qt), [ka] "v"(c.kma[KO >> 16]), [kt] "v"(c.kta[KT >> 16]), \
                 [va] "v"(c.va[V1 >> 16]), [vb] "v"(c.va[V0 >> 16]), [ko] "i"(KO & 0xffff), [kto] "i"(KT & 0xffff), [vo1] "i"(V1 & 0xffff),  \
                 [vo0] "i"(V0 & 0xffff)                                                                                            \
               : "v74", "v75", "v76", "v77")
template <int T> DI void attn_tile(AttnC& c) {
  constexpr int KO = k_off(T < 62 ? T + 2 : 0), KT = kt_off(T < 61 ? T + 3 : 0), V1 = v_off(T) + 608, V0 = v_off(T < 63 ? T + 1 : 0);
  if constexpr (T == 0) AT_STMT(AT_EVEN(AT_TAIL("v[50:65]", "v[92:93]", "v[104:105]"), "", AT_MAIN("v[50:65]"), AT_R1 AT_RK AT_R0));
  else if constexpr (T == 61) AT_STMT(AT_ODD(AT_TAIL("v[34:49]", "v[94:95]", "v[102:103]"), AT_PV1, AT_MAIN("v[34:49]"), AT_R1 AT_RK AT_R0));
  else if constexpr (T == 62) AT_STMT(AT_EVEN(AT_TAIL("v[50:65]", "v[94:95]", "v[104:105]"), AT_PV1, AT_MAIN("v[50:65]"), AT_R1 AT_R0));
  else if constexpr (T == 63) AT_STMT(AT_ODD("", AT_PV1, "", AT_R1));
  else if constexpr ((T & 3) == 0) AT_STMT(AT_EVEN(AT_TAIL("v[50:65]", "v[92:93]", "v[104:105]"), AT_PV1, AT_MAIN("v[50:65]"), AT_R1 AT_RK AT_R0));
  else if constexpr ((T & 3) == 1) AT_STMT(AT_ODD(AT_TAIL("v[34:49]", "v[94:95]", "v[102:103]"), AT_PV1, AT_MAIN("v[34:49]"), AT_R1 AT_RK AT_R0 AT_RT("v[92:93]")));
  else if constexpr ((T & 3) == 2) AT_STMT(AT_EVEN(AT_TAIL("v[50:65]", "v[94:95]", "v[104:105]"), AT_PV1, AT_MAIN("v[50:65]"), AT_R1 AT_RK AT_R0));
  else AT_STMT(AT_ODD(AT_TAIL("v[34:49]", "v[92:93]", "v[102:103]"), AT_PV1, AT_MAIN("v[34:49]"), AT_R1 AT_RK AT_R0 AT_RT("v[94:95]")));
}
DI void attn_group_slow(LDSP const char* km, LDSP const char* kt, LDSP const char* vp, const u32x4& qm, unsigned qtl, const f32x16& negm, f32x16& o) {
#pragma unroll
  for (int t = 0; t < 4; ++t) {
    const u32x2 k2 = *(LDSP const u32x2*)(kt + (t >> 1) * 256);
    const u32x4 ktl = {k2[0], k2[1], 0, 0};
    const u32x4 kf = ldsr16(km + t * 1024);
    const u32x4 v0 = ldsr16(vp + t * 1216), v1 = ldsr16(vp + t * 1216 + 608);
    u32x4 qq = {0, 0, 0, 0}; qq[t & 1] = qtl;
    f32x16 s = mfma32(ktl, qq, negm);
    s = mfma32(kf, qm, s);
    u32x4 p0, p1;
#pragma unroll
    for (int i = 0; i < 4; ++i) {
      p0[i] = cvtpk(__builtin_amdgcn_exp2f(s[2 * i]), __builtin_amdgcn_exp2f(s[2 * i + 1]));
      p1[i] = cvtpk(__builtin_amdgcn_exp2f(s[8 + 2 * i]), __builtin_amdgcn_exp2f(s[8 + 2 * i + 1]));
    }
    o = mfma32(v0, p0, o);
    o = mfma32(v1, p1, o);
  }
}
constexpr float L_MIN = 5.421011e-20f;
DI unsigned k3_src(int p, int cg) {
  const int st = (p * 205) >> 10, t = p - 5 * st;
  return p < 90 ? (unsigned)WS_WO + (st * 36 + 9 * (cg >> 1) + 5 * (cg & 1) + min(t, 4 - (cg & 1))) * 1024 : (unsigned)WS_WBAR + (p - 90) * 1024;
}
template <int G> DI void attn_group(AttnC& c) {
  __builtin_amdgcn_s_setprio(G < 5 ? 3 - (G & 3) : (G < 8 ? 3 : (G < 11 ? 2 : (G < 14 ? 1 : 0))));
  attn_tile<4 * G + 0>(c); attn_tile<4 * G + 1>(c); attn_tile<4 * G + 2>(c); attn_tile<4 * G + 3>(c);
}
DI void attn_helper(const u16* wg, const u16* wu, const u16* wd, char* ws, int head, int j, int lane, LDSP char* lds) {
  const unsigned lds0 = (unsigned)(uintptr_t)lds;
  __builtin_amdgcn_s_setprio(3);
  const char* kv = ws + WS_KV + (size_t)head * KV_HEAD_B;
  const unsigned lane16 = lane * 16;
#define KV_DMA(o) dma16(kv + (o), lane16, lds0 + (o))
#pragma unroll
  for (int i = 0; i < 4; ++i) KV_DMA(((i >> 1) * 16 + (i & 1) * 4 + j) * 1024);
#pragma unroll
  for (int i = 0; i < 6; ++i) { const int p = i < 2 ? 8 + 4 * i + j : min(16 + 4 * i + j, 36); KV_DMA(p * 1024); }
  const int hw = blockIdx.x * 4 + j;
  PrepJob jb[4];
#pragma unroll
  for (int i = 0; i < 4; ++i) { prep_setup(jb[i], min(4 * hw + i, NJ_W1 + NJ_WD - 1), lane, wd, wg, wu, wd); PIN(jb[i].voff); }
  volatile LDSP unsigned* vote = (volatile LDSP unsigned*)(lds + K2_VOTE);
  if (j == 0) vote[lane & 1] = 0;
  SBAR();
  asm volatile("s_waitcnt vmcnt(6) lgkmcnt(0)\n\ts_barrier" ::: "memory");
#pragma unroll
  for (int i = 0; i < 10; ++i) KV_DMA((unsigned)CHUNK_B + min(4 * i + j, 36) * 1024);
  asm volatile("s_waitcnt vmcnt(10)\n\ts_barrier" ::: "memory");
#pragma unroll
  for (int i = 0; i < 19; ++i) KV_DMA(2 * (unsigned)CHUNK_B + min(4 * i + j, 73) * 1024);
#undef KV_DMA
  u32x2 r[4][8];
  unsigned pf;
  prep_load_all(jb[0], r[0]);
  asm volatile("s_waitcnt vmcnt(27)\n\ts_barrier" ::: "memory");
  prep_load_all(jb[1], r[1]); prep_load_all(jb[2], r[2]);
  asm volatile("s_waitcnt vmcnt(24)\n\ts_barrier" ::: "memory");
  const __amdgpu_buffer_rsrc_t wrs = mkrsrc(ws, (unsigned)WS_END);
  const unsigned pso = ((lane >> 2) & 3) * 256 + ((lane & 3) * 4 + (lane >> 4)) * 16;
  prep_load_all(jb[3], r[3]);
  CODE_PF(pf, k_attn, k_end, 0, 2 * j + 1 + ((blockIdx.x >> 3) & 1), (blockIdx.x >> 3) < 2 ? 128u : 0u);
  asm volatile("s_waitcnt vmcnt(25)" : PIN8(r[0])::"memory");
  prep_store(jb[0], r[0], wrs, pso);
  asm volatile("s_waitcnt vmcnt(21)" : PIN8(r[1])::"memory");
  prep_store(jb[1], r[1], wrs, pso);
  asm volatile("s_waitcnt vmcnt(17)" : PIN8(r[2])::"memory");
  prep_store(jb[2], r[2], wrs, pso);
  asm volatile("s_waitcnt vmcnt(12)" : PIN8(r[3]), "+v"(pf)::"memory");
  prep_store(jb[3], r[3], wrs, pso);
  unsigned v = 0;
  for (unsigned spin = 0; spin < (1u << 16) && ((v = rfl(vote[0])) & 0xff) < 8; ++spin) __builtin_amdgcn_s_sleep(8);
  if (v == 8) {
#pragma unroll 1
    for (int p = j; p < 93; p += 4) dma16(ws + k3_src(p, blockIdx.x >> 5), lane16, lds0 + p * 1024);
    asm volatile("s_waitcnt vmcnt(0)" ::: "memory");
    if (lane == 0) __hip_atomic_fetch_add((LDSP unsigned*)(lds + K2_VOTE + 4), 1u, __ATOMIC_RELAXED, __HIP_MEMORY_SCOPE_WORKGROUP);
    asm volatile("s_waitcnt lgkmcnt(0)" ::: "memory");
  }
}
extern "C" __global__ __launch_bounds__(768, 3) void k_attn() {
  extern __shared__ __attribute__((aligned(16))) char smem[];
  cptr_t gp = (cptr_t)(uintptr_t)g_ptr;
  const u16 *x = (const u16*)gp[0], *wg = (const u16*)gp[1], *wu = (const u16*)gp[2], *wd = (const u16*)gp[3]; char* ws = (char*)gp[4];
  asm volatile("" : "+s"(x), "+s"(wg), "+s"(wu), "+s"(wd), "+s"(ws));
  LDSP char* lds = (LDSP char*)smem;
  const int tid = threadIdx.x, lane = tid & 63, w = rfl(tid >> 6);
  const int xcd = blockIdx.x & 7, bi = blockIdx.x >> 3;
  const int head = 4 * xcd + (bi & 3), qb = bi >> 2;
  if (__builtin_expect(w >= 8, 0)) { attn_helper(wg, wu, wd, ws, head, w - 8, lane, lds); return; }
  const int r = lane & 31, h = lane >> 5;
  const int qt32 = 8 * qb + w;
  STAMP_DECL; STAMP(0);
  AttnC c;
  c.qm = gld16(ws + WS_QM, head * 65536 + qt32 * 1024 + lane * 16);
  unsigned qtl = gld4(ws + WS_QT, head * 8192 + (32 * qt32 + r) * 4);
  u32x4 km0 = gld16(ws + WS_KMAX, ((lane >> 1) * 32 + head) * 64 + (lane & 1) * 32);
  u32x4 km1 = gld16(ws + WS_KMAX, ((lane >> 1) * 32 + head) * 64 + (lane & 1) * 32 + 16);
  const int vrow = r < 19 ? r : ((r >= 20 && r < 28) ? 0 : 18);
  c.kmb = lds + lane * 16;
  c.ktb = lds + 16384 + r * 8;
  c.vb = lds + 18432 + h * 304 + vrow * 16;
#pragma unroll
  for (int i = 0; i < 3; ++i) {
    c.kma[i] = (unsigned)(uintptr_t)c.kmb + i * 65536; c.kta[i] = (unsigned)(uintptr_t)c.ktb + i * 65536; c.va[i] = (unsigned)(uintptr_t)c.vb + i * 65536;
    PIN(c.kma[i]); PIN(c.kta[i]); PIN(c.va[i]);
  }
#pragma unroll
  for (int i = 0; i < 16; ++i) c.o[i] = 0.f;
  PIN(c.o);
  const __amdgpu_buffer_rsrc_t wrs = mkrsrc(ws, (unsigned)WS_END);
  SBAR();
  asm volatile("s_waitcnt vmcnt(0)" : "+v"(c.qm), "+v"(qtl), "+v"(km0), "+v"(km1)::"memory");
  if (h) qtl = 0;
  float q2 = dot2(qtl, qtl, 0.f);
#pragma unroll
  for (int i = 0; i < 4; ++i) q2 = dot2(c.qm[i], c.qm[i], q2);
  unsigned k2 = max(max(max(km0[0], km0[1]), max(km0[2], km0[3])), max(max(km1[0], km1[1]), max(km1[2], km1[3])));
  { auto rr = __builtin_amdgcn_permlane32_swap(k2, k2, false, false); k2 = max(rr[0], rr[1]); }
  { auto rr = __builtin_amdgcn_permlane16_swap(k2, k2, false, false); k2 = max(rr[0], rr[1]); }
  k2 = max(k2, (unsigned)__builtin_amdgcn_update_dpp(0u, k2, 0x128, 0xf, 0xf, true));
  k2 = max(k2, (unsigned)__builtin_amdgcn_update_dpp(0u, k2, 0x124, 0xf, 0xf, true));
  k2 = max(k2, (unsigned)__builtin_amdgcn_update_dpp(0u, k2, 0x122, 0xf, 0xf, true));
  k2 = max(k2, (unsigned)__builtin_amdgcn_update_dpp(0u, k2, 0x121, 0xf, 0xf, true));
  q2 = xor32_sum(q2);
  const float mshift = __builtin_amdgcn_sqrtf(q2 * __uint_as_float(k2)) * 1.001f;
  c.qt = u32x4{qtl, 0, 0, qtl}; PIN(c.qt);
#pragma unroll
  for (int i = 0; i < 16; ++i) c.negm[i] = -mshift;
  PIN(c.negm);
  STAMP(1);
  asm volatile("s_barrier" ::: "memory");
  STAMP(2);
  asm volatile("ds_read_b64 v[92:93], %[kt]\n\tds_read_b128 v[70:73], %[ka]\n\tds_read_b128 v[66:69], %[ka] offset:1024\n\t"
               "ds_read_b128 v[88:91], %[va]\n\tds_read_b64 v[94:95], %[kt] offset:256\n\ts_waitcnt lgkmcnt(3)\n\t"
               "v_mfma_f32_32x32x8_bf16 v[34:49], v[92:93], v[102:103], v[18:33]\n\t"
               "v_mfma_f32_32x32x16_bf16 v[34:49], v[70:73], v[98:101], v[34:49]\n\ts_nop 11"
               : "={v[34:49]}"(c.sa), "={v[50:65]}"(c.sb), "={v[66:69]}"(c.kf), "={v[70:73]}"(c.p1), "={v[84:87]}"(c.v1), "={v[88:91]}"(c.v0), "={v[92:95]}"(c.ktl)
               : "{v[18:33]}"(c.negm), "{v[98:101]}"(c.qm), "{v[102:105]}"(c.qt), [ka] "v"(c.kma[0]), [kt] "v"(c.kta[0]), [va] "v"(c.va[0]));
  attn_group<0>(c);
  asm volatile("s_barrier" ::: "memory");
  attn_group<1>(c); attn_group<2>(c);
  asm volatile("s_barrier" ::: "memory");
  attn_group<3>(c); attn_group<4>(c);
  asm volatile("s_barrier" ::: "memory");
  STAMP(3);
  attn_group<5>(c); attn_group<6>(c); attn_group<7>(c); attn_group<8>(c); attn_group<9>(c); attn_group<10>(c);
  attn_group<11>(c); attn_group<12>(c);
  const int m = lane & 15, g = lane >> 4, kh = w >> 2, cg = bi >> 2;
  const int nt0 = 9 * (cg >> 1) + 5 * (cg & 1), mt = 16 * xcd + 4 * (bi & 3) + (w & 3), row = 16 * mt + m;
  const bool t4 = kh == 0 && !(cg & 1);
  u32x2 xr[3];
  unsigned xm;
  {
    asm volatile("s_nop 3" : "+{v[2:17]}"(c.o));
    const unsigned inc = __any(h == 0 && !(c.o[10] >= L_MIN)) ? 0x101u : 1u;
    if (lane == 0) asm volatile("ds_add_u32 %0, %1" ::"v"((unsigned)(uintptr_t)(lds + K2_VOTE)), "v"(inc) : "memory");
    const unsigned xo = row * 1152 + (nt0 + 2 * kh) * 32 + g * 8;
    asm volatile("global_load_dwordx2 %0, %1, %2" : "=v"(xr[0]) : "v"(xo), "s"(x));
    asm volatile("global_load_dwordx2 %0, %1, %2 offset:32" : "=v"(xr[1]) : "v"(xo), "s"(x));
    asm volatile("global_load_dwordx2 %0, %1, %2" : "=v"(xr[2]) : "v"(t4 ? xo + 128 : xo), "s"(x));
    xm = gld4(ws + WS_XMEAN, row * 4);
  }
  attn_group<13>(c); attn_group<14>(c); attn_group<15>(c);
  asm volatile("s_waitcnt vmcnt(0) lgkmcnt(0)\n\tv_mfma_f32_32x32x16_bf16 v[2:17], v[84:87], v[70:73], v[2:17]\n\ts_nop 11"
               : "+{v[2:17]}"(c.o), "+v"(xr[0]), "+v"(xr[1]), "+v"(xr[2]), "+v"(xm) : "{v[84:87]}"(c.v1), "{v[70:73]}"(c.p1), "{v[18:33]}"(c.negm));
  STAMP(4);
  f32x16 o = c.o;
  float l;
  { auto rr = __builtin_amdgcn_permlane32_swap(__float_as_uint(o[10]), __float_as_uint(o[10]), false, false); l = __uint_as_float(rr[0]); }
  if (__builtin_expect(__any(!(l >= L_MIN)), 0)) {
    float mx = -INFINITY;
    f32x16 z, negm;
#pragma unroll
    for (int i = 0; i < 16; ++i) z[i] = 0.f;
#pragma unroll 1
    for (int tt = 0; tt < 64; ++tt) {
      const int ck = tt >> 4, tl = tt & 15;
      const u32x2 k2 = *(LDSP const u32x2*)(c.ktb + ck * CHUNK_B + (tl >> 1) * 256);
      const u32x4 ktl = {k2[0], k2[1], 0, 0};
      const u32x4 kf = ldsr16(c.kmb + ck * CHUNK_B + tl * 1024);
      const u32x4 qq = {(tl & 1) ? 0u : qtl, (tl & 1) ? qtl : 0u, 0, 0};
      f32x16 sc = mfma32(ktl, qq, z);
      sc = mfma32(kf, c.qm, sc);
#pragma unroll
      for (int i = 0; i < 16; ++i) mx = fmaxf(mx, sc[i]);
    }
    mx = xor32_max(mx);
#pragma unroll
    for (int i = 0; i < 16; ++i) { negm[i] = -mx; o[i] = 0.f; }
#pragma unroll 1
    for (int gq = 0; gq < 16; ++gq) {
      const int ck = gq >> 2, gg = gq & 3;
      attn_group_slow(c.kmb + ck * CHUNK_B + gg * 4096, c.ktb + ck * CHUNK_B + gg * 512, c.vb + ck * CHUNK_B + gg * 4864, c.qm, qtl, negm, o);
    }
    auto rr = __builtin_amdgcn_permlane32_swap(__float_as_uint(o[10]), __float_as_uint(o[10]), false, false);
    l = __uint_as_float(rr[0]);
  }
  const float rl = 1.0f / l;
  u32x4 ou;
#pragma unroll
  for (int i = 0; i < 4; ++i) ou[i] = cvtpk(o[2 * i] * rl, o[2 * i + 1] * rl);
  const int q = 32 * qt32 + r;
  const unsigned ob = (unsigned)WS_OFR + (q >> 4) * 18432 + (q & 15) * 16;
  bst16<SC1>(ou, wrs, ob + (head >> 1) * 1024 + (2 * (head & 1) + h) * 256, 0);
  __builtin_amdgcn_raw_buffer_store_b32(cvtpk(o[8] * rl, o[9] * rl), wrs, h ? DROP : ob + (16 + (head >> 4)) * 1024 + ((head >> 2) & 3) * 256 + (head & 3) * 4, 0, SC1);
  STAMP(5);
  const unsigned lds0 = (unsigned)(uintptr_t)lds, lane16 = lane * 16;
  asm volatile("s_waitcnt vmcnt(0)\n\ts_barrier" ::: "memory");
  if (w == 0 && lane < K3_REP) __hip_atomic_fetch_add((gu32*)(ws + WS_CNT + (128 + qb * K3_REP) * 128) + lane * 32, 1u, __ATOMIC_RELAXED, __HIP_MEMORY_SCOPE_AGENT);
  if (rfl(*(volatile LDSP unsigned*)(lds + K2_VOTE + 4)) != 4) {
#pragma unroll 1
    for (int p = w; p < 93; p += 8) dma16(ws + k3_src(p, cg), lane16, lds0 + p * 1024);
  }
  const unsigned uo = mt * 18432 + (2 * nt0 + 4 * kh + (g >> 1)) * 256 + m * 16 + (g & 1) * 8;
  if (w == 0) wait_count((gu32*)(ws + WS_CNT + (128 + xcd * K3_REP + bi % K3_REP) * 128), 0u, 32u);
  LDSP const char* wa = lds + (45 * kh + 2 * kh) * 1024 + lane16;
  LDSP const char* wb = lds + (45 * kh + 2 - 2 * kh) * 1024 + lane16;
  LDSP const char* wc = lds + (45 * kh + 4) * 1024 + lane16;
  LDSP const char* wbl = lds + K3_WBAR + (m & 1) * 1152 + kh * 576 + g * 16;
  unsigned so = ((g & 1) ? (unsigned)WS_HC : (unsigned)WS_HU) + (uo & ~8u);
  PIN(wa); PIN(wb); PIN(wc); PIN(wbl); PIN(so);
  asm volatile("s_waitcnt vmcnt(0)\n\ts_barrier" ::: "memory");
  STAMP(6);
  u32x4 O[9], wf[6];
  const char* obase = ws + WS_OFR + rfl((mt * 18 + 9 * kh) * 1024);
#pragma unroll
  for (int i = 0; i < 9; ++i)
    asm volatile("global_load_dwordx4 %0, %1, %2 sc1" : "=v"(O[i]) : "v"(lane16), "s"(obase + i * 1024));
#define K3_A(f) (((f) + 2) % 6)
#define K3_RD(f) (K3_A(f) == 5 ? ldsr16(wbl + ((f) / 6) * 64) : ldsr16((K3_A(f) < 2 ? wa : (K3_A(f) < 4 ? wb : wc)) + ((f) / 6) * 5120 + (K3_A(f) < 4 ? ((f) & 1) * 1024 : 0)))
#pragma unroll
  for (int f = 0; f < 5; ++f) wf[f] = K3_RD(f);
  f32x4 acc[6];
#pragma unroll
  for (int f = 0; f < 54; ++f) {
    if (f % 6 == 0) asm volatile("s_waitcnt vmcnt(%1)" : "+v"(O[f / 6]) : "i"(8 - f / 6));
    if (f + 5 < 54) wf[(f + 5) % 6] = K3_RD(f + 5);
    if (f < 6) G_MFMA0(acc[K3_A(f)], wf[f % 6], O[f / 6]);
    else G_MFMA(acc[K3_A(f)], wf[f % 6], O[f / 6]);
    SBAR();
  }
#undef K3_RD
#undef K3_A
  asm volatile("s_nop 7" : "+v"(acc[0]), "+v"(acc[1]), "+v"(acc[2]), "+v"(acc[3]), "+v"(acc[4]), "+v"(acc[5]));
  LDSP char* xc = lds + K3_XC + lane16;
#pragma unroll
  for (int i = 0; i < 4; ++i) *(LDSP f32x4*)(xc + w * 4096 + i * 1024) = acc[2 + i];
  asm volatile("s_waitcnt lgkmcnt(0)\n\ts_barrier" ::: "memory");
  f32x4 v[4];
#pragma unroll
  for (int i = 0; i < 4; ++i) v[i] = *(LDSP const f32x4*)(xc + (w ^ 4) * 4096 + i * 1024);
  SBAR();
  const float mean = __uint_as_float(xm) + (acc[5][0] + v[3][0]) + (acc[5][1] + v[3][1]);
  float ssq = 0.f;
#pragma unroll
  for (int t = 0; t < 3; ++t) {
    const f32x4 a = (t < 2 ? acc[t] : acc[4]) + v[t];
    const unsigned h0 = cvtpk(bflo(xr[t][0]) + a[0], bfhi(xr[t][0]) + a[1]);
    const unsigned h1 = cvtpk(bflo(xr[t][1]) + a[2], bfhi(xr[t][1]) + a[3]);
    const unsigned c0 = cvtpk(bflo(h0) - mean, bfhi(h0) - mean);
    const unsigned c1 = cvtpk(bflo(h1) - mean, bfhi(h1) - mean);
    if (t < 2 || t4) { ssq = dot2(c0, c0, ssq); ssq = dot2(c1, c1, ssq); }
    const auto r0 = __builtin_amdgcn_permlane16_swap(h0, c0, false, false);
    const auto r1 = __builtin_amdgcn_permlane16_swap(h1, c1, false, false);
    bst16<SC1>(u32x4{r0[0], r1[0], r0[1], r1[1]}, wrs, t < 2 ? so + t * 512 : (t4 ? so + 2048 : DROP), 0);
  }
  ssq = xor32_sum(xor16_sum(ssq));
  __builtin_amdgcn_raw_buffer_store_b32(__float_as_uint(ssq), wrs, (unsigned)WS_SSQ + ((2 * cg + kh) * 2048 + row) * 4, 0, SC1);
  STAMP(7); STAMP_OUT(2, 8);
}

constexpr int K4_SLOT = 36864, K4_LDS = 163840;
constexpr int K4_PF = 4, K4_POLL = 10, K5_SIG = 154;
constexpr int K5_PERM[18] = {0, 1, 2, 6, 7, 8, 9, 10, 11, 15, 16, 17, 3, 4, 5, 12, 13, 14};
constexpr int K5_R[4] = {36864, 73728, 110592, 0};
constexpr int K5_OC[9] = {65536, 102400, 139264, 147456, 155648, 0, 8192, 16384, 24576};
struct K5P { const char* wb; unsigned q[4], qs[4], qo, qos, lds0; };
template <int Z> DI void k5_piece(const K5P& k, int i, int w, unsigned lane16) {
  constexpr int P0 = K5_PERM[2 * Z] * 9216, D = (K5_PERM[2 * Z + 1] - K5_PERM[2 * Z]) * 9216;
  if (i < 3 || (w >> 2) == (Z & 1))
    dma16(k.wb + (P0 + k.q[i] + k.qs[i] * D), lane16, k.lds0 + K5_R[Z & 3] + (i < 3 ? 8 * i + w : 24 + (w & 3)) * 1024);
}
template <int Z> DI void k5_own(const K5P& k, int w, unsigned lane16) {
  constexpr int P0 = K5_PERM[2 * Z] * 9216, D = (K5_PERM[2 * Z + 1] - K5_PERM[2 * Z]) * 9216;
  dma16(k.wb + (P0 + k.qo + k.qos * D), lane16, k.lds0 + K5_OC[Z] + w * 1024);
}
#define POLL(dst, p) asm volatile("global_load_dword %0, %1, %2 sc1" : "=v"(dst) : "v"(0u), "s"(p))
struct K4C {
  const char *wb, *xb; unsigned lane16, lds0; int w, dir;
  unsigned pf1, poll;
  K5P wd;
  gu32 *cntA, *cntB;
  const char *ab, *ab2; u32x4 A0[2][4];
  LDSP const char* wl[4];
  u32x4 X[2][18], wf[K4_PF + 1];
  f32x4 acc[4][4];
  float c1[2], c2[2];
  __amdgpu_buffer_rsrc_t ars; unsigned avo; int T0;
  float ea[2][2], et[2][2]; unsigned pk[2][4];
};
DI void k4_piece(const K4C& c, int tile, int p) {
  dma16(c.wb + (tile * c.dir * K4_SLOT + p * 1024), c.lane16, c.lds0 + (tile & 3) * K4_SLOT + p * 1024);
}
template <int ZW> DI void k4_issue_w(K4C& c, int i) {
  if (ZW < 9) {
    if (i == 0) k4_piece(c, c.w >> 2, 4 * ZW + (c.w & 3));
    else if ((c.w >> 2) == (ZW & 1)) k4_piece(c, 2, 4 * ZW + (c.w & 3));
  } else if (ZW == 9) { k4_piece(c, 3, 8 * i + c.w); }
}
template <int Z, int I> DI void k4_issue1(K4C& c) {
  if (I == 0) k4_issue_w<Z + 1>(c, 0);
  else if (I < 5) {
    c.X[(I - 1) >> 1][2 * Z + ((I - 1) & 1)] = gld16(c.xb + (((I - 1) >> 1) * 18432 + (2 * Z + ((I - 1) & 1)) * 1024), c.lane16);
  }
  else k4_issue_w<Z + 1>(c, 1);
}
template <int TL, int I> DI void k4_issue2(K4C& c) {
  if (I < 4) k4_piece(c, TL, 8 * I + c.w);
  else if ((c.w >> 2) == (TL & 1)) k4_piece(c, TL, 32 + (c.w & 3));
}
template <int TL, int E, int U> DI void k4_epi(K4C& c) {
  constexpr int e = U >> 2, u = U & 3, mt = e >> 2, i = e & 3, q = e & 1;
  if constexpr (U < 32) {
    if (u == 0) { const float a = c.acc[TL & 3][mt][i] * c.c1[mt]; c.ea[E][q] = a; c.et[E][q] = a * c.acc[TL & 3][2 + mt][i]; PIN(c.ea[E][q]); PIN(c.et[E][q]); }
    if (u == 1) { c.ea[E][q] = __builtin_amdgcn_exp2f(c.ea[E][q]); PIN(c.ea[E][q]); }
    if (u == 2) { c.ea[E][q] = __builtin_amdgcn_rcpf(__builtin_fmaf(c.ea[E][q], c.c2[mt], c.c2[mt])); PIN(c.ea[E][q]); }
    if (u == 3) { c.et[E][q] *= c.ea[E][q]; PIN(c.et[E][q]); if (q) { c.pk[E][e >> 1] = cvtpk(c.et[E][0], c.et[E][1]); PIN(c.pk[E][e >> 1]); } }
  } else if constexpr (U == 32) {
    auto r0 = __builtin_amdgcn_permlane16_swap(c.pk[E][0], c.pk[E][2], false, false);
    auto r1 = __builtin_amdgcn_permlane16_swap(c.pk[E][1], c.pk[E][3], false, false);
    c.pk[E][0] = r0[0]; c.pk[E][1] = r1[0]; c.pk[E][2] = r0[1]; c.pk[E][3] = r1[1];
  } else if constexpr (U == 33) {
    const int T = c.T0 + TL * c.dir;
    bst16<SC1>(u32x4{c.pk[E][0], c.pk[E][1], c.pk[E][2], c.pk[E][3]}, c.ars, c.avo, (T >> 1) * 1024 + (T & 1) * 512);
  }
}
constexpr int k4_tile(int F) { return F < 108 ? (F % 6) >> 1 : 3 + (F - 108) / 36; }
constexpr int k4_step(int F) { return F < 108 ? F / 6 : ((F - 108) % 36) >> 1; }
constexpr int k4_addr(int F) { return k4_step(F) * 2048 + (F & 1) * 1024; }
template <int F> DI void k4_mem(K4C& c) {
  constexpr int TL = k4_tile(F);
  if constexpr (F < 108) {
    constexpr int Z = F / 12, I = F % 12;
    if constexpr (Z == 0 && I < 4 && !(I & 1)) k4_issue1<2, I / 2>(c);
    if constexpr (Z < 7 && I < 8 && (I & 1)) k4_issue1<Z + 2, 2 + I / 2>(c);
    if constexpr (Z < 6 && I > 8 && (I & 1)) k4_issue1<Z + 3, (I - 9) / 2>(c);
    if constexpr (Z == 6 && I == 10) k4_issue2<3, 2>(c);
    if constexpr (Z == 7 && (I == 1 || I == 3)) k4_issue2<3, 3 + I / 2>(c);
  } else {
    constexpr int P = (F - 108) % 36;
    if constexpr (TL == 3) { if constexpr (P % 3 == 0 && P < 15) k4_issue2<4, P / 3>(c); if constexpr (P % 3 == 0 && P >= 15 && P < 30) k4_issue2<5, (P - 15) / 3>(c); }
    else if constexpr (TL + 2 < 9 && P % 4 == 0 && P < 20) k4_issue2<TL + 2, P / 4>(c);
    if constexpr (TL == 7 && P == 0) CODE_PF(c.pf1, k_ffn, k_end, 0, 2 + (c.w & 1), ((blockIdx.x >> 3) == 0 && c.w < 2) ? 128u : 0u);
    if constexpr (TL == 7 && P % 3 == 2 && P <= 11) k5_piece<0>(c.wd, P / 3, c.w, c.lane16);
    if constexpr (TL == 8 && P < 4) k5_piece<1>(c.wd, P, c.w, c.lane16);
    if constexpr (TL == 8 && P >= 4 && P < 8) k5_piece<2>(c.wd, P - 4, c.w, c.lane16);
    if constexpr (TL == 8 && P == K4_POLL) POLL(c.poll, c.cntA);
    if constexpr (TL == 8 && P == K4_POLL + 12) { asm volatile("s_waitcnt vmcnt(0)" : "+v"(c.poll), "+v"(c.pf1)); wait_count(c.cntA, c.poll, 8u); }
    if constexpr (TL == 8 && P > K4_POLL + 12 && P <= K4_POLL + 20) {
      constexpr int i = P - K4_POLL - 13, z = i >> 2, j = (i & 1) * 2 + ((i >> 1) & 1);
      asm volatile("global_load_dwordx4 %0, %1, %2 sc1" : "=v"(c.A0[z][j]) : "v"(c.lane16), "s"(((j >> 1) ? c.ab2 : c.ab) + K5_PERM[2 * z + (j & 1)] * 1024));
    }
  }
}
template <int F> DI void k4_frag(K4C& c) {
  constexpr int TL = k4_tile(F), S = k4_step(F), M = F & 1, N = F + K4_PF;
  const u32x4 a = c.wf[F % (K4_PF + 1)];
  if constexpr (F < 108) { if constexpr ((F + 4) % 3 == 0) __builtin_amdgcn_s_setprio(3 - ((F + 4) % 12) / 3); }
  else if constexpr ((F - 108 + 4) % 9 == 0) __builtin_amdgcn_s_setprio(3 - ((F - 108 + 4) % 36) / 9);
  if (S == 0) {
    G_MFMA0(c.acc[TL & 3][2 * M], a, c.X[0][S]);
    G_MFMA0(c.acc[TL & 3][2 * M + 1], a, c.X[1][S]);
  } else {
    G_MFMA(c.acc[TL & 3][3 * M], a, c.X[M][S]);
    if constexpr (F < 108) { SBAR(); k4_mem<F>(c); SBAR(); }
    if constexpr (F >= 108 && (F - 108) % 36 >= 2) {
      SBAR();
      k4_epi<(TL == 3 ? 0 : (TL == 4 ? 2 : TL - 1)), 0, (F - 108) % 36 - 2>(c);
      SBAR();
    }
    G_MFMA(c.acc[TL & 3][M + 1], a, c.X[1 - M][S]);
  }
  if constexpr (N < 108 && N % 12 == 0) {
    constexpr int Z = N / 12;
    if (Z < 8) asm volatile("s_waitcnt vmcnt(5)\n\ts_barrier" : "+v"(c.X[0][2 * Z]), "+v"(c.X[0][2 * Z + 1]), "+v"(c.X[1][2 * Z]), "+v"(c.X[1][2 * Z + 1])::"memory");
    else asm volatile("s_waitcnt vmcnt(2)\n\ts_barrier" : "+v"(c.X[0][2 * Z]), "+v"(c.X[0][2 * Z + 1]), "+v"(c.X[1][2 * Z]), "+v"(c.X[1][2 * Z + 1])::"memory");
  } else if constexpr (N >= 108 && N < 324 && (N - 108) % 36 == 0) {
    constexpr int NT = k4_tile(N);
    if (NT == 3) asm volatile("s_waitcnt vmcnt(0) lgkmcnt(0)\n\ts_barrier" ::: "memory");
    else if (NT == 8) {
      asm volatile("s_waitcnt vmcnt(0) lgkmcnt(0)\n\ts_barrier" ::: "memory");
      if (c.w == 0 && c.lane16 == 0) __hip_atomic_fetch_add(c.cntA, 1u, __ATOMIC_RELAXED, __HIP_MEMORY_SCOPE_AGENT);
    }
    else asm volatile("s_waitcnt vmcnt(4) lgkmcnt(0)\n\ts_barrier" ::: "memory");
  }
  if constexpr (N < 324) c.wf[N % (K4_PF + 1)] = ldsr16(c.wl[k4_tile(N) & 3] + k4_addr(N));
  if constexpr (F >= 108 || S == 0) k4_mem<F>(c);
  if constexpr (F >= 108) {
    constexpr int P = (F - 108) % 36;
    if constexpr (P >= 2 && TL == 3) k4_epi<1, 1, P - 2>(c);
    if constexpr (P >= 2 && TL == 4) k4_epi<3, 1, P - 2>(c);
  }
  SBAR();
}
template <int B, int... Fs> DI void k4_run(K4C& c, std::integer_sequence<int, Fs...>) { (k4_frag<B + Fs>(c), ...); }
DI void k4_last(K4C& c) {
  float a[8], t[8];
#pragma unroll
  for (int e = 0; e < 8; ++e) { a[e] = c.acc[0][e >> 2][e & 3] * c.c1[e >> 2]; t[e] = a[e] * c.acc[0][2 + (e >> 2)][e & 3]; }
#pragma unroll
  for (int e = 0; e < 8; ++e) a[e] = __builtin_amdgcn_exp2f(a[e]);
#pragma unroll
  for (int e = 0; e < 8; ++e) a[e] = __builtin_amdgcn_rcpf(__builtin_fmaf(a[e], c.c2[e >> 2], c.c2[e >> 2]));
#pragma unroll
  for (int e = 0; e < 4; ++e) c.pk[0][e] = cvtpk(t[2 * e] * a[2 * e], t[2 * e + 1] * a[2 * e + 1]);
  k4_epi<8, 0, 32>(c); k4_epi<8, 0, 33>(c);
}
DI f32x4 addbf(f32x4 a, unsigned lo, unsigned hi) { return a + f32x4{bflo(lo), bfhi(lo), bflo(hi), bfhi(hi)}; }
struct K5C {
  const char *ab, *ab2;
  LDSP const char *ra[4], *oa[3], *xr;
  LDSP char* xw;
  u32x4 A[9][4], wf[5];
  f32x4 acc[2][7], own[2][2], v[4];
  const char* hb; unsigned ho; int kq; u32x2 hr[3];
  __amdgpu_buffer_rsrc_t wrs; unsigned tw, p8s;
  gu32* cnt;
};
template <int Z, int J> DI void k5_issue(K5C& c, const K4C& k) {
  if constexpr (Z < 7 && J < 4) asm volatile("global_load_dwordx4 %0, %1, %2 sc1" : "=v"(c.A[Z + 2][J]) : "v"(k.lane16), "s"(((J >> 1) ? c.ab2 : c.ab) + K5_PERM[2 * (Z + 2) + (J & 1)] * 1024));
  if constexpr (Z < 6 && J >= 4) k5_piece<Z + 3>(k.wd, J - 4, k.w, k.lane16);
  if constexpr (Z == 6 && J >= 4) k5_own<J - 4>(k.wd, k.w, k.lane16);
  if constexpr (Z == 7 && J == 0) k5_own<4>(k.wd, k.w, k.lane16);
  if constexpr (Z == 7 && J == 1) asm volatile("global_load_dwordx2 %0, %1, %2" : "=v"(c.hr[0]) : "v"(c.ho), "s"(c.hb + c.kq * 1024));
  if constexpr (Z == 7 && J == 2) asm volatile("global_load_dwordx2 %0, %1, %2 offset:512" : "=v"(c.hr[1]) : "v"(c.ho), "s"(c.hb + c.kq * 1024));
  if constexpr (Z == 7 && J == 3) asm volatile("global_load_dwordx2 %0, %1, %2" : "=v"(c.hr[2]) : "v"(c.ho), "s"(c.hb + 4096));
  if constexpr (Z == 8 && J < 4) k5_own<5 + J>(k.wd, k.w, k.lane16);
}
template <int N> DI u32x4 k5_read(const K5C& c) {
  if constexpr (N < 126) return ldsr16(c.ra[(N / 14) & 3] + (N % 14) * 1024);
  else { constexpr int o = K5_OC[(N - 126) / 4] + ((N - 126) & 3) * 1024; return ldsr16(c.oa[o >> 16] + (o & 0xffff)); }
}
template <int F> DI void k5_mem(K5C& c, K4C& k) {
    constexpr int Z = F / 14, f = F % 14;
    if constexpr (Z == 3 && f == 1) POLL(k.poll, k.cntB);
    constexpr int slot = f == 0 ? 2 : (f == 2 ? 3 : (f == 4 ? 4 : (f == 5 ? 5 : (f == 7 ? 6 : (f == 9 ? 7 : -1)))));
    if constexpr (F == 0) { k5_issue<0, 0>(c, k); k5_issue<0, 1>(c, k); }
    if constexpr (slot >= 0) k5_issue<Z, slot>(c, k);
    if constexpr (Z < 8 && f > 10 && (f & 1)) k5_issue<Z + 1, (f - 11) / 2>(c, k);
}
template <int F> DI void k5_frag(K5C& c, K4C& k) {
  constexpr int N = F + 4;
  const u32x4 a = c.wf[F % 5];
  if constexpr (F < 126) {
    constexpr int Z = F / 14, f = F % 14, st = f / 7, j = f % 7;
    if (F < 7) { G_MFMA0(c.acc[0][j], a, c.A[Z][st]); G_MFMA0(c.acc[1][j], a, c.A[Z][2 + st]); }
    else { G_MFMA(c.acc[1][j], a, c.A[Z][2 + st]); G_MFMA(c.acc[0][j], a, c.A[Z][st]); }
    if constexpr (f == 10 && Z == 8) asm volatile("s_waitcnt vmcnt(4)\n\ts_barrier" : "+v"(c.hr[0]), "+v"(c.hr[1]), "+v"(c.hr[2])::"memory");
    if constexpr (f == 10 && Z < 8) {
      u32x4(&A)[4] = c.A[Z + 1];
      if (Z == 0) asm volatile("s_waitcnt vmcnt(9) lgkmcnt(0)\n\ts_barrier" : "+v"(A[0]), "+v"(A[1]), "+v"(A[2]), "+v"(A[3])::"memory");
      else if (Z == 3) asm volatile("s_waitcnt vmcnt(4) lgkmcnt(0)\n\ts_barrier" : "+v"(A[0]), "+v"(A[1]), "+v"(A[2]), "+v"(A[3]), "+v"(k.poll)::"memory");
      else if (Z < 7) asm volatile("s_waitcnt vmcnt(7) lgkmcnt(0)\n\ts_barrier" : "+v"(A[0]), "+v"(A[1]), "+v"(A[2]), "+v"(A[3])::"memory");
      else asm volatile("s_waitcnt vmcnt(8) lgkmcnt(0)\n\ts_barrier" : "+v"(A[0]), "+v"(A[1]), "+v"(A[2]), "+v"(A[3])::"memory");
      if (Z == 1 && k.w == 0 && k.lane16 == 0) __hip_atomic_fetch_add(k.cntB, 1u, __ATOMIC_RELAXED, __HIP_MEMORY_SCOPE_AGENT);
      if (Z == 3) wait_count(k.cntB, k.poll, 8u);
    }
    c.wf[N % 5] = k5_read<N>(c);
    k5_mem<F>(c, k);
  } else {
    constexpr int B = F - 126, z = B / 4, st = (B >> 1) & 1, t = B & 1;
    if (B < 2) { G_MFMA0(c.own[0][t], a, c.A[z][st]); G_MFMA0(c.own[1][t], a, c.A[z][2 + st]); }
    else { G_MFMA(c.own[1][t], a, c.A[z][2 + st]); G_MFMA(c.own[0][t], a, c.A[z][st]); }
    if constexpr (F == 131) asm volatile("s_waitcnt vmcnt(0) lgkmcnt(0)\n\ts_barrier" ::: "memory");
    if constexpr (F == K5_SIG) {
      asm volatile("s_waitcnt vmcnt(0)\n\ts_barrier" ::: "memory");
      if (k.w == 0 && k.lane16 == 0) __hip_atomic_fetch_add(c.cnt, 1u, __ATOMIC_RELAXED, __HIP_MEMORY_SCOPE_AGENT);
    }
    if constexpr (N < 162) c.wf[N % 5] = k5_read<N>(c);
  }
  if constexpr (F >= 123 && F < 130) { PIN(c.acc[1][F - 123]); *(LDSP f32x4*)(c.xw + (F - 123) * 1024) = c.acc[1][F - 123]; }
  if constexpr (F == 131) { _Pragma("unroll") for (int i = 0; i < 4; ++i) c.v[i] = *(LDSP const f32x4*)(c.xr + i * 1024); }
  if constexpr (F == 133 || F == 135 || F == 138) {
    constexpr int j = F == 133 ? 0 : (F == 135 ? 1 : 2), o = 2 * (j & 1);
    const f32x4 x = c.acc[0][2 * j] + c.v[o], y = c.acc[0][2 * j + 1] + c.v[o + 1];
    bst16<SC1>(u32x4{cvtpk(x[0], x[1]), cvtpk(x[2], x[3]), cvtpk(y[0], y[1]), cvtpk(y[2], y[3])}, c.wrs, k.lane16,
              (unsigned)WS_PART + ((((c.kq + j + 1) & 3) * 512 + c.tw) * 3 + j) * 1024);
    if constexpr (F == 135) { _Pragma("unroll") for (int i = 0; i < 3; ++i) c.v[i] = *(LDSP const f32x4*)(c.xr + (4 + i) * 1024); }
  }
  if constexpr (F == 140) {
    c.acc[0][6] += c.v[2];
    __builtin_amdgcn_raw_buffer_store_b64(u32x2{cvtpk(c.acc[0][6][0], c.acc[0][6][1]), cvtpk(c.acc[0][6][2], c.acc[0][6][3])}, c.wrs, c.p8s, c.kq * 512, SC1);
    PIN(c.acc[0][6]);
  }
  SBAR();
}
template <int B, int... Fs> DI void k5_run(K5C& c, K4C& k, std::integer_sequence<int, Fs...>) { (k5_frag<B + Fs>(c, k), ...); }

extern "C" __global__ __launch_bounds__(512, 2) void k_ffn() {
  extern __shared__ __attribute__((aligned(16))) char smem[];
  char* ws = (char*)((cptr_t)(uintptr_t)g_ptr)[4]; char* out = (char*)((cptr_t)(uintptr_t)g_ptr)[5];
  asm volatile("" : "+s"(out), "+s"(ws));
  LDSP char* lds = (LDSP char*)smem;
  const int tid = threadIdx.x, lane = tid & 63, w = rfl(tid >> 6);
  const int xcd = blockIdx.x & 7, bi = blockIdx.x >> 3;
  const int m = lane & 15, g = lane >> 4, kq = xcd >> 1;
  STAMP_DECL; STAMP(0);
  K4C c;
  {
    const int rb = 4 * (xcd & 1) + (bi & 3), fs = 8 * kq + (bi >> 2), mt = 16 * rb + 2 * w;
    c.lds0 = (unsigned)(uintptr_t)lds; c.lane16 = lane * 16; c.w = w;
    c.T0 = 9 * fs + 8 * (fs & 1); c.dir = 1 - 2 * (fs & 1);
    c.wb = ws + WS_W1 + (size_t)c.T0 * K4_SLOT;
    c.xb = ws + WS_HC + (size_t)mt * 18432;
    k4_issue_w<0>(c, 0); k4_issue1<0, 1>(c); k4_issue1<0, 3>(c); k4_issue_w<0>(c, 1); k4_issue1<0, 2>(c); k4_issue1<0, 4>(c);
    k4_issue1<0, 0>(c); k4_issue1<0, 5>(c);
    unsigned sq[8];
#pragma unroll
    for (int i = 0; i < 8; ++i) sq[i] = gld4(ws + WS_SSQ + (i >> 1) * 8192 + (i & 1) * 64, 4 * g * 8192 + (16 * mt + m) * 4);
    k4_issue1<1, 0>(c); k4_issue1<1, 1>(c); k4_issue1<1, 2>(c); k4_issue1<1, 3>(c); k4_issue1<1, 4>(c); k4_issue1<1, 5>(c);
    SBAR();
#pragma unroll
    for (int i = 0; i < 4; ++i) { c.wl[i] = lds + c.lane16 + i * K4_SLOT; PIN(c.wl[i]); }
    c.ars = mkrsrc(ws + WS_ACT, 128u * 144 * 1024);
    c.avo = (mt + (g & 1)) * (144 * 1024) + (g >> 1) * 256 + m * 16;
    c.wd.wb = ws + WS_WD + ((size_t)(bi >> 3) * 144 + 36 * kq) * 9 * 1024;
#pragma unroll
    for (int i = 0; i < 4; ++i) {
      const int p = i < 3 ? 8 * i + w : 24 + (w & 3), st = (p * 37) >> 8, t = p - 7 * st + 2;
      c.wd.q[i] = ((st >> 1) * 162 + (t < 8 ? (t + 2 * kq) & 7 : 8)) * 1024;
      c.wd.qs[i] = st & 1;
      asm volatile("" : "+s"(c.wd.q[i]), "+s"(c.wd.qs[i]));
    }
    c.wd.qo = ((w >> 2) * 162 + 2 * kq + (w & 1)) * 1024; c.wd.qos = (w >> 1) & 1; c.wd.lds0 = c.lds0;
    c.cntA = (gu32*)(ws + WS_CNT + (64 + rb * 4 + kq) * 128);
    STAMP(1);
    asm volatile("s_waitcnt vmcnt(15)\n\ts_barrier" : "+v"(c.X[0][0]), "+v"(c.X[0][1]), "+v"(c.X[1][0]), "+v"(c.X[1][1])::"memory");
#pragma unroll
    for (int i = 0; i < K4_PF; ++i) c.wf[i] = ldsr16(c.wl[k4_tile(i)] + k4_addr(i));
    SBAR();
    k4_run<0>(c, std::make_integer_sequence<int, 12>{});
    asm volatile("" : "+v"(sq[0]), "+v"(sq[1]), "+v"(sq[2]), "+v"(sq[3]), "+v"(sq[4]), "+v"(sq[5]), "+v"(sq[6]), "+v"(sq[7]));
#pragma unroll
    for (int t = 0; t < 2; ++t) {
      float ss = (__uint_as_float(sq[t]) + __uint_as_float(sq[2 + t])) + (__uint_as_float(sq[4 + t]) + __uint_as_float(sq[6 + t]));
      ss = xor32_sum(xor16_sum(ss));
      const float v = ss * (1.0f / DM) + LN_EPS, rs = __builtin_amdgcn_rsqf(v);
      c.c1[t] = -LOG2E * rs; c.c2[t] = -LOG2E * (v * rs);
      PIN(c.c1[t]); PIN(c.c2[t]);
    }
    SBAR();
    k4_run<12>(c, std::make_integer_sequence<int, 276>{});
    c.cntB = c.cntA + 32 * 32;
  }
  const int rb = 8 * (xcd & 1) + (bi & 7), nb = bi >> 3, mg = w & 3, kh = w >> 2, mu = 8 * rb + 2 * mg + kh;
  gu32* prodB = c.cntB;
  c.cntA = (gu32*)(ws + WS_CNT + (64 + (rb >> 1) * 4 + kq) * 128);
  c.ab = ws + WS_ACT + ((size_t)mu * 144 + 36 * kq + 18 * kh) * 1024;
  c.ab2 = ws + WS_ACT + ((size_t)(mu ^ 1) * 144 + 36 * kq + 18 * kh) * 1024;
  k4_run<288>(c, std::make_integer_sequence<int, 36>{});
  STAMP(2);
  K5C d;
  d.ab = c.ab; d.ab2 = c.ab2;
  asm volatile("s_nop 7" : "+v"(c.acc[0][0]), "+v"(c.acc[0][1]), "+v"(c.acc[0][2]), "+v"(c.acc[0][3]));
  k4_last(c);
  SBAR();
  gu32* consB = c.cntA + 32 * 32;
  d.hb = ws + WS_HU + (size_t)mu * 18432 + nb * 4608; d.ho = (g >> 1) * 256 + m * 16 + (g & 1) * 8; d.kq = kq;
#pragma unroll
  for (int i = 0; i < 4; ++i) { d.ra[i] = lds + K5_R[i] + kh * 14336 + c.lane16; PIN(d.ra[i]); }
#pragma unroll
  for (int i = 0; i < 3; ++i) { d.oa[i] = lds + i * 65536 + kh * 4096 + c.lane16; PIN(d.oa[i]); }
  d.xw = lds + (kh ? K5_R[2] : K5_R[1]) + mg * 7168 + c.lane16; PIN(d.xw);
  d.xr = lds + (kh ? K5_R[1] : K5_R[2]) + mg * 7168 + c.lane16; PIN(d.xr);
  const __amdgpu_buffer_rsrc_t wrs = mkrsrc(ws, (unsigned)WS_END), ors = mkrsrc(out, SEQ * DM * 2);
  const int tile = rb * 4 + nb;
  const bool own8 = (mu & 3) == kq;
  const unsigned tw = tile * 8 + w;
  const unsigned p8 = (unsigned)WS_PART8 + tw * 2048 + lane * 8;
  d.cnt = (gu32*)(ws + WS_CNT + tile * 128);
  d.wrs = wrs; d.tw = tw; d.p8s = own8 ? DROP : p8; PIN(d.p8s);
#pragma unroll
  for (int i = 0; i < 4; ++i) { d.A[0][i] = c.A0[0][i]; d.A[1][i] = c.A0[1][i]; }
  asm volatile("s_waitcnt vmcnt(6) lgkmcnt(0)\n\ts_barrier" : "+v"(d.A[0][0]), "+v"(d.A[0][1]), "+v"(d.A[0][2]), "+v"(d.A[0][3])::"memory");
  STAMP(3);
  d.wf[0] = k5_read<0>(d); d.wf[1] = k5_read<1>(d); d.wf[2] = k5_read<2>(d); d.wf[3] = k5_read<3>(d);
  SBAR();
  c.cntB = prodB;
  k5_run<0>(d, c, std::make_integer_sequence<int, 28>{});
  c.cntB = consB;
  k5_run<28>(d, c, std::make_integer_sequence<int, 134>{});
  asm volatile("s_nop 7" : "+v"(d.own[0][0]), "+v"(d.own[0][1]), "+v"(d.own[1][0]), "+v"(d.own[1][1]));
  STAMP(4);
  LDSP char* xo = lds + K5_R[0] + c.lane16;
  *(LDSP f32x4*)(xo + w * 2048) = d.own[1][0];
  *(LDSP f32x4*)(xo + w * 2048 + 1024) = d.own[1][1];
  unsigned oo = (16 * mu + m) * 1152 + nb * 288;
  unsigned oo8 = own8 ? oo + 256 + g * 8 : DROP;
  oo += kq * 64 + (g & 1) * 32 + (g >> 1) * 16;
  PIN(oo); PIN(oo8);
  STAMP(5);
  if (w == 0) wait_count(d.cnt, 0u, 4u);
  asm volatile("s_waitcnt lgkmcnt(0)\n\ts_barrier" ::: "memory");
  STAMP(6);
  u32x4 r[3]; u32x2 r8[3];
#pragma unroll
  for (int j = 0; j < 3; ++j) {
    r[j] = __builtin_amdgcn_raw_buffer_load_b128(wrs, c.lane16, (unsigned)WS_PART + ((kq * 512 + tw) * 3 + j) * 1024, SC1);
    r8[j] = __builtin_amdgcn_raw_buffer_load_b64(wrs, own8 ? p8 : DROP, ((kq + 1 + j) & 3) * 512, SC1);
  }
  SBAR();
  f32x4 e0 = addbf(d.own[0][0] + *(LDSP const f32x4*)(xo + (w ^ 4) * 2048), d.hr[0][0], d.hr[0][1]);
  f32x4 e1 = addbf(d.own[0][1] + *(LDSP const f32x4*)(xo + (w ^ 4) * 2048 + 1024), d.hr[1][0], d.hr[1][1]);
  f32x4 e8 = addbf(d.acc[0][6], d.hr[2][0], d.hr[2][1]);
#pragma unroll
  for (int j = 0; j < 3; ++j) { e0 = addbf(e0, r[j][0], r[j][1]); e1 = addbf(e1, r[j][2], r[j][3]); e8 = addbf(e8, r8[j][0], r8[j][1]); }
  {
    const auto s0 = __builtin_amdgcn_permlane16_swap(cvtpk(e0[0], e0[1]), cvtpk(e1[0], e1[1]), false, false);
    const auto s1 = __builtin_amdgcn_permlane16_swap(cvtpk(e0[2], e0[3]), cvtpk(e1[2], e1[3]), false, false);
    bst16<SC1>(u32x4{s0[0], s1[0], s0[1], s1[1]}, ors, oo, 0);
    __builtin_amdgcn_raw_buffer_store_b64(u32x2{cvtpk(e8[0], e8[1]), cvtpk(e8[2], e8[3])}, ors, oo8, 0, SC1);
  }
  STAMP(7);
  STAMP_OUT(4, 8);
}
extern "C" __global__ void k_end() {}

extern "C" void kernel_launch(void* const* d_in, const int* in_sizes, int n_in, void* d_out, int out_size, void* d_ws, size_t ws_size,
                              hipStream_t stream) {
  const u16* x = (const u16*)d_in[0];
  const u16* wqkv = (const u16*)d_in[1];
  const u16* wo = (const u16*)d_in[2];
  const u16* wg = (const u16*)d_in[4];
  const u16* wu = (const u16*)d_in[5];
  const u16* wd = (const u16*)d_in[6];
  char* ws = (char*)d_ws;
  (void)hipFuncSetAttribute((const void*)k_qkv, hipFuncAttributeMaxDynamicSharedMemorySize, K1_LDS);
  (void)hipFuncSetAttribute((const void*)k_attn, hipFuncAttributeMaxDynamicSharedMemorySize, K2_LDS);
  (void)hipFuncSetAttribute((const void*)k_ffn, hipFuncAttributeMaxDynamicSharedMemorySize, K4_LDS);
  k_qkv<<<256, 768, K1_LDS, stream>>>(x, wqkv, wo, ws, wg, wu, wd, (char*)d_out);
  k_attn<<<256, 768, K2_LDS, stream>>>();
  k_ffn<<<256, 512, K4_LDS, stream>>>();
}
```
